```python
import torch

def _softmax(x, axis=-1):
    return torch.softmax(x, dim=axis)
def _gelu(x):
    return torch.nn.functional.gelu(x, approximate="tanh")
def _silu(x):
    return torch.nn.functional.silu(x)
def _sigmoid(x):
    return torch.sigmoid(x)
def _layernorm(x, axis=-1, eps=1e-5):
    m = x.mean(dim=axis, keepdim=True)
    v = x.var(dim=axis, keepdim=True, unbiased=False)
    return (x - m) / torch.sqrt(v + eps)
def _scatter_add(tgt, idx, src, axis=0):
    return tgt.clone().index_add_(axis, idx, src)
def _segment_sum(src, seg, n):
    out = torch.zeros((n,) + src.shape[1:], dtype=src.dtype)
    return out.index_add_(0, seg, src)
def _pad(x, pw):
    flat = tuple(v for pair in reversed(pw) for v in pair)
    return torch.nn.functional.pad(x, flat)

kernel_name = 'recurrent_scan__gqa_block_cached__glu_gated_swiglu__prompt'

def setup_inputs():

    _w = (lambda a: (lambda t: t.to(torch.bfloat16) if t.is_floating_point() else t)(torch.as_tensor(a)))
    def _A_setup():
        B, Sq, D = 1, 2048, 576
        Hq, Hkv, Sk, Dh = 32, 16, 2048, 128
        assert Hq % Hkv == 0, (Hq, Hkv)
        return {
            "x": _w((lambda *s: torch.randn(*s, dtype=torch.bfloat16) * 0.05)(B, Sq, D)),
            "k_cache": _w((lambda *s: torch.randn(*s, dtype=torch.bfloat16) * 1.6)(B, Hkv, Sk, Dh)),
            "v_cache": _w((lambda *s: torch.randn(*s, dtype=torch.bfloat16) * 1.6)(B, Hkv, Sk, Dh)),
            "wq": _w((lambda *s: torch.randn(*s, dtype=torch.bfloat16) * 1.6)(D, Hq * Dh) * (D ** -0.5)),
            "wo": _w((lambda *s: torch.randn(*s, dtype=torch.bfloat16) * 1.6)(Hq * Dh, D) * ((Hq * Dh) ** -0.5)),
        }
    _A_in = _A_setup()

    def _B_setup():
        B, S, D, F = 1, 2048, 576, 4608
        return {
            "x": _w((lambda *s: torch.randn(*s, dtype=torch.bfloat16) * 0.05)(B, S, D)),
            "w1": _w((lambda *s: torch.randn(*s, dtype=torch.bfloat16) * 0.2)(D, F) * (2.0 / D) ** 0.5),
            "w3": _w((lambda *s: torch.randn(*s, dtype=torch.bfloat16) * 0.2)(D, F) * (2.0 / D) ** 0.5),
            "w2": _w((lambda *s: torch.randn(*s, dtype=torch.bfloat16) * 0.2)(F, D) * (2.0 / F) ** 0.5),
        }
    _B_in = _B_setup()

    return {
        "A_x": _A_in["x"],
        "A_k_cache": _A_in["k_cache"],
        "A_v_cache": _A_in["v_cache"],
        "A_wq": _A_in["wq"],
        "A_wo": _A_in["wo"],
        "B_x": _B_in["x"],
        "B_w1": _B_in["w1"],
        "B_w3": _B_in["w3"],
        "B_w2": _B_in["w2"],
    }

def reference(A_x, A_k_cache, A_v_cache, A_wq, A_wo, B_x, B_w1, B_w3, B_w2):
    def _A_ref(x, k_cache, v_cache, wq, wo):
        B, Sq, D = x.shape
        Hq, Dh = 32, 128
        Hkv = k_cache.shape[1]
        rep = Hq // Hkv
        q = (lambda x, *d: x.permute(*d))((x @ wq).reshape(B, Sq, Hq, Dh), 0, 2, 1, 3)
        k = (lambda x, n, axis: torch.repeat_interleave(x, n, dim=axis))(k_cache, rep, axis=1)
        v = (lambda x, n, axis: torch.repeat_interleave(x, n, dim=axis))(v_cache, rep, axis=1)
        scores = torch.einsum("bhqd,bhkd->bhqk", q, k) * (Dh ** -0.5)
        p = _softmax(scores, axis=-1)
        o = torch.einsum("bhqk,bhkd->bhqd", p, v)
        return (lambda x, *d: x.permute(*d))(o, 0, 2, 1, 3).reshape(B, Sq, Hq * Dh) @ wo

    def _B_ref(x, w1, w3, w2):
        gate = torch.einsum("bsd,df->bsf", x, w1)
        up = torch.einsum("bsd,df->bsf", x, w3)
        h = _silu(gate) * up
        out = torch.einsum("bsf,fd->bsd", h, w2)
        return out

    x = A_x
    xn = _layernorm(x, axis=-1)
    s = _A_ref(xn, A_k_cache, A_v_cache, A_wq, A_wo)
    g = _silu(_B_ref(xn, B_w1, B_w3, B_w2))
    return x + s * g
```

```cpp
#include <hip/hip_runtime.h>
#include <hip/hip_bf16.h>
#include <stdint.h>
typedef unsigned short u16;
constexpr int S = 2048, D = 576, HQ = 32, HKV = 16, DH = 128, F = 4608, QD = 4096;
__device__ __forceinline__ float bf2f(u16 b) { return __uint_as_float(((unsigned)b) << 16); }
__device__ __forceinline__ u16 f2bf(float f) { __bf16 b = (__bf16)f; return __builtin_bit_cast(u16, b); }
__device__ __forceinline__ float rbf(float f) { return bf2f(f2bf(f)); }
__device__ __forceinline__ size_t fidx(int t, int k, int nks) {
  int tb = t >> 5, r = t & 31, ks = k >> 4, kk = k & 15;
  int h = (kk >> 2) & 1, j = (kk & 3) | ((kk >> 3) << 2);
  return (((size_t)tb * nks + ks) * 64 + h * 32 + r) * 8 + j;
}
__global__ void k_ln(const u16* x, u16* xn) {
  int row = blockIdx.x * 4 + (threadIdx.x >> 6), lane = threadIdx.x & 63;
  float v[9]; float s = 0;
  for (int i = 0; i < 9; ++i) { v[i] = bf2f(x[row * D + lane + 64 * i]); s += v[i]; }
  for (int o = 32; o >= 1; o >>= 1) s += __shfl_xor(s, o);
  float mean = s * (1.0f / D); float q = 0;
  for (int i = 0; i < 9; ++i) { float d = v[i] - mean; q += d * d; }
  for (int o = 32; o >= 1; o >>= 1) q += __shfl_xor(q, o);
  float mb = rbf(mean), vb = rbf(q * (1.0f / D));
  float den = rbf(sqrtf(rbf(vb + 1e-5f)));
  for (int i = 0; i < 9; ++i) xn[fidx(row, lane + 64 * i, 36)] = f2bf(rbf(v[i] - mb) / den);
}
__global__ void k_gateup(const u16* xn, const u16* w1, const u16* w3, u16* h) {
  int f = blockIdx.x * 256 + threadIdx.x, t = blockIdx.y;
  __shared__ float xs[D];
  for (int k = threadIdx.x; k < D; k += 256) xs[k] = bf2f(xn[fidx(t, k, 36)]);
  __syncthreads();
  float g = 0, u = 0;
  for (int k = 0; k < D; ++k) { g += xs[k] * bf2f(w1[(size_t)k * F + f]); u += xs[k] * bf2f(w3[(size_t)k * F + f]); }
  float sg = g / (1.f + __expf(-g));
  h[fidx(t, f, 288)] = f2bf(sg * u);
}
__global__ void k_qproj(const u16* xn, const u16* wq, u16* q) {
  int n = blockIdx.x * 256 + threadIdx.x, t = blockIdx.y;
  __shared__ float xs[D];
  for (int k = threadIdx.x; k < D; k += 256) xs[k] = bf2f(xn[fidx(t, k, 36)]);
  __syncthreads();
  float g = 0;
  for (int k = 0; k < D; ++k) g += xs[k] * bf2f(wq[(size_t)k * QD + n]);
  q[(size_t)t * QD + n] = f2bf(g * 0.12751743082f);
}
__global__ void k_attn(const u16* q, const u16* kc, const u16* vc, u16* o) {
  int t = blockIdx.x, hq = blockIdx.y, hk = hq >> 1, tid = threadIdx.x;
  __shared__ float qs[DH]; __shared__ float p[S]; __shared__ float red[256];
  if (tid < DH) qs[tid] = bf2f(q[(size_t)t * QD + hq * DH + tid]);
  __syncthreads();
  float m = -1e30f;
  for (int key = tid; key < S; key += 256) { const u16* kr = kc + ((size_t)hk * S + key) * DH; float s = 0;
    for (int d = 0; d < DH; ++d) s += qs[d] * bf2f(kr[d]); p[key] = s; m = fmaxf(m, s); }
  red[tid] = m; __syncthreads();
  for (int o2 = 128; o2 >= 1; o2 >>= 1) { if (tid < o2) red[tid] = fmaxf(red[tid], red[tid + o2]); __syncthreads(); }
  m = red[0]; __syncthreads();
  float l = 0;
  for (int key = tid; key < S; key += 256) { float e = exp2f(p[key] - m); l += e; p[key] = rbf(e); }
  red[tid] = l; __syncthreads();
  for (int o2 = 128; o2 >= 1; o2 >>= 1) { if (tid < o2) red[tid] += red[tid + o2]; __syncthreads(); }
  l = red[0];
  if (tid < DH) { float a = 0; for (int key = 0; key < S; ++key) a += p[key] * bf2f(vc[((size_t)hk * S + key) * DH + tid]);
    o[fidx(t, hq * DH + tid, 256)] = f2bf(a / l); }
}
__global__ void k_out(const u16* x, const u16* o, const u16* h, const u16* wo, const u16* w2, u16* out) {
  int t = blockIdx.x, n = threadIdx.x;
  __shared__ float os[QD]; __shared__ float hs[F];
  for (int k = n; k < QD; k += D) os[k] = bf2f(o[fidx(t, k, 256)]);
  for (int k = n; k < F; k += D) hs[k] = bf2f(h[fidx(t, k, 288)]);
  __syncthreads();
  float s = 0, f = 0;
  for (int k = 0; k < QD; ++k) s += os[k] * bf2f(wo[(size_t)k * D + n]);
  for (int k = 0; k < F; ++k) f += hs[k] * bf2f(w2[(size_t)k * D + n]);
  float g = f / (1.f + __expf(-f));
  out[(size_t)t * D + n] = f2bf(bf2f(x[(size_t)t * D + n]) + s * g);
}
extern "C" void kernel_launch(void* const* d_in, const int* in_sizes, int n_in, void* d_out, int out_size, void* d_ws, size_t ws_size, hipStream_t stream) {
  const u16* x = (const u16*)d_in[0]; const u16* kc = (const u16*)d_in[1]; const u16* vc = (const u16*)d_in[2];
  const u16* wq = (const u16*)d_in[3]; const u16* wo = (const u16*)d_in[4];
  const u16* w1 = (const u16*)d_in[6]; const u16* w3 = (const u16*)d_in[7]; const u16* w2 = (const u16*)d_in[8];
  char* ws = (char*)d_ws;
  u16* xn = (u16*)ws; u16* h = (u16*)(ws + (4u << 20)); u16* o = (u16*)(ws + (32u << 20)); u16* q = (u16*)(ws + (64u << 20));
  k_ln<<<S / 4, 256, 0, stream>>>(x, xn);
  k_gateup<<<dim3(F / 256, S), 256, 0, stream>>>(xn, w1, w3, h);
  k_qproj<<<dim3(QD / 256, S), 256, 0, stream>>>(xn, wq, q);
  k_attn<<<dim3(S, HQ), 256, 0, stream>>>(q, kc, vc, o);
  k_out<<<S, D, 0, stream>>>(x, o, h, wo, w2, (u16*)d_out);
}
```

```cpp
#include <hip/hip_runtime.h>

extern "C" __global__ __launch_bounds__(256, 1) void k_main(const void* x, const void* k, const void* v, const void* wq, const void* wo, const void* w1, const void* w3, const void* w2, void* ws, void* out) {
  __shared__ __attribute__((aligned(16))) char lds[163840];
  asm volatile(
"KSTART_%=:\n"
"v_mov_b32 v4, %10\n"
"s_mov_b32 s73, %11\n"
"v_and_b32 v5, 63, v4\n"
"v_lshrrev_b32 v122, 6, v4\n"
"s_nop 0\n"
"v_readfirstlane_b32 s72, v122\n"
"s_and_b32 s76, s73, 7\n"
"s_lshr_b32 s77, s73, 3\n"
"s_and_b32 s98, s77, 3\n"
"s_lshl_b32 s99, s76, 2\n"
"s_add_u32 s74, s99, s98\n"
"s_lshr_b32 s75, s77, 2\n"
"s_mov_b64 s[76:77], %0\n"
"s_mov_b32 s56, s76\n"
"s_and_b32 s57, s77, 0xffff\n"
"s_mov_b32 s58, 0x240000\n"
"s_mov_b32 s59, 0x20000\n"
"s_lshl_b32 s76, s75, 8\n"
"s_lshl_b32 s77, s74, 3\n"
"s_add_u32 s76, s76, s77\n"
"s_lshl_b32 s77, s72, 1\n"
"s_add_u32 s76, s76, s77\n"
"s_mul_i32 s76, s76, 0x480\n"
"v_lshrrev_b32 v26, 5, v5\n"
"v_mul_u32_u24 v27, 0x480, v26\n"
"v_and_b32 v28, 31, v5\n"
"v_lshl_add_u32 v27, v28, 3, v27\n"
"v_cmp_gt_u32 vcc, 16, v28\n"
"v_mov_b32 v29, 0x7ff00000\n"
"s_nop 0\n"
"v_cndmask_b32 v29, v29, v27\n"
"buffer_load_dwordx2 v[16:17], v27, s[56:59], s76 offen\n"
"buffer_load_dwordx2 v[18:19], v27, s[56:59], s76 offen offset:256\n"
"buffer_load_dwordx2 v[20:21], v27, s[56:59], s76 offen offset:512\n"
"buffer_load_dwordx2 v[22:23], v27, s[56:59], s76 offen offset:768\n"
"buffer_load_dwordx2 v[24:25], v29, s[56:59], s76 offen offset:1024\n"
"s_getpc_b64 s[52:53]\n"
"PCREF_%=:\n"
"s_sub_u32 s52, s52, PCREF_%=-KSTART_%=\n"
"s_subb_u32 s53, s53, 0\n"
"s_and_b32 s53, s53, 0xffff\n"
"s_mov_b32 s54, KEND_%=-KSTART_%=\n"
"s_mov_b32 s55, 0x20000\n"
"s_lshr_b32 s76, %11, 3\n"
"s_lshl_b32 s76, s76, 2\n"
"s_add_u32 s76, s76, s72\n"
"s_lshl_b32 s76, s76, 13\n"
"v_lshlrev_b32 v14, 7, v5\n"
"v_add_u32 v14, s76, v14\n"
"buffer_load_dword v11, v14, s[52:55], 0 offen\n"
"s_mov_b64 s[92:93], %8\n"
"s_lshl_b32 s76, s75, 8\n"
"v_mov_b32 v12, s76\n"
"global_load_dwordx2 v[8:9], v12, s[92:93] sc1\n"
"v_mov_b32 v13, 0x200000\n"
"global_load_dword v11, v13, s[92:93]\n"
"s_and_b32 s76, s73, 7\n"
"s_lshr_b32 s77, s73, 3\n"
"s_and_b32 s98, s77, 3\n"
"s_mov_b32 s80, s76\n"
"s_mov_b32 s82, s98\n"
"s_lshl_b32 s83, s75, 2\n"
"s_add_u32 s83, s83, s82\n"
"s_lshr_b32 s81, s80, 1\n"
"s_and_b32 s73, s80, 1\n"
"s_xor_b32 s73, s73, 1\n"
"s_mov_b64 s[76:77], %8\n"
"s_add_u32 s76, s76, 0x200000\n"
"s_addc_u32 s77, s77, 0\n"
"s_mov_b32 s64, s76\n"
"s_and_b32 s65, s77, 0xffff\n"
"s_mov_b32 s66, 0x240000\n"
"s_mov_b32 s67, 0x20000\n"
"s_lshl_b32 s76, s75, 4\n"
"s_lshl_b32 s77, s72, 2\n"
"s_add_u32 s91, s76, s77\n"
"s_mul_i32 s90, s91, 0x4800\n"
"v_lshlrev_b32 v121, 4, v5\n"
"s_mov_b64 s[98:99], %5\n"
"s_mov_b64 s[76:77], %6\n"
"s_cmp_ge_u32 s72, 2\n"
"s_cselect_b32 s60, s76, s98\n"
"s_cselect_b32 s61, s77, s99\n"
"s_and_b32 s61, s61, 0xffff\n"
"s_mov_b32 s62, 0x510000\n"
"s_mov_b32 s63, 0x20000\n"
"s_and_b32 s76, s72, 1\n"
"s_mul_i32 s77, s76, 0x48000\n"
"s_mul_i32 s79, s74, 0x120\n"
"s_add_u32 s79, s79, s77\n"
"s_mul_i32 s78, s72, 0x2400\n"
"v_mov_b32 v125, 0xe38f\n"
"v_mov_b32 v114, 0x2400\n"
"v_lshlrev_b32 v122, 4, v5\n"
"v_add_u32 v122, 0, v122\n"
"v_mul_lo_u32 v123, v125, v122\n"
"v_lshrrev_b32 v123, 24, v123\n"
"v_mul_u32_u24 v124, 0x120, v123\n"
"v_sub_u32 v122, v122, v124\n"
"v_mul_lo_u32 v123, v114, v123\n"
"v_add_u32 v104, v122, v123\n"
"v_lshlrev_b32 v122, 4, v5\n"
"v_add_u32 v122, 0x400, v122\n"
"v_mul_lo_u32 v123, v125, v122\n"
"v_lshrrev_b32 v123, 24, v123\n"
"v_mul_u32_u24 v124, 0x120, v123\n"
"v_sub_u32 v122, v122, v124\n"
"v_mul_lo_u32 v123, v114, v123\n"
"v_add_u32 v105, v122, v123\n"
"v_lshlrev_b32 v122, 4, v5\n"
"v_add_u32 v122, 0x800, v122\n"
"v_mul_lo_u32 v123, v125, v122\n"
"v_lshrrev_b32 v123, 24, v123\n"
"v_mul_u32_u24 v124, 0x120, v123\n"
"v_sub_u32 v122, v122, v124\n"
"v_mul_lo_u32 v123, v114, v123\n"
"v_add_u32 v106, v122, v123\n"
"v_lshlrev_b32 v122, 4, v5\n"
"v_add_u32 v122, 0xc00, v122\n"
"v_mul_lo_u32 v123, v125, v122\n"
"v_lshrrev_b32 v123, 24, v123\n"
"v_mul_u32_u24 v124, 0x120, v123\n"
"v_sub_u32 v122, v122, v124\n"
"v_mul_lo_u32 v123, v114, v123\n"
"v_add_u32 v107, v122, v123\n"
"v_lshlrev_b32 v122, 4, v5\n"
"v_add_u32 v122, 0x1000, v122\n"
"v_mul_lo_u32 v123, v125, v122\n"
"v_lshrrev_b32 v123, 24, v123\n"
"v_mul_u32_u24 v124, 0x120, v123\n"
"v_sub_u32 v122, v122, v124\n"
"v_mul_lo_u32 v123, v114, v123\n"
"v_add_u32 v108, v122, v123\n"
"v_lshlrev_b32 v122, 4, v5\n"
"v_add_u32 v122, 0x1400, v122\n"
"v_mul_lo_u32 v123, v125, v122\n"
"v_lshrrev_b32 v123, 24, v123\n"
"v_mul_u32_u24 v124, 0x120, v123\n"
"v_sub_u32 v122, v122, v124\n"
"v_mul_lo_u32 v123, v114, v123\n"
"v_add_u32 v109, v122, v123\n"
"v_lshlrev_b32 v122, 4, v5\n"
"v_add_u32 v122, 0x1800, v122\n"
"v_mul_lo_u32 v123, v125, v122\n"
"v_lshrrev_b32 v123, 24, v123\n"
"v_mul_u32_u24 v124, 0x120, v123\n"
"v_sub_u32 v122, v122, v124\n"
"v_mul_lo_u32 v123, v114, v123\n"
"v_add_u32 v110, v122, v123\n"
"v_lshlrev_b32 v122, 4, v5\n"
"v_add_u32 v122, 0x1c00, v122\n"
"v_mul_lo_u32 v123, v125, v122\n"
"v_lshrrev_b32 v123, 24, v123\n"
"v_mul_u32_u24 v124, 0x120, v123\n"
"v_sub_u32 v122, v122, v124\n"
"v_mul_lo_u32 v123, v114, v123\n"
"v_add_u32 v111, v122, v123\n"
"v_lshlrev_b32 v122, 4, v5\n"
"v_add_u32 v122, 0x2000, v122\n"
"v_mul_lo_u32 v123, v125, v122\n"
"v_lshrrev_b32 v123, 24, v123\n"
"v_mul_u32_u24 v124, 0x120, v123\n"
"v_sub_u32 v122, v122, v124\n"
"v_mul_lo_u32 v123, v114, v123\n"
"v_add_u32 v112, v122, v123\n"
"s_add_u32 m0, s78, 0\n"
"s_nop 0\n"
"buffer_load_dwordx4 v104, s[60:63], s79 offen lds\n"
"s_add_u32 m0, s78, 0x400\n"
"s_nop 0\n"
"buffer_load_dwordx4 v105, s[60:63], s79 offen lds\n"
"s_add_u32 m0, s78, 0x800\n"
"s_nop 0\n"
"buffer_load_dwordx4 v106, s[60:63], s79 offen lds\n"
"s_add_u32 m0, s78, 0xc00\n"
"s_nop 0\n"
"buffer_load_dwordx4 v107, s[60:63], s79 offen lds\n"
"s_add_u32 m0, s78, 0x1000\n"
"s_nop 0\n"
"buffer_load_dwordx4 v108, s[60:63], s79 offen lds\n"
"s_add_u32 m0, s78, 0x1400\n"
"s_nop 0\n"
"buffer_load_dwordx4 v109, s[60:63], s79 offen lds\n"
"s_add_u32 m0, s78, 0x1800\n"
"s_nop 0\n"
"buffer_load_dwordx4 v110, s[60:63], s79 offen lds\n"
"s_add_u32 m0, s78, 0x1c00\n"
"s_nop 0\n"
"buffer_load_dwordx4 v111, s[60:63], s79 offen lds\n"
"s_add_u32 m0, s78, 0x2000\n"
"s_nop 0\n"
"buffer_load_dwordx4 v112, s[60:63], s79 offen lds\n"
"s_add_u32 s79, s79, 0x90000\n"
"s_waitcnt vmcnt(16)\n"
"v_lshlrev_b32 v32, 16, v16\n"
"v_and_b32 v33, 0xffff0000, v16\n"
"v_lshlrev_b32 v34, 16, v17\n"
"v_and_b32 v35, 0xffff0000, v17\n"
"s_waitcnt vmcnt(15)\n"
"v_lshlrev_b32 v36, 16, v18\n"
"v_and_b32 v37, 0xffff0000, v18\n"
"v_lshlrev_b32 v38, 16, v19\n"
"v_and_b32 v39, 0xffff0000, v19\n"
"s_waitcnt vmcnt(14)\n"
"v_lshlrev_b32 v40, 16, v20\n"
"v_and_b32 v41, 0xffff0000, v20\n"
"v_lshlrev_b32 v42, 16, v21\n"
"v_and_b32 v43, 0xffff0000, v21\n"
"s_waitcnt vmcnt(13)\n"
"v_lshlrev_b32 v44, 16, v22\n"
"v_and_b32 v45, 0xffff0000, v22\n"
"v_lshlrev_b32 v46, 16, v23\n"
"v_and_b32 v47, 0xffff0000, v23\n"
"s_waitcnt vmcnt(12)\n"
"v_lshlrev_b32 v48, 16, v24\n"
"v_and_b32 v49, 0xffff0000, v24\n"
"v_lshlrev_b32 v50, 16, v25\n"
"v_and_b32 v51, 0xffff0000, v25\n"
"v_add_f32 v52, v32, v33\n"
"v_add_f32 v53, v34, v35\n"
"v_add_f32 v52, v52, v36\n"
"v_add_f32 v53, v53, v37\n"
"v_add_f32 v52, v52, v38\n"
"v_add_f32 v53, v53, v39\n"
"v_add_f32 v52, v52, v40\n"
"v_add_f32 v53, v53, v41\n"
"v_add_f32 v52, v52, v42\n"
"v_add_f32 v53, v53, v43\n"
"v_add_f32 v52, v52, v44\n"
"v_add_f32 v53, v53, v45\n"
"v_add_f32 v52, v52, v46\n"
"v_add_f32 v53, v53, v47\n"
"v_add_f32 v52, v52, v48\n"
"v_add_f32 v53, v53, v49\n"
"v_add_f32 v52, v52, v50\n"
"v_add_f32 v53, v53, v51\n"
"v_add_f32 v52, v52, v53\n"
"s_nop 1\n"
"v_add_f32_dpp v52, v52, v52 quad_perm:[1,0,3,2] row_mask:0xf bank_mask:0xf\n"
"s_nop 1\n"
"v_add_f32_dpp v52, v52, v52 quad_perm:[2,3,0,1] row_mask:0xf bank_mask:0xf\n"
"s_nop 1\n"
"v_add_f32_dpp v52, v52, v52 row_half_mirror row_mask:0xf bank_mask:0xf\n"
"s_nop 1\n"
"v_add_f32_dpp v52, v52, v52 row_mirror row_mask:0xf bank_mask:0xf\n"
"v_mov_b32 v53, v52\n"
"s_nop 1\n"
"v_permlane16_swap_b32 v52, v53\n"
"v_add_f32 v52, v52, v53\n"
"v_mul_f32 v54, 0x3ae38e39, v52\n"
"v_sub_f32 v56, v32, v54\n"
"v_sub_f32 v57, v33, v54\n"
"v_sub_f32 v58, v34, v54\n"
"v_sub_f32 v59, v35, v54\n"
"v_sub_f32 v60, v36, v54\n"
"v_sub_f32 v61, v37, v54\n"
"v_sub_f32 v62, v38, v54\n"
"v_sub_f32 v63, v39, v54\n"
"v_sub_f32 v64, v40, v54\n"
"v_sub_f32 v65, v41, v54\n"
"v_sub_f32 v66, v42, v54\n"
"v_sub_f32 v67, v43, v54\n"
"v_sub_f32 v68, v44, v54\n"
"v_sub_f32 v69, v45, v54\n"
"v_sub_f32 v70, v46, v54\n"
"v_sub_f32 v71, v47, v54\n"
"v_sub_f32 v72, v48, v54\n"
"v_sub_f32 v73, v49, v54\n"
"v_sub_f32 v74, v50, v54\n"
"v_sub_f32 v75, v51, v54\n"
"v_cmp_gt_u32 vcc, 16, v28\n"
"s_nop 1\n"
"v_cndmask_b32 v72, 0, v72\n"
"v_cndmask_b32 v73, 0, v73\n"
"v_cndmask_b32 v74, 0, v74\n"
"v_cndmask_b32 v75, 0, v75\n"
"v_mul_f32 v52, v56, v56\n"
"v_mul_f32 v53, v57, v57\n"
"v_fmac_f32 v52, v58, v58\n"
"v_fmac_f32 v53, v59, v59\n"
"v_fmac_f32 v52, v60, v60\n"
"v_fmac_f32 v53, v61, v61\n"
"v_fmac_f32 v52, v62, v62\n"
"v_fmac_f32 v53, v63, v63\n"
"v_fmac_f32 v52, v64, v64\n"
"v_fmac_f32 v53, v65, v65\n"
"v_fmac_f32 v52, v66, v66\n"
"v_fmac_f32 v53, v67, v67\n"
"v_fmac_f32 v52, v68, v68\n"
"v_fmac_f32 v53, v69, v69\n"
"v_fmac_f32 v52, v70, v70\n"
"v_fmac_f32 v53, v71, v71\n"
"v_fmac_f32 v52, v72, v72\n"
"v_fmac_f32 v53, v73, v73\n"
"v_fmac_f32 v52, v74, v74\n"
"v_fmac_f32 v53, v75, v75\n"
"v_add_f32 v52, v52, v53\n"
"s_nop 1\n"
"v_add_f32_dpp v52, v52, v52 quad_perm:[1,0,3,2] row_mask:0xf bank_mask:0xf\n"
"s_nop 1\n"
"v_add_f32_dpp v52, v52, v52 quad_perm:[2,3,0,1] row_mask:0xf bank_mask:0xf\n"
"s_nop 1\n"
"v_add_f32_dpp v52, v52, v52 row_half_mirror row_mask:0xf bank_mask:0xf\n"
"s_nop 1\n"
"v_add_f32_dpp v52, v52, v52 row_mirror row_mask:0xf bank_mask:0xf\n"
"v_mov_b32 v53, v52\n"
"s_nop 1\n"
"v_permlane16_swap_b32 v52, v53\n"
"v_add_f32 v52, v52, v53\n"
"v_mov_b32 v55, v54\n"
"v_cvt_pk_bf16_f32 v55, v55, v55\n"
"v_lshlrev_b32 v55, 16, v55\n"
"v_mul_f32 v52, 0x3ae38e39, v52\n"
"v_cvt_pk_bf16_f32 v52, v52, v52\n"
"v_lshlrev_b32 v52, 16, v52\n"
"v_add_f32 v52, 0x3727c5ac, v52\n"
"v_cvt_pk_bf16_f32 v52, v52, v52\n"
"v_lshlrev_b32 v52, 16, v52\n"
"v_sqrt_f32 v52, v52\n"
"s_nop 0\n"
"v_cvt_pk_bf16_f32 v52, v52, v52\n"
"v_lshlrev_b32 v52, 16, v52\n"
"v_rcp_f32 v52, v52\n"
"v_sub_f32 v32, v32, v55\n"
"v_sub_f32 v33, v33, v55\n"
"v_sub_f32 v34, v34, v55\n"
"v_sub_f32 v35, v35, v55\n"
"v_sub_f32 v36, v36, v55\n"
"v_sub_f32 v37, v37, v55\n"
"v_sub_f32 v38, v38, v55\n"
"v_sub_f32 v39, v39, v55\n"
"v_sub_f32 v40, v40, v55\n"
"v_sub_f32 v41, v41, v55\n"
"v_sub_f32 v42, v42, v55\n"
"v_sub_f32 v43, v43, v55\n"
"v_sub_f32 v44, v44, v55\n"
"v_sub_f32 v45, v45, v55\n"
"v_sub_f32 v46, v46, v55\n"
"v_sub_f32 v47, v47, v55\n"
"v_sub_f32 v48, v48, v55\n"
"v_sub_f32 v49, v49, v55\n"
"v_sub_f32 v50, v50, v55\n"
"v_sub_f32 v51, v51, v55\n"
"v_cvt_pk_bf16_f32 v56, v32, v33\n"
"v_cvt_pk_bf16_f32 v58, v34, v35\n"
"v_cvt_pk_bf16_f32 v60, v36, v37\n"
"v_cvt_pk_bf16_f32 v62, v38, v39\n"
"v_cvt_pk_bf16_f32 v64, v40, v41\n"
"v_cvt_pk_bf16_f32 v66, v42, v43\n"
"v_cvt_pk_bf16_f32 v68, v44, v45\n"
"v_cvt_pk_bf16_f32 v70, v46, v47\n"
"v_cvt_pk_bf16_f32 v72, v48, v49\n"
"v_cvt_pk_bf16_f32 v74, v50, v51\n"
"v_and_b32 v33, 0xffff0000, v56\n"
"v_lshlrev_b32 v32, 16, v56\n"
"v_and_b32 v35, 0xffff0000, v58\n"
"v_lshlrev_b32 v34, 16, v58\n"
"v_and_b32 v37, 0xffff0000, v60\n"
"v_lshlrev_b32 v36, 16, v60\n"
"v_and_b32 v39, 0xffff0000, v62\n"
"v_lshlrev_b32 v38, 16, v62\n"
"v_and_b32 v41, 0xffff0000, v64\n"
"v_lshlrev_b32 v40, 16, v64\n"
"v_and_b32 v43, 0xffff0000, v66\n"
"v_lshlrev_b32 v42, 16, v66\n"
"v_and_b32 v45, 0xffff0000, v68\n"
"v_lshlrev_b32 v44, 16, v68\n"
"v_and_b32 v47, 0xffff0000, v70\n"
"v_lshlrev_b32 v46, 16, v70\n"
"v_and_b32 v49, 0xffff0000, v72\n"
"v_lshlrev_b32 v48, 16, v72\n"
"v_and_b32 v51, 0xffff0000, v74\n"
"v_lshlrev_b32 v50, 16, v74\n"
"v_mul_f32 v32, v32, v52\n"
"v_mul_f32 v33, v33, v52\n"
"v_mul_f32 v34, v34, v52\n"
"v_mul_f32 v35, v35, v52\n"
"v_mul_f32 v36, v36, v52\n"
"v_mul_f32 v37, v37, v52\n"
"v_mul_f32 v38, v38, v52\n"
"v_mul_f32 v39, v39, v52\n"
"v_mul_f32 v40, v40, v52\n"
"v_mul_f32 v41, v41, v52\n"
"v_mul_f32 v42, v42, v52\n"
"v_mul_f32 v43, v43, v52\n"
"v_mul_f32 v44, v44, v52\n"
"v_mul_f32 v45, v45, v52\n"
"v_mul_f32 v46, v46, v52\n"
"v_mul_f32 v47, v47, v52\n"
"v_mul_f32 v48, v48, v52\n"
"v_mul_f32 v49, v49, v52\n"
"v_mul_f32 v50, v50, v52\n"
"v_mul_f32 v51, v51, v52\n"
"v_cvt_pk_bf16_f32 v16, v32, v33\n"
"v_cvt_pk_bf16_f32 v17, v34, v35\n"
"v_cvt_pk_bf16_f32 v18, v36, v37\n"
"v_cvt_pk_bf16_f32 v19, v38, v39\n"
"v_cvt_pk_bf16_f32 v20, v40, v41\n"
"v_cvt_pk_bf16_f32 v21, v42, v43\n"
"v_cvt_pk_bf16_f32 v22, v44, v45\n"
"v_cvt_pk_bf16_f32 v23, v46, v47\n"
"v_cvt_pk_bf16_f32 v24, v48, v49\n"
"v_cvt_pk_bf16_f32 v25, v50, v51\n"
"s_and_b32 s76, s74, 1\n"
"s_lshl_b32 s76, s76, 3\n"
"s_lshl_b32 s77, s72, 1\n"
"s_add_u32 s76, s76, s77\n"
"v_add_u32 v30, s76, v26\n"
"v_and_b32 v31, 3, v28\n"
"v_lshl_add_u32 v30, v31, 4, v30\n"
"v_lshlrev_b32 v30, 4, v30\n"
"v_lshrrev_b32 v31, 3, v28\n"
"v_lshl_add_u32 v30, v31, 10, v30\n"
"v_bfe_u32 v31, v28, 2, 1\n"
"v_lshl_add_u32 v30, v31, 3, v30\n"
"v_mov_b32 v31, 0x7ff00000\n"
"v_cndmask_b32 v31, v31, v30\n"
"s_lshl_b32 s76, s75, 4\n"
"s_lshr_b32 s77, s74, 1\n"
"s_add_u32 s76, s76, s77\n"
"s_mul_i32 s76, s76, 0x4800\n"
"buffer_store_dwordx2 v[16:17], v30, s[64:67], s76 offen sc1\n"
"s_add_u32 s76, s76, 0x1000\n"
"buffer_store_dwordx2 v[18:19], v30, s[64:67], s76 offen sc1\n"
"s_add_u32 s76, s76, 0x1000\n"
"buffer_store_dwordx2 v[20:21], v30, s[64:67], s76 offen sc1\n"
"s_add_u32 s76, s76, 0x1000\n"
"buffer_store_dwordx2 v[22:23], v30, s[64:67], s76 offen sc1\n"
"s_add_u32 s76, s76, 0x1000\n"
"buffer_store_dwordx2 v[24:25], v31, s[64:67], s76 offen sc1\n"
"s_add_u32 m0, s78, 0x9000\n"
"s_nop 0\n"
"buffer_load_dwordx4 v104, s[60:63], s79 offen lds\n"
"s_add_u32 m0, s78, 0x9400\n"
"s_nop 0\n"
"buffer_load_dwordx4 v105, s[60:63], s79 offen lds\n"
"s_add_u32 m0, s78, 0x9800\n"
"s_nop 0\n"
"buffer_load_dwordx4 v106, s[60:63], s79 offen lds\n"
"s_add_u32 m0, s78, 0x9c00\n"
"s_nop 0\n"
"buffer_load_dwordx4 v107, s[60:63], s79 offen lds\n"
"s_add_u32 m0, s78, 0xa000\n"
"s_nop 0\n"
"buffer_load_dwordx4 v108, s[60:63], s79 offen lds\n"
"s_add_u32 m0, s78, 0xa400\n"
"s_nop 0\n"
"buffer_load_dwordx4 v109, s[60:63], s79 offen lds\n"
"s_add_u32 m0, s78, 0xa800\n"
"s_nop 0\n"
"buffer_load_dwordx4 v110, s[60:63], s79 offen lds\n"
"s_add_u32 m0, s78, 0xac00\n"
"s_nop 0\n"
"buffer_load_dwordx4 v111, s[60:63], s79 offen lds\n"
"s_add_u32 m0, s78, 0xb000\n"
"s_nop 0\n"
"buffer_load_dwordx4 v112, s[60:63], s79 offen lds\n"
"s_add_u32 s79, s79, 0x90000\n"
"s_waitcnt vmcnt(24)\n"
"v_readfirstlane_b32 s88, v8\n"
"v_readfirstlane_b32 s89, v9\n"
"v_lshrrev_b32 v122, 2, v5\n"
"v_mul_u32_u24 v122, 0x120, v122\n"
"v_and_b32 v123, 3, v5\n"
"v_lshl_add_u32 v113, v123, 3, v122\n"
"v_add_u32 v114, 0x9000, v113\n"
"v_add_u32 v115, 0x12000, v113\n"
"v_add_u32 v116, 0x1b000, v113\n"
"s_add_u32 s88, s88, 1\n"
"s_addc_u32 s89, s89, 0\n"
"s_waitcnt vmcnt(9)\n"
"s_barrier\n"
"s_cmp_lg_u32 s72, 0\n"
"s_cbranch_scc1 NOSIGLN_%=\n"
"s_lshl_b32 s76, s75, 5\n"
"s_add_u32 s76, s76, s74\n"
"s_lshl_b32 s76, s76, 3\n"
"s_add_u32 s76, s76, 0x1000\n"
"v_mov_b32 v16, s76\n"
"v_mov_b32 v18, s88\n"
"v_mov_b32 v19, s89\n"
"s_mov_b64 exec, 1\n"
"global_store_dwordx2 v16, v[18:19], s[92:93] sc1\n"
"s_mov_b64 exec, -1\n"
"NOSIGLN_%=:\n"
"s_lshl_b32 s76, s75, 8\n"
"s_add_u32 s76, s76, 0x1000\n"
"v_and_b32 v20, 31, v5\n"
"v_lshlrev_b32 v20, 3, v20\n"
"v_add_u32 v20, s76, v20\n"
"global_load_dwordx2 v[22:23], v20, s[92:93] sc1\n"
"s_mov_b64 s[76:77], %8\n"
"s_add_u32 s76, s76, 0x800000\n"
"s_addc_u32 s77, s77, 0\n"
"s_mov_b32 s68, s76\n"
"s_and_b32 s69, s77, 0xffff\n"
"s_mov_b32 s70, 0x1200000\n"
"s_mov_b32 s71, 0x20000\n"
"s_mov_b64 s[76:77], %8\n"
"s_add_u32 s76, s76, 0x2000000\n"
"s_addc_u32 s77, s77, 0\n"
"s_mov_b32 s48, s76\n"
"s_and_b32 s49, s77, 0xffff\n"
"s_mov_b32 s50, 0x1000000\n"
"s_mov_b32 s51, 0x20000\n"
"s_lshr_b32 s98, s74, 1\n"
"s_lshl_b32 s98, s98, 19\n"
"s_mov_b64 s[76:77], %1\n"
"s_add_u32 s76, s76, s98\n"
"s_addc_u32 s77, s77, 0\n"
"s_mov_b32 s40, s76\n"
"s_and_b32 s41, s77, 0xffff\n"
"s_mov_b32 s42, 0\n"
"s_mov_b32 s43, 0x20000\n"
"s_sub_u32 s42, 0x800000, s98\n"
"s_mov_b64 s[76:77], %2\n"
"s_add_u32 s76, s76, s98\n"
"s_addc_u32 s77, s77, 0\n"
"s_mov_b32 s44, s76\n"
"s_and_b32 s45, s77, 0xffff\n"
"s_mov_b32 s46, 0\n"
"s_mov_b32 s47, 0x20000\n"
"s_sub_u32 s46, 0x800000, s98\n"
"s_lshl_b32 s98, s72, 3\n"
"s_lshl_b32 s98, 0xff, s98\n"
"s_waitcnt vmcnt(0)\n"
"v_cmp_eq_u64 vcc, v[22:23], s[88:89]\n"
"s_nop 0\n"
"s_and_b32 s76, vcc_lo, s98\n"
"s_mov_b32 s99, 0\n"
"s_cmp_eq_u32 s76, s98\n"
"s_cbranch_scc1 POLLDLN_%=\n"
"global_load_dwordx2 v[6:7], v20, s[92:93] sc1\n"
"s_sleep 3\n"
"global_load_dwordx2 v[8:9], v20, s[92:93] sc1\n"
"s_sleep 3\n"
"global_load_dwordx2 v[10:11], v20, s[92:93] sc1\n"
"s_sleep 3\n"
"POLLLN_%=:\n"
"global_load_dwordx2 v[12:13], v20, s[92:93] sc1\n"
"s_waitcnt vmcnt(3)\n"
"v_cmp_eq_u64 vcc, v[6:7], s[88:89]\n"
"s_nop 3\n"
"s_and_b32 s76, vcc_lo, s98\n"
"s_cmp_eq_u32 s76, s98\n"
"s_cbranch_scc1 POLLDLN_%=\n"
"s_sleep 2\n"
"global_load_dwordx2 v[6:7], v20, s[92:93] sc1\n"
"s_waitcnt vmcnt(3)\n"
"v_cmp_eq_u64 vcc, v[8:9], s[88:89]\n"
"s_nop 3\n"
"s_and_b32 s76, vcc_lo, s98\n"
"s_cmp_eq_u32 s76, s98\n"
"s_cbranch_scc1 POLLDLN_%=\n"
"s_sleep 2\n"
"global_load_dwordx2 v[8:9], v20, s[92:93] sc1\n"
"s_waitcnt vmcnt(3)\n"
"v_cmp_eq_u64 vcc, v[10:11], s[88:89]\n"
"s_nop 3\n"
"s_and_b32 s76, vcc_lo, s98\n"
"s_cmp_eq_u32 s76, s98\n"
"s_cbranch_scc1 POLLDLN_%=\n"
"s_sleep 2\n"
"global_load_dwordx2 v[10:11], v20, s[92:93] sc1\n"
"s_waitcnt vmcnt(3)\n"
"v_cmp_eq_u64 vcc, v[12:13], s[88:89]\n"
"s_nop 3\n"
"s_and_b32 s76, vcc_lo, s98\n"
"s_cmp_eq_u32 s76, s98\n"
"s_cbranch_scc1 POLLDLN_%=\n"
"s_sleep 2\n"
"s_add_u32 s99, s99, 1\n"
"s_cmp_gt_u32 s99, 0x40000\n"
"s_cbranch_scc0 POLLLN_%=\n"
"POLLDLN_%=:\n"
"s_add_u32 s76, s90, 0\n"
"buffer_load_dwordx4 v[16:19], v121, s[64:67], s76 offen sc1\n"
"s_add_u32 s76, s90, 0x400\n"
"buffer_load_dwordx4 v[20:23], v121, s[64:67], s76 offen sc1\n"
"s_add_u32 s76, s90, 0x4800\n"
"buffer_load_dwordx4 v[24:27], v121, s[64:67], s76 offen sc1\n"
"s_add_u32 s76, s90, 0x4c00\n"
"buffer_load_dwordx4 v[28:31], v121, s[64:67], s76 offen sc1\n"
"s_add_u32 s76, s90, 0x9000\n"
"buffer_load_dwordx4 v[32:35], v121, s[64:67], s76 offen sc1\n"
"s_add_u32 s76, s90, 0x9400\n"
"buffer_load_dwordx4 v[36:39], v121, s[64:67], s76 offen sc1\n"
"s_add_u32 s76, s90, 0xd800\n"
"buffer_load_dwordx4 v[40:43], v121, s[64:67], s76 offen sc1\n"
"s_add_u32 s76, s90, 0xdc00\n"
"buffer_load_dwordx4 v[44:47], v121, s[64:67], s76 offen sc1\n"
"ds_read_b64_tr_b16 v[80:81], v113\n"
"ds_read_b64_tr_b16 v[82:83], v113 offset:4608\n"
"ds_read_b64_tr_b16 v[84:85], v113 offset:9216\n"
"ds_read_b64_tr_b16 v[86:87], v113 offset:13824\n"
"ds_read_b64_tr_b16 v[88:89], v113 offset:18432\n"
"ds_read_b64_tr_b16 v[90:91], v113 offset:23040\n"
"ds_read_b64_tr_b16 v[92:93], v113 offset:27648\n"
"ds_read_b64_tr_b16 v[94:95], v113 offset:32256\n"
"s_waitcnt vmcnt(7) lgkmcnt(6)\n"
"v_mfma_f32_16x16x32_bf16 v[128:131], v[80:83], v[16:19], 0\n"
"s_waitcnt vmcnt(6) lgkmcnt(4)\n"
"v_mfma_f32_16x16x32_bf16 v[128:131], v[84:87], v[20:23], v[128:131]\n"
"s_add_u32 s76, s90, 0x800\n"
"ds_read_b64_tr_b16 v[96:97], v113 offset:32\n"
"s_waitcnt vmcnt(4)\n"
"v_mfma_f32_16x16x32_bf16 v[132:135], v[84:87], v[28:31], 0\n"
"buffer_load_dwordx4 v[48:51], v121, s[64:67], s76 offen sc1\n"
"v_mfma_f32_16x16x32_bf16 v[132:135], v[80:83], v[24:27], v[132:135]\n"
"ds_read_b64_tr_b16 v[98:99], v113 offset:4640\n"
"s_waitcnt vmcnt(4)\n"
"v_mfma_f32_16x16x32_bf16 v[136:139], v[80:83], v[32:35], 0\n"
"s_waitcnt vmcnt(3)\n"
"v_mfma_f32_16x16x32_bf16 v[136:139], v[84:87], v[36:39], v[136:139]\n"
"s_add_u32 s76, s90, 0xc00\n"
"ds_read_b64_tr_b16 v[100:101], v113 offset:9248\n"
"s_waitcnt vmcnt(1)\n"
"v_mfma_f32_16x16x32_bf16 v[140:143], v[84:87], v[44:47], 0\n"
"buffer_load_dwordx4 v[52:55], v121, s[64:67], s76 offen sc1\n"
"v_mfma_f32_16x16x32_bf16 v[140:143], v[80:83], v[40:43], v[140:143]\n"
"ds_read_b64_tr_b16 v[102:103], v113 offset:13856\n"
"s_waitcnt lgkmcnt(6)\n"
"v_mfma_f32_16x16x32_bf16 a[0:3], v[88:91], v[16:19], 0\n"
"s_waitcnt lgkmcnt(4)\n"
"v_mfma_f32_16x16x32_bf16 a[0:3], v[92:95], v[20:23], a[0:3]\n"
"s_add_u32 s76, s90, 0x5000\n"
"ds_read_b64_tr_b16 v[80:81], v113 offset:18464\n"
"v_mfma_f32_16x16x32_bf16 a[4:7], v[92:95], v[28:31], 0\n"
"buffer_load_dwordx4 v[56:59], v121, s[64:67], s76 offen sc1\n"
"v_mfma_f32_16x16x32_bf16 a[4:7], v[88:91], v[24:27], a[4:7]\n"
"ds_read_b64_tr_b16 v[82:83], v113 offset:23072\n"
"v_mfma_f32_16x16x32_bf16 a[8:11], v[88:91], v[32:35], 0\n"
"v_mfma_f32_16x16x32_bf16 a[8:11], v[92:95], v[36:39], a[8:11]\n"
"s_add_u32 s76, s90, 0x5400\n"
"ds_read_b64_tr_b16 v[84:85], v113 offset:27680\n"
"v_mfma_f32_16x16x32_bf16 a[12:15], v[92:95], v[44:47], 0\n"
"buffer_load_dwordx4 v[60:63], v121, s[64:67], s76 offen sc1\n"
"v_mfma_f32_16x16x32_bf16 a[12:15], v[88:91], v[40:43], a[12:15]\n"
"ds_read_b64_tr_b16 v[86:87], v113 offset:32288\n"
"s_waitcnt lgkmcnt(6)\n"
"v_mfma_f32_16x16x32_bf16 v[144:147], v[96:99], v[16:19], 0\n"
"s_waitcnt lgkmcnt(4)\n"
"v_mfma_f32_16x16x32_bf16 v[144:147], v[100:103], v[20:23], v[144:147]\n"
"s_add_u32 s76, s90, 0x9800\n"
"ds_read_b64_tr_b16 v[88:89], v113 offset:64\n"
"v_mfma_f32_16x16x32_bf16 v[148:151], v[100:103], v[28:31], 0\n"
"buffer_load_dwordx4 v[64:67], v121, s[64:67], s76 offen sc1\n"
"v_mfma_f32_16x16x32_bf16 v[148:151], v[96:99], v[24:27], v[148:151]\n"
"ds_read_b64_tr_b16 v[90:91], v113 offset:4672\n"
"v_mfma_f32_16x16x32_bf16 v[152:155], v[96:99], v[32:35], 0\n"
"v_mfma_f32_16x16x32_bf16 v[152:155], v[100:103], v[36:39], v[152:155]\n"
"s_add_u32 s76, s90, 0x9c00\n"
"ds_read_b64_tr_b16 v[92:93], v113 offset:9280\n"
"v_mfma_f32_16x16x32_bf16 v[156:159], v[100:103], v[44:47], 0\n"
"buffer_load_dwordx4 v[68:71], v121, s[64:67], s76 offen sc1\n"
"v_mfma_f32_16x16x32_bf16 v[156:159], v[96:99], v[40:43], v[156:159]\n"
"ds_read_b64_tr_b16 v[94:95], v113 offset:13888\n"
"s_waitcnt lgkmcnt(6)\n"
"v_mfma_f32_16x16x32_bf16 a[16:19], v[80:83], v[16:19], 0\n"
"s_waitcnt lgkmcnt(4)\n"
"v_mfma_f32_16x16x32_bf16 a[16:19], v[84:87], v[20:23], a[16:19]\n"
"s_add_u32 s76, s90, 0xe000\n"
"ds_read_b64_tr_b16 v[96:97], v113 offset:18496\n"
"v_mfma_f32_16x16x32_bf16 a[20:23], v[84:87], v[28:31], 0\n"
"buffer_load_dwordx4 v[72:75], v121, s[64:67], s76 offen sc1\n"
"v_mfma_f32_16x16x32_bf16 a[20:23], v[80:83], v[24:27], a[20:23]\n"
"ds_read_b64_tr_b16 v[98:99], v113 offset:23104\n"
"v_mfma_f32_16x16x32_bf16 a[24:27], v[80:83], v[32:35], 0\n"
"v_mfma_f32_16x16x32_bf16 a[24:27], v[84:87], v[36:39], a[24:27]\n"
"s_add_u32 s76, s90, 0xe400\n"
"ds_read_b64_tr_b16 v[100:101], v113 offset:27712\n"
"v_mfma_f32_16x16x32_bf16 a[28:31], v[84:87], v[44:47], 0\n"
"buffer_load_dwordx4 v[76:79], v121, s[64:67], s76 offen sc1\n"
"v_mfma_f32_16x16x32_bf16 a[28:31], v[80:83], v[40:43], a[28:31]\n"
"ds_read_b64_tr_b16 v[102:103], v113 offset:32320\n"
"s_waitcnt lgkmcnt(6)\n"
"v_mfma_f32_16x16x32_bf16 v[160:163], v[88:91], v[16:19], 0\n"
"s_waitcnt lgkmcnt(4)\n"
"v_mfma_f32_16x16x32_bf16 v[160:163], v[92:95], v[20:23], v[160:163]\n"
"s_add_u32 m0, s78, 0x12000\n"
"ds_read_b64_tr_b16 v[80:81], v113 offset:96\n"
"v_mfma_f32_16x16x32_bf16 v[164:167], v[92:95], v[28:31], 0\n"
"buffer_load_dwordx4 v104, s[60:63], s79 offen lds\n"
"v_mfma_f32_16x16x32_bf16 v[164:167], v[88:91], v[24:27], v[164:167]\n"
"ds_read_b64_tr_b16 v[82:83], v113 offset:4704\n"
"v_mfma_f32_16x16x32_bf16 v[168:171], v[88:91], v[32:35], 0\n"
"v_mfma_f32_16x16x32_bf16 v[168:171], v[92:95], v[36:39], v[168:171]\n"
"ds_read_b64_tr_b16 v[84:85], v113 offset:9312\n"
"v_mfma_f32_16x16x32_bf16 v[172:175], v[92:95], v[44:47], 0\n"
"v_mfma_f32_16x16x32_bf16 v[172:175], v[88:91], v[40:43], v[172:175]\n"
"s_add_u32 m0, s78, 0x12400\n"
"ds_read_b64_tr_b16 v[86:87], v113 offset:13920\n"
"s_waitcnt lgkmcnt(6)\n"
"v_mfma_f32_16x16x32_bf16 a[32:35], v[96:99], v[16:19], 0\n"
"buffer_load_dwordx4 v105, s[60:63], s79 offen lds\n"
"s_waitcnt lgkmcnt(4)\n"
"v_mfma_f32_16x16x32_bf16 a[32:35], v[100:103], v[20:23], a[32:35]\n"
"ds_read_b64_tr_b16 v[88:89], v113 offset:18528\n"
"v_mfma_f32_16x16x32_bf16 a[36:39], v[100:103], v[28:31], 0\n"
"v_mfma_f32_16x16x32_bf16 a[36:39], v[96:99], v[24:27], a[36:39]\n"
"ds_read_b64_tr_b16 v[90:91], v113 offset:23136\n"
"v_mfma_f32_16x16x32_bf16 a[40:43], v[96:99], v[32:35], 0\n"
"v_mfma_f32_16x16x32_bf16 a[40:43], v[100:103], v[36:39], a[40:43]\n"
"s_add_u32 m0, s78, 0x12800\n"
"ds_read_b64_tr_b16 v[92:93], v113 offset:27744\n"
"v_mfma_f32_16x16x32_bf16 a[44:47], v[100:103], v[44:47], 0\n"
"buffer_load_dwordx4 v106, s[60:63], s79 offen lds\n"
"v_mfma_f32_16x16x32_bf16 a[44:47], v[96:99], v[40:43], a[44:47]\n"
"ds_read_b64_tr_b16 v[94:95], v113 offset:32352\n"
"s_waitcnt lgkmcnt(6)\n"
"v_mfma_f32_16x16x32_bf16 v[176:179], v[80:83], v[16:19], 0\n"
"s_waitcnt lgkmcnt(4)\n"
"v_mfma_f32_16x16x32_bf16 v[176:179], v[84:87], v[20:23], v[176:179]\n"
"ds_read_b64_tr_b16 v[96:97], v113 offset:128\n"
"v_mfma_f32_16x16x32_bf16 v[180:183], v[84:87], v[28:31], 0\n"
"v_mfma_f32_16x16x32_bf16 v[180:183], v[80:83], v[24:27], v[180:183]\n"
"s_add_u32 m0, s78, 0x12c00\n"
"ds_read_b64_tr_b16 v[98:99], v113 offset:4736\n"
"v_mfma_f32_16x16x32_bf16 v[184:187], v[80:83], v[32:35], 0\n"
"buffer_load_dwordx4 v107, s[60:63], s79 offen lds\n"
"v_mfma_f32_16x16x32_bf16 v[184:187], v[84:87], v[36:39], v[184:187]\n"
"ds_read_b64_tr_b16 v[100:101], v113 offset:9344\n"
"v_mfma_f32_16x16x32_bf16 v[188:191], v[84:87], v[44:47], 0\n"
"v_mfma_f32_16x16x32_bf16 v[188:191], v[80:83], v[40:43], v[188:191]\n"
"ds_read_b64_tr_b16 v[102:103], v113 offset:13952\n"
"s_waitcnt lgkmcnt(6)\n"
"v_mfma_f32_16x16x32_bf16 a[48:51], v[88:91], v[16:19], 0\n"
"s_waitcnt lgkmcnt(4)\n"
"v_mfma_f32_16x16x32_bf16 a[48:51], v[92:95], v[20:23], a[48:51]\n"
"s_add_u32 m0, s78, 0x13000\n"
"ds_read_b64_tr_b16 v[80:81], v113 offset:18560\n"
"v_mfma_f32_16x16x32_bf16 a[52:55], v[92:95], v[28:31], 0\n"
"buffer_load_dwordx4 v108, s[60:63], s79 offen lds\n"
"v_mfma_f32_16x16x32_bf16 a[52:55], v[88:91], v[24:27], a[52:55]\n"
"ds_read_b64_tr_b16 v[82:83], v113 offset:23168\n"
"v_mfma_f32_16x16x32_bf16 a[56:59], v[88:91], v[32:35], 0\n"
"v_mfma_f32_16x16x32_bf16 a[56:59], v[92:95], v[36:39], a[56:59]\n"
"ds_read_b64_tr_b16 v[84:85], v113 offset:27776\n"
"v_mfma_f32_16x16x32_bf16 a[60:63], v[92:95], v[44:47], 0\n"
"v_mfma_f32_16x16x32_bf16 a[60:63], v[88:91], v[40:43], a[60:63]\n"
"s_add_u32 m0, s78, 0x13400\n"
"ds_read_b64_tr_b16 v[86:87], v113 offset:32384\n"
"s_waitcnt lgkmcnt(6)\n"
"v_mfma_f32_16x16x32_bf16 v[192:195], v[96:99], v[16:19], 0\n"
"buffer_load_dwordx4 v109, s[60:63], s79 offen lds\n"
"s_waitcnt lgkmcnt(4)\n"
"v_mfma_f32_16x16x32_bf16 v[192:195], v[100:103], v[20:23], v[192:195]\n"
"ds_read_b64_tr_b16 v[88:89], v113 offset:160\n"
"v_mfma_f32_16x16x32_bf16 v[196:199], v[100:103], v[28:31], 0\n"
"v_mfma_f32_16x16x32_bf16 v[196:199], v[96:99], v[24:27], v[196:199]\n"
"ds_read_b64_tr_b16 v[90:91], v113 offset:4768\n"
"v_mfma_f32_16x16x32_bf16 v[200:203], v[96:99], v[32:35], 0\n"
"v_mfma_f32_16x16x32_bf16 v[200:203], v[100:103], v[36:39], v[200:203]\n"
"s_add_u32 m0, s78, 0x13800\n"
"ds_read_b64_tr_b16 v[92:93], v113 offset:9376\n"
"v_mfma_f32_16x16x32_bf16 v[204:207], v[100:103], v[44:47], 0\n"
"buffer_load_dwordx4 v110, s[60:63], s79 offen lds\n"
"v_mfma_f32_16x16x32_bf16 v[204:207], v[96:99], v[40:43], v[204:207]\n"
"ds_read_b64_tr_b16 v[94:95], v113 offset:13984\n"
"s_waitcnt lgkmcnt(6)\n"
"v_mfma_f32_16x16x32_bf16 a[64:67], v[80:83], v[16:19], 0\n"
"s_waitcnt lgkmcnt(4)\n"
"v_mfma_f32_16x16x32_bf16 a[64:67], v[84:87], v[20:23], a[64:67]\n"
"ds_read_b64_tr_b16 v[96:97], v113 offset:18592\n"
"v_mfma_f32_16x16x32_bf16 a[68:71], v[84:87], v[28:31], 0\n"
"v_mfma_f32_16x16x32_bf16 a[68:71], v[80:83], v[24:27], a[68:71]\n"
"s_add_u32 m0, s78, 0x13c00\n"
"ds_read_b64_tr_b16 v[98:99], v113 offset:23200\n"
"v_mfma_f32_16x16x32_bf16 a[72:75], v[80:83], v[32:35], 0\n"
"buffer_load_dwordx4 v111, s[60:63], s79 offen lds\n"
"v_mfma_f32_16x16x32_bf16 a[72:75], v[84:87], v[36:39], a[72:75]\n"
"ds_read_b64_tr_b16 v[100:101], v113 offset:27808\n"
"v_mfma_f32_16x16x32_bf16 a[76:79], v[84:87], v[44:47], 0\n"
"v_mfma_f32_16x16x32_bf16 a[76:79], v[80:83], v[40:43], a[76:79]\n"
"ds_read_b64_tr_b16 v[102:103], v113 offset:32416\n"
"s_waitcnt lgkmcnt(6)\n"
"v_mfma_f32_16x16x32_bf16 v[208:211], v[88:91], v[16:19], 0\n"
"s_waitcnt lgkmcnt(4)\n"
"v_mfma_f32_16x16x32_bf16 v[208:211], v[92:95], v[20:23], v[208:211]\n"
"s_add_u32 m0, s78, 0x14000\n"
"ds_read_b64_tr_b16 v[80:81], v113 offset:192\n"
"v_mfma_f32_16x16x32_bf16 v[212:215], v[92:95], v[28:31], 0\n"
"buffer_load_dwordx4 v112, s[60:63], s79 offen lds\n"
"v_mfma_f32_16x16x32_bf16 v[212:215], v[88:91], v[24:27], v[212:215]\n"
"ds_read_b64_tr_b16 v[82:83], v113 offset:4800\n"
"v_mfma_f32_16x16x32_bf16 v[216:219], v[88:91], v[32:35], 0\n"
"v_mfma_f32_16x16x32_bf16 v[216:219], v[92:95], v[36:39], v[216:219]\n"
"ds_read_b64_tr_b16 v[84:85], v113 offset:9408\n"
"v_mfma_f32_16x16x32_bf16 v[220:223], v[92:95], v[44:47], 0\n"
"v_mfma_f32_16x16x32_bf16 v[220:223], v[88:91], v[40:43], v[220:223]\n"
"ds_read_b64_tr_b16 v[86:87], v113 offset:14016\n"
"s_waitcnt lgkmcnt(6)\n"
"v_mfma_f32_16x16x32_bf16 a[80:83], v[96:99], v[16:19], 0\n"
"s_add_u32 s79, s79, 0x90000\n"
"s_waitcnt lgkmcnt(4)\n"
"v_mfma_f32_16x16x32_bf16 a[80:83], v[100:103], v[20:23], a[80:83]\n"
"ds_read_b64_tr_b16 v[88:89], v113 offset:18624\n"
"v_mfma_f32_16x16x32_bf16 a[84:87], v[100:103], v[28:31], 0\n"
"s_mov_b64 s[76:77], %8\n"
"s_mov_b32 s56, s76\n"
"s_and_b32 s57, s77, 0xffff\n"
"s_mov_b32 s58, 0x6000000\n"
"s_mov_b32 s59, 0x20000\n"
"v_lshlrev_b32 v123, 21, v5\n"
"v_mfma_f32_16x16x32_bf16 a[84:87], v[96:99], v[24:27], a[84:87]\n"
"ds_read_b64_tr_b16 v[90:91], v113 offset:23232\n"
"v_mfma_f32_16x16x32_bf16 a[88:91], v[96:99], v[32:35], 0\n"
"v_mfma_f32_16x16x32_bf16 a[88:91], v[100:103], v[36:39], a[88:91]\n"
"ds_read_b64_tr_b16 v[92:93], v113 offset:27840\n"
"v_mfma_f32_16x16x32_bf16 a[92:95], v[100:103], v[44:47], 0\n"
"buffer_load_dword v11, v123, s[56:59], 0 offen\n"
"v_mfma_f32_16x16x32_bf16 a[92:95], v[96:99], v[40:43], a[92:95]\n"
"ds_read_b64_tr_b16 v[94:95], v113 offset:32448\n"
"s_waitcnt lgkmcnt(6)\n"
"v_mfma_f32_16x16x32_bf16 v[224:227], v[80:83], v[16:19], 0\n"
"s_mov_b64 s[76:77], %9\n"
"s_mov_b32 s56, s76\n"
"s_and_b32 s57, s77, 0xffff\n"
"s_mov_b32 s58, 0x240000\n"
"s_mov_b32 s59, 0x20000\n"
"s_waitcnt lgkmcnt(4)\n"
"v_mfma_f32_16x16x32_bf16 v[224:227], v[84:87], v[20:23], v[224:227]\n"
"ds_read_b64_tr_b16 v[96:97], v113 offset:224\n"
"v_mfma_f32_16x16x32_bf16 v[228:231], v[84:87], v[28:31], 0\n"
"v_mfma_f32_16x16x32_bf16 v[228:231], v[80:83], v[24:27], v[228:231]\n"
"ds_read_b64_tr_b16 v[98:99], v113 offset:4832\n"
"v_mfma_f32_16x16x32_bf16 v[232:235], v[80:83], v[32:35], 0\n"
"buffer_load_dword v11, v123, s[56:59], 0 offen\n"
"v_mfma_f32_16x16x32_bf16 v[232:235], v[84:87], v[36:39], v[232:235]\n"
"ds_read_b64_tr_b16 v[100:101], v113 offset:9440\n"
"v_mfma_f32_16x16x32_bf16 v[236:239], v[84:87], v[44:47], 0\n"
"v_mfma_f32_16x16x32_bf16 v[236:239], v[80:83], v[40:43], v[236:239]\n"
"ds_read_b64_tr_b16 v[102:103], v113 offset:14048\n"
"s_waitcnt lgkmcnt(6)\n"
"v_mfma_f32_16x16x32_bf16 a[96:99], v[88:91], v[16:19], 0\n"
"s_waitcnt lgkmcnt(4)\n"
"v_mfma_f32_16x16x32_bf16 a[96:99], v[92:95], v[20:23], a[96:99]\n"
"ds_read_b64_tr_b16 v[80:81], v113 offset:18656\n"
"v_mfma_f32_16x16x32_bf16 a[100:103], v[92:95], v[28:31], 0\n"
"v_mfma_f32_16x16x32_bf16 a[100:103], v[88:91], v[24:27], a[100:103]\n"
"ds_read_b64_tr_b16 v[82:83], v113 offset:23264\n"
"v_mfma_f32_16x16x32_bf16 a[104:107], v[88:91], v[32:35], 0\n"
"v_mfma_f32_16x16x32_bf16 a[104:107], v[92:95], v[36:39], a[104:107]\n"
"ds_read_b64_tr_b16 v[84:85], v113 offset:27872\n"
"v_mfma_f32_16x16x32_bf16 a[108:111], v[92:95], v[44:47], 0\n"
"v_mfma_f32_16x16x32_bf16 a[108:111], v[88:91], v[40:43], a[108:111]\n"
"ds_read_b64_tr_b16 v[86:87], v113 offset:32480\n"
"s_waitcnt lgkmcnt(6)\n"
"v_mfma_f32_16x16x32_bf16 v[240:243], v[96:99], v[16:19], 0\n"
"s_waitcnt lgkmcnt(4)\n"
"v_mfma_f32_16x16x32_bf16 v[240:243], v[100:103], v[20:23], v[240:243]\n"
"ds_read_b64_tr_b16 v[88:89], v113 offset:256\n"
"v_mfma_f32_16x16x32_bf16 v[244:247], v[100:103], v[28:31], 0\n"
"v_mfma_f32_16x16x32_bf16 v[244:247], v[96:99], v[24:27], v[244:247]\n"
"ds_read_b64_tr_b16 v[90:91], v113 offset:4864\n"
"v_mfma_f32_16x16x32_bf16 v[248:251], v[96:99], v[32:35], 0\n"
"v_mfma_f32_16x16x32_bf16 v[248:251], v[100:103], v[36:39], v[248:251]\n"
"ds_read_b64_tr_b16 v[92:93], v113 offset:9472\n"
"v_mfma_f32_16x16x32_bf16 v[252:255], v[100:103], v[44:47], 0\n"
"v_mfma_f32_16x16x32_bf16 v[252:255], v[96:99], v[40:43], v[252:255]\n"
"ds_read_b64_tr_b16 v[94:95], v113 offset:14080\n"
"s_waitcnt lgkmcnt(6)\n"
"v_mfma_f32_16x16x32_bf16 a[112:115], v[80:83], v[16:19], 0\n"
"s_waitcnt lgkmcnt(4)\n"
"v_mfma_f32_16x16x32_bf16 a[112:115], v[84:87], v[20:23], a[112:115]\n"
"ds_read_b64_tr_b16 v[96:97], v113 offset:18688\n"
"v_mfma_f32_16x16x32_bf16 a[116:119], v[84:87], v[28:31], 0\n"
"v_mfma_f32_16x16x32_bf16 a[116:119], v[80:83], v[24:27], a[116:119]\n"
"ds_read_b64_tr_b16 v[98:99], v113 offset:23296\n"
"v_mfma_f32_16x16x32_bf16 a[120:123], v[80:83], v[32:35], 0\n"
"v_mfma_f32_16x16x32_bf16 a[120:123], v[84:87], v[36:39], a[120:123]\n"
"ds_read_b64_tr_b16 v[100:101], v113 offset:27904\n"
"v_mfma_f32_16x16x32_bf16 a[124:127], v[84:87], v[44:47], 0\n"
"v_mfma_f32_16x16x32_bf16 a[124:127], v[80:83], v[40:43], a[124:127]\n"
"s_nop 0\n"
"s_barrier\n"
"ds_read_b64_tr_b16 v[102:103], v113 offset:32512\n"
"s_waitcnt lgkmcnt(6)\n"
"v_mfma_f32_16x16x32_bf16 a[144:147], v[88:91], v[16:19], 0\n"
"s_waitcnt lgkmcnt(4)\n"
"v_mfma_f32_16x16x32_bf16 a[144:147], v[92:95], v[20:23], a[144:147]\n"
"ds_read_b64_tr_b16 v[80:81], v114\n"
"v_mfma_f32_16x16x32_bf16 a[148:151], v[92:95], v[28:31], 0\n"
"v_mfma_f32_16x16x32_bf16 a[148:151], v[88:91], v[24:27], a[148:151]\n"
"ds_read_b64_tr_b16 v[82:83], v114 offset:4608\n"
"v_mfma_f32_16x16x32_bf16 a[152:155], v[88:91], v[32:35], 0\n"
"v_mfma_f32_16x16x32_bf16 a[152:155], v[92:95], v[36:39], a[152:155]\n"
"ds_read_b64_tr_b16 v[84:85], v114 offset:9216\n"
"v_mfma_f32_16x16x32_bf16 a[156:159], v[92:95], v[44:47], 0\n"
"v_mfma_f32_16x16x32_bf16 a[156:159], v[88:91], v[40:43], a[156:159]\n"
"ds_read_b64_tr_b16 v[86:87], v114 offset:13824\n"
"s_waitcnt lgkmcnt(6)\n"
"v_mfma_f32_16x16x32_bf16 a[128:131], v[96:99], v[16:19], 0\n"
"s_waitcnt lgkmcnt(4)\n"
"v_mfma_f32_16x16x32_bf16 a[128:131], v[100:103], v[20:23], a[128:131]\n"
"ds_read_b64_tr_b16 v[88:89], v114 offset:18432\n"
"v_mfma_f32_16x16x32_bf16 a[132:135], v[100:103], v[28:31], 0\n"
"v_mfma_f32_16x16x32_bf16 a[132:135], v[96:99], v[24:27], a[132:135]\n"
"ds_read_b64_tr_b16 v[90:91], v114 offset:23040\n"
"v_mfma_f32_16x16x32_bf16 a[136:139], v[96:99], v[32:35], 0\n"
"v_mfma_f32_16x16x32_bf16 a[136:139], v[100:103], v[36:39], a[136:139]\n"
"ds_read_b64_tr_b16 v[92:93], v114 offset:27648\n"
"v_mfma_f32_16x16x32_bf16 a[140:143], v[100:103], v[44:47], 0\n"
"v_mfma_f32_16x16x32_bf16 a[140:143], v[96:99], v[40:43], a[140:143]\n"
"ds_read_b64_tr_b16 v[94:95], v114 offset:32256\n"
"s_waitcnt vmcnt(18) lgkmcnt(6)\n"
"v_mfma_f32_16x16x32_bf16 v[128:131], v[80:83], v[48:51], v[128:131]\n"
"s_waitcnt vmcnt(17) lgkmcnt(4)\n"
"v_mfma_f32_16x16x32_bf16 v[128:131], v[84:87], v[52:55], v[128:131]\n"
"s_add_u32 s76, s90, 0x1000\n"
"ds_read_b64_tr_b16 v[96:97], v114 offset:32\n"
"s_waitcnt vmcnt(15)\n"
"v_mfma_f32_16x16x32_bf16 v[132:135], v[84:87], v[60:63], v[132:135]\n"
"buffer_load_dwordx4 v[16:19], v121, s[64:67], s76 offen sc1\n"
"v_mfma_f32_16x16x32_bf16 v[132:135], v[80:83], v[56:59], v[132:135]\n"
"ds_read_b64_tr_b16 v[98:99], v114 offset:4640\n"
"s_waitcnt vmcnt(15)\n"
"v_mfma_f32_16x16x32_bf16 v[136:139], v[80:83], v[64:67], v[136:139]\n"
"s_waitcnt vmcnt(14)\n"
"v_mfma_f32_16x16x32_bf16 v[136:139], v[84:87], v[68:71], v[136:139]\n"
"s_add_u32 s76, s90, 0x1400\n"
"ds_read_b64_tr_b16 v[100:101], v114 offset:9248\n"
"s_waitcnt vmcnt(12)\n"
"v_mfma_f32_16x16x32_bf16 v[140:143], v[84:87], v[76:79], v[140:143]\n"
"buffer_load_dwordx4 v[20:23], v121, s[64:67], s76 offen sc1\n"
"v_mfma_f32_16x16x32_bf16 v[140:143], v[80:83], v[72:75], v[140:143]\n"
"ds_read_b64_tr_b16 v[102:103], v114 offset:13856\n"
"s_waitcnt lgkmcnt(6)\n"
"v_mfma_f32_16x16x32_bf16 a[0:3], v[88:91], v[48:51], a[0:3]\n"
"s_waitcnt lgkmcnt(4)\n"
"v_mfma_f32_16x16x32_bf16 a[0:3], v[92:95], v[52:55], a[0:3]\n"
"s_add_u32 s76, s90, 0x5800\n"
"ds_read_b64_tr_b16 v[80:81], v114 offset:18464\n"
"v_mfma_f32_16x16x32_bf16 a[4:7], v[92:95], v[60:63], a[4:7]\n"
"buffer_load_dwordx4 v[24:27], v121, s[64:67], s76 offen sc1\n"
"v_mfma_f32_16x16x32_bf16 a[4:7], v[88:91], v[56:59], a[4:7]\n"
"ds_read_b64_tr_b16 v[82:83], v114 offset:23072\n"
"v_mfma_f32_16x16x32_bf16 a[8:11], v[88:91], v[64:67], a[8:11]\n"
"v_mfma_f32_16x16x32_bf16 a[8:11], v[92:95], v[68:71], a[8:11]\n"
"s_add_u32 s76, s90, 0x5c00\n"
"ds_read_b64_tr_b16 v[84:85], v114 offset:27680\n"
"v_mfma_f32_16x16x32_bf16 a[12:15], v[92:95], v[76:79], a[12:15]\n"
"buffer_load_dwordx4 v[28:31], v121, s[64:67], s76 offen sc1\n"
"v_mfma_f32_16x16x32_bf16 a[12:15], v[88:91], v[72:75], a[12:15]\n"
"ds_read_b64_tr_b16 v[86:87], v114 offset:32288\n"
"s_waitcnt lgkmcnt(6)\n"
"v_mfma_f32_16x16x32_bf16 v[144:147], v[96:99], v[48:51], v[144:147]\n"
"s_waitcnt lgkmcnt(4)\n"
"v_mfma_f32_16x16x32_bf16 v[144:147], v[100:103], v[52:55], v[144:147]\n"
"s_add_u32 s76, s90, 0xa000\n"
"ds_read_b64_tr_b16 v[88:89], v114 offset:64\n"
"v_mfma_f32_16x16x32_bf16 v[148:151], v[100:103], v[60:63], v[148:151]\n"
"buffer_load_dwordx4 v[32:35], v121, s[64:67], s76 offen sc1\n"
"v_mfma_f32_16x16x32_bf16 v[148:151], v[96:99], v[56:59], v[148:151]\n"
"ds_read_b64_tr_b16 v[90:91], v114 offset:4672\n"
"v_mfma_f32_16x16x32_bf16 v[152:155], v[96:99], v[64:67], v[152:155]\n"
"v_mfma_f32_16x16x32_bf16 v[152:155], v[100:103], v[68:71], v[152:155]\n"
"s_add_u32 s76, s90, 0xa400\n"
"ds_read_b64_tr_b16 v[92:93], v114 offset:9280\n"
"v_mfma_f32_16x16x32_bf16 v[156:159], v[100:103], v[76:79], v[156:159]\n"
"buffer_load_dwordx4 v[36:39], v121, s[64:67], s76 offen sc1\n"
"v_mfma_f32_16x16x32_bf16 v[156:159], v[96:99], v[72:75], v[156:159]\n"
"ds_read_b64_tr_b16 v[94:95], v114 offset:13888\n"
"s_waitcnt lgkmcnt(6)\n"
"v_mfma_f32_16x16x32_bf16 a[16:19], v[80:83], v[48:51], a[16:19]\n"
"s_waitcnt lgkmcnt(4)\n"
"v_mfma_f32_16x16x32_bf16 a[16:19], v[84:87], v[52:55], a[16:19]\n"
"s_add_u32 s76, s90, 0xe800\n"
"ds_read_b64_tr_b16 v[96:97], v114 offset:18496\n"
"v_mfma_f32_16x16x32_bf16 a[20:23], v[84:87], v[60:63], a[20:23]\n"
"buffer_load_dwordx4 v[40:43], v121, s[64:67], s76 offen sc1\n"
"v_mfma_f32_16x16x32_bf16 a[20:23], v[80:83], v[56:59], a[20:23]\n"
"ds_read_b64_tr_b16 v[98:99], v114 offset:23104\n"
"v_mfma_f32_16x16x32_bf16 a[24:27], v[80:83], v[64:67], a[24:27]\n"
"v_mfma_f32_16x16x32_bf16 a[24:27], v[84:87], v[68:71], a[24:27]\n"
"s_add_u32 s76, s90, 0xec00\n"
"ds_read_b64_tr_b16 v[100:101], v114 offset:27712\n"
"v_mfma_f32_16x16x32_bf16 a[28:31], v[84:87], v[76:79], a[28:31]\n"
"buffer_load_dwordx4 v[44:47], v121, s[64:67], s76 offen sc1\n"
"v_mfma_f32_16x16x32_bf16 a[28:31], v[80:83], v[72:75], a[28:31]\n"
"ds_read_b64_tr_b16 v[102:103], v114 offset:32320\n"
"s_waitcnt lgkmcnt(6)\n"
"v_mfma_f32_16x16x32_bf16 v[160:163], v[88:91], v[48:51], v[160:163]\n"
"s_waitcnt lgkmcnt(4)\n"
"v_mfma_f32_16x16x32_bf16 v[160:163], v[92:95], v[52:55], v[160:163]\n"
"s_add_u32 m0, s78, 0x1b000\n"
"ds_read_b64_tr_b16 v[80:81], v114 offset:96\n"
"v_mfma_f32_16x16x32_bf16 v[164:167], v[92:95], v[60:63], v[164:167]\n"
"buffer_load_dwordx4 v104, s[60:63], s79 offen lds\n"
"v_mfma_f32_16x16x32_bf16 v[164:167], v[88:91], v[56:59], v[164:167]\n"
"ds_read_b64_tr_b16 v[82:83], v114 offset:4704\n"
"v_mfma_f32_16x16x32_bf16 v[168:171], v[88:91], v[64:67], v[168:171]\n"
"v_mfma_f32_16x16x32_bf16 v[168:171], v[92:95], v[68:71], v[168:171]\n"
"ds_read_b64_tr_b16 v[84:85], v114 offset:9312\n"
"v_mfma_f32_16x16x32_bf16 v[172:175], v[92:95], v[76:79], v[172:175]\n"
"v_mfma_f32_16x16x32_bf16 v[172:175], v[88:91], v[72:75], v[172:175]\n"
"s_add_u32 m0, s78, 0x1b400\n"
"ds_read_b64_tr_b16 v[86:87], v114 offset:13920\n"
"s_waitcnt lgkmcnt(6)\n"
"v_mfma_f32_16x16x32_bf16 a[32:35], v[96:99], v[48:51], a[32:35]\n"
"buffer_load_dwordx4 v105, s[60:63], s79 offen lds\n"
"s_waitcnt lgkmcnt(4)\n"
"v_mfma_f32_16x16x32_bf16 a[32:35], v[100:103], v[52:55], a[32:35]\n"
"ds_read_b64_tr_b16 v[88:89], v114 offset:18528\n"
"v_mfma_f32_16x16x32_bf16 a[36:39], v[100:103], v[60:63], a[36:39]\n"
"v_mfma_f32_16x16x32_bf16 a[36:39], v[96:99], v[56:59], a[36:39]\n"
"ds_read_b64_tr_b16 v[90:91], v114 offset:23136\n"
"v_mfma_f32_16x16x32_bf16 a[40:43], v[96:99], v[64:67], a[40:43]\n"
"v_mfma_f32_16x16x32_bf16 a[40:43], v[100:103], v[68:71], a[40:43]\n"
"s_add_u32 m0, s78, 0x1b800\n"
"ds_read_b64_tr_b16 v[92:93], v114 offset:27744\n"
"v_mfma_f32_16x16x32_bf16 a[44:47], v[100:103], v[76:79], a[44:47]\n"
"buffer_load_dwordx4 v106, s[60:63], s79 offen lds\n"
"v_mfma_f32_16x16x32_bf16 a[44:47], v[96:99], v[72:75], a[44:47]\n"
"ds_read_b64_tr_b16 v[94:95], v114 offset:32352\n"
"s_waitcnt lgkmcnt(6)\n"
"v_mfma_f32_16x16x32_bf16 v[176:179], v[80:83], v[48:51], v[176:179]\n"
"s_waitcnt lgkmcnt(4)\n"
"v_mfma_f32_16x16x32_bf16 v[176:179], v[84:87], v[52:55], v[176:179]\n"
"ds_read_b64_tr_b16 v[96:97], v114 offset:128\n"
"v_mfma_f32_16x16x32_bf16 v[180:183], v[84:87], v[60:63], v[180:183]\n"
"v_mfma_f32_16x16x32_bf16 v[180:183], v[80:83], v[56:59], v[180:183]\n"
"s_add_u32 m0, s78, 0x1bc00\n"
"ds_read_b64_tr_b16 v[98:99], v114 offset:4736\n"
"v_mfma_f32_16x16x32_bf16 v[184:187], v[80:83], v[64:67], v[184:187]\n"
"buffer_load_dwordx4 v107, s[60:63], s79 offen lds\n"
"v_mfma_f32_16x16x32_bf16 v[184:187], v[84:87], v[68:71], v[184:187]\n"
"ds_read_b64_tr_b16 v[100:101], v114 offset:9344\n"
"v_mfma_f32_16x16x32_bf16 v[188:191], v[84:87], v[76:79], v[188:191]\n"
"v_mfma_f32_16x16x32_bf16 v[188:191], v[80:83], v[72:75], v[188:191]\n"
"ds_read_b64_tr_b16 v[102:103], v114 offset:13952\n"
"s_waitcnt lgkmcnt(6)\n"
"v_mfma_f32_16x16x32_bf16 a[48:51], v[88:91], v[48:51], a[48:51]\n"
"s_waitcnt lgkmcnt(4)\n"
"v_mfma_f32_16x16x32_bf16 a[48:51], v[92:95], v[52:55], a[48:51]\n"
"s_add_u32 m0, s78, 0x1c000\n"
"ds_read_b64_tr_b16 v[80:81], v114 offset:18560\n"
"v_mfma_f32_16x16x32_bf16 a[52:55], v[92:95], v[60:63], a[52:55]\n"
"buffer_load_dwordx4 v108, s[60:63], s79 offen lds\n"
"v_mfma_f32_16x16x32_bf16 a[52:55], v[88:91], v[56:59], a[52:55]\n"
"ds_read_b64_tr_b16 v[82:83], v114 offset:23168\n"
"v_mfma_f32_16x16x32_bf16 a[56:59], v[88:91], v[64:67], a[56:59]\n"
"v_mfma_f32_16x16x32_bf16 a[56:59], v[92:95], v[68:71], a[56:59]\n"
"ds_read_b64_tr_b16 v[84:85], v114 offset:27776\n"
"v_mfma_f32_16x16x32_bf16 a[60:63], v[92:95], v[76:79], a[60:63]\n"
"v_mfma_f32_16x16x32_bf16 a[60:63], v[88:91], v[72:75], a[60:63]\n"
"s_add_u32 m0, s78, 0x1c400\n"
"ds_read_b64_tr_b16 v[86:87], v114 offset:32384\n"
"s_waitcnt lgkmcnt(6)\n"
"v_mfma_f32_16x16x32_bf16 v[192:195], v[96:99], v[48:51], v[192:195]\n"
"buffer_load_dwordx4 v109, s[60:63], s79 offen lds\n"
"s_waitcnt lgkmcnt(4)\n"
"v_mfma_f32_16x16x32_bf16 v[192:195], v[100:103], v[52:55], v[192:195]\n"
"ds_read_b64_tr_b16 v[88:89], v114 offset:160\n"
"v_mfma_f32_16x16x32_bf16 v[196:199], v[100:103], v[60:63], v[196:199]\n"
"v_mfma_f32_16x16x32_bf16 v[196:199], v[96:99], v[56:59], v[196:199]\n"
"ds_read_b64_tr_b16 v[90:91], v114 offset:4768\n"
"v_mfma_f32_16x16x32_bf16 v[200:203], v[96:99], v[64:67], v[200:203]\n"
"v_mfma_f32_16x16x32_bf16 v[200:203], v[100:103], v[68:71], v[200:203]\n"
"s_add_u32 m0, s78, 0x1c800\n"
"ds_read_b64_tr_b16 v[92:93], v114 offset:9376\n"
"v_mfma_f32_16x16x32_bf16 v[204:207], v[100:103], v[76:79], v[204:207]\n"
"buffer_load_dwordx4 v110, s[60:63], s79 offen lds\n"
"v_mfma_f32_16x16x32_bf16 v[204:207], v[96:99], v[72:75], v[204:207]\n"
"ds_read_b64_tr_b16 v[94:95], v114 offset:13984\n"
"s_waitcnt lgkmcnt(6)\n"
"v_mfma_f32_16x16x32_bf16 a[64:67], v[80:83], v[48:51], a[64:67]\n"
"s_waitcnt lgkmcnt(4)\n"
"v_mfma_f32_16x16x32_bf16 a[64:67], v[84:87], v[52:55], a[64:67]\n"
"ds_read_b64_tr_b16 v[96:97], v114 offset:18592\n"
"v_mfma_f32_16x16x32_bf16 a[68:71], v[84:87], v[60:63], a[68:71]\n"
"v_mfma_f32_16x16x32_bf16 a[68:71], v[80:83], v[56:59], a[68:71]\n"
"s_add_u32 m0, s78, 0x1cc00\n"
"ds_read_b64_tr_b16 v[98:99], v114 offset:23200\n"
"v_mfma_f32_16x16x32_bf16 a[72:75], v[80:83], v[64:67], a[72:75]\n"
"buffer_load_dwordx4 v111, s[60:63], s79 offen lds\n"
"v_mfma_f32_16x16x32_bf16 a[72:75], v[84:87], v[68:71], a[72:75]\n"
"ds_read_b64_tr_b16 v[100:101], v114 offset:27808\n"
"v_mfma_f32_16x16x32_bf16 a[76:79], v[84:87], v[76:79], a[76:79]\n"
"v_mfma_f32_16x16x32_bf16 a[76:79], v[80:83], v[72:75], a[76:79]\n"
"ds_read_b64_tr_b16 v[102:103], v114 offset:32416\n"
"s_waitcnt lgkmcnt(6)\n"
"v_mfma_f32_16x16x32_bf16 v[208:211], v[88:91], v[48:51], v[208:211]\n"
"s_waitcnt lgkmcnt(4)\n"
"v_mfma_f32_16x16x32_bf16 v[208:211], v[92:95], v[52:55], v[208:211]\n"
"s_add_u32 m0, s78, 0x1d000\n"
"ds_read_b64_tr_b16 v[80:81], v114 offset:192\n"
"v_mfma_f32_16x16x32_bf16 v[212:215], v[92:95], v[60:63], v[212:215]\n"
"buffer_load_dwordx4 v112, s[60:63], s79 offen lds\n"
"v_mfma_f32_16x16x32_bf16 v[212:215], v[88:91], v[56:59], v[212:215]\n"
"ds_read_b64_tr_b16 v[82:83], v114 offset:4800\n"
"v_mfma_f32_16x16x32_bf16 v[216:219], v[88:91], v[64:67], v[216:219]\n"
"v_mfma_f32_16x16x32_bf16 v[216:219], v[92:95], v[68:71], v[216:219]\n"
"ds_read_b64_tr_b16 v[84:85], v114 offset:9408\n"
"v_mfma_f32_16x16x32_bf16 v[220:223], v[92:95], v[76:79], v[220:223]\n"
"v_mfma_f32_16x16x32_bf16 v[220:223], v[88:91], v[72:75], v[220:223]\n"
"ds_read_b64_tr_b16 v[86:87], v114 offset:14016\n"
"s_waitcnt lgkmcnt(6)\n"
"v_mfma_f32_16x16x32_bf16 a[80:83], v[96:99], v[48:51], a[80:83]\n"
"s_add_u32 s79, s79, 0x90000\n"
"s_waitcnt lgkmcnt(4)\n"
"v_mfma_f32_16x16x32_bf16 a[80:83], v[100:103], v[52:55], a[80:83]\n"
"ds_read_b64_tr_b16 v[88:89], v114 offset:18624\n"
"v_mfma_f32_16x16x32_bf16 a[84:87], v[100:103], v[60:63], a[84:87]\n"
"s_mov_b64 s[76:77], %3\n"
"s_mov_b32 s56, s76\n"
"s_and_b32 s57, s77, 0xffff\n"
"s_mov_b32 s58, 0x480000\n"
"s_mov_b32 s59, 0x20000\n"
"s_mul_i32 s76, s75, 0x48\n"
"s_mul_i32 s77, s72, 18\n"
"s_add_u32 s76, s76, s77\n"
"s_lshl_b32 s76, s76, 13\n"
"s_lshl_b32 s77, s74, 8\n"
"s_add_u32 s98, s76, s77\n"
"v_mfma_f32_16x16x32_bf16 a[84:87], v[96:99], v[56:59], a[84:87]\n"
"ds_read_b64_tr_b16 v[90:91], v114 offset:23232\n"
"v_mfma_f32_16x16x32_bf16 a[88:91], v[96:99], v[64:67], a[88:91]\n"
"v_lshrrev_b32 v125, 1, v5\n"
"v_mfma_f32_16x16x32_bf16 a[88:91], v[100:103], v[68:71], a[88:91]\n"
"ds_read_b64_tr_b16 v[92:93], v114 offset:27840\n"
"v_mfma_f32_16x16x32_bf16 a[92:95], v[100:103], v[76:79], a[92:95]\n"
"v_min_u32 v125, 17, v125\n"
"v_mfma_f32_16x16x32_bf16 a[92:95], v[96:99], v[72:75], a[92:95]\n"
"ds_read_b64_tr_b16 v[94:95], v114 offset:32448\n"
"s_waitcnt lgkmcnt(6)\n"
"v_mfma_f32_16x16x32_bf16 v[224:227], v[80:83], v[48:51], v[224:227]\n"
"v_lshlrev_b32 v125, 13, v125\n"
"s_waitcnt lgkmcnt(4)\n"
"v_mfma_f32_16x16x32_bf16 v[224:227], v[84:87], v[52:55], v[224:227]\n"
"ds_read_b64_tr_b16 v[96:97], v114 offset:224\n"
"v_mfma_f32_16x16x32_bf16 v[228:231], v[84:87], v[60:63], v[228:231]\n"
"v_and_b32 v124, 1, v5\n"
"v_mfma_f32_16x16x32_bf16 v[228:231], v[80:83], v[56:59], v[228:231]\n"
"ds_read_b64_tr_b16 v[98:99], v114 offset:4832\n"
"v_mfma_f32_16x16x32_bf16 v[232:235], v[80:83], v[64:67], v[232:235]\n"
"v_lshl_add_u32 v125, v124, 7, v125\n"
"v_mfma_f32_16x16x32_bf16 v[232:235], v[84:87], v[68:71], v[232:235]\n"
"ds_read_b64_tr_b16 v[100:101], v114 offset:9440\n"
"v_mfma_f32_16x16x32_bf16 v[236:239], v[84:87], v[76:79], v[236:239]\n"
"buffer_load_dword v11, v125, s[56:59], s98 offen\n"
"v_mfma_f32_16x16x32_bf16 v[236:239], v[80:83], v[72:75], v[236:239]\n"
"ds_read_b64_tr_b16 v[102:103], v114 offset:14048\n"
"s_waitcnt lgkmcnt(6)\n"
"v_mfma_f32_16x16x32_bf16 a[96:99], v[88:91], v[48:51], a[96:99]\n"
"s_and_b32 s76, s74, 1\n"
"s_lshl_b32 s76, s76, 3\n"
"s_add_u32 s76, s76, s75\n"
"s_lshl_b32 s76, s76, 12\n"
"s_and_b32 s77, s72, 1\n"
"s_lshl_b32 s77, s77, 11\n"
"s_add_u32 s99, s76, s77\n"
"s_waitcnt lgkmcnt(4)\n"
"v_mfma_f32_16x16x32_bf16 a[96:99], v[92:95], v[52:55], a[96:99]\n"
"ds_read_b64_tr_b16 v[80:81], v114 offset:18656\n"
"v_mfma_f32_16x16x32_bf16 a[100:103], v[92:95], v[60:63], a[100:103]\n"
"v_and_b32 v125, 15, v5\n"
"v_mfma_f32_16x16x32_bf16 a[100:103], v[88:91], v[56:59], a[100:103]\n"
"ds_read_b64_tr_b16 v[82:83], v114 offset:23264\n"
"v_mfma_f32_16x16x32_bf16 a[104:107], v[88:91], v[64:67], a[104:107]\n"
"v_lshlrev_b32 v125, 7, v125\n"
"v_mfma_f32_16x16x32_bf16 a[104:107], v[92:95], v[68:71], a[104:107]\n"
"ds_read_b64_tr_b16 v[84:85], v114 offset:27872\n"
"v_mfma_f32_16x16x32_bf16 a[108:111], v[92:95], v[76:79], a[108:111]\n"
"buffer_load_dword v11, v125, s[40:43], s99 offen\n"
"v_mfma_f32_16x16x32_bf16 a[108:111], v[88:91], v[72:75], a[108:111]\n"
"ds_read_b64_tr_b16 v[86:87], v114 offset:32480\n"
"s_waitcnt lgkmcnt(6)\n"
"v_mfma_f32_16x16x32_bf16 v[240:243], v[96:99], v[48:51], v[240:243]\n"
"buffer_load_dword v11, v125, s[44:47], s99 offen\n"
"s_waitcnt lgkmcnt(4)\n"
"v_mfma_f32_16x16x32_bf16 v[240:243], v[100:103], v[52:55], v[240:243]\n"
"ds_read_b64_tr_b16 v[88:89], v114 offset:256\n"
"v_mfma_f32_16x16x32_bf16 v[244:247], v[100:103], v[60:63], v[244:247]\n"
"v_mfma_f32_16x16x32_bf16 v[244:247], v[96:99], v[56:59], v[244:247]\n"
"ds_read_b64_tr_b16 v[90:91], v114 offset:4864\n"
"v_mfma_f32_16x16x32_bf16 v[248:251], v[96:99], v[64:67], v[248:251]\n"
"v_mfma_f32_16x16x32_bf16 v[248:251], v[100:103], v[68:71], v[248:251]\n"
"ds_read_b64_tr_b16 v[92:93], v114 offset:9472\n"
"v_mfma_f32_16x16x32_bf16 v[252:255], v[100:103], v[76:79], v[252:255]\n"
"v_mfma_f32_16x16x32_bf16 v[252:255], v[96:99], v[72:75], v[252:255]\n"
"ds_read_b64_tr_b16 v[94:95], v114 offset:14080\n"
"s_waitcnt lgkmcnt(6)\n"
"v_mfma_f32_16x16x32_bf16 a[112:115], v[80:83], v[48:51], a[112:115]\n"
"s_waitcnt lgkmcnt(4)\n"
"v_mfma_f32_16x16x32_bf16 a[112:115], v[84:87], v[52:55], a[112:115]\n"
"ds_read_b64_tr_b16 v[96:97], v114 offset:18688\n"
"v_mfma_f32_16x16x32_bf16 a[116:119], v[84:87], v[60:63], a[116:119]\n"
"v_mfma_f32_16x16x32_bf16 a[116:119], v[80:83], v[56:59], a[116:119]\n"
"ds_read_b64_tr_b16 v[98:99], v114 offset:23296\n"
"v_mfma_f32_16x16x32_bf16 a[120:123], v[80:83], v[64:67], a[120:123]\n"
"v_mfma_f32_16x16x32_bf16 a[120:123], v[84:87], v[68:71], a[120:123]\n"
"ds_read_b64_tr_b16 v[100:101], v114 offset:27904\n"
"v_mfma_f32_16x16x32_bf16 a[124:127], v[84:87], v[76:79], a[124:127]\n"
"v_mfma_f32_16x16x32_bf16 a[124:127], v[80:83], v[72:75], a[124:127]\n"
"s_waitcnt vmcnt(22)\n"
"s_barrier\n"
"ds_read_b64_tr_b16 v[102:103], v114 offset:32512\n"
"s_waitcnt lgkmcnt(6)\n"
"v_mfma_f32_16x16x32_bf16 a[144:147], v[88:91], v[48:51], a[144:147]\n"
"s_waitcnt lgkmcnt(4)\n"
"v_mfma_f32_16x16x32_bf16 a[144:147], v[92:95], v[52:55], a[144:147]\n"
"ds_read_b64_tr_b16 v[80:81], v115\n"
"v_mfma_f32_16x16x32_bf16 a[148:151], v[92:95], v[60:63], a[148:151]\n"
"v_mfma_f32_16x16x32_bf16 a[148:151], v[88:91], v[56:59], a[148:151]\n"
"ds_read_b64_tr_b16 v[82:83], v115 offset:4608\n"
"v_mfma_f32_16x16x32_bf16 a[152:155], v[88:91], v[64:67], a[152:155]\n"
"v_mfma_f32_16x16x32_bf16 a[152:155], v[92:95], v[68:71], a[152:155]\n"
"ds_read_b64_tr_b16 v[84:85], v115 offset:9216\n"
"v_mfma_f32_16x16x32_bf16 a[156:159], v[92:95], v[76:79], a[156:159]\n"
"v_mfma_f32_16x16x32_bf16 a[156:159], v[88:91], v[72:75], a[156:159]\n"
"ds_read_b64_tr_b16 v[86:87], v115 offset:13824\n"
"s_waitcnt lgkmcnt(6)\n"
"v_mfma_f32_16x16x32_bf16 a[128:131], v[96:99], v[48:51], a[128:131]\n"
"s_waitcnt lgkmcnt(4)\n"
"v_mfma_f32_16x16x32_bf16 a[128:131], v[100:103], v[52:55], a[128:131]\n"
"ds_read_b64_tr_b16 v[88:89], v115 offset:18432\n"
"v_mfma_f32_16x16x32_bf16 a[132:135], v[100:103], v[60:63], a[132:135]\n"
"v_mfma_f32_16x16x32_bf16 a[132:135], v[96:99], v[56:59], a[132:135]\n"
"ds_read_b64_tr_b16 v[90:91], v115 offset:23040\n"
"v_mfma_f32_16x16x32_bf16 a[136:139], v[96:99], v[64:67], a[136:139]\n"
"v_mfma_f32_16x16x32_bf16 a[136:139], v[100:103], v[68:71], a[136:139]\n"
"ds_read_b64_tr_b16 v[92:93], v115 offset:27648\n"
"v_mfma_f32_16x16x32_bf16 a[140:143], v[100:103], v[76:79], a[140:143]\n"
"v_mfma_f32_16x16x32_bf16 a[140:143], v[96:99], v[72:75], a[140:143]\n"
"ds_read_b64_tr_b16 v[94:95], v115 offset:32256\n"
"s_waitcnt vmcnt(19) lgkmcnt(6)\n"
"v_mfma_f32_16x16x32_bf16 v[128:131], v[80:83], v[16:19], v[128:131]\n"
"s_waitcnt vmcnt(18) lgkmcnt(4)\n"
"v_mfma_f32_16x16x32_bf16 v[128:131], v[84:87], v[20:23], v[128:131]\n"
"s_add_u32 s76, s90, 0x1800\n"
"ds_read_b64_tr_b16 v[96:97], v115 offset:32\n"
"s_waitcnt vmcnt(16)\n"
"v_mfma_f32_16x16x32_bf16 v[132:135], v[84:87], v[28:31], v[132:135]\n"
"buffer_load_dwordx4 v[48:51], v121, s[64:67], s76 offen sc1\n"
"v_mfma_f32_16x16x32_bf16 v[132:135], v[80:83], v[24:27], v[132:135]\n"
"ds_read_b64_tr_b16 v[98:99], v115 offset:4640\n"
"s_waitcnt vmcnt(16)\n"
"v_mfma_f32_16x16x32_bf16 v[136:139], v[80:83], v[32:35], v[136:139]\n"
"s_waitcnt vmcnt(15)\n"
"v_mfma_f32_16x16x32_bf16 v[136:139], v[84:87], v[36:39], v[136:139]\n"
"s_add_u32 s76, s90, 0x1c00\n"
"ds_read_b64_tr_b16 v[100:101], v115 offset:9248\n"
"s_waitcnt vmcnt(13)\n"
"v_mfma_f32_16x16x32_bf16 v[140:143], v[84:87], v[44:47], v[140:143]\n"
"buffer_load_dwordx4 v[52:55], v121, s[64:67], s76 offen sc1\n"
"v_mfma_f32_16x16x32_bf16 v[140:143], v[80:83], v[40:43], v[140:143]\n"
"ds_read_b64_tr_b16 v[102:103], v115 offset:13856\n"
"s_waitcnt lgkmcnt(6)\n"
"v_mfma_f32_16x16x32_bf16 a[0:3], v[88:91], v[16:19], a[0:3]\n"
"s_waitcnt lgkmcnt(4)\n"
"v_mfma_f32_16x16x32_bf16 a[0:3], v[92:95], v[20:23], a[0:3]\n"
"s_add_u32 s76, s90, 0x6000\n"
"ds_read_b64_tr_b16 v[80:81], v115 offset:18464\n"
"v_mfma_f32_16x16x32_bf16 a[4:7], v[92:95], v[28:31], a[4:7]\n"
"buffer_load_dwordx4 v[56:59], v121, s[64:67], s76 offen sc1\n"
"v_mfma_f32_16x16x32_bf16 a[4:7], v[88:91], v[24:27], a[4:7]\n"
"ds_read_b64_tr_b16 v[82:83], v115 offset:23072\n"
"v_mfma_f32_16x16x32_bf16 a[8:11], v[88:91], v[32:35], a[8:11]\n"
"v_mfma_f32_16x16x32_bf16 a[8:11], v[92:95], v[36:39], a[8:11]\n"
"s_add_u32 s76, s90, 0x6400\n"
"ds_read_b64_tr_b16 v[84:85], v115 offset:27680\n"
"v_mfma_f32_16x16x32_bf16 a[12:15], v[92:95], v[44:47], a[12:15]\n"
"buffer_load_dwordx4 v[60:63], v121, s[64:67], s76 offen sc1\n"
"v_mfma_f32_16x16x32_bf16 a[12:15], v[88:91], v[40:43], a[12:15]\n"
"ds_read_b64_tr_b16 v[86:87], v115 offset:32288\n"
"s_waitcnt lgkmcnt(6)\n"
"v_mfma_f32_16x16x32_bf16 v[144:147], v[96:99], v[16:19], v[144:147]\n"
"s_waitcnt lgkmcnt(4)\n"
"v_mfma_f32_16x16x32_bf16 v[144:147], v[100:103], v[20:23], v[144:147]\n"
"s_add_u32 s76, s90, 0xa800\n"
"ds_read_b64_tr_b16 v[88:89], v115 offset:64\n"
"v_mfma_f32_16x16x32_bf16 v[148:151], v[100:103], v[28:31], v[148:151]\n"
"buffer_load_dwordx4 v[64:67], v121, s[64:67], s76 offen sc1\n"
"v_mfma_f32_16x16x32_bf16 v[148:151], v[96:99], v[24:27], v[148:151]\n"
"ds_read_b64_tr_b16 v[90:91], v115 offset:4672\n"
"v_mfma_f32_16x16x32_bf16 v[152:155], v[96:99], v[32:35], v[152:155]\n"
"v_mfma_f32_16x16x32_bf16 v[152:155], v[100:103], v[36:39], v[152:155]\n"
"s_add_u32 s76, s90, 0xac00\n"
"ds_read_b64_tr_b16 v[92:93], v115 offset:9280\n"
"v_mfma_f32_16x16x32_bf16 v[156:159], v[100:103], v[44:47], v[156:159]\n"
"buffer_load_dwordx4 v[68:71], v121, s[64:67], s76 offen sc1\n"
"v_mfma_f32_16x16x32_bf16 v[156:159], v[96:99], v[40:43], v[156:159]\n"
"ds_read_b64_tr_b16 v[94:95], v115 offset:13888\n"
"s_waitcnt lgkmcnt(6)\n"
"v_mfma_f32_16x16x32_bf16 a[16:19], v[80:83], v[16:19], a[16:19]\n"
"s_waitcnt lgkmcnt(4)\n"
"v_mfma_f32_16x16x32_bf16 a[16:19], v[84:87], v[20:23], a[16:19]\n"
"s_add_u32 s76, s90, 0xf000\n"
"ds_read_b64_tr_b16 v[96:97], v115 offset:18496\n"
"v_mfma_f32_16x16x32_bf16 a[20:23], v[84:87], v[28:31], a[20:23]\n"
"buffer_load_dwordx4 v[72:75], v121, s[64:67], s76 offen sc1\n"
"v_mfma_f32_16x16x32_bf16 a[20:23], v[80:83], v[24:27], a[20:23]\n"
"ds_read_b64_tr_b16 v[98:99], v115 offset:23104\n"
"v_mfma_f32_16x16x32_bf16 a[24:27], v[80:83], v[32:35], a[24:27]\n"
"v_mfma_f32_16x16x32_bf16 a[24:27], v[84:87], v[36:39], a[24:27]\n"
"s_add_u32 s76, s90, 0xf400\n"
"ds_read_b64_tr_b16 v[100:101], v115 offset:27712\n"
"v_mfma_f32_16x16x32_bf16 a[28:31], v[84:87], v[44:47], a[28:31]\n"
"buffer_load_dwordx4 v[76:79], v121, s[64:67], s76 offen sc1\n"
"v_mfma_f32_16x16x32_bf16 a[28:31], v[80:83], v[40:43], a[28:31]\n"
"ds_read_b64_tr_b16 v[102:103], v115 offset:32320\n"
"s_waitcnt lgkmcnt(6)\n"
"v_mfma_f32_16x16x32_bf16 v[160:163], v[88:91], v[16:19], v[160:163]\n"
"s_waitcnt lgkmcnt(4)\n"
"v_mfma_f32_16x16x32_bf16 v[160:163], v[92:95], v[20:23], v[160:163]\n"
"s_add_u32 m0, s78, 0\n"
"ds_read_b64_tr_b16 v[80:81], v115 offset:96\n"
"v_mfma_f32_16x16x32_bf16 v[164:167], v[92:95], v[28:31], v[164:167]\n"
"buffer_load_dwordx4 v104, s[60:63], s79 offen lds\n"
"v_mfma_f32_16x16x32_bf16 v[164:167], v[88:91], v[24:27], v[164:167]\n"
"ds_read_b64_tr_b16 v[82:83], v115 offset:4704\n"
"v_mfma_f32_16x16x32_bf16 v[168:171], v[88:91], v[32:35], v[168:171]\n"
"v_mfma_f32_16x16x32_bf16 v[168:171], v[92:95], v[36:39], v[168:171]\n"
"ds_read_b64_tr_b16 v[84:85], v115 offset:9312\n"
"v_mfma_f32_16x16x32_bf16 v[172:175], v[92:95], v[44:47], v[172:175]\n"
"v_mfma_f32_16x16x32_bf16 v[172:175], v[88:91], v[40:43], v[172:175]\n"
"s_add_u32 m0, s78, 0x400\n"
"ds_read_b64_tr_b16 v[86:87], v115 offset:13920\n"
"s_waitcnt lgkmcnt(6)\n"
"v_mfma_f32_16x16x32_bf16 a[32:35], v[96:99], v[16:19], a[32:35]\n"
"buffer_load_dwordx4 v105, s[60:63], s79 offen lds\n"
"s_waitcnt lgkmcnt(4)\n"
"v_mfma_f32_16x16x32_bf16 a[32:35], v[100:103], v[20:23], a[32:35]\n"
"ds_read_b64_tr_b16 v[88:89], v115 offset:18528\n"
"v_mfma_f32_16x16x32_bf16 a[36:39], v[100:103], v[28:31], a[36:39]\n"
"v_mfma_f32_16x16x32_bf16 a[36:39], v[96:99], v[24:27], a[36:39]\n"
"ds_read_b64_tr_b16 v[90:91], v115 offset:23136\n"
"v_mfma_f32_16x16x32_bf16 a[40:43], v[96:99], v[32:35], a[40:43]\n"
"v_mfma_f32_16x16x32_bf16 a[40:43], v[100:103], v[36:39], a[40:43]\n"
"s_add_u32 m0, s78, 0x800\n"
"ds_read_b64_tr_b16 v[92:93], v115 offset:27744\n"
"v_mfma_f32_16x16x32_bf16 a[44:47], v[100:103], v[44:47], a[44:47]\n"
"buffer_load_dwordx4 v106, s[60:63], s79 offen lds\n"
"v_mfma_f32_16x16x32_bf16 a[44:47], v[96:99], v[40:43], a[44:47]\n"
"ds_read_b64_tr_b16 v[94:95], v115 offset:32352\n"
"s_waitcnt lgkmcnt(6)\n"
"v_mfma_f32_16x16x32_bf16 v[176:179], v[80:83], v[16:19], v[176:179]\n"
"s_waitcnt lgkmcnt(4)\n"
"v_mfma_f32_16x16x32_bf16 v[176:179], v[84:87], v[20:23], v[176:179]\n"
"ds_read_b64_tr_b16 v[96:97], v115 offset:128\n"
"v_mfma_f32_16x16x32_bf16 v[180:183], v[84:87], v[28:31], v[180:183]\n"
"v_mfma_f32_16x16x32_bf16 v[180:183], v[80:83], v[24:27], v[180:183]\n"
"s_add_u32 m0, s78, 0xc00\n"
"ds_read_b64_tr_b16 v[98:99], v115 offset:4736\n"
"v_mfma_f32_16x16x32_bf16 v[184:187], v[80:83], v[32:35], v[184:187]\n"
"buffer_load_dwordx4 v107, s[60:63], s79 offen lds\n"
"v_mfma_f32_16x16x32_bf16 v[184:187], v[84:87], v[36:39], v[184:187]\n"
"ds_read_b64_tr_b16 v[100:101], v115 offset:9344\n"
"v_mfma_f32_16x16x32_bf16 v[188:191], v[84:87], v[44:47], v[188:191]\n"
"v_mfma_f32_16x16x32_bf16 v[188:191], v[80:83], v[40:43], v[188:191]\n"
"ds_read_b64_tr_b16 v[102:103], v115 offset:13952\n"
"s_waitcnt lgkmcnt(6)\n"
"v_mfma_f32_16x16x32_bf16 a[48:51], v[88:91], v[16:19], a[48:51]\n"
"s_waitcnt lgkmcnt(4)\n"
"v_mfma_f32_16x16x32_bf16 a[48:51], v[92:95], v[20:23], a[48:51]\n"
"s_add_u32 m0, s78, 0x1000\n"
"ds_read_b64_tr_b16 v[80:81], v115 offset:18560\n"
"v_mfma_f32_16x16x32_bf16 a[52:55], v[92:95], v[28:31], a[52:55]\n"
"buffer_load_dwordx4 v108, s[60:63], s79 offen lds\n"
"v_mfma_f32_16x16x32_bf16 a[52:55], v[88:91], v[24:27], a[52:55]\n"
"ds_read_b64_tr_b16 v[82:83], v115 offset:23168\n"
"v_mfma_f32_16x16x32_bf16 a[56:59], v[88:91], v[32:35], a[56:59]\n"
"v_mfma_f32_16x16x32_bf16 a[56:59], v[92:95], v[36:39], a[56:59]\n"
"ds_read_b64_tr_b16 v[84:85], v115 offset:27776\n"
"v_mfma_f32_16x16x32_bf16 a[60:63], v[92:95], v[44:47], a[60:63]\n"
"v_mfma_f32_16x16x32_bf16 a[60:63], v[88:91], v[40:43], a[60:63]\n"
"s_add_u32 m0, s78, 0x1400\n"
"ds_read_b64_tr_b16 v[86:87], v115 offset:32384\n"
"s_waitcnt lgkmcnt(6)\n"
"v_mfma_f32_16x16x32_bf16 v[192:195], v[96:99], v[16:19], v[192:195]\n"
"buffer_load_dwordx4 v109, s[60:63], s79 offen lds\n"
"s_waitcnt lgkmcnt(4)\n"
"v_mfma_f32_16x16x32_bf16 v[192:195], v[100:103], v[20:23], v[192:195]\n"
"ds_read_b64_tr_b16 v[88:89], v115 offset:160\n"
"v_mfma_f32_16x16x32_bf16 v[196:199], v[100:103], v[28:31], v[196:199]\n"
"v_mfma_f32_16x16x32_bf16 v[196:199], v[96:99], v[24:27], v[196:199]\n"
"ds_read_b64_tr_b16 v[90:91], v115 offset:4768\n"
"v_mfma_f32_16x16x32_bf16 v[200:203], v[96:99], v[32:35], v[200:203]\n"
"v_mfma_f32_16x16x32_bf16 v[200:203], v[100:103], v[36:39], v[200:203]\n"
"s_add_u32 m0, s78, 0x1800\n"
"ds_read_b64_tr_b16 v[92:93], v115 offset:9376\n"
"v_mfma_f32_16x16x32_bf16 v[204:207], v[100:103], v[44:47], v[204:207]\n"
"buffer_load_dwordx4 v110, s[60:63], s79 offen lds\n"
"v_mfma_f32_16x16x32_bf16 v[204:207], v[96:99], v[40:43], v[204:207]\n"
"ds_read_b64_tr_b16 v[94:95], v115 offset:13984\n"
"s_waitcnt lgkmcnt(6)\n"
"v_mfma_f32_16x16x32_bf16 a[64:67], v[80:83], v[16:19], a[64:67]\n"
"s_waitcnt lgkmcnt(4)\n"
"v_mfma_f32_16x16x32_bf16 a[64:67], v[84:87], v[20:23], a[64:67]\n"
"ds_read_b64_tr_b16 v[96:97], v115 offset:18592\n"
"v_mfma_f32_16x16x32_bf16 a[68:71], v[84:87], v[28:31], a[68:71]\n"
"v_mfma_f32_16x16x32_bf16 a[68:71], v[80:83], v[24:27], a[68:71]\n"
"s_add_u32 m0, s78, 0x1c00\n"
"ds_read_b64_tr_b16 v[98:99], v115 offset:23200\n"
"v_mfma_f32_16x16x32_bf16 a[72:75], v[80:83], v[32:35], a[72:75]\n"
"buffer_load_dwordx4 v111, s[60:63], s79 offen lds\n"
"v_mfma_f32_16x16x32_bf16 a[72:75], v[84:87], v[36:39], a[72:75]\n"
"ds_read_b64_tr_b16 v[100:101], v115 offset:27808\n"
"v_mfma_f32_16x16x32_bf16 a[76:79], v[84:87], v[44:47], a[76:79]\n"
"v_mfma_f32_16x16x32_bf16 a[76:79], v[80:83], v[40:43], a[76:79]\n"
"ds_read_b64_tr_b16 v[102:103], v115 offset:32416\n"
"s_waitcnt lgkmcnt(6)\n"
"v_mfma_f32_16x16x32_bf16 v[208:211], v[88:91], v[16:19], v[208:211]\n"
"s_waitcnt lgkmcnt(4)\n"
"v_mfma_f32_16x16x32_bf16 v[208:211], v[92:95], v[20:23], v[208:211]\n"
"s_add_u32 m0, s78, 0x2000\n"
"ds_read_b64_tr_b16 v[80:81], v115 offset:192\n"
"v_mfma_f32_16x16x32_bf16 v[212:215], v[92:95], v[28:31], v[212:215]\n"
"buffer_load_dwordx4 v112, s[60:63], s79 offen lds\n"
"v_mfma_f32_16x16x32_bf16 v[212:215], v[88:91], v[24:27], v[212:215]\n"
"ds_read_b64_tr_b16 v[82:83], v115 offset:4800\n"
"v_mfma_f32_16x16x32_bf16 v[216:219], v[88:91], v[32:35], v[216:219]\n"
"v_mfma_f32_16x16x32_bf16 v[216:219], v[92:95], v[36:39], v[216:219]\n"
"ds_read_b64_tr_b16 v[84:85], v115 offset:9408\n"
"v_mfma_f32_16x16x32_bf16 v[220:223], v[92:95], v[44:47], v[220:223]\n"
"v_mfma_f32_16x16x32_bf16 v[220:223], v[88:91], v[40:43], v[220:223]\n"
"ds_read_b64_tr_b16 v[86:87], v115 offset:14016\n"
"s_waitcnt lgkmcnt(6)\n"
"v_mfma_f32_16x16x32_bf16 a[80:83], v[96:99], v[16:19], a[80:83]\n"
"s_add_u32 s79, s79, 0x90000\n"
"s_waitcnt lgkmcnt(4)\n"
"v_mfma_f32_16x16x32_bf16 a[80:83], v[100:103], v[20:23], a[80:83]\n"
"ds_read_b64_tr_b16 v[88:89], v115 offset:18624\n"
"v_mfma_f32_16x16x32_bf16 a[84:87], v[100:103], v[28:31], a[84:87]\n"
"v_mfma_f32_16x16x32_bf16 a[84:87], v[96:99], v[24:27], a[84:87]\n"
"ds_read_b64_tr_b16 v[90:91], v115 offset:23232\n"
"v_mfma_f32_16x16x32_bf16 a[88:91], v[96:99], v[32:35], a[88:91]\n"
"v_mfma_f32_16x16x32_bf16 a[88:91], v[100:103], v[36:39], a[88:91]\n"
"ds_read_b64_tr_b16 v[92:93], v115 offset:27840\n"
"v_mfma_f32_16x16x32_bf16 a[92:95], v[100:103], v[44:47], a[92:95]\n"
"v_mfma_f32_16x16x32_bf16 a[92:95], v[96:99], v[40:43], a[92:95]\n"
"ds_read_b64_tr_b16 v[94:95], v115 offset:32448\n"
"s_waitcnt lgkmcnt(6)\n"
"v_mfma_f32_16x16x32_bf16 v[224:227], v[80:83], v[16:19], v[224:227]\n"
"s_waitcnt lgkmcnt(4)\n"
"v_mfma_f32_16x16x32_bf16 v[224:227], v[84:87], v[20:23], v[224:227]\n"
"ds_read_b64_tr_b16 v[96:97], v115 offset:224\n"
"v_mfma_f32_16x16x32_bf16 v[228:231], v[84:87], v[28:31], v[228:231]\n"
"v_mfma_f32_16x16x32_bf16 v[228:231], v[80:83], v[24:27], v[228:231]\n"
"ds_read_b64_tr_b16 v[98:99], v115 offset:4832\n"
"v_mfma_f32_16x16x32_bf16 v[232:235], v[80:83], v[32:35], v[232:235]\n"
"v_mfma_f32_16x16x32_bf16 v[232:235], v[84:87], v[36:39], v[232:235]\n"
"ds_read_b64_tr_b16 v[100:101], v115 offset:9440\n"
"v_mfma_f32_16x16x32_bf16 v[236:239], v[84:87], v[44:47], v[236:239]\n"
"v_mfma_f32_16x16x32_bf16 v[236:239], v[80:83], v[40:43], v[236:239]\n"
"ds_read_b64_tr_b16 v[102:103], v115 offset:14048\n"
"s_waitcnt lgkmcnt(6)\n"
"v_mfma_f32_16x16x32_bf16 a[96:99], v[88:91], v[16:19], a[96:99]\n"
"s_waitcnt lgkmcnt(4)\n"
"v_mfma_f32_16x16x32_bf16 a[96:99], v[92:95], v[20:23], a[96:99]\n"
"ds_read_b64_tr_b16 v[80:81], v115 offset:18656\n"
"v_mfma_f32_16x16x32_bf16 a[100:103], v[92:95], v[28:31], a[100:103]\n"
"v_mfma_f32_16x16x32_bf16 a[100:103], v[88:91], v[24:27], a[100:103]\n"
"ds_read_b64_tr_b16 v[82:83], v115 offset:23264\n"
"v_mfma_f32_16x16x32_bf16 a[104:107], v[88:91], v[32:35], a[104:107]\n"
"v_mfma_f32_16x16x32_bf16 a[104:107], v[92:95], v[36:39], a[104:107]\n"
"ds_read_b64_tr_b16 v[84:85], v115 offset:27872\n"
"v_mfma_f32_16x16x32_bf16 a[108:111], v[92:95], v[44:47], a[108:111]\n"
"v_mfma_f32_16x16x32_bf16 a[108:111], v[88:91], v[40:43], a[108:111]\n"
"ds_read_b64_tr_b16 v[86:87], v115 offset:32480\n"
"s_waitcnt lgkmcnt(6)\n"
"v_mfma_f32_16x16x32_bf16 v[240:243], v[96:99], v[16:19], v[240:243]\n"
"s_waitcnt lgkmcnt(4)\n"
"v_mfma_f32_16x16x32_bf16 v[240:243], v[100:103], v[20:23], v[240:243]\n"
"ds_read_b64_tr_b16 v[88:89], v115 offset:256\n"
"v_mfma_f32_16x16x32_bf16 v[244:247], v[100:103], v[28:31], v[244:247]\n"
"v_mfma_f32_16x16x32_bf16 v[244:247], v[96:99], v[24:27], v[244:247]\n"
"ds_read_b64_tr_b16 v[90:91], v115 offset:4864\n"
"v_mfma_f32_16x16x32_bf16 v[248:251], v[96:99], v[32:35], v[248:251]\n"
"v_mfma_f32_16x16x32_bf16 v[248:251], v[100:103], v[36:39], v[248:251]\n"
"ds_read_b64_tr_b16 v[92:93], v115 offset:9472\n"
"v_mfma_f32_16x16x32_bf16 v[252:255], v[100:103], v[44:47], v[252:255]\n"
"v_mfma_f32_16x16x32_bf16 v[252:255], v[96:99], v[40:43], v[252:255]\n"
"ds_read_b64_tr_b16 v[94:95], v115 offset:14080\n"
"s_waitcnt lgkmcnt(6)\n"
"v_mfma_f32_16x16x32_bf16 a[112:115], v[80:83], v[16:19], a[112:115]\n"
"s_waitcnt lgkmcnt(4)\n"
"v_mfma_f32_16x16x32_bf16 a[112:115], v[84:87], v[20:23], a[112:115]\n"
"ds_read_b64_tr_b16 v[96:97], v115 offset:18688\n"
"v_mfma_f32_16x16x32_bf16 a[116:119], v[84:87], v[28:31], a[116:119]\n"
"v_mfma_f32_16x16x32_bf16 a[116:119], v[80:83], v[24:27], a[116:119]\n"
"ds_read_b64_tr_b16 v[98:99], v115 offset:23296\n"
"v_mfma_f32_16x16x32_bf16 a[120:123], v[80:83], v[32:35], a[120:123]\n"
"v_mfma_f32_16x16x32_bf16 a[120:123], v[84:87], v[36:39], a[120:123]\n"
"ds_read_b64_tr_b16 v[100:101], v115 offset:27904\n"
"v_mfma_f32_16x16x32_bf16 a[124:127], v[84:87], v[44:47], a[124:127]\n"
"v_mfma_f32_16x16x32_bf16 a[124:127], v[80:83], v[40:43], a[124:127]\n"
"s_waitcnt vmcnt(20)\n"
"s_barrier\n"
"ds_read_b64_tr_b16 v[102:103], v115 offset:32512\n"
"s_waitcnt lgkmcnt(6)\n"
"v_mfma_f32_16x16x32_bf16 a[144:147], v[88:91], v[16:19], a[144:147]\n"
"s_waitcnt lgkmcnt(4)\n"
"v_mfma_f32_16x16x32_bf16 a[144:147], v[92:95], v[20:23], a[144:147]\n"
"ds_read_b64_tr_b16 v[80:81], v116\n"
"v_mfma_f32_16x16x32_bf16 a[148:151], v[92:95], v[28:31], a[148:151]\n"
"v_mfma_f32_16x16x32_bf16 a[148:151], v[88:91], v[24:27], a[148:151]\n"
"ds_read_b64_tr_b16 v[82:83], v116 offset:4608\n"
"v_mfma_f32_16x16x32_bf16 a[152:155], v[88:91], v[32:35], a[152:155]\n"
"v_mfma_f32_16x16x32_bf16 a[152:155], v[92:95], v[36:39], a[152:155]\n"
"ds_read_b64_tr_b16 v[84:85], v116 offset:9216\n"
"v_mfma_f32_16x16x32_bf16 a[156:159], v[92:95], v[44:47], a[156:159]\n"
"v_mfma_f32_16x16x32_bf16 a[156:159], v[88:91], v[40:43], a[156:159]\n"
"ds_read_b64_tr_b16 v[86:87], v116 offset:13824\n"
"s_waitcnt lgkmcnt(6)\n"
"v_mfma_f32_16x16x32_bf16 a[128:131], v[96:99], v[16:19], a[128:131]\n"
"s_waitcnt lgkmcnt(4)\n"
"v_mfma_f32_16x16x32_bf16 a[128:131], v[100:103], v[20:23], a[128:131]\n"
"ds_read_b64_tr_b16 v[88:89], v116 offset:18432\n"
"v_mfma_f32_16x16x32_bf16 a[132:135], v[100:103], v[28:31], a[132:135]\n"
"v_mfma_f32_16x16x32_bf16 a[132:135], v[96:99], v[24:27], a[132:135]\n"
"ds_read_b64_tr_b16 v[90:91], v116 offset:23040\n"
"v_mfma_f32_16x16x32_bf16 a[136:139], v[96:99], v[32:35], a[136:139]\n"
"v_mfma_f32_16x16x32_bf16 a[136:139], v[100:103], v[36:39], a[136:139]\n"
"ds_read_b64_tr_b16 v[92:93], v116 offset:27648\n"
"v_mfma_f32_16x16x32_bf16 a[140:143], v[100:103], v[44:47], a[140:143]\n"
"v_mfma_f32_16x16x32_bf16 a[140:143], v[96:99], v[40:43], a[140:143]\n"
"ds_read_b64_tr_b16 v[94:95], v116 offset:32256\n"
"s_waitcnt vmcnt(16) lgkmcnt(6)\n"
"v_mfma_f32_16x16x32_bf16 v[128:131], v[80:83], v[48:51], v[128:131]\n"
"s_waitcnt vmcnt(15) lgkmcnt(4)\n"
"v_mfma_f32_16x16x32_bf16 v[128:131], v[84:87], v[52:55], v[128:131]\n"
"s_add_u32 s76, s90, 0x2000\n"
"ds_read_b64_tr_b16 v[96:97], v116 offset:32\n"
"s_waitcnt vmcnt(13)\n"
"v_mfma_f32_16x16x32_bf16 v[132:135], v[84:87], v[60:63], v[132:135]\n"
"buffer_load_dwordx4 v[16:19], v121, s[64:67], s76 offen sc1\n"
"v_mfma_f32_16x16x32_bf16 v[132:135], v[80:83], v[56:59], v[132:135]\n"
"ds_read_b64_tr_b16 v[98:99], v116 offset:4640\n"
"s_waitcnt vmcnt(13)\n"
"v_mfma_f32_16x16x32_bf16 v[136:139], v[80:83], v[64:67], v[136:139]\n"
"s_waitcnt vmcnt(12)\n"
"v_mfma_f32_16x16x32_bf16 v[136:139], v[84:87], v[68:71], v[136:139]\n"
"s_add_u32 s76, s90, 0x2400\n"
"ds_read_b64_tr_b16 v[100:101], v116 offset:9248\n"
"s_waitcnt vmcnt(10)\n"
"v_mfma_f32_16x16x32_bf16 v[140:143], v[84:87], v[76:79], v[140:143]\n"
"buffer_load_dwordx4 v[20:23], v121, s[64:67], s76 offen sc1\n"
"v_mfma_f32_16x16x32_bf16 v[140:143], v[80:83], v[72:75], v[140:143]\n"
"ds_read_b64_tr_b16 v[102:103], v116 offset:13856\n"
"s_waitcnt lgkmcnt(6)\n"
"v_mfma_f32_16x16x32_bf16 a[0:3], v[88:91], v[48:51], a[0:3]\n"
"s_waitcnt lgkmcnt(4)\n"
"v_mfma_f32_16x16x32_bf16 a[0:3], v[92:95], v[52:55], a[0:3]\n"
"s_add_u32 s76, s90, 0x6800\n"
"ds_read_b64_tr_b16 v[80:81], v116 offset:18464\n"
"v_mfma_f32_16x16x32_bf16 a[4:7], v[92:95], v[60:63], a[4:7]\n"
"buffer_load_dwordx4 v[24:27], v121, s[64:67], s76 offen sc1\n"
"v_mfma_f32_16x16x32_bf16 a[4:7], v[88:91], v[56:59], a[4:7]\n"
"ds_read_b64_tr_b16 v[82:83], v116 offset:23072\n"
"v_mfma_f32_16x16x32_bf16 a[8:11], v[88:91], v[64:67], a[8:11]\n"
"v_mfma_f32_16x16x32_bf16 a[8:11], v[92:95], v[68:71], a[8:11]\n"
"s_add_u32 s76, s90, 0x6c00\n"
"ds_read_b64_tr_b16 v[84:85], v116 offset:27680\n"
"v_mfma_f32_16x16x32_bf16 a[12:15], v[92:95], v[76:79], a[12:15]\n"
"buffer_load_dwordx4 v[28:31], v121, s[64:67], s76 offen sc1\n"
"v_mfma_f32_16x16x32_bf16 a[12:15], v[88:91], v[72:75], a[12:15]\n"
"ds_read_b64_tr_b16 v[86:87], v116 offset:32288\n"
"s_waitcnt lgkmcnt(6)\n"
"v_mfma_f32_16x16x32_bf16 v[144:147], v[96:99], v[48:51], v[144:147]\n"
"s_waitcnt lgkmcnt(4)\n"
"v_mfma_f32_16x16x32_bf16 v[144:147], v[100:103], v[52:55], v[144:147]\n"
"s_add_u32 s76, s90, 0xb000\n"
"ds_read_b64_tr_b16 v[88:89], v116 offset:64\n"
"v_mfma_f32_16x16x32_bf16 v[148:151], v[100:103], v[60:63], v[148:151]\n"
"buffer_load_dwordx4 v[32:35], v121, s[64:67], s76 offen sc1\n"
"v_mfma_f32_16x16x32_bf16 v[148:151], v[96:99], v[56:59], v[148:151]\n"
"ds_read_b64_tr_b16 v[90:91], v116 offset:4672\n"
"v_mfma_f32_16x16x32_bf16 v[152:155], v[96:99], v[64:67], v[152:155]\n"
"v_mfma_f32_16x16x32_bf16 v[152:155], v[100:103], v[68:71], v[152:155]\n"
"s_add_u32 s76, s90, 0xb400\n"
"ds_read_b64_tr_b16 v[92:93], v116 offset:9280\n"
"v_mfma_f32_16x16x32_bf16 v[156:159], v[100:103], v[76:79], v[156:159]\n"
"buffer_load_dwordx4 v[36:39], v121, s[64:67], s76 offen sc1\n"
"v_mfma_f32_16x16x32_bf16 v[156:159], v[96:99], v[72:75], v[156:159]\n"
"ds_read_b64_tr_b16 v[94:95], v116 offset:13888\n"
"s_waitcnt lgkmcnt(6)\n"
"v_mfma_f32_16x16x32_bf16 a[16:19], v[80:83], v[48:51], a[16:19]\n"
"s_waitcnt lgkmcnt(4)\n"
"v_mfma_f32_16x16x32_bf16 a[16:19], v[84:87], v[52:55], a[16:19]\n"
"s_add_u32 s76, s90, 0xf800\n"
"ds_read_b64_tr_b16 v[96:97], v116 offset:18496\n"
"v_mfma_f32_16x16x32_bf16 a[20:23], v[84:87], v[60:63], a[20:23]\n"
"buffer_load_dwordx4 v[40:43], v121, s[64:67], s76 offen sc1\n"
"v_mfma_f32_16x16x32_bf16 a[20:23], v[80:83], v[56:59], a[20:23]\n"
"ds_read_b64_tr_b16 v[98:99], v116 offset:23104\n"
"v_mfma_f32_16x16x32_bf16 a[24:27], v[80:83], v[64:67], a[24:27]\n"
"v_mfma_f32_16x16x32_bf16 a[24:27], v[84:87], v[68:71], a[24:27]\n"
"s_add_u32 s76, s90, 0xfc00\n"
"ds_read_b64_tr_b16 v[100:101], v116 offset:27712\n"
"v_mfma_f32_16x16x32_bf16 a[28:31], v[84:87], v[76:79], a[28:31]\n"
"buffer_load_dwordx4 v[44:47], v121, s[64:67], s76 offen sc1\n"
"v_mfma_f32_16x16x32_bf16 a[28:31], v[80:83], v[72:75], a[28:31]\n"
"ds_read_b64_tr_b16 v[102:103], v116 offset:32320\n"
"s_waitcnt lgkmcnt(6)\n"
"v_mfma_f32_16x16x32_bf16 v[160:163], v[88:91], v[48:51], v[160:163]\n"
"s_waitcnt lgkmcnt(4)\n"
"v_mfma_f32_16x16x32_bf16 v[160:163], v[92:95], v[52:55], v[160:163]\n"
"s_add_u32 m0, s78, 0x9000\n"
"ds_read_b64_tr_b16 v[80:81], v116 offset:96\n"
"v_mfma_f32_16x16x32_bf16 v[164:167], v[92:95], v[60:63], v[164:167]\n"
"buffer_load_dwordx4 v104, s[60:63], s79 offen lds\n"
"v_mfma_f32_16x16x32_bf16 v[164:167], v[88:91], v[56:59], v[164:167]\n"
"ds_read_b64_tr_b16 v[82:83], v116 offset:4704\n"
"v_mfma_f32_16x16x32_bf16 v[168:171], v[88:91], v[64:67], v[168:171]\n"
"v_mfma_f32_16x16x32_bf16 v[168:171], v[92:95], v[68:71], v[168:171]\n"
"ds_read_b64_tr_b16 v[84:85], v116 offset:9312\n"
"v_mfma_f32_16x16x32_bf16 v[172:175], v[92:95], v[76:79], v[172:175]\n"
"v_mfma_f32_16x16x32_bf16 v[172:175], v[88:91], v[72:75], v[172:175]\n"
"s_add_u32 m0, s78, 0x9400\n"
"ds_read_b64_tr_b16 v[86:87], v116 offset:13920\n"
"s_waitcnt lgkmcnt(6)\n"
"v_mfma_f32_16x16x32_bf16 a[32:35], v[96:99], v[48:51], a[32:35]\n"
"buffer_load_dwordx4 v105, s[60:63], s79 offen lds\n"
"s_waitcnt lgkmcnt(4)\n"
"v_mfma_f32_16x16x32_bf16 a[32:35], v[100:103], v[52:55], a[32:35]\n"
"ds_read_b64_tr_b16 v[88:89], v116 offset:18528\n"
"v_mfma_f32_16x16x32_bf16 a[36:39], v[100:103], v[60:63], a[36:39]\n"
"v_mfma_f32_16x16x32_bf16 a[36:39], v[96:99], v[56:59], a[36:39]\n"
"ds_read_b64_tr_b16 v[90:91], v116 offset:23136\n"
"v_mfma_f32_16x16x32_bf16 a[40:43], v[96:99], v[64:67], a[40:43]\n"
"v_mfma_f32_16x16x32_bf16 a[40:43], v[100:103], v[68:71], a[40:43]\n"
"s_add_u32 m0, s78, 0x9800\n"
"ds_read_b64_tr_b16 v[92:93], v116 offset:27744\n"
"v_mfma_f32_16x16x32_bf16 a[44:47], v[100:103], v[76:79], a[44:47]\n"
"buffer_load_dwordx4 v106, s[60:63], s79 offen lds\n"
"v_mfma_f32_16x16x32_bf16 a[44:47], v[96:99], v[72:75], a[44:47]\n"
"ds_read_b64_tr_b16 v[94:95], v116 offset:32352\n"
"s_waitcnt lgkmcnt(6)\n"
"v_mfma_f32_16x16x32_bf16 v[176:179], v[80:83], v[48:51], v[176:179]\n"
"s_waitcnt lgkmcnt(4)\n"
"v_mfma_f32_16x16x32_bf16 v[176:179], v[84:87], v[52:55], v[176:179]\n"
"ds_read_b64_tr_b16 v[96:97], v116 offset:128\n"
"v_mfma_f32_16x16x32_bf16 v[180:183], v[84:87], v[60:63], v[180:183]\n"
"v_mfma_f32_16x16x32_bf16 v[180:183], v[80:83], v[56:59], v[180:183]\n"
"s_add_u32 m0, s78, 0x9c00\n"
"ds_read_b64_tr_b16 v[98:99], v116 offset:4736\n"
"v_mfma_f32_16x16x32_bf16 v[184:187], v[80:83], v[64:67], v[184:187]\n"
"buffer_load_dwordx4 v107, s[60:63], s79 offen lds\n"
"v_mfma_f32_16x16x32_bf16 v[184:187], v[84:87], v[68:71], v[184:187]\n"
"ds_read_b64_tr_b16 v[100:101], v116 offset:9344\n"
"v_mfma_f32_16x16x32_bf16 v[188:191], v[84:87], v[76:79], v[188:191]\n"
"v_mfma_f32_16x16x32_bf16 v[188:191], v[80:83], v[72:75], v[188:191]\n"
"ds_read_b64_tr_b16 v[102:103], v116 offset:13952\n"
"s_waitcnt lgkmcnt(6)\n"
"v_mfma_f32_16x16x32_bf16 a[48:51], v[88:91], v[48:51], a[48:51]\n"
"s_waitcnt lgkmcnt(4)\n"
"v_mfma_f32_16x16x32_bf16 a[48:51], v[92:95], v[52:55], a[48:51]\n"
"s_add_u32 m0, s78, 0xa000\n"
"ds_read_b64_tr_b16 v[80:81], v116 offset:18560\n"
"v_mfma_f32_16x16x32_bf16 a[52:55], v[92:95], v[60:63], a[52:55]\n"
"buffer_load_dwordx4 v108, s[60:63], s79 offen lds\n"
"v_mfma_f32_16x16x32_bf16 a[52:55], v[88:91], v[56:59], a[52:55]\n"
"ds_read_b64_tr_b16 v[82:83], v116 offset:23168\n"
"v_mfma_f32_16x16x32_bf16 a[56:59], v[88:91], v[64:67], a[56:59]\n"
"v_mfma_f32_16x16x32_bf16 a[56:59], v[92:95], v[68:71], a[56:59]\n"
"ds_read_b64_tr_b16 v[84:85], v116 offset:27776\n"
"v_mfma_f32_16x16x32_bf16 a[60:63], v[92:95], v[76:79], a[60:63]\n"
"v_mfma_f32_16x16x32_bf16 a[60:63], v[88:91], v[72:75], a[60:63]\n"
"s_add_u32 m0, s78, 0xa400\n"
"ds_read_b64_tr_b16 v[86:87], v116 offset:32384\n"
"s_waitcnt lgkmcnt(6)\n"
"v_mfma_f32_16x16x32_bf16 v[192:195], v[96:99], v[48:51], v[192:195]\n"
"buffer_load_dwordx4 v109, s[60:63], s79 offen lds\n"
"s_waitcnt lgkmcnt(4)\n"
"v_mfma_f32_16x16x32_bf16 v[192:195], v[100:103], v[52:55], v[192:195]\n"
"ds_read_b64_tr_b16 v[88:89], v116 offset:160\n"
"v_mfma_f32_16x16x32_bf16 v[196:199], v[100:103], v[60:63], v[196:199]\n"
"v_mfma_f32_16x16x32_bf16 v[196:199], v[96:99], v[56:59], v[196:199]\n"
"ds_read_b64_tr_b16 v[90:91], v116 offset:4768\n"
"v_mfma_f32_16x16x32_bf16 v[200:203], v[96:99], v[64:67], v[200:203]\n"
"v_mfma_f32_16x16x32_bf16 v[200:203], v[100:103], v[68:71], v[200:203]\n"
"s_add_u32 m0, s78, 0xa800\n"
"ds_read_b64_tr_b16 v[92:93], v116 offset:9376\n"
"v_mfma_f32_16x16x32_bf16 v[204:207], v[100:103], v[76:79], v[204:207]\n"
"buffer_load_dwordx4 v110, s[60:63], s79 offen lds\n"
"v_mfma_f32_16x16x32_bf16 v[204:207], v[96:99], v[72:75], v[204:207]\n"
"ds_read_b64_tr_b16 v[94:95], v116 offset:13984\n"
"s_waitcnt lgkmcnt(6)\n"
"v_mfma_f32_16x16x32_bf16 a[64:67], v[80:83], v[48:51], a[64:67]\n"
"s_waitcnt lgkmcnt(4)\n"
"v_mfma_f32_16x16x32_bf16 a[64:67], v[84:87], v[52:55], a[64:67]\n"
"ds_read_b64_tr_b16 v[96:97], v116 offset:18592\n"
"v_mfma_f32_16x16x32_bf16 a[68:71], v[84:87], v[60:63], a[68:71]\n"
"v_mfma_f32_16x16x32_bf16 a[68:71], v[80:83], v[56:59], a[68:71]\n"
"s_add_u32 m0, s78, 0xac00\n"
"ds_read_b64_tr_b16 v[98:99], v116 offset:23200\n"
"v_mfma_f32_16x16x32_bf16 a[72:75], v[80:83], v[64:67], a[72:75]\n"
"buffer_load_dwordx4 v111, s[60:63], s79 offen lds\n"
"v_mfma_f32_16x16x32_bf16 a[72:75], v[84:87], v[68:71], a[72:75]\n"
"ds_read_b64_tr_b16 v[100:101], v116 offset:27808\n"
"v_mfma_f32_16x16x32_bf16 a[76:79], v[84:87], v[76:79], a[76:79]\n"
"v_mfma_f32_16x16x32_bf16 a[76:79], v[80:83], v[72:75], a[76:79]\n"
"ds_read_b64_tr_b16 v[102:103], v116 offset:32416\n"
"s_waitcnt lgkmcnt(6)\n"
"v_mfma_f32_16x16x32_bf16 v[208:211], v[88:91], v[48:51], v[208:211]\n"
"s_waitcnt lgkmcnt(4)\n"
"v_mfma_f32_16x16x32_bf16 v[208:211], v[92:95], v[52:55], v[208:211]\n"
"s_add_u32 m0, s78, 0xb000\n"
"ds_read_b64_tr_b16 v[80:81], v116 offset:192\n"
"v_mfma_f32_16x16x32_bf16 v[212:215], v[92:95], v[60:63], v[212:215]\n"
"buffer_load_dwordx4 v112, s[60:63], s79 offen lds\n"
"v_mfma_f32_16x16x32_bf16 v[212:215], v[88:91], v[56:59], v[212:215]\n"
"ds_read_b64_tr_b16 v[82:83], v116 offset:4800\n"
"v_mfma_f32_16x16x32_bf16 v[216:219], v[88:91], v[64:67], v[216:219]\n"
"v_mfma_f32_16x16x32_bf16 v[216:219], v[92:95], v[68:71], v[216:219]\n"
"ds_read_b64_tr_b16 v[84:85], v116 offset:9408\n"
"v_mfma_f32_16x16x32_bf16 v[220:223], v[92:95], v[76:79], v[220:223]\n"
"v_mfma_f32_16x16x32_bf16 v[220:223], v[88:91], v[72:75], v[220:223]\n"
"ds_read_b64_tr_b16 v[86:87], v116 offset:14016\n"
"s_waitcnt lgkmcnt(6)\n"
"v_mfma_f32_16x16x32_bf16 a[80:83], v[96:99], v[48:51], a[80:83]\n"
"s_add_u32 s79, s79, 0x90000\n"
"s_waitcnt lgkmcnt(4)\n"
"v_mfma_f32_16x16x32_bf16 a[80:83], v[100:103], v[52:55], a[80:83]\n"
"ds_read_b64_tr_b16 v[88:89], v116 offset:18624\n"
"v_mfma_f32_16x16x32_bf16 a[84:87], v[100:103], v[60:63], a[84:87]\n"
"v_mfma_f32_16x16x32_bf16 a[84:87], v[96:99], v[56:59], a[84:87]\n"
"ds_read_b64_tr_b16 v[90:91], v116 offset:23232\n"
"v_mfma_f32_16x16x32_bf16 a[88:91], v[96:99], v[64:67], a[88:91]\n"
"v_mfma_f32_16x16x32_bf16 a[88:91], v[100:103], v[68:71], a[88:91]\n"
"ds_read_b64_tr_b16 v[92:93], v116 offset:27840\n"
"v_mfma_f32_16x16x32_bf16 a[92:95], v[100:103], v[76:79], a[92:95]\n"
"v_mfma_f32_16x16x32_bf16 a[92:95], v[96:99], v[72:75], a[92:95]\n"
"ds_read_b64_tr_b16 v[94:95], v116 offset:32448\n"
"s_waitcnt lgkmcnt(6)\n"
"v_mfma_f32_16x16x32_bf16 v[224:227], v[80:83], v[48:51], v[224:227]\n"
"s_waitcnt lgkmcnt(4)\n"
"v_mfma_f32_16x16x32_bf16 v[224:227], v[84:87], v[52:55], v[224:227]\n"
"ds_read_b64_tr_b16 v[96:97], v116 offset:224\n"
"v_mfma_f32_16x16x32_bf16 v[228:231], v[84:87], v[60:63], v[228:231]\n"
"v_mfma_f32_16x16x32_bf16 v[228:231], v[80:83], v[56:59], v[228:231]\n"
"ds_read_b64_tr_b16 v[98:99], v116 offset:4832\n"
"v_mfma_f32_16x16x32_bf16 v[232:235], v[80:83], v[64:67], v[232:235]\n"
"v_mfma_f32_16x16x32_bf16 v[232:235], v[84:87], v[68:71], v[232:235]\n"
"ds_read_b64_tr_b16 v[100:101], v116 offset:9440\n"
"v_mfma_f32_16x16x32_bf16 v[236:239], v[84:87], v[76:79], v[236:239]\n"
"v_mfma_f32_16x16x32_bf16 v[236:239], v[80:83], v[72:75], v[236:239]\n"
"ds_read_b64_tr_b16 v[102:103], v116 offset:14048\n"
"s_waitcnt lgkmcnt(6)\n"
"v_mfma_f32_16x16x32_bf16 a[96:99], v[88:91], v[48:51], a[96:99]\n"
"s_waitcnt lgkmcnt(4)\n"
"v_mfma_f32_16x16x32_bf16 a[96:99], v[92:95], v[52:55], a[96:99]\n"
"ds_read_b64_tr_b16 v[80:81], v116 offset:18656\n"
"v_mfma_f32_16x16x32_bf16 a[100:103], v[92:95], v[60:63], a[100:103]\n"
"v_mfma_f32_16x16x32_bf16 a[100:103], v[88:91], v[56:59], a[100:103]\n"
"ds_read_b64_tr_b16 v[82:83], v116 offset:23264\n"
"v_mfma_f32_16x16x32_bf16 a[104:107], v[88:91], v[64:67], a[104:107]\n"
"v_mfma_f32_16x16x32_bf16 a[104:107], v[92:95], v[68:71], a[104:107]\n"
"ds_read_b64_tr_b16 v[84:85], v116 offset:27872\n"
"v_mfma_f32_16x16x32_bf16 a[108:111], v[92:95], v[76:79], a[108:111]\n"
"v_mfma_f32_16x16x32_bf16 a[108:111], v[88:91], v[72:75], a[108:111]\n"
"ds_read_b64_tr_b16 v[86:87], v116 offset:32480\n"
"s_waitcnt lgkmcnt(6)\n"
"v_mfma_f32_16x16x32_bf16 v[240:243], v[96:99], v[48:51], v[240:243]\n"
"s_waitcnt lgkmcnt(4)\n"
"v_mfma_f32_16x16x32_bf16 v[240:243], v[100:103], v[52:55], v[240:243]\n"
"ds_read_b64_tr_b16 v[88:89], v116 offset:256\n"
"v_mfma_f32_16x16x32_bf16 v[244:247], v[100:103], v[60:63], v[244:247]\n"
"v_mfma_f32_16x16x32_bf16 v[244:247], v[96:99], v[56:59], v[244:247]\n"
"ds_read_b64_tr_b16 v[90:91], v116 offset:4864\n"
"v_mfma_f32_16x16x32_bf16 v[248:251], v[96:99], v[64:67], v[248:251]\n"
"v_mfma_f32_16x16x32_bf16 v[248:251], v[100:103], v[68:71], v[248:251]\n"
"ds_read_b64_tr_b16 v[92:93], v116 offset:9472\n"
"v_mfma_f32_16x16x32_bf16 v[252:255], v[100:103], v[76:79], v[252:255]\n"
"v_mfma_f32_16x16x32_bf16 v[252:255], v[96:99], v[72:75], v[252:255]\n"
"ds_read_b64_tr_b16 v[94:95], v116 offset:14080\n"
"s_waitcnt lgkmcnt(6)\n"
"v_mfma_f32_16x16x32_bf16 a[112:115], v[80:83], v[48:51], a[112:115]\n"
"s_waitcnt lgkmcnt(4)\n"
"v_mfma_f32_16x16x32_bf16 a[112:115], v[84:87], v[52:55], a[112:115]\n"
"ds_read_b64_tr_b16 v[96:97], v116 offset:18688\n"
"v_mfma_f32_16x16x32_bf16 a[116:119], v[84:87], v[60:63], a[116:119]\n"
"v_mfma_f32_16x16x32_bf16 a[116:119], v[80:83], v[56:59], a[116:119]\n"
"ds_read_b64_tr_b16 v[98:99], v116 offset:23296\n"
"v_mfma_f32_16x16x32_bf16 a[120:123], v[80:83], v[64:67], a[120:123]\n"
"v_mfma_f32_16x16x32_bf16 a[120:123], v[84:87], v[68:71], a[120:123]\n"
"ds_read_b64_tr_b16 v[100:101], v116 offset:27904\n"
"v_mfma_f32_16x16x32_bf16 a[124:127], v[84:87], v[76:79], a[124:127]\n"
"v_mfma_f32_16x16x32_bf16 a[124:127], v[80:83], v[72:75], a[124:127]\n"
"s_waitcnt vmcnt(17)\n"
"s_barrier\n"
"ds_read_b64_tr_b16 v[102:103], v116 offset:32512\n"
"s_waitcnt lgkmcnt(6)\n"
"v_mfma_f32_16x16x32_bf16 a[144:147], v[88:91], v[48:51], a[144:147]\n"
"s_waitcnt lgkmcnt(4)\n"
"v_mfma_f32_16x16x32_bf16 a[144:147], v[92:95], v[52:55], a[144:147]\n"
"ds_read_b64_tr_b16 v[80:81], v113\n"
"v_mfma_f32_16x16x32_bf16 a[148:151], v[92:95], v[60:63], a[148:151]\n"
"v_mfma_f32_16x16x32_bf16 a[148:151], v[88:91], v[56:59], a[148:151]\n"
"ds_read_b64_tr_b16 v[82:83], v113 offset:4608\n"
"v_mfma_f32_16x16x32_bf16 a[152:155], v[88:91], v[64:67], a[152:155]\n"
"v_mfma_f32_16x16x32_bf16 a[152:155], v[92:95], v[68:71], a[152:155]\n"
"ds_read_b64_tr_b16 v[84:85], v113 offset:9216\n"
"v_mfma_f32_16x16x32_bf16 a[156:159], v[92:95], v[76:79], a[156:159]\n"
"v_mfma_f32_16x16x32_bf16 a[156:159], v[88:91], v[72:75], a[156:159]\n"
"ds_read_b64_tr_b16 v[86:87], v113 offset:13824\n"
"s_waitcnt lgkmcnt(6)\n"
"v_mfma_f32_16x16x32_bf16 a[128:131], v[96:99], v[48:51], a[128:131]\n"
"s_waitcnt lgkmcnt(4)\n"
"v_mfma_f32_16x16x32_bf16 a[128:131], v[100:103], v[52:55], a[128:131]\n"
"ds_read_b64_tr_b16 v[88:89], v113 offset:18432\n"
"v_mfma_f32_16x16x32_bf16 a[132:135], v[100:103], v[60:63], a[132:135]\n"
"v_mfma_f32_16x16x32_bf16 a[132:135], v[96:99], v[56:59], a[132:135]\n"
"ds_read_b64_tr_b16 v[90:91], v113 offset:23040\n"
"v_mfma_f32_16x16x32_bf16 a[136:139], v[96:99], v[64:67], a[136:139]\n"
"v_mfma_f32_16x16x32_bf16 a[136:139], v[100:103], v[68:71], a[136:139]\n"
"ds_read_b64_tr_b16 v[92:93], v113 offset:27648\n"
"v_mfma_f32_16x16x32_bf16 a[140:143], v[100:103], v[76:79], a[140:143]\n"
"v_mfma_f32_16x16x32_bf16 a[140:143], v[96:99], v[72:75], a[140:143]\n"
"ds_read_b64_tr_b16 v[94:95], v113 offset:32256\n"
"s_waitcnt vmcnt(16) lgkmcnt(6)\n"
"v_mfma_f32_16x16x32_bf16 v[128:131], v[80:83], v[16:19], v[128:131]\n"
"s_waitcnt vmcnt(15) lgkmcnt(4)\n"
"v_mfma_f32_16x16x32_bf16 v[128:131], v[84:87], v[20:23], v[128:131]\n"
"s_add_u32 s76, s90, 0x2800\n"
"ds_read_b64_tr_b16 v[96:97], v113 offset:32\n"
"s_waitcnt vmcnt(13)\n"
"v_mfma_f32_16x16x32_bf16 v[132:135], v[84:87], v[28:31], v[132:135]\n"
"buffer_load_dwordx4 v[48:51], v121, s[64:67], s76 offen sc1\n"
"v_mfma_f32_16x16x32_bf16 v[132:135], v[80:83], v[24:27], v[132:135]\n"
"ds_read_b64_tr_b16 v[98:99], v113 offset:4640\n"
"s_waitcnt vmcnt(13)\n"
"v_mfma_f32_16x16x32_bf16 v[136:139], v[80:83], v[32:35], v[136:139]\n"
"s_waitcnt vmcnt(12)\n"
"v_mfma_f32_16x16x32_bf16 v[136:139], v[84:87], v[36:39], v[136:139]\n"
"s_add_u32 s76, s90, 0x2c00\n"
"ds_read_b64_tr_b16 v[100:101], v113 offset:9248\n"
"s_waitcnt vmcnt(10)\n"
"v_mfma_f32_16x16x32_bf16 v[140:143], v[84:87], v[44:47], v[140:143]\n"
"buffer_load_dwordx4 v[52:55], v121, s[64:67], s76 offen sc1\n"
"v_mfma_f32_16x16x32_bf16 v[140:143], v[80:83], v[40:43], v[140:143]\n"
"ds_read_b64_tr_b16 v[102:103], v113 offset:13856\n"
"s_waitcnt lgkmcnt(6)\n"
"v_mfma_f32_16x16x32_bf16 a[0:3], v[88:91], v[16:19], a[0:3]\n"
"s_waitcnt lgkmcnt(4)\n"
"v_mfma_f32_16x16x32_bf16 a[0:3], v[92:95], v[20:23], a[0:3]\n"
"s_add_u32 s76, s90, 0x7000\n"
"ds_read_b64_tr_b16 v[80:81], v113 offset:18464\n"
"v_mfma_f32_16x16x32_bf16 a[4:7], v[92:95], v[28:31], a[4:7]\n"
"buffer_load_dwordx4 v[56:59], v121, s[64:67], s76 offen sc1\n"
"v_mfma_f32_16x16x32_bf16 a[4:7], v[88:91], v[24:27], a[4:7]\n"
"ds_read_b64_tr_b16 v[82:83], v113 offset:23072\n"
"v_mfma_f32_16x16x32_bf16 a[8:11], v[88:91], v[32:35], a[8:11]\n"
"v_mfma_f32_16x16x32_bf16 a[8:11], v[92:95], v[36:39], a[8:11]\n"
"s_add_u32 s76, s90, 0x7400\n"
"ds_read_b64_tr_b16 v[84:85], v113 offset:27680\n"
"v_mfma_f32_16x16x32_bf16 a[12:15], v[92:95], v[44:47], a[12:15]\n"
"buffer_load_dwordx4 v[60:63], v121, s[64:67], s76 offen sc1\n"
"v_mfma_f32_16x16x32_bf16 a[12:15], v[88:91], v[40:43], a[12:15]\n"
"ds_read_b64_tr_b16 v[86:87], v113 offset:32288\n"
"s_waitcnt lgkmcnt(6)\n"
"v_mfma_f32_16x16x32_bf16 v[144:147], v[96:99], v[16:19], v[144:147]\n"
"s_waitcnt lgkmcnt(4)\n"
"v_mfma_f32_16x16x32_bf16 v[144:147], v[100:103], v[20:23], v[144:147]\n"
"s_add_u32 s76, s90, 0xb800\n"
"ds_read_b64_tr_b16 v[88:89], v113 offset:64\n"
"v_mfma_f32_16x16x32_bf16 v[148:151], v[100:103], v[28:31], v[148:151]\n"
"buffer_load_dwordx4 v[64:67], v121, s[64:67], s76 offen sc1\n"
"v_mfma_f32_16x16x32_bf16 v[148:151], v[96:99], v[24:27], v[148:151]\n"
"ds_read_b64_tr_b16 v[90:91], v113 offset:4672\n"
"v_mfma_f32_16x16x32_bf16 v[152:155], v[96:99], v[32:35], v[152:155]\n"
"v_mfma_f32_16x16x32_bf16 v[152:155], v[100:103], v[36:39], v[152:155]\n"
"s_add_u32 s76, s90, 0xbc00\n"
"ds_read_b64_tr_b16 v[92:93], v113 offset:9280\n"
"v_mfma_f32_16x16x32_bf16 v[156:159], v[100:103], v[44:47], v[156:159]\n"
"buffer_load_dwordx4 v[68:71], v121, s[64:67], s76 offen sc1\n"
"v_mfma_f32_16x16x32_bf16 v[156:159], v[96:99], v[40:43], v[156:159]\n"
"ds_read_b64_tr_b16 v[94:95], v113 offset:13888\n"
"s_waitcnt lgkmcnt(6)\n"
"v_mfma_f32_16x16x32_bf16 a[16:19], v[80:83], v[16:19], a[16:19]\n"
"s_waitcnt lgkmcnt(4)\n"
"v_mfma_f32_16x16x32_bf16 a[16:19], v[84:87], v[20:23], a[16:19]\n"
"s_add_u32 s76, s90, 0x10000\n"
"ds_read_b64_tr_b16 v[96:97], v113 offset:18496\n"
"v_mfma_f32_16x16x32_bf16 a[20:23], v[84:87], v[28:31], a[20:23]\n"
"buffer_load_dwordx4 v[72:75], v121, s[64:67], s76 offen sc1\n"
"v_mfma_f32_16x16x32_bf16 a[20:23], v[80:83], v[24:27], a[20:23]\n"
"ds_read_b64_tr_b16 v[98:99], v113 offset:23104\n"
"v_mfma_f32_16x16x32_bf16 a[24:27], v[80:83], v[32:35], a[24:27]\n"
"v_mfma_f32_16x16x32_bf16 a[24:27], v[84:87], v[36:39], a[24:27]\n"
"s_add_u32 s76, s90, 0x10400\n"
"ds_read_b64_tr_b16 v[100:101], v113 offset:27712\n"
"v_mfma_f32_16x16x32_bf16 a[28:31], v[84:87], v[44:47], a[28:31]\n"
"buffer_load_dwordx4 v[76:79], v121, s[64:67], s76 offen sc1\n"
"v_mfma_f32_16x16x32_bf16 a[28:31], v[80:83], v[40:43], a[28:31]\n"
"ds_read_b64_tr_b16 v[102:103], v113 offset:32320\n"
"s_waitcnt lgkmcnt(6)\n"
"v_mfma_f32_16x16x32_bf16 v[160:163], v[88:91], v[16:19], v[160:163]\n"
"s_waitcnt lgkmcnt(4)\n"
"v_mfma_f32_16x16x32_bf16 v[160:163], v[92:95], v[20:23], v[160:163]\n"
"s_add_u32 m0, s78, 0x12000\n"
"ds_read_b64_tr_b16 v[80:81], v113 offset:96\n"
"v_mfma_f32_16x16x32_bf16 v[164:167], v[92:95], v[28:31], v[164:167]\n"
"buffer_load_dwordx4 v104, s[60:63], s79 offen lds\n"
"v_mfma_f32_16x16x32_bf16 v[164:167], v[88:91], v[24:27], v[164:167]\n"
"ds_read_b64_tr_b16 v[82:83], v113 offset:4704\n"
"v_mfma_f32_16x16x32_bf16 v[168:171], v[88:91], v[32:35], v[168:171]\n"
"v_mfma_f32_16x16x32_bf16 v[168:171], v[92:95], v[36:39], v[168:171]\n"
"ds_read_b64_tr_b16 v[84:85], v113 offset:9312\n"
"v_mfma_f32_16x16x32_bf16 v[172:175], v[92:95], v[44:47], v[172:175]\n"
"v_mfma_f32_16x16x32_bf16 v[172:175], v[88:91], v[40:43], v[172:175]\n"
"s_add_u32 m0, s78, 0x12400\n"
"ds_read_b64_tr_b16 v[86:87], v113 offset:13920\n"
"s_waitcnt lgkmcnt(6)\n"
"v_mfma_f32_16x16x32_bf16 a[32:35], v[96:99], v[16:19], a[32:35]\n"
"buffer_load_dwordx4 v105, s[60:63], s79 offen lds\n"
"s_waitcnt lgkmcnt(4)\n"
"v_mfma_f32_16x16x32_bf16 a[32:35], v[100:103], v[20:23], a[32:35]\n"
"ds_read_b64_tr_b16 v[88:89], v113 offset:18528\n"
"v_mfma_f32_16x16x32_bf16 a[36:39], v[100:103], v[28:31], a[36:39]\n"
"v_mfma_f32_16x16x32_bf16 a[36:39], v[96:99], v[24:27], a[36:39]\n"
"ds_read_b64_tr_b16 v[90:91], v113 offset:23136\n"
"v_mfma_f32_16x16x32_bf16 a[40:43], v[96:99], v[32:35], a[40:43]\n"
"v_mfma_f32_16x16x32_bf16 a[40:43], v[100:103], v[36:39], a[40:43]\n"
"s_add_u32 m0, s78, 0x12800\n"
"ds_read_b64_tr_b16 v[92:93], v113 offset:27744\n"
"v_mfma_f32_16x16x32_bf16 a[44:47], v[100:103], v[44:47], a[44:47]\n"
"buffer_load_dwordx4 v106, s[60:63], s79 offen lds\n"
"v_mfma_f32_16x16x32_bf16 a[44:47], v[96:99], v[40:43], a[44:47]\n"
"ds_read_b64_tr_b16 v[94:95], v113 offset:32352\n"
"s_waitcnt lgkmcnt(6)\n"
"v_mfma_f32_16x16x32_bf16 v[176:179], v[80:83], v[16:19], v[176:179]\n"
"s_waitcnt lgkmcnt(4)\n"
"v_mfma_f32_16x16x32_bf16 v[176:179], v[84:87], v[20:23], v[176:179]\n"
"ds_read_b64_tr_b16 v[96:97], v113 offset:128\n"
"v_mfma_f32_16x16x32_bf16 v[180:183], v[84:87], v[28:31], v[180:183]\n"
"v_mfma_f32_16x16x32_bf16 v[180:183], v[80:83], v[24:27], v[180:183]\n"
"s_add_u32 m0, s78, 0x12c00\n"
"ds_read_b64_tr_b16 v[98:99], v113 offset:4736\n"
"v_mfma_f32_16x16x32_bf16 v[184:187], v[80:83], v[32:35], v[184:187]\n"
"buffer_load_dwordx4 v107, s[60:63], s79 offen lds\n"
"v_mfma_f32_16x16x32_bf16 v[184:187], v[84:87], v[36:39], v[184:187]\n"
"ds_read_b64_tr_b16 v[100:101], v113 offset:9344\n"
"v_mfma_f32_16x16x32_bf16 v[188:191], v[84:87], v[44:47], v[188:191]\n"
"v_mfma_f32_16x16x32_bf16 v[188:191], v[80:83], v[40:43], v[188:191]\n"
"ds_read_b64_tr_b16 v[102:103], v113 offset:13952\n"
"s_waitcnt lgkmcnt(6)\n"
"v_mfma_f32_16x16x32_bf16 a[48:51], v[88:91], v[16:19], a[48:51]\n"
"s_waitcnt lgkmcnt(4)\n"
"v_mfma_f32_16x16x32_bf16 a[48:51], v[92:95], v[20:23], a[48:51]\n"
"s_add_u32 m0, s78, 0x13000\n"
"ds_read_b64_tr_b16 v[80:81], v113 offset:18560\n"
"v_mfma_f32_16x16x32_bf16 a[52:55], v[92:95], v[28:31], a[52:55]\n"
"buffer_load_dwordx4 v108, s[60:63], s79 offen lds\n"
"v_mfma_f32_16x16x32_bf16 a[52:55], v[88:91], v[24:27], a[52:55]\n"
"ds_read_b64_tr_b16 v[82:83], v113 offset:23168\n"
"v_mfma_f32_16x16x32_bf16 a[56:59], v[88:91], v[32:35], a[56:59]\n"
"v_mfma_f32_16x16x32_bf16 a[56:59], v[92:95], v[36:39], a[56:59]\n"
"ds_read_b64_tr_b16 v[84:85], v113 offset:27776\n"
"v_mfma_f32_16x16x32_bf16 a[60:63], v[92:95], v[44:47], a[60:63]\n"
"v_mfma_f32_16x16x32_bf16 a[60:63], v[88:91], v[40:43], a[60:63]\n"
"s_add_u32 m0, s78, 0x13400\n"
"ds_read_b64_tr_b16 v[86:87], v113 offset:32384\n"
"s_waitcnt lgkmcnt(6)\n"
"v_mfma_f32_16x16x32_bf16 v[192:195], v[96:99], v[16:19], v[192:195]\n"
"buffer_load_dwordx4 v109, s[60:63], s79 offen lds\n"
"s_waitcnt lgkmcnt(4)\n"
"v_mfma_f32_16x16x32_bf16 v[192:195], v[100:103], v[20:23], v[192:195]\n"
"ds_read_b64_tr_b16 v[88:89], v113 offset:160\n"
"v_mfma_f32_16x16x32_bf16 v[196:199], v[100:103], v[28:31], v[196:199]\n"
"v_mfma_f32_16x16x32_bf16 v[196:199], v[96:99], v[24:27], v[196:199]\n"
"ds_read_b64_tr_b16 v[90:91], v113 offset:4768\n"
"v_mfma_f32_16x16x32_bf16 v[200:203], v[96:99], v[32:35], v[200:203]\n"
"v_mfma_f32_16x16x32_bf16 v[200:203], v[100:103], v[36:39], v[200:203]\n"
"s_add_u32 m0, s78, 0x13800\n"
"ds_read_b64_tr_b16 v[92:93], v113 offset:9376\n"
"v_mfma_f32_16x16x32_bf16 v[204:207], v[100:103], v[44:47], v[204:207]\n"
"buffer_load_dwordx4 v110, s[60:63], s79 offen lds\n"
"v_mfma_f32_16x16x32_bf16 v[204:207], v[96:99], v[40:43], v[204:207]\n"
"ds_read_b64_tr_b16 v[94:95], v113 offset:13984\n"
"s_waitcnt lgkmcnt(6)\n"
"v_mfma_f32_16x16x32_bf16 a[64:67], v[80:83], v[16:19], a[64:67]\n"
"s_waitcnt lgkmcnt(4)\n"
"v_mfma_f32_16x16x32_bf16 a[64:67], v[84:87], v[20:23], a[64:67]\n"
"ds_read_b64_tr_b16 v[96:97], v113 offset:18592\n"
"v_mfma_f32_16x16x32_bf16 a[68:71], v[84:87], v[28:31], a[68:71]\n"
"v_mfma_f32_16x16x32_bf16 a[68:71], v[80:83], v[24:27], a[68:71]\n"
"s_add_u32 m0, s78, 0x13c00\n"
"ds_read_b64_tr_b16 v[98:99], v113 offset:23200\n"
"v_mfma_f32_16x16x32_bf16 a[72:75], v[80:83], v[32:35], a[72:75]\n"
"buffer_load_dwordx4 v111, s[60:63], s79 offen lds\n"
"v_mfma_f32_16x16x32_bf16 a[72:75], v[84:87], v[36:39], a[72:75]\n"
"ds_read_b64_tr_b16 v[100:101], v113 offset:27808\n"
"v_mfma_f32_16x16x32_bf16 a[76:79], v[84:87], v[44:47], a[76:79]\n"
"v_mfma_f32_16x16x32_bf16 a[76:79], v[80:83], v[40:43], a[76:79]\n"
"ds_read_b64_tr_b16 v[102:103], v113 offset:32416\n"
"s_waitcnt lgkmcnt(6)\n"
"v_mfma_f32_16x16x32_bf16 v[208:211], v[88:91], v[16:19], v[208:211]\n"
"s_waitcnt lgkmcnt(4)\n"
"v_mfma_f32_16x16x32_bf16 v[208:211], v[92:95], v[20:23], v[208:211]\n"
"s_add_u32 m0, s78, 0x14000\n"
"ds_read_b64_tr_b16 v[80:81], v113 offset:192\n"
"v_mfma_f32_16x16x32_bf16 v[212:215], v[92:95], v[28:31], v[212:215]\n"
"buffer_load_dwordx4 v112, s[60:63], s79 offen lds\n"
"v_mfma_f32_16x16x32_bf16 v[212:215], v[88:91], v[24:27], v[212:215]\n"
"ds_read_b64_tr_b16 v[82:83], v113 offset:4800\n"
"v_mfma_f32_16x16x32_bf16 v[216:219], v[88:91], v[32:35], v[216:219]\n"
"v_mfma_f32_16x16x32_bf16 v[216:219], v[92:95], v[36:39], v[216:219]\n"
"ds_read_b64_tr_b16 v[84:85], v113 offset:9408\n"
"v_mfma_f32_16x16x32_bf16 v[220:223], v[92:95], v[44:47], v[220:223]\n"
"v_mfma_f32_16x16x32_bf16 v[220:223], v[88:91], v[40:43], v[220:223]\n"
"ds_read_b64_tr_b16 v[86:87], v113 offset:14016\n"
"s_waitcnt lgkmcnt(6)\n"
"v_mfma_f32_16x16x32_bf16 a[80:83], v[96:99], v[16:19], a[80:83]\n"
"s_add_u32 s79, s79, 0x90000\n"
"s_waitcnt lgkmcnt(4)\n"
"v_mfma_f32_16x16x32_bf16 a[80:83], v[100:103], v[20:23], a[80:83]\n"
"ds_read_b64_tr_b16 v[88:89], v113 offset:18624\n"
"v_mfma_f32_16x16x32_bf16 a[84:87], v[100:103], v[28:31], a[84:87]\n"
"v_mfma_f32_16x16x32_bf16 a[84:87], v[96:99], v[24:27], a[84:87]\n"
"ds_read_b64_tr_b16 v[90:91], v113 offset:23232\n"
"v_mfma_f32_16x16x32_bf16 a[88:91], v[96:99], v[32:35], a[88:91]\n"
"v_mfma_f32_16x16x32_bf16 a[88:91], v[100:103], v[36:39], a[88:91]\n"
"ds_read_b64_tr_b16 v[92:93], v113 offset:27840\n"
"v_mfma_f32_16x16x32_bf16 a[92:95], v[100:103], v[44:47], a[92:95]\n"
"v_mfma_f32_16x16x32_bf16 a[92:95], v[96:99], v[40:43], a[92:95]\n"
"ds_read_b64_tr_b16 v[94:95], v113 offset:32448\n"
"s_waitcnt lgkmcnt(6)\n"
"v_mfma_f32_16x16x32_bf16 v[224:227], v[80:83], v[16:19], v[224:227]\n"
"s_waitcnt lgkmcnt(4)\n"
"v_mfma_f32_16x16x32_bf16 v[224:227], v[84:87], v[20:23], v[224:227]\n"
"ds_read_b64_tr_b16 v[96:97], v113 offset:224\n"
"v_mfma_f32_16x16x32_bf16 v[228:231], v[84:87], v[28:31], v[228:231]\n"
"v_mfma_f32_16x16x32_bf16 v[228:231], v[80:83], v[24:27], v[228:231]\n"
"ds_read_b64_tr_b16 v[98:99], v113 offset:4832\n"
"v_mfma_f32_16x16x32_bf16 v[232:235], v[80:83], v[32:35], v[232:235]\n"
"v_mfma_f32_16x16x32_bf16 v[232:235], v[84:87], v[36:39], v[232:235]\n"
"ds_read_b64_tr_b16 v[100:101], v113 offset:9440\n"
"v_mfma_f32_16x16x32_bf16 v[236:239], v[84:87], v[44:47], v[236:239]\n"
"v_mfma_f32_16x16x32_bf16 v[236:239], v[80:83], v[40:43], v[236:239]\n"
"ds_read_b64_tr_b16 v[102:103], v113 offset:14048\n"
"s_waitcnt lgkmcnt(6)\n"
"v_mfma_f32_16x16x32_bf16 a[96:99], v[88:91], v[16:19], a[96:99]\n"
"s_waitcnt lgkmcnt(4)\n"
"v_mfma_f32_16x16x32_bf16 a[96:99], v[92:95], v[20:23], a[96:99]\n"
"ds_read_b64_tr_b16 v[80:81], v113 offset:18656\n"
"v_mfma_f32_16x16x32_bf16 a[100:103], v[92:95], v[28:31], a[100:103]\n"
"v_mfma_f32_16x16x32_bf16 a[100:103], v[88:91], v[24:27], a[100:103]\n"
"ds_read_b64_tr_b16 v[82:83], v113 offset:23264\n"
"v_mfma_f32_16x16x32_bf16 a[104:107], v[88:91], v[32:35], a[104:107]\n"
"v_mfma_f32_16x16x32_bf16 a[104:107], v[92:95], v[36:39], a[104:107]\n"
"ds_read_b64_tr_b16 v[84:85], v113 offset:27872\n"
"v_mfma_f32_16x16x32_bf16 a[108:111], v[92:95], v[44:47], a[108:111]\n"
"v_mfma_f32_16x16x32_bf16 a[108:111], v[88:91], v[40:43], a[108:111]\n"
"ds_read_b64_tr_b16 v[86:87], v113 offset:32480\n"
"s_waitcnt lgkmcnt(6)\n"
"v_mfma_f32_16x16x32_bf16 v[240:243], v[96:99], v[16:19], v[240:243]\n"
"s_waitcnt lgkmcnt(4)\n"
"v_mfma_f32_16x16x32_bf16 v[240:243], v[100:103], v[20:23], v[240:243]\n"
"ds_read_b64_tr_b16 v[88:89], v113 offset:256\n"
"v_mfma_f32_16x16x32_bf16 v[244:247], v[100:103], v[28:31], v[244:247]\n"
"v_mfma_f32_16x16x32_bf16 v[244:247], v[96:99], v[24:27], v[244:247]\n"
"ds_read_b64_tr_b16 v[90:91], v113 offset:4864\n"
"v_mfma_f32_16x16x32_bf16 v[248:251], v[96:99], v[32:35], v[248:251]\n"
"v_mfma_f32_16x16x32_bf16 v[248:251], v[100:103], v[36:39], v[248:251]\n"
"ds_read_b64_tr_b16 v[92:93], v113 offset:9472\n"
"v_mfma_f32_16x16x32_bf16 v[252:255], v[100:103], v[44:47], v[252:255]\n"
"v_mfma_f32_16x16x32_bf16 v[252:255], v[96:99], v[40:43], v[252:255]\n"
"ds_read_b64_tr_b16 v[94:95], v113 offset:14080\n"
"s_waitcnt lgkmcnt(6)\n"
"v_mfma_f32_16x16x32_bf16 a[112:115], v[80:83], v[16:19], a[112:115]\n"
"s_waitcnt lgkmcnt(4)\n"
"v_mfma_f32_16x16x32_bf16 a[112:115], v[84:87], v[20:23], a[112:115]\n"
"ds_read_b64_tr_b16 v[96:97], v113 offset:18688\n"
"v_mfma_f32_16x16x32_bf16 a[116:119], v[84:87], v[28:31], a[116:119]\n"
"v_mfma_f32_16x16x32_bf16 a[116:119], v[80:83], v[24:27], a[116:119]\n"
"ds_read_b64_tr_b16 v[98:99], v113 offset:23296\n"
"v_mfma_f32_16x16x32_bf16 a[120:123], v[80:83], v[32:35], a[120:123]\n"
"v_mfma_f32_16x16x32_bf16 a[120:123], v[84:87], v[36:39], a[120:123]\n"
"ds_read_b64_tr_b16 v[100:101], v113 offset:27904\n"
"v_mfma_f32_16x16x32_bf16 a[124:127], v[84:87], v[44:47], a[124:127]\n"
"v_mfma_f32_16x16x32_bf16 a[124:127], v[80:83], v[40:43], a[124:127]\n"
"s_waitcnt vmcnt(17)\n"
"s_barrier\n"
"ds_read_b64_tr_b16 v[102:103], v113 offset:32512\n"
"s_waitcnt lgkmcnt(6)\n"
"v_mfma_f32_16x16x32_bf16 a[144:147], v[88:91], v[16:19], a[144:147]\n"
"s_waitcnt lgkmcnt(4)\n"
"v_mfma_f32_16x16x32_bf16 a[144:147], v[92:95], v[20:23], a[144:147]\n"
"ds_read_b64_tr_b16 v[80:81], v114\n"
"v_mfma_f32_16x16x32_bf16 a[148:151], v[92:95], v[28:31], a[148:151]\n"
"v_mfma_f32_16x16x32_bf16 a[148:151], v[88:91], v[24:27], a[148:151]\n"
"ds_read_b64_tr_b16 v[82:83], v114 offset:4608\n"
"v_mfma_f32_16x16x32_bf16 a[152:155], v[88:91], v[32:35], a[152:155]\n"
"v_mfma_f32_16x16x32_bf16 a[152:155], v[92:95], v[36:39], a[152:155]\n"
"ds_read_b64_tr_b16 v[84:85], v114 offset:9216\n"
"v_mfma_f32_16x16x32_bf16 a[156:159], v[92:95], v[44:47], a[156:159]\n"
"v_mfma_f32_16x16x32_bf16 a[156:159], v[88:91], v[40:43], a[156:159]\n"
"ds_read_b64_tr_b16 v[86:87], v114 offset:13824\n"
"s_waitcnt lgkmcnt(6)\n"
"v_mfma_f32_16x16x32_bf16 a[128:131], v[96:99], v[16:19], a[128:131]\n"
"s_waitcnt lgkmcnt(4)\n"
"v_mfma_f32_16x16x32_bf16 a[128:131], v[100:103], v[20:23], a[128:131]\n"
"ds_read_b64_tr_b16 v[88:89], v114 offset:18432\n"
"v_mfma_f32_16x16x32_bf16 a[132:135], v[100:103], v[28:31], a[132:135]\n"
"v_mfma_f32_16x16x32_bf16 a[132:135], v[96:99], v[24:27], a[132:135]\n"
"ds_read_b64_tr_b16 v[90:91], v114 offset:23040\n"
"v_mfma_f32_16x16x32_bf16 a[136:139], v[96:99], v[32:35], a[136:139]\n"
"v_mfma_f32_16x16x32_bf16 a[136:139], v[100:103], v[36:39], a[136:139]\n"
"ds_read_b64_tr_b16 v[92:93], v114 offset:27648\n"
"v_mfma_f32_16x16x32_bf16 a[140:143], v[100:103], v[44:47], a[140:143]\n"
"v_mfma_f32_16x16x32_bf16 a[140:143], v[96:99], v[40:43], a[140:143]\n"
"ds_read_b64_tr_b16 v[94:95], v114 offset:32256\n"
"s_waitcnt vmcnt(16) lgkmcnt(6)\n"
"v_mfma_f32_16x16x32_bf16 v[128:131], v[80:83], v[48:51], v[128:131]\n"
"s_waitcnt vmcnt(15) lgkmcnt(4)\n"
"v_mfma_f32_16x16x32_bf16 v[128:131], v[84:87], v[52:55], v[128:131]\n"
"s_add_u32 s76, s90, 0x3000\n"
"ds_read_b64_tr_b16 v[96:97], v114 offset:32\n"
"s_waitcnt vmcnt(13)\n"
"v_mfma_f32_16x16x32_bf16 v[132:135], v[84:87], v[60:63], v[132:135]\n"
"buffer_load_dwordx4 v[16:19], v121, s[64:67], s76 offen sc1\n"
"v_mfma_f32_16x16x32_bf16 v[132:135], v[80:83], v[56:59], v[132:135]\n"
"ds_read_b64_tr_b16 v[98:99], v114 offset:4640\n"
"s_waitcnt vmcnt(13)\n"
"v_mfma_f32_16x16x32_bf16 v[136:139], v[80:83], v[64:67], v[136:139]\n"
"s_waitcnt vmcnt(12)\n"
"v_mfma_f32_16x16x32_bf16 v[136:139], v[84:87], v[68:71], v[136:139]\n"
"s_add_u32 s76, s90, 0x3400\n"
"ds_read_b64_tr_b16 v[100:101], v114 offset:9248\n"
"s_waitcnt vmcnt(10)\n"
"v_mfma_f32_16x16x32_bf16 v[140:143], v[84:87], v[76:79], v[140:143]\n"
"buffer_load_dwordx4 v[20:23], v121, s[64:67], s76 offen sc1\n"
"v_mfma_f32_16x16x32_bf16 v[140:143], v[80:83], v[72:75], v[140:143]\n"
"ds_read_b64_tr_b16 v[102:103], v114 offset:13856\n"
"s_waitcnt lgkmcnt(6)\n"
"v_mfma_f32_16x16x32_bf16 a[0:3], v[88:91], v[48:51], a[0:3]\n"
"s_waitcnt lgkmcnt(4)\n"
"v_mfma_f32_16x16x32_bf16 a[0:3], v[92:95], v[52:55], a[0:3]\n"
"s_add_u32 s76, s90, 0x7800\n"
"ds_read_b64_tr_b16 v[80:81], v114 offset:18464\n"
"v_mfma_f32_16x16x32_bf16 a[4:7], v[92:95], v[60:63], a[4:7]\n"
"buffer_load_dwordx4 v[24:27], v121, s[64:67], s76 offen sc1\n"
"v_mfma_f32_16x16x32_bf16 a[4:7], v[88:91], v[56:59], a[4:7]\n"
"ds_read_b64_tr_b16 v[82:83], v114 offset:23072\n"
"v_mfma_f32_16x16x32_bf16 a[8:11], v[88:91], v[64:67], a[8:11]\n"
"v_mfma_f32_16x16x32_bf16 a[8:11], v[92:95], v[68:71], a[8:11]\n"
"s_add_u32 s76, s90, 0x7c00\n"
"ds_read_b64_tr_b16 v[84:85], v114 offset:27680\n"
"v_mfma_f32_16x16x32_bf16 a[12:15], v[92:95], v[76:79], a[12:15]\n"
"buffer_load_dwordx4 v[28:31], v121, s[64:67], s76 offen sc1\n"
"v_mfma_f32_16x16x32_bf16 a[12:15], v[88:91], v[72:75], a[12:15]\n"
"ds_read_b64_tr_b16 v[86:87], v114 offset:32288\n"
"s_waitcnt lgkmcnt(6)\n"
"v_mfma_f32_16x16x32_bf16 v[144:147], v[96:99], v[48:51], v[144:147]\n"
"s_waitcnt lgkmcnt(4)\n"
"v_mfma_f32_16x16x32_bf16 v[144:147], v[100:103], v[52:55], v[144:147]\n"
"s_add_u32 s76, s90, 0xc000\n"
"ds_read_b64_tr_b16 v[88:89], v114 offset:64\n"
"v_mfma_f32_16x16x32_bf16 v[148:151], v[100:103], v[60:63], v[148:151]\n"
"buffer_load_dwordx4 v[32:35], v121, s[64:67], s76 offen sc1\n"
"v_mfma_f32_16x16x32_bf16 v[148:151], v[96:99], v[56:59], v[148:151]\n"
"ds_read_b64_tr_b16 v[90:91], v114 offset:4672\n"
"v_mfma_f32_16x16x32_bf16 v[152:155], v[96:99], v[64:67], v[152:155]\n"
"v_mfma_f32_16x16x32_bf16 v[152:155], v[100:103], v[68:71], v[152:155]\n"
"s_add_u32 s76, s90, 0xc400\n"
"ds_read_b64_tr_b16 v[92:93], v114 offset:9280\n"
"v_mfma_f32_16x16x32_bf16 v[156:159], v[100:103], v[76:79], v[156:159]\n"
"buffer_load_dwordx4 v[36:39], v121, s[64:67], s76 offen sc1\n"
"v_mfma_f32_16x16x32_bf16 v[156:159], v[96:99], v[72:75], v[156:159]\n"
"ds_read_b64_tr_b16 v[94:95], v114 offset:13888\n"
"s_waitcnt lgkmcnt(6)\n"
"v_mfma_f32_16x16x32_bf16 a[16:19], v[80:83], v[48:51], a[16:19]\n"
"s_waitcnt lgkmcnt(4)\n"
"v_mfma_f32_16x16x32_bf16 a[16:19], v[84:87], v[52:55], a[16:19]\n"
"s_add_u32 s76, s90, 0x10800\n"
"ds_read_b64_tr_b16 v[96:97], v114 offset:18496\n"
"v_mfma_f32_16x16x32_bf16 a[20:23], v[84:87], v[60:63], a[20:23]\n"
"buffer_load_dwordx4 v[40:43], v121, s[64:67], s76 offen sc1\n"
"v_mfma_f32_16x16x32_bf16 a[20:23], v[80:83], v[56:59], a[20:23]\n"
"ds_read_b64_tr_b16 v[98:99], v114 offset:23104\n"
"v_mfma_f32_16x16x32_bf16 a[24:27], v[80:83], v[64:67], a[24:27]\n"
"v_mfma_f32_16x16x32_bf16 a[24:27], v[84:87], v[68:71], a[24:27]\n"
"s_add_u32 s76, s90, 0x10c00\n"
"ds_read_b64_tr_b16 v[100:101], v114 offset:27712\n"
"v_mfma_f32_16x16x32_bf16 a[28:31], v[84:87], v[76:79], a[28:31]\n"
"buffer_load_dwordx4 v[44:47], v121, s[64:67], s76 offen sc1\n"
"v_mfma_f32_16x16x32_bf16 a[28:31], v[80:83], v[72:75], a[28:31]\n"
"ds_read_b64_tr_b16 v[102:103], v114 offset:32320\n"
"s_waitcnt lgkmcnt(6)\n"
"v_mfma_f32_16x16x32_bf16 v[160:163], v[88:91], v[48:51], v[160:163]\n"
"s_waitcnt lgkmcnt(4)\n"
"v_mfma_f32_16x16x32_bf16 v[160:163], v[92:95], v[52:55], v[160:163]\n"
"s_add_u32 m0, s78, 0x1b000\n"
"ds_read_b64_tr_b16 v[80:81], v114 offset:96\n"
"v_mfma_f32_16x16x32_bf16 v[164:167], v[92:95], v[60:63], v[164:167]\n"
"buffer_load_dwordx4 v104, s[60:63], s79 offen lds\n"
"v_mfma_f32_16x16x32_bf16 v[164:167], v[88:91], v[56:59], v[164:167]\n"
"ds_read_b64_tr_b16 v[82:83], v114 offset:4704\n"
"v_mfma_f32_16x16x32_bf16 v[168:171], v[88:91], v[64:67], v[168:171]\n"
"v_mfma_f32_16x16x32_bf16 v[168:171], v[92:95], v[68:71], v[168:171]\n"
"ds_read_b64_tr_b16 v[84:85], v114 offset:9312\n"
"v_mfma_f32_16x16x32_bf16 v[172:175], v[92:95], v[76:79], v[172:175]\n"
"v_mfma_f32_16x16x32_bf16 v[172:175], v[88:91], v[72:75], v[172:175]\n"
"s_add_u32 m0, s78, 0x1b400\n"
"ds_read_b64_tr_b16 v[86:87], v114 offset:13920\n"
"s_waitcnt lgkmcnt(6)\n"
"v_mfma_f32_16x16x32_bf16 a[32:35], v[96:99], v[48:51], a[32:35]\n"
"buffer_load_dwordx4 v105, s[60:63], s79 offen lds\n"
"s_waitcnt lgkmcnt(4)\n"
"v_mfma_f32_16x16x32_bf16 a[32:35], v[100:103], v[52:55], a[32:35]\n"
"ds_read_b64_tr_b16 v[88:89], v114 offset:18528\n"
"v_mfma_f32_16x16x32_bf16 a[36:39], v[100:103], v[60:63], a[36:39]\n"
"v_mfma_f32_16x16x32_bf16 a[36:39], v[96:99], v[56:59], a[36:39]\n"
"ds_read_b64_tr_b16 v[90:91], v114 offset:23136\n"
"v_mfma_f32_16x16x32_bf16 a[40:43], v[96:99], v[64:67], a[40:43]\n"
"v_mfma_f32_16x16x32_bf16 a[40:43], v[100:103], v[68:71], a[40:43]\n"
"s_add_u32 m0, s78, 0x1b800\n"
"ds_read_b64_tr_b16 v[92:93], v114 offset:27744\n"
"v_mfma_f32_16x16x32_bf16 a[44:47], v[100:103], v[76:79], a[44:47]\n"
"buffer_load_dwordx4 v106, s[60:63], s79 offen lds\n"
"v_mfma_f32_16x16x32_bf16 a[44:47], v[96:99], v[72:75], a[44:47]\n"
"ds_read_b64_tr_b16 v[94:95], v114 offset:32352\n"
"s_waitcnt lgkmcnt(6)\n"
"v_mfma_f32_16x16x32_bf16 v[176:179], v[80:83], v[48:51], v[176:179]\n"
"s_waitcnt lgkmcnt(4)\n"
"v_mfma_f32_16x16x32_bf16 v[176:179], v[84:87], v[52:55], v[176:179]\n"
"ds_read_b64_tr_b16 v[96:97], v114 offset:128\n"
"v_mfma_f32_16x16x32_bf16 v[180:183], v[84:87], v[60:63], v[180:183]\n"
"v_mfma_f32_16x16x32_bf16 v[180:183], v[80:83], v[56:59], v[180:183]\n"
"s_add_u32 m0, s78, 0x1bc00\n"
"ds_read_b64_tr_b16 v[98:99], v114 offset:4736\n"
"v_mfma_f32_16x16x32_bf16 v[184:187], v[80:83], v[64:67], v[184:187]\n"
"buffer_load_dwordx4 v107, s[60:63], s79 offen lds\n"
"v_mfma_f32_16x16x32_bf16 v[184:187], v[84:87], v[68:71], v[184:187]\n"
"ds_read_b64_tr_b16 v[100:101], v114 offset:9344\n"
"v_mfma_f32_16x16x32_bf16 v[188:191], v[84:87], v[76:79], v[188:191]\n"
"v_mfma_f32_16x16x32_bf16 v[188:191], v[80:83], v[72:75], v[188:191]\n"
"ds_read_b64_tr_b16 v[102:103], v114 offset:13952\n"
"s_waitcnt lgkmcnt(6)\n"
"v_mfma_f32_16x16x32_bf16 a[48:51], v[88:91], v[48:51], a[48:51]\n"
"s_waitcnt lgkmcnt(4)\n"
"v_mfma_f32_16x16x32_bf16 a[48:51], v[92:95], v[52:55], a[48:51]\n"
"s_add_u32 m0, s78, 0x1c000\n"
"ds_read_b64_tr_b16 v[80:81], v114 offset:18560\n"
"v_mfma_f32_16x16x32_bf16 a[52:55], v[92:95], v[60:63], a[52:55]\n"
"buffer_load_dwordx4 v108, s[60:63], s79 offen lds\n"
"v_mfma_f32_16x16x32_bf16 a[52:55], v[88:91], v[56:59], a[52:55]\n"
"ds_read_b64_tr_b16 v[82:83], v114 offset:23168\n"
"v_mfma_f32_16x16x32_bf16 a[56:59], v[88:91], v[64:67], a[56:59]\n"
"v_mfma_f32_16x16x32_bf16 a[56:59], v[92:95], v[68:71], a[56:59]\n"
"ds_read_b64_tr_b16 v[84:85], v114 offset:27776\n"
"v_mfma_f32_16x16x32_bf16 a[60:63], v[92:95], v[76:79], a[60:63]\n"
"v_mfma_f32_16x16x32_bf16 a[60:63], v[88:91], v[72:75], a[60:63]\n"
"s_add_u32 m0, s78, 0x1c400\n"
"ds_read_b64_tr_b16 v[86:87], v114 offset:32384\n"
"s_waitcnt lgkmcnt(6)\n"
"v_mfma_f32_16x16x32_bf16 v[192:195], v[96:99], v[48:51], v[192:195]\n"
"buffer_load_dwordx4 v109, s[60:63], s79 offen lds\n"
"s_waitcnt lgkmcnt(4)\n"
"v_mfma_f32_16x16x32_bf16 v[192:195], v[100:103], v[52:55], v[192:195]\n"
"ds_read_b64_tr_b16 v[88:89], v114 offset:160\n"
"v_mfma_f32_16x16x32_bf16 v[196:199], v[100:103], v[60:63], v[196:199]\n"
"v_mfma_f32_16x16x32_bf16 v[196:199], v[96:99], v[56:59], v[196:199]\n"
"ds_read_b64_tr_b16 v[90:91], v114 offset:4768\n"
"v_mfma_f32_16x16x32_bf16 v[200:203], v[96:99], v[64:67], v[200:203]\n"
"v_mfma_f32_16x16x32_bf16 v[200:203], v[100:103], v[68:71], v[200:203]\n"
"s_add_u32 m0, s78, 0x1c800\n"
"ds_read_b64_tr_b16 v[92:93], v114 offset:9376\n"
"v_mfma_f32_16x16x32_bf16 v[204:207], v[100:103], v[76:79], v[204:207]\n"
"buffer_load_dwordx4 v110, s[60:63], s79 offen lds\n"
"v_mfma_f32_16x16x32_bf16 v[204:207], v[96:99], v[72:75], v[204:207]\n"
"ds_read_b64_tr_b16 v[94:95], v114 offset:13984\n"
"s_waitcnt lgkmcnt(6)\n"
"v_mfma_f32_16x16x32_bf16 a[64:67], v[80:83], v[48:51], a[64:67]\n"
"s_waitcnt lgkmcnt(4)\n"
"v_mfma_f32_16x16x32_bf16 a[64:67], v[84:87], v[52:55], a[64:67]\n"
"ds_read_b64_tr_b16 v[96:97], v114 offset:18592\n"
"v_mfma_f32_16x16x32_bf16 a[68:71], v[84:87], v[60:63], a[68:71]\n"
"v_mfma_f32_16x16x32_bf16 a[68:71], v[80:83], v[56:59], a[68:71]\n"
"s_add_u32 m0, s78, 0x1cc00\n"
"ds_read_b64_tr_b16 v[98:99], v114 offset:23200\n"
"v_mfma_f32_16x16x32_bf16 a[72:75], v[80:83], v[64:67], a[72:75]\n"
"buffer_load_dwordx4 v111, s[60:63], s79 offen lds\n"
"v_mfma_f32_16x16x32_bf16 a[72:75], v[84:87], v[68:71], a[72:75]\n"
"ds_read_b64_tr_b16 v[100:101], v114 offset:27808\n"
"v_mfma_f32_16x16x32_bf16 a[76:79], v[84:87], v[76:79], a[76:79]\n"
"v_mfma_f32_16x16x32_bf16 a[76:79], v[80:83], v[72:75], a[76:79]\n"
"ds_read_b64_tr_b16 v[102:103], v114 offset:32416\n"
"s_waitcnt lgkmcnt(6)\n"
"v_mfma_f32_16x16x32_bf16 v[208:211], v[88:91], v[48:51], v[208:211]\n"
"s_waitcnt lgkmcnt(4)\n"
"v_mfma_f32_16x16x32_bf16 v[208:211], v[92:95], v[52:55], v[208:211]\n"
"s_add_u32 m0, s78, 0x1d000\n"
"ds_read_b64_tr_b16 v[80:81], v114 offset:192\n"
"v_mfma_f32_16x16x32_bf16 v[212:215], v[92:95], v[60:63], v[212:215]\n"
"buffer_load_dwordx4 v112, s[60:63], s79 offen lds\n"
"v_mfma_f32_16x16x32_bf16 v[212:215], v[88:91], v[56:59], v[212:215]\n"
"ds_read_b64_tr_b16 v[82:83], v114 offset:4800\n"
"v_mfma_f32_16x16x32_bf16 v[216:219], v[88:91], v[64:67], v[216:219]\n"
"v_mfma_f32_16x16x32_bf16 v[216:219], v[92:95], v[68:71], v[216:219]\n"
"ds_read_b64_tr_b16 v[84:85], v114 offset:9408\n"
"v_mfma_f32_16x16x32_bf16 v[220:223], v[92:95], v[76:79], v[220:223]\n"
"v_mfma_f32_16x16x32_bf16 v[220:223], v[88:91], v[72:75], v[220:223]\n"
"ds_read_b64_tr_b16 v[86:87], v114 offset:14016\n"
"s_waitcnt lgkmcnt(6)\n"
"v_mfma_f32_16x16x32_bf16 a[80:83], v[96:99], v[48:51], a[80:83]\n"
"s_add_u32 s79, s79, 0x90000\n"
"s_waitcnt lgkmcnt(4)\n"
"v_mfma_f32_16x16x32_bf16 a[80:83], v[100:103], v[52:55], a[80:83]\n"
"ds_read_b64_tr_b16 v[88:89], v114 offset:18624\n"
"v_mfma_f32_16x16x32_bf16 a[84:87], v[100:103], v[60:63], a[84:87]\n"
"v_mfma_f32_16x16x32_bf16 a[84:87], v[96:99], v[56:59], a[84:87]\n"
"ds_read_b64_tr_b16 v[90:91], v114 offset:23232\n"
"v_mfma_f32_16x16x32_bf16 a[88:91], v[96:99], v[64:67], a[88:91]\n"
"v_mfma_f32_16x16x32_bf16 a[88:91], v[100:103], v[68:71], a[88:91]\n"
"ds_read_b64_tr_b16 v[92:93], v114 offset:27840\n"
"v_mfma_f32_16x16x32_bf16 a[92:95], v[100:103], v[76:79], a[92:95]\n"
"v_mfma_f32_16x16x32_bf16 a[92:95], v[96:99], v[72:75], a[92:95]\n"
"ds_read_b64_tr_b16 v[94:95], v114 offset:32448\n"
"s_waitcnt lgkmcnt(6)\n"
"v_mfma_f32_16x16x32_bf16 v[224:227], v[80:83], v[48:51], v[224:227]\n"
"s_waitcnt lgkmcnt(4)\n"
"v_mfma_f32_16x16x32_bf16 v[224:227], v[84:87], v[52:55], v[224:227]\n"
"ds_read_b64_tr_b16 v[96:97], v114 offset:224\n"
"v_mfma_f32_16x16x32_bf16 v[228:231], v[84:87], v[60:63], v[228:231]\n"
"v_mfma_f32_16x16x32_bf16 v[228:231], v[80:83], v[56:59], v[228:231]\n"
"ds_read_b64_tr_b16 v[98:99], v114 offset:4832\n"
"v_mfma_f32_16x16x32_bf16 v[232:235], v[80:83], v[64:67], v[232:235]\n"
"v_mfma_f32_16x16x32_bf16 v[232:235], v[84:87], v[68:71], v[232:235]\n"
"ds_read_b64_tr_b16 v[100:101], v114 offset:9440\n"
"v_mfma_f32_16x16x32_bf16 v[236:239], v[84:87], v[76:79], v[236:239]\n"
"v_mfma_f32_16x16x32_bf16 v[236:239], v[80:83], v[72:75], v[236:239]\n"
"ds_read_b64_tr_b16 v[102:103], v114 offset:14048\n"
"s_waitcnt lgkmcnt(6)\n"
"v_mfma_f32_16x16x32_bf16 a[96:99], v[88:91], v[48:51], a[96:99]\n"
"s_waitcnt lgkmcnt(4)\n"
"v_mfma_f32_16x16x32_bf16 a[96:99], v[92:95], v[52:55], a[96:99]\n"
"ds_read_b64_tr_b16 v[80:81], v114 offset:18656\n"
"v_mfma_f32_16x16x32_bf16 a[100:103], v[92:95], v[60:63], a[100:103]\n"
"v_mfma_f32_16x16x32_bf16 a[100:103], v[88:91], v[56:59], a[100:103]\n"
"ds_read_b64_tr_b16 v[82:83], v114 offset:23264\n"
"v_mfma_f32_16x16x32_bf16 a[104:107], v[88:91], v[64:67], a[104:107]\n"
"v_mfma_f32_16x16x32_bf16 a[104:107], v[92:95], v[68:71], a[104:107]\n"
"ds_read_b64_tr_b16 v[84:85], v114 offset:27872\n"
"v_mfma_f32_16x16x32_bf16 a[108:111], v[92:95], v[76:79], a[108:111]\n"
"v_mfma_f32_16x16x32_bf16 a[108:111], v[88:91], v[72:75], a[108:111]\n"
"ds_read_b64_tr_b16 v[86:87], v114 offset:32480\n"
"s_waitcnt lgkmcnt(6)\n"
"v_mfma_f32_16x16x32_bf16 v[240:243], v[96:99], v[48:51], v[240:243]\n"
"s_waitcnt lgkmcnt(4)\n"
"v_mfma_f32_16x16x32_bf16 v[240:243], v[100:103], v[52:55], v[240:243]\n"
"ds_read_b64_tr_b16 v[88:89], v114 offset:256\n"
"v_mfma_f32_16x16x32_bf16 v[244:247], v[100:103], v[60:63], v[244:247]\n"
"v_mfma_f32_16x16x32_bf16 v[244:247], v[96:99], v[56:59], v[244:247]\n"
"ds_read_b64_tr_b16 v[90:91], v114 offset:4864\n"
"v_mfma_f32_16x16x32_bf16 v[248:251], v[96:99], v[64:67], v[248:251]\n"
"v_mfma_f32_16x16x32_bf16 v[248:251], v[100:103], v[68:71], v[248:251]\n"
"ds_read_b64_tr_b16 v[92:93], v114 offset:9472\n"
"v_mfma_f32_16x16x32_bf16 v[252:255], v[100:103], v[76:79], v[252:255]\n"
"v_mfma_f32_16x16x32_bf16 v[252:255], v[96:99], v[72:75], v[252:255]\n"
"ds_read_b64_tr_b16 v[94:95], v114 offset:14080\n"
"s_waitcnt lgkmcnt(6)\n"
"v_mfma_f32_16x16x32_bf16 a[112:115], v[80:83], v[48:51], a[112:115]\n"
"s_waitcnt lgkmcnt(4)\n"
"v_mfma_f32_16x16x32_bf16 a[112:115], v[84:87], v[52:55], a[112:115]\n"
"ds_read_b64_tr_b16 v[96:97], v114 offset:18688\n"
"v_mfma_f32_16x16x32_bf16 a[116:119], v[84:87], v[60:63], a[116:119]\n"
"v_mfma_f32_16x16x32_bf16 a[116:119], v[80:83], v[56:59], a[116:119]\n"
"ds_read_b64_tr_b16 v[98:99], v114 offset:23296\n"
"v_mfma_f32_16x16x32_bf16 a[120:123], v[80:83], v[64:67], a[120:123]\n"
"v_mfma_f32_16x16x32_bf16 a[120:123], v[84:87], v[68:71], a[120:123]\n"
"ds_read_b64_tr_b16 v[100:101], v114 offset:27904\n"
"v_mfma_f32_16x16x32_bf16 a[124:127], v[84:87], v[76:79], a[124:127]\n"
"v_mfma_f32_16x16x32_bf16 a[124:127], v[80:83], v[72:75], a[124:127]\n"
"s_waitcnt vmcnt(17)\n"
"s_barrier\n"
"ds_read_b64_tr_b16 v[102:103], v114 offset:32512\n"
"s_waitcnt lgkmcnt(6)\n"
"v_mfma_f32_16x16x32_bf16 a[144:147], v[88:91], v[48:51], a[144:147]\n"
"s_waitcnt lgkmcnt(4)\n"
"v_mfma_f32_16x16x32_bf16 a[144:147], v[92:95], v[52:55], a[144:147]\n"
"ds_read_b64_tr_b16 v[80:81], v115\n"
"v_mfma_f32_16x16x32_bf16 a[148:151], v[92:95], v[60:63], a[148:151]\n"
"v_mfma_f32_16x16x32_bf16 a[148:151], v[88:91], v[56:59], a[148:151]\n"
"ds_read_b64_tr_b16 v[82:83], v115 offset:4608\n"
"v_mfma_f32_16x16x32_bf16 a[152:155], v[88:91], v[64:67], a[152:155]\n"
"v_mfma_f32_16x16x32_bf16 a[152:155], v[92:95], v[68:71], a[152:155]\n"
"ds_read_b64_tr_b16 v[84:85], v115 offset:9216\n"
"v_mfma_f32_16x16x32_bf16 a[156:159], v[92:95], v[76:79], a[156:159]\n"
"v_mfma_f32_16x16x32_bf16 a[156:159], v[88:91], v[72:75], a[156:159]\n"
"ds_read_b64_tr_b16 v[86:87], v115 offset:13824\n"
"s_waitcnt lgkmcnt(6)\n"
"v_mfma_f32_16x16x32_bf16 a[128:131], v[96:99], v[48:51], a[128:131]\n"
"s_waitcnt lgkmcnt(4)\n"
"v_mfma_f32_16x16x32_bf16 a[128:131], v[100:103], v[52:55], a[128:131]\n"
"ds_read_b64_tr_b16 v[88:89], v115 offset:18432\n"
"v_mfma_f32_16x16x32_bf16 a[132:135], v[100:103], v[60:63], a[132:135]\n"
"v_mfma_f32_16x16x32_bf16 a[132:135], v[96:99], v[56:59], a[132:135]\n"
"ds_read_b64_tr_b16 v[90:91], v115 offset:23040\n"
"v_mfma_f32_16x16x32_bf16 a[136:139], v[96:99], v[64:67], a[136:139]\n"
"v_mfma_f32_16x16x32_bf16 a[136:139], v[100:103], v[68:71], a[136:139]\n"
"ds_read_b64_tr_b16 v[92:93], v115 offset:27648\n"
"v_mfma_f32_16x16x32_bf16 a[140:143], v[100:103], v[76:79], a[140:143]\n"
"v_mfma_f32_16x16x32_bf16 a[140:143], v[96:99], v[72:75], a[140:143]\n"
"ds_read_b64_tr_b16 v[94:95], v115 offset:32256\n"
"s_waitcnt vmcnt(16) lgkmcnt(6)\n"
"v_mfma_f32_16x16x32_bf16 v[128:131], v[80:83], v[16:19], v[128:131]\n"
"s_waitcnt vmcnt(15) lgkmcnt(4)\n"
"v_mfma_f32_16x16x32_bf16 v[128:131], v[84:87], v[20:23], v[128:131]\n"
"s_add_u32 s76, s90, 0x3800\n"
"ds_read_b64_tr_b16 v[96:97], v115 offset:32\n"
"s_waitcnt vmcnt(13)\n"
"v_mfma_f32_16x16x32_bf16 v[132:135], v[84:87], v[28:31], v[132:135]\n"
"buffer_load_dwordx4 v[48:51], v121, s[64:67], s76 offen sc1\n"
"v_mfma_f32_16x16x32_bf16 v[132:135], v[80:83], v[24:27], v[132:135]\n"
"ds_read_b64_tr_b16 v[98:99], v115 offset:4640\n"
"s_waitcnt vmcnt(13)\n"
"v_mfma_f32_16x16x32_bf16 v[136:139], v[80:83], v[32:35], v[136:139]\n"
"s_waitcnt vmcnt(12)\n"
"v_mfma_f32_16x16x32_bf16 v[136:139], v[84:87], v[36:39], v[136:139]\n"
"s_add_u32 s76, s90, 0x3c00\n"
"ds_read_b64_tr_b16 v[100:101], v115 offset:9248\n"
"s_waitcnt vmcnt(10)\n"
"v_mfma_f32_16x16x32_bf16 v[140:143], v[84:87], v[44:47], v[140:143]\n"
"buffer_load_dwordx4 v[52:55], v121, s[64:67], s76 offen sc1\n"
"v_mfma_f32_16x16x32_bf16 v[140:143], v[80:83], v[40:43], v[140:143]\n"
"ds_read_b64_tr_b16 v[102:103], v115 offset:13856\n"
"s_waitcnt lgkmcnt(6)\n"
"v_mfma_f32_16x16x32_bf16 a[0:3], v[88:91], v[16:19], a[0:3]\n"
"s_waitcnt lgkmcnt(4)\n"
"v_mfma_f32_16x16x32_bf16 a[0:3], v[92:95], v[20:23], a[0:3]\n"
"s_add_u32 s76, s90, 0x8000\n"
"ds_read_b64_tr_b16 v[80:81], v115 offset:18464\n"
"v_mfma_f32_16x16x32_bf16 a[4:7], v[92:95], v[28:31], a[4:7]\n"
"buffer_load_dwordx4 v[56:59], v121, s[64:67], s76 offen sc1\n"
"v_mfma_f32_16x16x32_bf16 a[4:7], v[88:91], v[24:27], a[4:7]\n"
"ds_read_b64_tr_b16 v[82:83], v115 offset:23072\n"
"v_mfma_f32_16x16x32_bf16 a[8:11], v[88:91], v[32:35], a[8:11]\n"
"v_mfma_f32_16x16x32_bf16 a[8:11], v[92:95], v[36:39], a[8:11]\n"
"s_add_u32 s76, s90, 0x8400\n"
"ds_read_b64_tr_b16 v[84:85], v115 offset:27680\n"
"v_mfma_f32_16x16x32_bf16 a[12:15], v[92:95], v[44:47], a[12:15]\n"
"buffer_load_dwordx4 v[60:63], v121, s[64:67], s76 offen sc1\n"
"v_mfma_f32_16x16x32_bf16 a[12:15], v[88:91], v[40:43], a[12:15]\n"
"ds_read_b64_tr_b16 v[86:87], v115 offset:32288\n"
"s_waitcnt lgkmcnt(6)\n"
"v_mfma_f32_16x16x32_bf16 v[144:147], v[96:99], v[16:19], v[144:147]\n"
"s_waitcnt lgkmcnt(4)\n"
"v_mfma_f32_16x16x32_bf16 v[144:147], v[100:103], v[20:23], v[144:147]\n"
"s_add_u32 s76, s90, 0xc800\n"
"ds_read_b64_tr_b16 v[88:89], v115 offset:64\n"
"v_mfma_f32_16x16x32_bf16 v[148:151], v[100:103], v[28:31], v[148:151]\n"
"buffer_load_dwordx4 v[64:67], v121, s[64:67], s76 offen sc1\n"
"v_mfma_f32_16x16x32_bf16 v[148:151], v[96:99], v[24:27], v[148:151]\n"
"ds_read_b64_tr_b16 v[90:91], v115 offset:4672\n"
"v_mfma_f32_16x16x32_bf16 v[152:155], v[96:99], v[32:35], v[152:155]\n"
"v_mfma_f32_16x16x32_bf16 v[152:155], v[100:103], v[36:39], v[152:155]\n"
"s_add_u32 s76, s90, 0xcc00\n"
"ds_read_b64_tr_b16 v[92:93], v115 offset:9280\n"
"v_mfma_f32_16x16x32_bf16 v[156:159], v[100:103], v[44:47], v[156:159]\n"
"buffer_load_dwordx4 v[68:71], v121, s[64:67], s76 offen sc1\n"
"v_mfma_f32_16x16x32_bf16 v[156:159], v[96:99], v[40:43], v[156:159]\n"
"ds_read_b64_tr_b16 v[94:95], v115 offset:13888\n"
"s_waitcnt lgkmcnt(6)\n"
"v_mfma_f32_16x16x32_bf16 a[16:19], v[80:83], v[16:19], a[16:19]\n"
"s_waitcnt lgkmcnt(4)\n"
"v_mfma_f32_16x16x32_bf16 a[16:19], v[84:87], v[20:23], a[16:19]\n"
"s_add_u32 s76, s90, 0x11000\n"
"ds_read_b64_tr_b16 v[96:97], v115 offset:18496\n"
"v_mfma_f32_16x16x32_bf16 a[20:23], v[84:87], v[28:31], a[20:23]\n"
"buffer_load_dwordx4 v[72:75], v121, s[64:67], s76 offen sc1\n"
"v_mfma_f32_16x16x32_bf16 a[20:23], v[80:83], v[24:27], a[20:23]\n"
"ds_read_b64_tr_b16 v[98:99], v115 offset:23104\n"
"v_mfma_f32_16x16x32_bf16 a[24:27], v[80:83], v[32:35], a[24:27]\n"
"v_mfma_f32_16x16x32_bf16 a[24:27], v[84:87], v[36:39], a[24:27]\n"
"s_add_u32 s76, s90, 0x11400\n"
"ds_read_b64_tr_b16 v[100:101], v115 offset:27712\n"
"v_mfma_f32_16x16x32_bf16 a[28:31], v[84:87], v[44:47], a[28:31]\n"
"buffer_load_dwordx4 v[76:79], v121, s[64:67], s76 offen sc1\n"
"v_mfma_f32_16x16x32_bf16 a[28:31], v[80:83], v[40:43], a[28:31]\n"
"ds_read_b64_tr_b16 v[102:103], v115 offset:32320\n"
"s_waitcnt lgkmcnt(6)\n"
"v_mfma_f32_16x16x32_bf16 v[160:163], v[88:91], v[16:19], v[160:163]\n"
"s_waitcnt lgkmcnt(4)\n"
"v_mfma_f32_16x16x32_bf16 v[160:163], v[92:95], v[20:23], v[160:163]\n"
"s_add_u32 m0, s78, 0\n"
"ds_read_b64_tr_b16 v[80:81], v115 offset:96\n"
"v_mfma_f32_16x16x32_bf16 v[164:167], v[92:95], v[28:31], v[164:167]\n"
"buffer_load_dwordx4 v104, s[60:63], s79 offen lds\n"
"v_mfma_f32_16x16x32_bf16 v[164:167], v[88:91], v[24:27], v[164:167]\n"
"ds_read_b64_tr_b16 v[82:83], v115 offset:4704\n"
"v_mfma_f32_16x16x32_bf16 v[168:171], v[88:91], v[32:35], v[168:171]\n"
"v_mfma_f32_16x16x32_bf16 v[168:171], v[92:95], v[36:39], v[168:171]\n"
"ds_read_b64_tr_b16 v[84:85], v115 offset:9312\n"
"v_mfma_f32_16x16x32_bf16 v[172:175], v[92:95], v[44:47], v[172:175]\n"
"v_mfma_f32_16x16x32_bf16 v[172:175], v[88:91], v[40:43], v[172:175]\n"
"s_add_u32 m0, s78, 0x400\n"
"ds_read_b64_tr_b16 v[86:87], v115 offset:13920\n"
"s_waitcnt lgkmcnt(6)\n"
"v_mfma_f32_16x16x32_bf16 a[32:35], v[96:99], v[16:19], a[32:35]\n"
"buffer_load_dwordx4 v105, s[60:63], s79 offen lds\n"
"s_waitcnt lgkmcnt(4)\n"
"v_mfma_f32_16x16x32_bf16 a[32:35], v[100:103], v[20:23], a[32:35]\n"
"ds_read_b64_tr_b16 v[88:89], v115 offset:18528\n"
"v_mfma_f32_16x16x32_bf16 a[36:39], v[100:103], v[28:31], a[36:39]\n"
"v_mfma_f32_16x16x32_bf16 a[36:39], v[96:99], v[24:27], a[36:39]\n"
"ds_read_b64_tr_b16 v[90:91], v115 offset:23136\n"
"v_mfma_f32_16x16x32_bf16 a[40:43], v[96:99], v[32:35], a[40:43]\n"
"v_mfma_f32_16x16x32_bf16 a[40:43], v[100:103], v[36:39], a[40:43]\n"
"s_add_u32 m0, s78, 0x800\n"
"ds_read_b64_tr_b16 v[92:93], v115 offset:27744\n"
"v_mfma_f32_16x16x32_bf16 a[44:47], v[100:103], v[44:47], a[44:47]\n"
"buffer_load_dwordx4 v106, s[60:63], s79 offen lds\n"
"v_mfma_f32_16x16x32_bf16 a[44:47], v[96:99], v[40:43], a[44:47]\n"
"ds_read_b64_tr_b16 v[94:95], v115 offset:32352\n"
"s_waitcnt lgkmcnt(6)\n"
"v_mfma_f32_16x16x32_bf16 v[176:179], v[80:83], v[16:19], v[176:179]\n"
"s_waitcnt lgkmcnt(4)\n"
"v_mfma_f32_16x16x32_bf16 v[176:179], v[84:87], v[20:23], v[176:179]\n"
"ds_read_b64_tr_b16 v[96:97], v115 offset:128\n"
"v_mfma_f32_16x16x32_bf16 v[180:183], v[84:87], v[28:31], v[180:183]\n"
"v_mfma_f32_16x16x32_bf16 v[180:183], v[80:83], v[24:27], v[180:183]\n"
"s_add_u32 m0, s78, 0xc00\n"
"ds_read_b64_tr_b16 v[98:99], v115 offset:4736\n"
"v_mfma_f32_16x16x32_bf16 v[184:187], v[80:83], v[32:35], v[184:187]\n"
"buffer_load_dwordx4 v107, s[60:63], s79 offen lds\n"
"v_mfma_f32_16x16x32_bf16 v[184:187], v[84:87], v[36:39], v[184:187]\n"
"ds_read_b64_tr_b16 v[100:101], v115 offset:9344\n"
"v_mfma_f32_16x16x32_bf16 v[188:191], v[84:87], v[44:47], v[188:191]\n"
"v_mfma_f32_16x16x32_bf16 v[188:191], v[80:83], v[40:43], v[188:191]\n"
"ds_read_b64_tr_b16 v[102:103], v115 offset:13952\n"
"s_waitcnt lgkmcnt(6)\n"
"v_mfma_f32_16x16x32_bf16 a[48:51], v[88:91], v[16:19], a[48:51]\n"
"s_waitcnt lgkmcnt(4)\n"
"v_mfma_f32_16x16x32_bf16 a[48:51], v[92:95], v[20:23], a[48:51]\n"
"s_add_u32 m0, s78, 0x1000\n"
"ds_read_b64_tr_b16 v[80:81], v115 offset:18560\n"
"v_mfma_f32_16x16x32_bf16 a[52:55], v[92:95], v[28:31], a[52:55]\n"
"buffer_load_dwordx4 v108, s[60:63], s79 offen lds\n"
"v_mfma_f32_16x16x32_bf16 a[52:55], v[88:91], v[24:27], a[52:55]\n"
"ds_read_b64_tr_b16 v[82:83], v115 offset:23168\n"
"v_mfma_f32_16x16x32_bf16 a[56:59], v[88:91], v[32:35], a[56:59]\n"
"v_mfma_f32_16x16x32_bf16 a[56:59], v[92:95], v[36:39], a[56:59]\n"
"ds_read_b64_tr_b16 v[84:85], v115 offset:27776\n"
"v_mfma_f32_16x16x32_bf16 a[60:63], v[92:95], v[44:47], a[60:63]\n"
"v_mfma_f32_16x16x32_bf16 a[60:63], v[88:91], v[40:43], a[60:63]\n"
"s_add_u32 m0, s78, 0x1400\n"
"ds_read_b64_tr_b16 v[86:87], v115 offset:32384\n"
"s_waitcnt lgkmcnt(6)\n"
"v_mfma_f32_16x16x32_bf16 v[192:195], v[96:99], v[16:19], v[192:195]\n"
"buffer_load_dwordx4 v109, s[60:63], s79 offen lds\n"
"s_waitcnt lgkmcnt(4)\n"
"v_mfma_f32_16x16x32_bf16 v[192:195], v[100:103], v[20:23], v[192:195]\n"
"ds_read_b64_tr_b16 v[88:89], v115 offset:160\n"
"v_mfma_f32_16x16x32_bf16 v[196:199], v[100:103], v[28:31], v[196:199]\n"
"v_mfma_f32_16x16x32_bf16 v[196:199], v[96:99], v[24:27], v[196:199]\n"
"ds_read_b64_tr_b16 v[90:91], v115 offset:4768\n"
"v_mfma_f32_16x16x32_bf16 v[200:203], v[96:99], v[32:35], v[200:203]\n"
"v_mfma_f32_16x16x32_bf16 v[200:203], v[100:103], v[36:39], v[200:203]\n"
"s_add_u32 m0, s78, 0x1800\n"
"ds_read_b64_tr_b16 v[92:93], v115 offset:9376\n"
"v_mfma_f32_16x16x32_bf16 v[204:207], v[100:103], v[44:47], v[204:207]\n"
"buffer_load_dwordx4 v110, s[60:63], s79 offen lds\n"
"v_mfma_f32_16x16x32_bf16 v[204:207], v[96:99], v[40:43], v[204:207]\n"
"ds_read_b64_tr_b16 v[94:95], v115 offset:13984\n"
"s_waitcnt lgkmcnt(6)\n"
"v_mfma_f32_16x16x32_bf16 a[64:67], v[80:83], v[16:19], a[64:67]\n"
"s_waitcnt lgkmcnt(4)\n"
"v_mfma_f32_16x16x32_bf16 a[64:67], v[84:87], v[20:23], a[64:67]\n"
"ds_read_b64_tr_b16 v[96:97], v115 offset:18592\n"
"v_mfma_f32_16x16x32_bf16 a[68:71], v[84:87], v[28:31], a[68:71]\n"
"v_mfma_f32_16x16x32_bf16 a[68:71], v[80:83], v[24:27], a[68:71]\n"
"s_add_u32 m0, s78, 0x1c00\n"
"ds_read_b64_tr_b16 v[98:99], v115 offset:23200\n"
"v_mfma_f32_16x16x32_bf16 a[72:75], v[80:83], v[32:35], a[72:75]\n"
"buffer_load_dwordx4 v111, s[60:63], s79 offen lds\n"
"v_mfma_f32_16x16x32_bf16 a[72:75], v[84:87], v[36:39], a[72:75]\n"
"ds_read_b64_tr_b16 v[100:101], v115 offset:27808\n"
"v_mfma_f32_16x16x32_bf16 a[76:79], v[84:87], v[44:47], a[76:79]\n"
"v_mfma_f32_16x16x32_bf16 a[76:79], v[80:83], v[40:43], a[76:79]\n"
"ds_read_b64_tr_b16 v[102:103], v115 offset:32416\n"
"s_waitcnt lgkmcnt(6)\n"
"v_mfma_f32_16x16x32_bf16 v[208:211], v[88:91], v[16:19], v[208:211]\n"
"s_waitcnt lgkmcnt(4)\n"
"v_mfma_f32_16x16x32_bf16 v[208:211], v[92:95], v[20:23], v[208:211]\n"
"s_add_u32 m0, s78, 0x2000\n"
"ds_read_b64_tr_b16 v[80:81], v115 offset:192\n"
"v_mfma_f32_16x16x32_bf16 v[212:215], v[92:95], v[28:31], v[212:215]\n"
"buffer_load_dwordx4 v112, s[60:63], s79 offen lds\n"
"v_mfma_f32_16x16x32_bf16 v[212:215], v[88:91], v[24:27], v[212:215]\n"
"ds_read_b64_tr_b16 v[82:83], v115 offset:4800\n"
"v_mfma_f32_16x16x32_bf16 v[216:219], v[88:91], v[32:35], v[216:219]\n"
"v_mfma_f32_16x16x32_bf16 v[216:219], v[92:95], v[36:39], v[216:219]\n"
"ds_read_b64_tr_b16 v[84:85], v115 offset:9408\n"
"v_mfma_f32_16x16x32_bf16 v[220:223], v[92:95], v[44:47], v[220:223]\n"
"v_mfma_f32_16x16x32_bf16 v[220:223], v[88:91], v[40:43], v[220:223]\n"
"ds_read_b64_tr_b16 v[86:87], v115 offset:14016\n"
"s_waitcnt lgkmcnt(6)\n"
"v_mfma_f32_16x16x32_bf16 a[80:83], v[96:99], v[16:19], a[80:83]\n"
"s_add_u32 s79, s79, 0x90000\n"
"s_waitcnt lgkmcnt(4)\n"
"v_mfma_f32_16x16x32_bf16 a[80:83], v[100:103], v[20:23], a[80:83]\n"
"ds_read_b64_tr_b16 v[88:89], v115 offset:18624\n"
"v_mfma_f32_16x16x32_bf16 a[84:87], v[100:103], v[28:31], a[84:87]\n"
"v_mfma_f32_16x16x32_bf16 a[84:87], v[96:99], v[24:27], a[84:87]\n"
"ds_read_b64_tr_b16 v[90:91], v115 offset:23232\n"
"v_mfma_f32_16x16x32_bf16 a[88:91], v[96:99], v[32:35], a[88:91]\n"
"v_mfma_f32_16x16x32_bf16 a[88:91], v[100:103], v[36:39], a[88:91]\n"
"ds_read_b64_tr_b16 v[92:93], v115 offset:27840\n"
"v_mfma_f32_16x16x32_bf16 a[92:95], v[100:103], v[44:47], a[92:95]\n"
"v_mfma_f32_16x16x32_bf16 a[92:95], v[96:99], v[40:43], a[92:95]\n"
"ds_read_b64_tr_b16 v[94:95], v115 offset:32448\n"
"s_waitcnt lgkmcnt(6)\n"
"v_mfma_f32_16x16x32_bf16 v[224:227], v[80:83], v[16:19], v[224:227]\n"
"s_waitcnt lgkmcnt(4)\n"
"v_mfma_f32_16x16x32_bf16 v[224:227], v[84:87], v[20:23], v[224:227]\n"
"ds_read_b64_tr_b16 v[96:97], v115 offset:224\n"
"v_mfma_f32_16x16x32_bf16 v[228:231], v[84:87], v[28:31], v[228:231]\n"
"v_mfma_f32_16x16x32_bf16 v[228:231], v[80:83], v[24:27], v[228:231]\n"
"ds_read_b64_tr_b16 v[98:99], v115 offset:4832\n"
"v_mfma_f32_16x16x32_bf16 v[232:235], v[80:83], v[32:35], v[232:235]\n"
"v_mfma_f32_16x16x32_bf16 v[232:235], v[84:87], v[36:39], v[232:235]\n"
"ds_read_b64_tr_b16 v[100:101], v115 offset:9440\n"
"v_mfma_f32_16x16x32_bf16 v[236:239], v[84:87], v[44:47], v[236:239]\n"
"v_mfma_f32_16x16x32_bf16 v[236:239], v[80:83], v[40:43], v[236:239]\n"
"ds_read_b64_tr_b16 v[102:103], v115 offset:14048\n"
"s_waitcnt lgkmcnt(6)\n"
"v_mfma_f32_16x16x32_bf16 a[96:99], v[88:91], v[16:19], a[96:99]\n"
"s_waitcnt lgkmcnt(4)\n"
"v_mfma_f32_16x16x32_bf16 a[96:99], v[92:95], v[20:23], a[96:99]\n"
"ds_read_b64_tr_b16 v[80:81], v115 offset:18656\n"
"v_mfma_f32_16x16x32_bf16 a[100:103], v[92:95], v[28:31], a[100:103]\n"
"v_mfma_f32_16x16x32_bf16 a[100:103], v[88:91], v[24:27], a[100:103]\n"
"ds_read_b64_tr_b16 v[82:83], v115 offset:23264\n"
"v_mfma_f32_16x16x32_bf16 a[104:107], v[88:91], v[32:35], a[104:107]\n"
"v_mfma_f32_16x16x32_bf16 a[104:107], v[92:95], v[36:39], a[104:107]\n"
"ds_read_b64_tr_b16 v[84:85], v115 offset:27872\n"
"v_mfma_f32_16x16x32_bf16 a[108:111], v[92:95], v[44:47], a[108:111]\n"
"v_mfma_f32_16x16x32_bf16 a[108:111], v[88:91], v[40:43], a[108:111]\n"
"ds_read_b64_tr_b16 v[86:87], v115 offset:32480\n"
"s_waitcnt lgkmcnt(6)\n"
"v_mfma_f32_16x16x32_bf16 v[240:243], v[96:99], v[16:19], v[240:243]\n"
"s_waitcnt lgkmcnt(4)\n"
"v_mfma_f32_16x16x32_bf16 v[240:243], v[100:103], v[20:23], v[240:243]\n"
"ds_read_b64_tr_b16 v[88:89], v115 offset:256\n"
"v_mfma_f32_16x16x32_bf16 v[244:247], v[100:103], v[28:31], v[244:247]\n"
"v_mfma_f32_16x16x32_bf16 v[244:247], v[96:99], v[24:27], v[244:247]\n"
"ds_read_b64_tr_b16 v[90:91], v115 offset:4864\n"
"v_mfma_f32_16x16x32_bf16 v[248:251], v[96:99], v[32:35], v[248:251]\n"
"v_mfma_f32_16x16x32_bf16 v[248:251], v[100:103], v[36:39], v[248:251]\n"
"ds_read_b64_tr_b16 v[92:93], v115 offset:9472\n"
"v_mfma_f32_16x16x32_bf16 v[252:255], v[100:103], v[44:47], v[252:255]\n"
"v_mfma_f32_16x16x32_bf16 v[252:255], v[96:99], v[40:43], v[252:255]\n"
"ds_read_b64_tr_b16 v[94:95], v115 offset:14080\n"
"s_waitcnt lgkmcnt(6)\n"
"v_mfma_f32_16x16x32_bf16 a[112:115], v[80:83], v[16:19], a[112:115]\n"
"s_waitcnt lgkmcnt(4)\n"
"v_mfma_f32_16x16x32_bf16 a[112:115], v[84:87], v[20:23], a[112:115]\n"
"ds_read_b64_tr_b16 v[96:97], v115 offset:18688\n"
"v_mfma_f32_16x16x32_bf16 a[116:119], v[84:87], v[28:31], a[116:119]\n"
"v_mfma_f32_16x16x32_bf16 a[116:119], v[80:83], v[24:27], a[116:119]\n"
"ds_read_b64_tr_b16 v[98:99], v115 offset:23296\n"
"v_mfma_f32_16x16x32_bf16 a[120:123], v[80:83], v[32:35], a[120:123]\n"
"v_mfma_f32_16x16x32_bf16 a[120:123], v[84:87], v[36:39], a[120:123]\n"
"ds_read_b64_tr_b16 v[100:101], v115 offset:27904\n"
"v_mfma_f32_16x16x32_bf16 a[124:127], v[84:87], v[44:47], a[124:127]\n"
"v_mfma_f32_16x16x32_bf16 a[124:127], v[80:83], v[40:43], a[124:127]\n"
"s_waitcnt vmcnt(17)\n"
"s_barrier\n"
"ds_read_b64_tr_b16 v[102:103], v115 offset:32512\n"
"s_waitcnt lgkmcnt(6)\n"
"v_mfma_f32_16x16x32_bf16 a[144:147], v[88:91], v[16:19], a[144:147]\n"
"s_waitcnt lgkmcnt(4)\n"
"v_mfma_f32_16x16x32_bf16 a[144:147], v[92:95], v[20:23], a[144:147]\n"
"ds_read_b64_tr_b16 v[80:81], v116\n"
"v_mfma_f32_16x16x32_bf16 a[148:151], v[92:95], v[28:31], a[148:151]\n"
"v_mfma_f32_16x16x32_bf16 a[148:151], v[88:91], v[24:27], a[148:151]\n"
"ds_read_b64_tr_b16 v[82:83], v116 offset:4608\n"
"v_mfma_f32_16x16x32_bf16 a[152:155], v[88:91], v[32:35], a[152:155]\n"
"v_mfma_f32_16x16x32_bf16 a[152:155], v[92:95], v[36:39], a[152:155]\n"
"ds_read_b64_tr_b16 v[84:85], v116 offset:9216\n"
"v_mfma_f32_16x16x32_bf16 a[156:159], v[92:95], v[44:47], a[156:159]\n"
"v_mfma_f32_16x16x32_bf16 a[156:159], v[88:91], v[40:43], a[156:159]\n"
"ds_read_b64_tr_b16 v[86:87], v116 offset:13824\n"
"s_waitcnt lgkmcnt(6)\n"
"v_mfma_f32_16x16x32_bf16 a[128:131], v[96:99], v[16:19], a[128:131]\n"
"s_waitcnt lgkmcnt(4)\n"
"v_mfma_f32_16x16x32_bf16 a[128:131], v[100:103], v[20:23], a[128:131]\n"
"ds_read_b64_tr_b16 v[88:89], v116 offset:18432\n"
"v_mfma_f32_16x16x32_bf16 a[132:135], v[100:103], v[28:31], a[132:135]\n"
"v_mfma_f32_16x16x32_bf16 a[132:135], v[96:99], v[24:27], a[132:135]\n"
"ds_read_b64_tr_b16 v[90:91], v116 offset:23040\n"
"v_mfma_f32_16x16x32_bf16 a[136:139], v[96:99], v[32:35], a[136:139]\n"
"v_mfma_f32_16x16x32_bf16 a[136:139], v[100:103], v[36:39], a[136:139]\n"
"ds_read_b64_tr_b16 v[92:93], v116 offset:27648\n"
"v_mfma_f32_16x16x32_bf16 a[140:143], v[100:103], v[44:47], a[140:143]\n"
"v_mfma_f32_16x16x32_bf16 a[140:143], v[96:99], v[40:43], a[140:143]\n"
"ds_read_b64_tr_b16 v[94:95], v116 offset:32256\n"
"s_waitcnt vmcnt(16) lgkmcnt(6)\n"
"v_mfma_f32_16x16x32_bf16 v[128:131], v[80:83], v[48:51], v[128:131]\n"
"s_waitcnt vmcnt(15) lgkmcnt(4)\n"
"v_mfma_f32_16x16x32_bf16 v[128:131], v[84:87], v[52:55], v[128:131]\n"
"s_add_u32 s76, s90, 0x4000\n"
"ds_read_b64_tr_b16 v[96:97], v116 offset:32\n"
"s_waitcnt vmcnt(13)\n"
"v_mfma_f32_16x16x32_bf16 v[132:135], v[84:87], v[60:63], v[132:135]\n"
"buffer_load_dwordx4 v[16:19], v121, s[64:67], s76 offen sc1\n"
"v_mfma_f32_16x16x32_bf16 v[132:135], v[80:83], v[56:59], v[132:135]\n"
"ds_read_b64_tr_b16 v[98:99], v116 offset:4640\n"
"s_waitcnt vmcnt(13)\n"
"v_mfma_f32_16x16x32_bf16 v[136:139], v[80:83], v[64:67], v[136:139]\n"
"s_waitcnt vmcnt(12)\n"
"v_mfma_f32_16x16x32_bf16 v[136:139], v[84:87], v[68:71], v[136:139]\n"
"s_add_u32 s76, s90, 0x4400\n"
"ds_read_b64_tr_b16 v[100:101], v116 offset:9248\n"
"s_waitcnt vmcnt(10)\n"
"v_mfma_f32_16x16x32_bf16 v[140:143], v[84:87], v[76:79], v[140:143]\n"
"buffer_load_dwordx4 v[20:23], v121, s[64:67], s76 offen sc1\n"
"v_mfma_f32_16x16x32_bf16 v[140:143], v[80:83], v[72:75], v[140:143]\n"
"ds_read_b64_tr_b16 v[102:103], v116 offset:13856\n"
"s_waitcnt lgkmcnt(6)\n"
"v_mfma_f32_16x16x32_bf16 a[0:3], v[88:91], v[48:51], a[0:3]\n"
"s_waitcnt lgkmcnt(4)\n"
"v_mfma_f32_16x16x32_bf16 a[0:3], v[92:95], v[52:55], a[0:3]\n"
"s_add_u32 s76, s90, 0x8800\n"
"ds_read_b64_tr_b16 v[80:81], v116 offset:18464\n"
"v_mfma_f32_16x16x32_bf16 a[4:7], v[92:95], v[60:63], a[4:7]\n"
"buffer_load_dwordx4 v[24:27], v121, s[64:67], s76 offen sc1\n"
"v_mfma_f32_16x16x32_bf16 a[4:7], v[88:91], v[56:59], a[4:7]\n"
"ds_read_b64_tr_b16 v[82:83], v116 offset:23072\n"
"v_mfma_f32_16x16x32_bf16 a[8:11], v[88:91], v[64:67], a[8:11]\n"
"v_mfma_f32_16x16x32_bf16 a[8:11], v[92:95], v[68:71], a[8:11]\n"
"s_add_u32 s76, s90, 0x8c00\n"
"ds_read_b64_tr_b16 v[84:85], v116 offset:27680\n"
"v_mfma_f32_16x16x32_bf16 a[12:15], v[92:95], v[76:79], a[12:15]\n"
"buffer_load_dwordx4 v[28:31], v121, s[64:67], s76 offen sc1\n"
"v_mfma_f32_16x16x32_bf16 a[12:15], v[88:91], v[72:75], a[12:15]\n"
"ds_read_b64_tr_b16 v[86:87], v116 offset:32288\n"
"s_waitcnt lgkmcnt(6)\n"
"v_mfma_f32_16x16x32_bf16 v[144:147], v[96:99], v[48:51], v[144:147]\n"
"s_waitcnt lgkmcnt(4)\n"
"v_mfma_f32_16x16x32_bf16 v[144:147], v[100:103], v[52:55], v[144:147]\n"
"s_add_u32 s76, s90, 0xd000\n"
"ds_read_b64_tr_b16 v[88:89], v116 offset:64\n"
"v_mfma_f32_16x16x32_bf16 v[148:151], v[100:103], v[60:63], v[148:151]\n"
"buffer_load_dwordx4 v[32:35], v121, s[64:67], s76 offen sc1\n"
"v_mfma_f32_16x16x32_bf16 v[148:151], v[96:99], v[56:59], v[148:151]\n"
"ds_read_b64_tr_b16 v[90:91], v116 offset:4672\n"
"v_mfma_f32_16x16x32_bf16 v[152:155], v[96:99], v[64:67], v[152:155]\n"
"v_mfma_f32_16x16x32_bf16 v[152:155], v[100:103], v[68:71], v[152:155]\n"
"s_add_u32 s76, s90, 0xd400\n"
"ds_read_b64_tr_b16 v[92:93], v116 offset:9280\n"
"v_mfma_f32_16x16x32_bf16 v[156:159], v[100:103], v[76:79], v[156:159]\n"
"buffer_load_dwordx4 v[36:39], v121, s[64:67], s76 offen sc1\n"
"v_mfma_f32_16x16x32_bf16 v[156:159], v[96:99], v[72:75], v[156:159]\n"
"ds_read_b64_tr_b16 v[94:95], v116 offset:13888\n"
"s_waitcnt lgkmcnt(6)\n"
"v_mfma_f32_16x16x32_bf16 a[16:19], v[80:83], v[48:51], a[16:19]\n"
"s_waitcnt lgkmcnt(4)\n"
"v_mfma_f32_16x16x32_bf16 a[16:19], v[84:87], v[52:55], a[16:19]\n"
"s_add_u32 s76, s90, 0x11800\n"
"ds_read_b64_tr_b16 v[96:97], v116 offset:18496\n"
"v_mfma_f32_16x16x32_bf16 a[20:23], v[84:87], v[60:63], a[20:23]\n"
"buffer_load_dwordx4 v[40:43], v121, s[64:67], s76 offen sc1\n"
"v_mfma_f32_16x16x32_bf16 a[20:23], v[80:83], v[56:59], a[20:23]\n"
"ds_read_b64_tr_b16 v[98:99], v116 offset:23104\n"
"v_mfma_f32_16x16x32_bf16 a[24:27], v[80:83], v[64:67], a[24:27]\n"
"v_mfma_f32_16x16x32_bf16 a[24:27], v[84:87], v[68:71], a[24:27]\n"
"s_add_u32 s76, s90, 0x11c00\n"
"ds_read_b64_tr_b16 v[100:101], v116 offset:27712\n"
"v_mfma_f32_16x16x32_bf16 a[28:31], v[84:87], v[76:79], a[28:31]\n"
"buffer_load_dwordx4 v[44:47], v121, s[64:67], s76 offen sc1\n"
"v_mfma_f32_16x16x32_bf16 a[28:31], v[80:83], v[72:75], a[28:31]\n"
"ds_read_b64_tr_b16 v[102:103], v116 offset:32320\n"
"s_waitcnt lgkmcnt(6)\n"
"v_mfma_f32_16x16x32_bf16 v[160:163], v[88:91], v[48:51], v[160:163]\n"
"s_waitcnt lgkmcnt(4)\n"
"v_mfma_f32_16x16x32_bf16 v[160:163], v[92:95], v[52:55], v[160:163]\n"
"ds_read_b64_tr_b16 v[80:81], v116 offset:96\n"
"v_mfma_f32_16x16x32_bf16 v[164:167], v[92:95], v[60:63], v[164:167]\n"
"v_mfma_f32_16x16x32_bf16 v[164:167], v[88:91], v[56:59], v[164:167]\n"
"ds_read_b64_tr_b16 v[82:83], v116 offset:4704\n"
"v_mfma_f32_16x16x32_bf16 v[168:171], v[88:91], v[64:67], v[168:171]\n"
"v_mfma_f32_16x16x32_bf16 v[168:171], v[92:95], v[68:71], v[168:171]\n"
"ds_read_b64_tr_b16 v[84:85], v116 offset:9312\n"
"v_mfma_f32_16x16x32_bf16 v[172:175], v[92:95], v[76:79], v[172:175]\n"
"v_mfma_f32_16x16x32_bf16 v[172:175], v[88:91], v[72:75], v[172:175]\n"
"ds_read_b64_tr_b16 v[86:87], v116 offset:13920\n"
"s_waitcnt lgkmcnt(6)\n"
"v_mfma_f32_16x16x32_bf16 a[32:35], v[96:99], v[48:51], a[32:35]\n"
"s_waitcnt lgkmcnt(4)\n"
"v_mfma_f32_16x16x32_bf16 a[32:35], v[100:103], v[52:55], a[32:35]\n"
"ds_read_b64_tr_b16 v[88:89], v116 offset:18528\n"
"v_mfma_f32_16x16x32_bf16 a[36:39], v[100:103], v[60:63], a[36:39]\n"
"v_mfma_f32_16x16x32_bf16 a[36:39], v[96:99], v[56:59], a[36:39]\n"
"ds_read_b64_tr_b16 v[90:91], v116 offset:23136\n"
"v_mfma_f32_16x16x32_bf16 a[40:43], v[96:99], v[64:67], a[40:43]\n"
"v_mfma_f32_16x16x32_bf16 a[40:43], v[100:103], v[68:71], a[40:43]\n"
"ds_read_b64_tr_b16 v[92:93], v116 offset:27744\n"
"v_mfma_f32_16x16x32_bf16 a[44:47], v[100:103], v[76:79], a[44:47]\n"
"v_mfma_f32_16x16x32_bf16 a[44:47], v[96:99], v[72:75], a[44:47]\n"
"ds_read_b64_tr_b16 v[94:95], v116 offset:32352\n"
"s_waitcnt lgkmcnt(6)\n"
"v_mfma_f32_16x16x32_bf16 v[176:179], v[80:83], v[48:51], v[176:179]\n"
"s_waitcnt lgkmcnt(4)\n"
"v_mfma_f32_16x16x32_bf16 v[176:179], v[84:87], v[52:55], v[176:179]\n"
"ds_read_b64_tr_b16 v[96:97], v116 offset:128\n"
"v_mfma_f32_16x16x32_bf16 v[180:183], v[84:87], v[60:63], v[180:183]\n"
"v_mfma_f32_16x16x32_bf16 v[180:183], v[80:83], v[56:59], v[180:183]\n"
"ds_read_b64_tr_b16 v[98:99], v116 offset:4736\n"
"v_mfma_f32_16x16x32_bf16 v[184:187], v[80:83], v[64:67], v[184:187]\n"
"v_mfma_f32_16x16x32_bf16 v[184:187], v[84:87], v[68:71], v[184:187]\n"
"ds_read_b64_tr_b16 v[100:101], v116 offset:9344\n"
"v_mfma_f32_16x16x32_bf16 v[188:191], v[84:87], v[76:79], v[188:191]\n"
"v_mfma_f32_16x16x32_bf16 v[188:191], v[80:83], v[72:75], v[188:191]\n"
"ds_read_b64_tr_b16 v[102:103], v116 offset:13952\n"
"s_waitcnt lgkmcnt(6)\n"
"v_mfma_f32_16x16x32_bf16 a[48:51], v[88:91], v[48:51], a[48:51]\n"
"s_waitcnt lgkmcnt(4)\n"
"v_mfma_f32_16x16x32_bf16 a[48:51], v[92:95], v[52:55], a[48:51]\n"
"ds_read_b64_tr_b16 v[80:81], v116 offset:18560\n"
"v_mfma_f32_16x16x32_bf16 a[52:55], v[92:95], v[60:63], a[52:55]\n"
"v_mfma_f32_16x16x32_bf16 a[52:55], v[88:91], v[56:59], a[52:55]\n"
"ds_read_b64_tr_b16 v[82:83], v116 offset:23168\n"
"v_mfma_f32_16x16x32_bf16 a[56:59], v[88:91], v[64:67], a[56:59]\n"
"s_lshl_b32 s84, s72, 12\n"
"s_lshl_b32 s85, s72, 4\n"
"s_mul_i32 s85, s85, 0x2000\n"
"s_lshl_b32 s76, s74, 8\n"
"s_add_u32 s85, s85, s76\n"
"v_mfma_f32_16x16x32_bf16 a[56:59], v[92:95], v[68:71], a[56:59]\n"
"ds_read_b64_tr_b16 v[84:85], v116 offset:27776\n"
"v_mfma_f32_16x16x32_bf16 a[60:63], v[92:95], v[76:79], a[60:63]\n"
"v_bfe_u32 v122, v5, 2, 3\n"
"v_mfma_f32_16x16x32_bf16 a[60:63], v[88:91], v[72:75], a[60:63]\n"
"ds_read_b64_tr_b16 v[86:87], v116 offset:32384\n"
"s_waitcnt lgkmcnt(6)\n"
"v_mfma_f32_16x16x32_bf16 v[192:195], v[96:99], v[48:51], v[192:195]\n"
"v_lshrrev_b32 v123, 2, v122\n"
"s_waitcnt lgkmcnt(4)\n"
"v_mfma_f32_16x16x32_bf16 v[192:195], v[100:103], v[52:55], v[192:195]\n"
"ds_read_b64_tr_b16 v[88:89], v116 offset:160\n"
"v_mfma_f32_16x16x32_bf16 v[196:199], v[100:103], v[60:63], v[196:199]\n"
"v_lshlrev_b32 v123, 1, v123\n"
"v_mfma_f32_16x16x32_bf16 v[196:199], v[96:99], v[56:59], v[196:199]\n"
"ds_read_b64_tr_b16 v[90:91], v116 offset:4768\n"
"v_mfma_f32_16x16x32_bf16 v[200:203], v[96:99], v[64:67], v[200:203]\n"
"v_and_b32 v124, 3, v5\n"
"v_mfma_f32_16x16x32_bf16 v[200:203], v[100:103], v[68:71], v[200:203]\n"
"ds_read_b64_tr_b16 v[92:93], v116 offset:9376\n"
"v_mfma_f32_16x16x32_bf16 v[204:207], v[100:103], v[76:79], v[204:207]\n"
"v_xor_b32 v123, v123, v124\n"
"v_mfma_f32_16x16x32_bf16 v[204:207], v[96:99], v[72:75], v[204:207]\n"
"ds_read_b64_tr_b16 v[94:95], v116 offset:13984\n"
"s_waitcnt lgkmcnt(6)\n"
"v_mfma_f32_16x16x32_bf16 a[64:67], v[80:83], v[48:51], a[64:67]\n"
"v_lshlrev_b32 v123, 4, v123\n"
"s_waitcnt lgkmcnt(4)\n"
"v_mfma_f32_16x16x32_bf16 a[64:67], v[84:87], v[52:55], a[64:67]\n"
"ds_read_b64_tr_b16 v[96:97], v116 offset:18592\n"
"v_mfma_f32_16x16x32_bf16 a[68:71], v[84:87], v[60:63], a[68:71]\n"
"v_lshl_add_u32 v123, v122, 13, v123\n"
"v_mfma_f32_16x16x32_bf16 a[68:71], v[80:83], v[56:59], a[68:71]\n"
"ds_read_b64_tr_b16 v[98:99], v116 offset:23200\n"
"v_mfma_f32_16x16x32_bf16 a[72:75], v[80:83], v[64:67], a[72:75]\n"
"v_lshrrev_b32 v124, 5, v5\n"
"v_mfma_f32_16x16x32_bf16 a[72:75], v[84:87], v[68:71], a[72:75]\n"
"ds_read_b64_tr_b16 v[100:101], v116 offset:27808\n"
"v_mfma_f32_16x16x32_bf16 a[76:79], v[84:87], v[76:79], a[76:79]\n"
"v_lshl_add_u32 v118, v124, 6, v123\n"
"v_mfma_f32_16x16x32_bf16 a[76:79], v[80:83], v[72:75], a[76:79]\n"
"ds_read_b64_tr_b16 v[102:103], v116 offset:32416\n"
"s_waitcnt lgkmcnt(6)\n"
"v_mfma_f32_16x16x32_bf16 v[208:211], v[88:91], v[48:51], v[208:211]\n"
"v_lshrrev_b32 v122, 5, v5\n"
"s_waitcnt lgkmcnt(4)\n"
"v_mfma_f32_16x16x32_bf16 v[208:211], v[92:95], v[52:55], v[208:211]\n"
"ds_read_b64_tr_b16 v[80:81], v116 offset:192\n"
"v_mfma_f32_16x16x32_bf16 v[212:215], v[92:95], v[60:63], v[212:215]\n"
"v_lshlrev_b32 v119, 11, v122\n"
"v_mfma_f32_16x16x32_bf16 v[212:215], v[88:91], v[56:59], v[212:215]\n"
"ds_read_b64_tr_b16 v[82:83], v116 offset:4800\n"
"v_mfma_f32_16x16x32_bf16 v[216:219], v[88:91], v[64:67], v[216:219]\n"
"v_bfe_u32 v122, v5, 4, 1\n"
"v_mfma_f32_16x16x32_bf16 v[216:219], v[92:95], v[68:71], v[216:219]\n"
"ds_read_b64_tr_b16 v[84:85], v116 offset:9408\n"
"v_mfma_f32_16x16x32_bf16 v[220:223], v[92:95], v[76:79], v[220:223]\n"
"v_lshl_add_u32 v119, v122, 8, v119\n"
"v_mfma_f32_16x16x32_bf16 v[220:223], v[88:91], v[72:75], v[220:223]\n"
"ds_read_b64_tr_b16 v[86:87], v116 offset:14016\n"
"s_waitcnt lgkmcnt(6)\n"
"v_mfma_f32_16x16x32_bf16 a[80:83], v[96:99], v[48:51], a[80:83]\n"
"v_xor_b32 v122, 0, v122\n"
"s_waitcnt lgkmcnt(4)\n"
"v_mfma_f32_16x16x32_bf16 a[80:83], v[100:103], v[52:55], a[80:83]\n"
"ds_read_b64_tr_b16 v[88:89], v116 offset:18624\n"
"v_mfma_f32_16x16x32_bf16 a[84:87], v[100:103], v[60:63], a[84:87]\n"
"v_lshl_add_u32 v119, v122, 5, v119\n"
"v_mfma_f32_16x16x32_bf16 a[84:87], v[96:99], v[56:59], a[84:87]\n"
"ds_read_b64_tr_b16 v[90:91], v116 offset:23232\n"
"v_mfma_f32_16x16x32_bf16 a[88:91], v[96:99], v[64:67], a[88:91]\n"
"v_bfe_u32 v122, v5, 2, 2\n"
"v_mfma_f32_16x16x32_bf16 a[88:91], v[100:103], v[68:71], a[88:91]\n"
"ds_read_b64_tr_b16 v[92:93], v116 offset:27840\n"
"v_mfma_f32_16x16x32_bf16 a[92:95], v[100:103], v[76:79], a[92:95]\n"
"v_lshl_add_u32 v119, v122, 6, v119\n"
"v_mfma_f32_16x16x32_bf16 a[92:95], v[96:99], v[72:75], a[92:95]\n"
"ds_read_b64_tr_b16 v[94:95], v116 offset:32448\n"
"s_waitcnt lgkmcnt(6)\n"
"v_mfma_f32_16x16x32_bf16 v[224:227], v[80:83], v[48:51], v[224:227]\n"
"v_and_b32 v122, 3, v5\n"
"s_waitcnt lgkmcnt(4)\n"
"v_mfma_f32_16x16x32_bf16 v[224:227], v[84:87], v[52:55], v[224:227]\n"
"ds_read_b64_tr_b16 v[96:97], v116 offset:224\n"
"v_mfma_f32_16x16x32_bf16 v[228:231], v[84:87], v[60:63], v[228:231]\n"
"v_lshl_add_u32 v119, v122, 3, v119\n"
"v_mfma_f32_16x16x32_bf16 v[228:231], v[80:83], v[56:59], v[228:231]\n"
"ds_read_b64_tr_b16 v[98:99], v116 offset:4832\n"
"v_mfma_f32_16x16x32_bf16 v[232:235], v[80:83], v[64:67], v[232:235]\n"
"v_add_u32 v119, 0x18000, v119\n"
"v_mfma_f32_16x16x32_bf16 v[232:235], v[84:87], v[68:71], v[232:235]\n"
"ds_read_b64_tr_b16 v[100:101], v116 offset:9440\n"
"v_mfma_f32_16x16x32_bf16 v[236:239], v[84:87], v[76:79], v[236:239]\n"
"v_lshrrev_b32 v122, 5, v5\n"
"v_mfma_f32_16x16x32_bf16 v[236:239], v[80:83], v[72:75], v[236:239]\n"
"ds_read_b64_tr_b16 v[102:103], v116 offset:14048\n"
"s_waitcnt lgkmcnt(6)\n"
"v_mfma_f32_16x16x32_bf16 a[96:99], v[88:91], v[48:51], a[96:99]\n"
"v_lshlrev_b32 v120, 11, v122\n"
"s_waitcnt lgkmcnt(4)\n"
"v_mfma_f32_16x16x32_bf16 a[96:99], v[92:95], v[52:55], a[96:99]\n"
"ds_read_b64_tr_b16 v[80:81], v116 offset:18656\n"
"v_mfma_f32_16x16x32_bf16 a[100:103], v[92:95], v[60:63], a[100:103]\n"
"v_bfe_u32 v122, v5, 4, 1\n"
"v_mfma_f32_16x16x32_bf16 a[100:103], v[88:91], v[56:59], a[100:103]\n"
"ds_read_b64_tr_b16 v[82:83], v116 offset:23264\n"
"v_mfma_f32_16x16x32_bf16 a[104:107], v[88:91], v[64:67], a[104:107]\n"
"v_lshl_add_u32 v120, v122, 8, v120\n"
"v_mfma_f32_16x16x32_bf16 a[104:107], v[92:95], v[68:71], a[104:107]\n"
"ds_read_b64_tr_b16 v[84:85], v116 offset:27872\n"
"v_mfma_f32_16x16x32_bf16 a[108:111], v[92:95], v[76:79], a[108:111]\n"
"v_xor_b32 v122, 1, v122\n"
"v_mfma_f32_16x16x32_bf16 a[108:111], v[88:91], v[72:75], a[108:111]\n"
"ds_read_b64_tr_b16 v[86:87], v116 offset:32480\n"
"s_waitcnt lgkmcnt(6)\n"
"v_mfma_f32_16x16x32_bf16 v[240:243], v[96:99], v[48:51], v[240:243]\n"
"v_lshl_add_u32 v120, v122, 5, v120\n"
"s_waitcnt lgkmcnt(4)\n"
"v_mfma_f32_16x16x32_bf16 v[240:243], v[100:103], v[52:55], v[240:243]\n"
"ds_read_b64_tr_b16 v[88:89], v116 offset:256\n"
"v_mfma_f32_16x16x32_bf16 v[244:247], v[100:103], v[60:63], v[244:247]\n"
"v_bfe_u32 v122, v5, 2, 2\n"
"v_mfma_f32_16x16x32_bf16 v[244:247], v[96:99], v[56:59], v[244:247]\n"
"ds_read_b64_tr_b16 v[90:91], v116 offset:4864\n"
"v_mfma_f32_16x16x32_bf16 v[248:251], v[96:99], v[64:67], v[248:251]\n"
"v_lshl_add_u32 v120, v122, 6, v120\n"
"v_mfma_f32_16x16x32_bf16 v[248:251], v[100:103], v[68:71], v[248:251]\n"
"ds_read_b64_tr_b16 v[92:93], v116 offset:9472\n"
"v_mfma_f32_16x16x32_bf16 v[252:255], v[100:103], v[76:79], v[252:255]\n"
"v_and_b32 v122, 3, v5\n"
"v_mfma_f32_16x16x32_bf16 v[252:255], v[96:99], v[72:75], v[252:255]\n"
"ds_read_b64_tr_b16 v[94:95], v116 offset:14080\n"
"s_waitcnt lgkmcnt(6)\n"
"v_mfma_f32_16x16x32_bf16 a[112:115], v[80:83], v[48:51], a[112:115]\n"
"v_lshl_add_u32 v120, v122, 3, v120\n"
"s_waitcnt lgkmcnt(4)\n"
"v_mfma_f32_16x16x32_bf16 a[112:115], v[84:87], v[52:55], a[112:115]\n"
"ds_read_b64_tr_b16 v[96:97], v116 offset:18688\n"
"v_mfma_f32_16x16x32_bf16 a[116:119], v[84:87], v[60:63], a[116:119]\n"
"v_add_u32 v120, 0x18000, v120\n"
"v_mfma_f32_16x16x32_bf16 a[116:119], v[80:83], v[56:59], a[116:119]\n"
"ds_read_b64_tr_b16 v[98:99], v116 offset:23296\n"
"v_mfma_f32_16x16x32_bf16 a[120:123], v[80:83], v[64:67], a[120:123]\n"
"v_mfma_f32_16x16x32_bf16 a[120:123], v[84:87], v[68:71], a[120:123]\n"
"ds_read_b64_tr_b16 v[100:101], v116 offset:27904\n"
"v_mfma_f32_16x16x32_bf16 a[124:127], v[84:87], v[76:79], a[124:127]\n"
"v_mfma_f32_16x16x32_bf16 a[124:127], v[80:83], v[72:75], a[124:127]\n"
"s_waitcnt vmcnt(8)\n"
"s_barrier\n"
"ds_read_b64_tr_b16 v[102:103], v116 offset:32512\n"
"s_waitcnt lgkmcnt(6)\n"
"v_mfma_f32_16x16x32_bf16 a[144:147], v[88:91], v[48:51], a[144:147]\n"
"s_waitcnt lgkmcnt(4)\n"
"v_mfma_f32_16x16x32_bf16 a[144:147], v[92:95], v[52:55], a[144:147]\n"
"ds_read_b64_tr_b16 v[80:81], v113\n"
"v_mfma_f32_16x16x32_bf16 a[148:151], v[92:95], v[60:63], a[148:151]\n"
"v_mfma_f32_16x16x32_bf16 a[148:151], v[88:91], v[56:59], a[148:151]\n"
"ds_read_b64_tr_b16 v[82:83], v113 offset:4608\n"
"v_mfma_f32_16x16x32_bf16 a[152:155], v[88:91], v[64:67], a[152:155]\n"
"v_mfma_f32_16x16x32_bf16 a[152:155], v[92:95], v[68:71], a[152:155]\n"
"ds_read_b64_tr_b16 v[84:85], v113 offset:9216\n"
"v_mfma_f32_16x16x32_bf16 a[156:159], v[92:95], v[76:79], a[156:159]\n"
"v_mfma_f32_16x16x32_bf16 a[156:159], v[88:91], v[72:75], a[156:159]\n"
"ds_read_b64_tr_b16 v[86:87], v113 offset:13824\n"
"s_waitcnt lgkmcnt(6)\n"
"v_mfma_f32_16x16x32_bf16 a[128:131], v[96:99], v[48:51], a[128:131]\n"
"s_waitcnt lgkmcnt(4)\n"
"v_mfma_f32_16x16x32_bf16 a[128:131], v[100:103], v[52:55], a[128:131]\n"
"ds_read_b64_tr_b16 v[88:89], v113 offset:18432\n"
"v_mfma_f32_16x16x32_bf16 a[132:135], v[100:103], v[60:63], a[132:135]\n"
"v_mfma_f32_16x16x32_bf16 a[132:135], v[96:99], v[56:59], a[132:135]\n"
"ds_read_b64_tr_b16 v[90:91], v113 offset:23040\n"
"v_mfma_f32_16x16x32_bf16 a[136:139], v[96:99], v[64:67], a[136:139]\n"
"v_mfma_f32_16x16x32_bf16 a[136:139], v[100:103], v[68:71], a[136:139]\n"
"ds_read_b64_tr_b16 v[92:93], v113 offset:27648\n"
"v_mfma_f32_16x16x32_bf16 a[140:143], v[100:103], v[76:79], a[140:143]\n"
"v_mfma_f32_16x16x32_bf16 a[140:143], v[96:99], v[72:75], a[140:143]\n"
"ds_read_b64_tr_b16 v[94:95], v113 offset:32256\n"
"s_waitcnt vmcnt(7) lgkmcnt(6)\n"
"v_mfma_f32_16x16x32_bf16 v[128:131], v[80:83], v[16:19], v[128:131]\n"
"s_waitcnt vmcnt(6) lgkmcnt(4)\n"
"v_mfma_f32_16x16x32_bf16 v[128:131], v[84:87], v[20:23], v[128:131]\n"
"s_add_u32 s77, s85, 0\n"
"s_add_u32 m0, s84, 0x24000\n"
"ds_read_b64_tr_b16 v[96:97], v113 offset:32\n"
"s_waitcnt vmcnt(4)\n"
"v_mfma_f32_16x16x32_bf16 v[132:135], v[84:87], v[28:31], v[132:135]\n"
"buffer_load_dwordx4 v118, s[56:59], s77 offen lds\n"
"v_mfma_f32_16x16x32_bf16 v[132:135], v[80:83], v[24:27], v[132:135]\n"
"ds_read_b64_tr_b16 v[98:99], v113 offset:4640\n"
"s_waitcnt vmcnt(4)\n"
"v_mfma_f32_16x16x32_bf16 v[136:139], v[80:83], v[32:35], v[136:139]\n"
"s_waitcnt vmcnt(3)\n"
"v_mfma_f32_16x16x32_bf16 v[136:139], v[84:87], v[36:39], v[136:139]\n"
"ds_read_b64_tr_b16 v[100:101], v113 offset:9248\n"
"s_waitcnt vmcnt(1)\n"
"v_mfma_f32_16x16x32_bf16 v[140:143], v[84:87], v[44:47], v[140:143]\n"
"s_add_u32 s77, s85, 0x80\n"
"s_add_u32 m0, s84, 0x24400\n"
"v_mfma_f32_16x16x32_bf16 v[140:143], v[80:83], v[40:43], v[140:143]\n"
"ds_read_b64_tr_b16 v[102:103], v113 offset:13856\n"
"buffer_load_dwordx4 v118, s[56:59], s77 offen lds\n"
"s_waitcnt lgkmcnt(6)\n"
"v_mfma_f32_16x16x32_bf16 a[0:3], v[88:91], v[16:19], a[0:3]\n"
"s_waitcnt lgkmcnt(4)\n"
"v_mfma_f32_16x16x32_bf16 a[0:3], v[92:95], v[20:23], a[0:3]\n"
"ds_read_b64_tr_b16 v[80:81], v113 offset:18464\n"
"v_mfma_f32_16x16x32_bf16 a[4:7], v[92:95], v[28:31], a[4:7]\n"
"v_mfma_f32_16x16x32_bf16 a[4:7], v[88:91], v[24:27], a[4:7]\n"
"s_add_u32 s77, s85, 0x10000\n"
"s_add_u32 m0, s84, 0x24800\n"
"ds_read_b64_tr_b16 v[82:83], v113 offset:23072\n"
"v_mfma_f32_16x16x32_bf16 a[8:11], v[88:91], v[32:35], a[8:11]\n"
"buffer_load_dwordx4 v118, s[56:59], s77 offen lds\n"
"v_mfma_f32_16x16x32_bf16 a[8:11], v[92:95], v[36:39], a[8:11]\n"
"ds_read_b64_tr_b16 v[84:85], v113 offset:27680\n"
"v_mfma_f32_16x16x32_bf16 a[12:15], v[92:95], v[44:47], a[12:15]\n"
"v_mfma_f32_16x16x32_bf16 a[12:15], v[88:91], v[40:43], a[12:15]\n"
"ds_read_b64_tr_b16 v[86:87], v113 offset:32288\n"
"s_waitcnt lgkmcnt(6)\n"
"v_mfma_f32_16x16x32_bf16 v[144:147], v[96:99], v[16:19], v[144:147]\n"
"s_add_u32 s77, s85, 0x10080\n"
"s_add_u32 m0, s84, 0x24c00\n"
"s_waitcnt lgkmcnt(4)\n"
"v_mfma_f32_16x16x32_bf16 v[144:147], v[100:103], v[20:23], v[144:147]\n"
"ds_read_b64_tr_b16 v[88:89], v113 offset:64\n"
"buffer_load_dwordx4 v118, s[56:59], s77 offen lds\n"
"v_mfma_f32_16x16x32_bf16 v[148:151], v[100:103], v[28:31], v[148:151]\n"
"v_mfma_f32_16x16x32_bf16 v[148:151], v[96:99], v[24:27], v[148:151]\n"
"ds_read_b64_tr_b16 v[90:91], v113 offset:4672\n"
"v_mfma_f32_16x16x32_bf16 v[152:155], v[96:99], v[32:35], v[152:155]\n"
"v_mfma_f32_16x16x32_bf16 v[152:155], v[100:103], v[36:39], v[152:155]\n"
"s_add_u32 s76, s90, 0\n"
"ds_read_b64_tr_b16 v[92:93], v113 offset:9280\n"
"v_mfma_f32_16x16x32_bf16 v[156:159], v[100:103], v[44:47], v[156:159]\n"
"buffer_load_dwordx4 a[160:163], v121, s[64:67], s76 offen sc1\n"
"v_mfma_f32_16x16x32_bf16 v[156:159], v[96:99], v[40:43], v[156:159]\n"
"ds_read_b64_tr_b16 v[94:95], v113 offset:13888\n"
"s_waitcnt lgkmcnt(6)\n"
"v_mfma_f32_16x16x32_bf16 a[16:19], v[80:83], v[16:19], a[16:19]\n"
"s_waitcnt lgkmcnt(4)\n"
"v_mfma_f32_16x16x32_bf16 a[16:19], v[84:87], v[20:23], a[16:19]\n"
"ds_read_b64_tr_b16 v[96:97], v113 offset:18496\n"
"v_mfma_f32_16x16x32_bf16 a[20:23], v[84:87], v[28:31], a[20:23]\n"
"s_add_u32 s76, s90, 0x400\n"
"v_mfma_f32_16x16x32_bf16 a[20:23], v[80:83], v[24:27], a[20:23]\n"
"ds_read_b64_tr_b16 v[98:99], v113 offset:23104\n"
"buffer_load_dwordx4 a[164:167], v121, s[64:67], s76 offen sc1\n"
"v_mfma_f32_16x16x32_bf16 a[24:27], v[80:83], v[32:35], a[24:27]\n"
"v_mfma_f32_16x16x32_bf16 a[24:27], v[84:87], v[36:39], a[24:27]\n"
"ds_read_b64_tr_b16 v[100:101], v113 offset:27712\n"
"v_mfma_f32_16x16x32_bf16 a[28:31], v[84:87], v[44:47], a[28:31]\n"
"v_mfma_f32_16x16x32_bf16 a[28:31], v[80:83], v[40:43], a[28:31]\n"
"s_add_u32 s76, s90, 0x4800\n"
"ds_read_b64_tr_b16 v[102:103], v113 offset:32320\n"
"s_waitcnt lgkmcnt(6)\n"
"v_mfma_f32_16x16x32_bf16 v[160:163], v[88:91], v[16:19], v[160:163]\n"
"buffer_load_dwordx4 a[168:171], v121, s[64:67], s76 offen sc1\n"
"s_waitcnt lgkmcnt(4)\n"
"v_mfma_f32_16x16x32_bf16 v[160:163], v[92:95], v[20:23], v[160:163]\n"
"ds_read_b64_tr_b16 v[80:81], v113 offset:96\n"
"v_mfma_f32_16x16x32_bf16 v[164:167], v[92:95], v[28:31], v[164:167]\n"
"v_mfma_f32_16x16x32_bf16 v[164:167], v[88:91], v[24:27], v[164:167]\n"
"ds_read_b64_tr_b16 v[82:83], v113 offset:4704\n"
"v_mfma_f32_16x16x32_bf16 v[168:171], v[88:91], v[32:35], v[168:171]\n"
"s_add_u32 s76, s90, 0x4c00\n"
"v_mfma_f32_16x16x32_bf16 v[168:171], v[92:95], v[36:39], v[168:171]\n"
"ds_read_b64_tr_b16 v[84:85], v113 offset:9312\n"
"buffer_load_dwordx4 a[172:175], v121, s[64:67], s76 offen sc1\n"
"v_mfma_f32_16x16x32_bf16 v[172:175], v[92:95], v[44:47], v[172:175]\n"
"v_mfma_f32_16x16x32_bf16 v[172:175], v[88:91], v[40:43], v[172:175]\n"
"ds_read_b64_tr_b16 v[86:87], v113 offset:13920\n"
"s_waitcnt lgkmcnt(6)\n"
"v_mfma_f32_16x16x32_bf16 a[32:35], v[96:99], v[16:19], a[32:35]\n"
"s_waitcnt lgkmcnt(4)\n"
"v_mfma_f32_16x16x32_bf16 a[32:35], v[100:103], v[20:23], a[32:35]\n"
"s_add_u32 s76, s90, 0x9000\n"
"ds_read_b64_tr_b16 v[88:89], v113 offset:18528\n"
"v_mfma_f32_16x16x32_bf16 a[36:39], v[100:103], v[28:31], a[36:39]\n"
"buffer_load_dwordx4 a[176:179], v121, s[64:67], s76 offen sc1\n"
"v_mfma_f32_16x16x32_bf16 a[36:39], v[96:99], v[24:27], a[36:39]\n"
"ds_read_b64_tr_b16 v[90:91], v113 offset:23136\n"
"v_mfma_f32_16x16x32_bf16 a[40:43], v[96:99], v[32:35], a[40:43]\n"
"v_mfma_f32_16x16x32_bf16 a[40:43], v[100:103], v[36:39], a[40:43]\n"
"ds_read_b64_tr_b16 v[92:93], v113 offset:27744\n"
"v_mfma_f32_16x16x32_bf16 a[44:47], v[100:103], v[44:47], a[44:47]\n"
"s_add_u32 s76, s90, 0x9400\n"
"v_mfma_f32_16x16x32_bf16 a[44:47], v[96:99], v[40:43], a[44:47]\n"
"ds_read_b64_tr_b16 v[94:95], v113 offset:32352\n"
"buffer_load_dwordx4 a[180:183], v121, s[64:67], s76 offen sc1\n"
"s_waitcnt lgkmcnt(6)\n"
"v_mfma_f32_16x16x32_bf16 v[176:179], v[80:83], v[16:19], v[176:179]\n"
"s_waitcnt lgkmcnt(4)\n"
"v_mfma_f32_16x16x32_bf16 v[176:179], v[84:87], v[20:23], v[176:179]\n"
"ds_read_b64_tr_b16 v[96:97], v113 offset:128\n"
"v_mfma_f32_16x16x32_bf16 v[180:183], v[84:87], v[28:31], v[180:183]\n"
"v_mfma_f32_16x16x32_bf16 v[180:183], v[80:83], v[24:27], v[180:183]\n"
"s_add_u32 s76, s90, 0xd800\n"
"ds_read_b64_tr_b16 v[98:99], v113 offset:4736\n"
"v_mfma_f32_16x16x32_bf16 v[184:187], v[80:83], v[32:35], v[184:187]\n"
"buffer_load_dwordx4 a[184:187], v121, s[64:67], s76 offen sc1\n"
"v_mfma_f32_16x16x32_bf16 v[184:187], v[84:87], v[36:39], v[184:187]\n"
"ds_read_b64_tr_b16 v[100:101], v113 offset:9344\n"
"v_mfma_f32_16x16x32_bf16 v[188:191], v[84:87], v[44:47], v[188:191]\n"
"v_mfma_f32_16x16x32_bf16 v[188:191], v[80:83], v[40:43], v[188:191]\n"
"ds_read_b64_tr_b16 v[102:103], v113 offset:13952\n"
"s_waitcnt lgkmcnt(6)\n"
"v_mfma_f32_16x16x32_bf16 a[48:51], v[88:91], v[16:19], a[48:51]\n"
"s_add_u32 s76, s90, 0xdc00\n"
"s_waitcnt lgkmcnt(4)\n"
"v_mfma_f32_16x16x32_bf16 a[48:51], v[92:95], v[20:23], a[48:51]\n"
"ds_read_b64_tr_b16 v[80:81], v113 offset:18560\n"
"buffer_load_dwordx4 a[188:191], v121, s[64:67], s76 offen sc1\n"
"v_mfma_f32_16x16x32_bf16 a[52:55], v[92:95], v[28:31], a[52:55]\n"
"v_mfma_f32_16x16x32_bf16 a[52:55], v[88:91], v[24:27], a[52:55]\n"
"ds_read_b64_tr_b16 v[82:83], v113 offset:23168\n"
"v_mfma_f32_16x16x32_bf16 a[56:59], v[88:91], v[32:35], a[56:59]\n"
"v_mfma_f32_16x16x32_bf16 a[56:59], v[92:95], v[36:39], a[56:59]\n"
"s_add_u32 s76, s90, 0x800\n"
"ds_read_b64_tr_b16 v[84:85], v113 offset:27776\n"
"v_mfma_f32_16x16x32_bf16 a[60:63], v[92:95], v[44:47], a[60:63]\n"
"buffer_load_dwordx4 a[192:195], v121, s[64:67], s76 offen sc1\n"
"v_mfma_f32_16x16x32_bf16 a[60:63], v[88:91], v[40:43], a[60:63]\n"
"ds_read_b64_tr_b16 v[86:87], v113 offset:32384\n"
"s_waitcnt lgkmcnt(6)\n"
"v_mfma_f32_16x16x32_bf16 v[192:195], v[96:99], v[16:19], v[192:195]\n"
"s_waitcnt lgkmcnt(4)\n"
"v_mfma_f32_16x16x32_bf16 v[192:195], v[100:103], v[20:23], v[192:195]\n"
"ds_read_b64_tr_b16 v[88:89], v113 offset:160\n"
"v_mfma_f32_16x16x32_bf16 v[196:199], v[100:103], v[28:31], v[196:199]\n"
"s_add_u32 s76, s90, 0xc00\n"
"v_mfma_f32_16x16x32_bf16 v[196:199], v[96:99], v[24:27], v[196:199]\n"
"ds_read_b64_tr_b16 v[90:91], v113 offset:4768\n"
"buffer_load_dwordx4 a[196:199], v121, s[64:67], s76 offen sc1\n"
"v_mfma_f32_16x16x32_bf16 v[200:203], v[96:99], v[32:35], v[200:203]\n"
"v_mfma_f32_16x16x32_bf16 v[200:203], v[100:103], v[36:39], v[200:203]\n"
"ds_read_b64_tr_b16 v[92:93], v113 offset:9376\n"
"v_mfma_f32_16x16x32_bf16 v[204:207], v[100:103], v[44:47], v[204:207]\n"
"v_mfma_f32_16x16x32_bf16 v[204:207], v[96:99], v[40:43], v[204:207]\n"
"s_add_u32 s76, s90, 0x5000\n"
"ds_read_b64_tr_b16 v[94:95], v113 offset:13984\n"
"s_waitcnt lgkmcnt(6)\n"
"v_mfma_f32_16x16x32_bf16 a[64:67], v[80:83], v[16:19], a[64:67]\n"
"buffer_load_dwordx4 a[200:203], v121, s[64:67], s76 offen sc1\n"
"s_waitcnt lgkmcnt(4)\n"
"v_mfma_f32_16x16x32_bf16 a[64:67], v[84:87], v[20:23], a[64:67]\n"
"ds_read_b64_tr_b16 v[96:97], v113 offset:18592\n"
"v_mfma_f32_16x16x32_bf16 a[68:71], v[84:87], v[28:31], a[68:71]\n"
"v_mfma_f32_16x16x32_bf16 a[68:71], v[80:83], v[24:27], a[68:71]\n"
"ds_read_b64_tr_b16 v[98:99], v113 offset:23200\n"
"v_mfma_f32_16x16x32_bf16 a[72:75], v[80:83], v[32:35], a[72:75]\n"
"s_add_u32 s76, s90, 0x5400\n"
"v_mfma_f32_16x16x32_bf16 a[72:75], v[84:87], v[36:39], a[72:75]\n"
"ds_read_b64_tr_b16 v[100:101], v113 offset:27808\n"
"buffer_load_dwordx4 a[204:207], v121, s[64:67], s76 offen sc1\n"
"v_mfma_f32_16x16x32_bf16 a[76:79], v[84:87], v[44:47], a[76:79]\n"
"v_mfma_f32_16x16x32_bf16 a[76:79], v[80:83], v[40:43], a[76:79]\n"
"ds_read_b64_tr_b16 v[102:103], v113 offset:32416\n"
"s_waitcnt lgkmcnt(6)\n"
"v_mfma_f32_16x16x32_bf16 v[208:211], v[88:91], v[16:19], v[208:211]\n"
"s_waitcnt lgkmcnt(4)\n"
"v_mfma_f32_16x16x32_bf16 v[208:211], v[92:95], v[20:23], v[208:211]\n"
"s_add_u32 s76, s90, 0x9800\n"
"ds_read_b64_tr_b16 v[80:81], v113 offset:192\n"
"v_mfma_f32_16x16x32_bf16 v[212:215], v[92:95], v[28:31], v[212:215]\n"
"buffer_load_dwordx4 a[208:211], v121, s[64:67], s76 offen sc1\n"
"v_mfma_f32_16x16x32_bf16 v[212:215], v[88:91], v[24:27], v[212:215]\n"
"ds_read_b64_tr_b16 v[82:83], v113 offset:4800\n"
"v_mfma_f32_16x16x32_bf16 v[216:219], v[88:91], v[32:35], v[216:219]\n"
"v_mfma_f32_16x16x32_bf16 v[216:219], v[92:95], v[36:39], v[216:219]\n"
"ds_read_b64_tr_b16 v[84:85], v113 offset:9408\n"
"v_mfma_f32_16x16x32_bf16 v[220:223], v[92:95], v[44:47], v[220:223]\n"
"s_add_u32 s76, s90, 0x9c00\n"
"v_mfma_f32_16x16x32_bf16 v[220:223], v[88:91], v[40:43], v[220:223]\n"
"ds_read_b64_tr_b16 v[86:87], v113 offset:14016\n"
"buffer_load_dwordx4 a[212:215], v121, s[64:67], s76 offen sc1\n"
"s_waitcnt lgkmcnt(6)\n"
"v_mfma_f32_16x16x32_bf16 a[80:83], v[96:99], v[16:19], a[80:83]\n"
"s_waitcnt lgkmcnt(4)\n"
"v_mfma_f32_16x16x32_bf16 a[80:83], v[100:103], v[20:23], a[80:83]\n"
"ds_read_b64_tr_b16 v[88:89], v113 offset:18624\n"
"v_mfma_f32_16x16x32_bf16 a[84:87], v[100:103], v[28:31], a[84:87]\n"
"v_mfma_f32_16x16x32_bf16 a[84:87], v[96:99], v[24:27], a[84:87]\n"
"s_add_u32 s76, s90, 0xe000\n"
"ds_read_b64_tr_b16 v[90:91], v113 offset:23232\n"
"v_mfma_f32_16x16x32_bf16 a[88:91], v[96:99], v[32:35], a[88:91]\n"
"buffer_load_dwordx4 a[216:219], v121, s[64:67], s76 offen sc1\n"
"v_mfma_f32_16x16x32_bf16 a[88:91], v[100:103], v[36:39], a[88:91]\n"
"ds_read_b64_tr_b16 v[92:93], v113 offset:27840\n"
"v_mfma_f32_16x16x32_bf16 a[92:95], v[100:103], v[44:47], a[92:95]\n"
"v_mfma_f32_16x16x32_bf16 a[92:95], v[96:99], v[40:43], a[92:95]\n"
"ds_read_b64_tr_b16 v[94:95], v113 offset:32448\n"
"s_waitcnt lgkmcnt(6)\n"
"v_mfma_f32_16x16x32_bf16 v[224:227], v[80:83], v[16:19], v[224:227]\n"
"s_add_u32 s76, s90, 0xe400\n"
"s_waitcnt lgkmcnt(4)\n"
"v_mfma_f32_16x16x32_bf16 v[224:227], v[84:87], v[20:23], v[224:227]\n"
"ds_read_b64_tr_b16 v[96:97], v113 offset:224\n"
"buffer_load_dwordx4 a[220:223], v121, s[64:67], s76 offen sc1\n"
"v_mfma_f32_16x16x32_bf16 v[228:231], v[84:87], v[28:31], v[228:231]\n"
"v_mfma_f32_16x16x32_bf16 v[228:231], v[80:83], v[24:27], v[228:231]\n"
"ds_read_b64_tr_b16 v[98:99], v113 offset:4832\n"
"v_mfma_f32_16x16x32_bf16 v[232:235], v[80:83], v[32:35], v[232:235]\n"
"v_mfma_f32_16x16x32_bf16 v[232:235], v[84:87], v[36:39], v[232:235]\n"
"s_add_u32 s76, s90, 0x1000\n"
"ds_read_b64_tr_b16 v[100:101], v113 offset:9440\n"
"v_mfma_f32_16x16x32_bf16 v[236:239], v[84:87], v[44:47], v[236:239]\n"
"buffer_load_dwordx4 a[224:227], v121, s[64:67], s76 offen sc1\n"
"v_mfma_f32_16x16x32_bf16 v[236:239], v[80:83], v[40:43], v[236:239]\n"
"ds_read_b64_tr_b16 v[102:103], v113 offset:14048\n"
"s_waitcnt lgkmcnt(6)\n"
"v_mfma_f32_16x16x32_bf16 a[96:99], v[88:91], v[16:19], a[96:99]\n"
"s_waitcnt lgkmcnt(4)\n"
"v_mfma_f32_16x16x32_bf16 a[96:99], v[92:95], v[20:23], a[96:99]\n"
"ds_read_b64_tr_b16 v[80:81], v113 offset:18656\n"
"v_mfma_f32_16x16x32_bf16 a[100:103], v[92:95], v[28:31], a[100:103]\n"
"s_add_u32 s76, s90, 0x1400\n"
"v_mfma_f32_16x16x32_bf16 a[100:103], v[88:91], v[24:27], a[100:103]\n"
"ds_read_b64_tr_b16 v[82:83], v113 offset:23264\n"
"buffer_load_dwordx4 a[228:231], v121, s[64:67], s76 offen sc1\n"
"v_mfma_f32_16x16x32_bf16 a[104:107], v[88:91], v[32:35], a[104:107]\n"
"v_mfma_f32_16x16x32_bf16 a[104:107], v[92:95], v[36:39], a[104:107]\n"
"ds_read_b64_tr_b16 v[84:85], v113 offset:27872\n"
"v_mfma_f32_16x16x32_bf16 a[108:111], v[92:95], v[44:47], a[108:111]\n"
"v_mfma_f32_16x16x32_bf16 a[108:111], v[88:91], v[40:43], a[108:111]\n"
"s_add_u32 s76, s90, 0x5800\n"
"ds_read_b64_tr_b16 v[86:87], v113 offset:32480\n"
"s_waitcnt lgkmcnt(6)\n"
"v_mfma_f32_16x16x32_bf16 v[240:243], v[96:99], v[16:19], v[240:243]\n"
"buffer_load_dwordx4 a[232:235], v121, s[64:67], s76 offen sc1\n"
"s_waitcnt lgkmcnt(4)\n"
"v_mfma_f32_16x16x32_bf16 v[240:243], v[100:103], v[20:23], v[240:243]\n"
"ds_read_b64_tr_b16 v[88:89], v113 offset:256\n"
"v_mfma_f32_16x16x32_bf16 v[244:247], v[100:103], v[28:31], v[244:247]\n"
"v_mfma_f32_16x16x32_bf16 v[244:247], v[96:99], v[24:27], v[244:247]\n"
"ds_read_b64_tr_b16 v[90:91], v113 offset:4864\n"
"v_mfma_f32_16x16x32_bf16 v[248:251], v[96:99], v[32:35], v[248:251]\n"
"s_add_u32 s76, s90, 0x5c00\n"
"v_mfma_f32_16x16x32_bf16 v[248:251], v[100:103], v[36:39], v[248:251]\n"
"ds_read_b64_tr_b16 v[92:93], v113 offset:9472\n"
"buffer_load_dwordx4 a[236:239], v121, s[64:67], s76 offen sc1\n"
"v_mfma_f32_16x16x32_bf16 v[252:255], v[100:103], v[44:47], v[252:255]\n"
"v_mfma_f32_16x16x32_bf16 v[252:255], v[96:99], v[40:43], v[252:255]\n"
"ds_read_b64_tr_b16 v[94:95], v113 offset:14080\n"
"s_waitcnt lgkmcnt(6)\n"
"v_mfma_f32_16x16x32_bf16 a[112:115], v[80:83], v[16:19], a[112:115]\n"
"s_waitcnt lgkmcnt(4)\n"
"v_mfma_f32_16x16x32_bf16 a[112:115], v[84:87], v[20:23], a[112:115]\n"
"s_add_u32 s76, s90, 0xa000\n"
"ds_read_b64_tr_b16 v[96:97], v113 offset:18688\n"
"v_mfma_f32_16x16x32_bf16 a[116:119], v[84:87], v[28:31], a[116:119]\n"
"buffer_load_dwordx4 a[240:243], v121, s[64:67], s76 offen sc1\n"
"v_mfma_f32_16x16x32_bf16 a[116:119], v[80:83], v[24:27], a[116:119]\n"
"ds_read_b64_tr_b16 v[98:99], v113 offset:23296\n"
"v_mfma_f32_16x16x32_bf16 a[120:123], v[80:83], v[32:35], a[120:123]\n"
"v_mfma_f32_16x16x32_bf16 a[120:123], v[84:87], v[36:39], a[120:123]\n"
"ds_read_b64_tr_b16 v[100:101], v113 offset:27904\n"
"v_mfma_f32_16x16x32_bf16 a[124:127], v[84:87], v[44:47], a[124:127]\n"
"s_add_u32 s76, s90, 0xa400\n"
"v_mfma_f32_16x16x32_bf16 a[124:127], v[80:83], v[40:43], a[124:127]\n"
"ds_read_b64_tr_b16 v[102:103], v113 offset:32512\n"
"buffer_load_dwordx4 a[244:247], v121, s[64:67], s76 offen sc1\n"
"s_waitcnt lgkmcnt(6)\n"
"v_mfma_f32_16x16x32_bf16 a[144:147], v[88:91], v[16:19], a[144:147]\n"
"s_waitcnt lgkmcnt(4)\n"
"v_mfma_f32_16x16x32_bf16 a[144:147], v[92:95], v[20:23], a[144:147]\n"
"v_mfma_f32_16x16x32_bf16 a[148:151], v[92:95], v[28:31], a[148:151]\n"
"v_mfma_f32_16x16x32_bf16 a[148:151], v[88:91], v[24:27], a[148:151]\n"
"s_add_u32 s76, s90, 0xe800\n"
"v_mfma_f32_16x16x32_bf16 a[152:155], v[88:91], v[32:35], a[152:155]\n"
"buffer_load_dwordx4 a[248:251], v121, s[64:67], s76 offen sc1\n"
"v_mfma_f32_16x16x32_bf16 a[152:155], v[92:95], v[36:39], a[152:155]\n"
"v_mfma_f32_16x16x32_bf16 a[156:159], v[92:95], v[44:47], a[156:159]\n"
"v_mfma_f32_16x16x32_bf16 a[156:159], v[88:91], v[40:43], a[156:159]\n"
"s_waitcnt lgkmcnt(2)\n"
"v_mfma_f32_16x16x32_bf16 a[128:131], v[96:99], v[16:19], a[128:131]\n"
"s_add_u32 s76, s90, 0xec00\n"
"s_waitcnt lgkmcnt(0)\n"
"v_mfma_f32_16x16x32_bf16 a[128:131], v[100:103], v[20:23], a[128:131]\n"
"buffer_load_dwordx4 a[252:255], v121, s[64:67], s76 offen sc1\n"
"v_mfma_f32_16x16x32_bf16 a[132:135], v[100:103], v[28:31], a[132:135]\n"
"v_mfma_f32_16x16x32_bf16 a[132:135], v[96:99], v[24:27], a[132:135]\n"
"v_mfma_f32_16x16x32_bf16 a[136:139], v[96:99], v[32:35], a[136:139]\n"
"v_mfma_f32_16x16x32_bf16 a[136:139], v[100:103], v[36:39], a[136:139]\n"
"v_mfma_f32_16x16x32_bf16 a[140:143], v[100:103], v[44:47], a[140:143]\n"
"v_mfma_f32_16x16x32_bf16 a[140:143], v[96:99], v[40:43], a[140:143]\n"
"buffer_load_dword v11, v14, s[52:55], 0 offen\n"
"s_waitcnt lgkmcnt(0)\n"
"s_barrier\n"
"s_lshl_b32 s78, s72, 12\n"
"s_mov_b32 s79, 0\n"
"v_mov_b32 v104, 0xbfb8aa3b\n"
"v_mov_b32 v106, 1.0\n"
"v_mov_b32 v105, 0xbfb8aa3b\n"
"v_mov_b32 v107, 1.0\n"
"s_mul_i32 s99, s91, 0x24000\n"
"s_mul_i32 s76, s74, 9\n"
"s_lshr_b32 s76, s76, 1\n"
"s_lshl_b32 s76, s76, 10\n"
"s_add_u32 s99, s99, s76\n"
"s_bitcmp1_b32 s74, 0\n"
"s_cbranch_scc1 HODD_%=\n"
"s_add_u32 s98, s99, 0\n"
"v_pk_mul_f32 v[24:25], v[128:129], v[104:105]\n"
"v_pk_mul_f32 v[36:37], v[144:145], v[104:105]\n"
"v_pk_mul_f32 v[26:27], v[130:131], v[104:105]\n"
"v_pk_mul_f32 v[38:39], v[146:147], v[104:105]\n"
"v_accvgpr_read_b32 v20, a0\n"
"v_accvgpr_read_b32 v32, a16\n"
"v_accvgpr_read_b32 v21, a1\n"
"v_accvgpr_read_b32 v33, a17\n"
"v_accvgpr_read_b32 v22, a2\n"
"v_accvgpr_read_b32 v34, a18\n"
"v_accvgpr_read_b32 v23, a3\n"
"v_accvgpr_read_b32 v35, a19\n"
"v_exp_f32 v24, v24\n"
"v_exp_f32 v36, v36\n"
"v_exp_f32 v25, v25\n"
"v_exp_f32 v37, v37\n"
"v_exp_f32 v26, v26\n"
"v_exp_f32 v38, v38\n"
"v_exp_f32 v27, v27\n"
"v_exp_f32 v39, v39\n"
"v_pk_mul_f32 v[20:21], v[20:21], v[128:129]\n"
"v_pk_mul_f32 v[32:33], v[32:33], v[144:145]\n"
"v_pk_mul_f32 v[22:23], v[22:23], v[130:131]\n"
"v_pk_mul_f32 v[34:35], v[34:35], v[146:147]\n"
"v_pk_add_f32 v[24:25], v[24:25], v[106:107]\n"
"v_pk_add_f32 v[36:37], v[36:37], v[106:107]\n"
"v_pk_add_f32 v[26:27], v[26:27], v[106:107]\n"
"v_pk_add_f32 v[38:39], v[38:39], v[106:107]\n"
"v_rcp_f32 v24, v24\n"
"v_rcp_f32 v36, v36\n"
"v_rcp_f32 v25, v25\n"
"v_rcp_f32 v37, v37\n"
"v_rcp_f32 v26, v26\n"
"v_rcp_f32 v38, v38\n"
"v_rcp_f32 v27, v27\n"
"v_rcp_f32 v39, v39\n"
"v_pk_mul_f32 v[20:21], v[20:21], v[24:25]\n"
"v_pk_mul_f32 v[32:33], v[32:33], v[36:37]\n"
"v_pk_mul_f32 v[22:23], v[22:23], v[26:27]\n"
"v_pk_mul_f32 v[34:35], v[34:35], v[38:39]\n"
"v_cvt_pk_bf16_f32 v76, v20, v21\n"
"v_cvt_pk_bf16_f32 v78, v32, v33\n"
"v_cvt_pk_bf16_f32 v77, v22, v23\n"
"v_cvt_pk_bf16_f32 v79, v34, v35\n"
"buffer_store_dwordx4 v[76:79], v121, s[68:71], s98 offen sc1\n"
"s_add_u32 m0, s84, 0x20000\n"
"s_add_u32 s77, s85, 0x80000\n"
"buffer_load_dwordx4 v118, s[56:59], s77 offen lds\n"
"v_pk_mul_f32 v[48:49], v[160:161], v[104:105]\n"
"v_pk_mul_f32 v[60:61], v[176:177], v[104:105]\n"
"v_pk_mul_f32 v[50:51], v[162:163], v[104:105]\n"
"v_pk_mul_f32 v[62:63], v[178:179], v[104:105]\n"
"v_accvgpr_read_b32 v44, a32\n"
"v_accvgpr_read_b32 v56, a48\n"
"v_accvgpr_read_b32 v45, a33\n"
"v_accvgpr_read_b32 v57, a49\n"
"v_accvgpr_read_b32 v46, a34\n"
"v_accvgpr_read_b32 v58, a50\n"
"v_accvgpr_read_b32 v47, a35\n"
"v_accvgpr_read_b32 v59, a51\n"
"v_exp_f32 v48, v48\n"
"v_exp_f32 v60, v60\n"
"v_exp_f32 v49, v49\n"
"v_exp_f32 v61, v61\n"
"v_exp_f32 v50, v50\n"
"v_exp_f32 v62, v62\n"
"v_exp_f32 v51, v51\n"
"v_exp_f32 v63, v63\n"
"v_pk_mul_f32 v[44:45], v[44:45], v[160:161]\n"
"v_pk_mul_f32 v[56:57], v[56:57], v[176:177]\n"
"v_pk_mul_f32 v[46:47], v[46:47], v[162:163]\n"
"v_pk_mul_f32 v[58:59], v[58:59], v[178:179]\n"
"v_pk_add_f32 v[48:49], v[48:49], v[106:107]\n"
"v_pk_add_f32 v[60:61], v[60:61], v[106:107]\n"
"v_pk_add_f32 v[50:51], v[50:51], v[106:107]\n"
"v_pk_add_f32 v[62:63], v[62:63], v[106:107]\n"
"v_rcp_f32 v48, v48\n"
"v_rcp_f32 v60, v60\n"
"v_rcp_f32 v49, v49\n"
"v_rcp_f32 v61, v61\n"
"v_rcp_f32 v50, v50\n"
"v_rcp_f32 v62, v62\n"
"v_rcp_f32 v51, v51\n"
"v_rcp_f32 v63, v63\n"
"v_pk_mul_f32 v[44:45], v[44:45], v[48:49]\n"
"v_pk_mul_f32 v[56:57], v[56:57], v[60:61]\n"
"v_pk_mul_f32 v[46:47], v[46:47], v[50:51]\n"
"v_pk_mul_f32 v[58:59], v[58:59], v[62:63]\n"
"v_cvt_pk_bf16_f32 v80, v44, v45\n"
"v_cvt_pk_bf16_f32 v82, v56, v57\n"
"v_cvt_pk_bf16_f32 v81, v46, v47\n"
"v_cvt_pk_bf16_f32 v83, v58, v59\n"
"buffer_store_dwordx4 v[80:83], v121, s[68:71], s98 offen offset:1024 sc1\n"
"s_add_u32 m0, s84, 0x20400\n"
"s_add_u32 s77, s85, 0x80080\n"
"buffer_load_dwordx4 v118, s[56:59], s77 offen lds\n"
"v_pk_mul_f32 v[72:73], v[192:193], v[104:105]\n"
"v_pk_mul_f32 v[24:25], v[208:209], v[104:105]\n"
"v_pk_mul_f32 v[74:75], v[194:195], v[104:105]\n"
"v_pk_mul_f32 v[26:27], v[210:211], v[104:105]\n"
"v_accvgpr_read_b32 v68, a64\n"
"v_accvgpr_read_b32 v20, a80\n"
"v_accvgpr_read_b32 v69, a65\n"
"v_accvgpr_read_b32 v21, a81\n"
"v_accvgpr_read_b32 v70, a66\n"
"v_accvgpr_read_b32 v22, a82\n"
"v_accvgpr_read_b32 v71, a67\n"
"v_accvgpr_read_b32 v23, a83\n"
"v_exp_f32 v72, v72\n"
"v_exp_f32 v24, v24\n"
"v_exp_f32 v73, v73\n"
"v_exp_f32 v25, v25\n"
"v_exp_f32 v74, v74\n"
"v_exp_f32 v26, v26\n"
"v_exp_f32 v75, v75\n"
"v_exp_f32 v27, v27\n"
"v_pk_mul_f32 v[68:69], v[68:69], v[192:193]\n"
"v_pk_mul_f32 v[20:21], v[20:21], v[208:209]\n"
"v_pk_mul_f32 v[70:71], v[70:71], v[194:195]\n"
"v_pk_mul_f32 v[22:23], v[22:23], v[210:211]\n"
"v_pk_add_f32 v[72:73], v[72:73], v[106:107]\n"
"v_pk_add_f32 v[24:25], v[24:25], v[106:107]\n"
"v_pk_add_f32 v[74:75], v[74:75], v[106:107]\n"
"v_pk_add_f32 v[26:27], v[26:27], v[106:107]\n"
"v_rcp_f32 v72, v72\n"
"v_rcp_f32 v24, v24\n"
"v_rcp_f32 v73, v73\n"
"v_rcp_f32 v25, v25\n"
"v_rcp_f32 v74, v74\n"
"v_rcp_f32 v26, v26\n"
"v_rcp_f32 v75, v75\n"
"v_rcp_f32 v27, v27\n"
"v_pk_mul_f32 v[68:69], v[68:69], v[72:73]\n"
"v_pk_mul_f32 v[20:21], v[20:21], v[24:25]\n"
"v_pk_mul_f32 v[70:71], v[70:71], v[74:75]\n"
"v_pk_mul_f32 v[22:23], v[22:23], v[26:27]\n"
"v_cvt_pk_bf16_f32 v84, v68, v69\n"
"v_cvt_pk_bf16_f32 v86, v20, v21\n"
"v_cvt_pk_bf16_f32 v85, v70, v71\n"
"v_cvt_pk_bf16_f32 v87, v22, v23\n"
"buffer_store_dwordx4 v[84:87], v121, s[68:71], s98 offen offset:2048 sc1\n"
"s_add_u32 m0, s84, 0x20800\n"
"s_add_u32 s77, s85, 0x90000\n"
"buffer_load_dwordx4 v118, s[56:59], s77 offen lds\n"
"v_pk_mul_f32 v[36:37], v[224:225], v[104:105]\n"
"v_pk_mul_f32 v[48:49], v[240:241], v[104:105]\n"
"v_pk_mul_f32 v[38:39], v[226:227], v[104:105]\n"
"v_pk_mul_f32 v[50:51], v[242:243], v[104:105]\n"
"v_accvgpr_read_b32 v32, a96\n"
"v_accvgpr_read_b32 v44, a112\n"
"v_accvgpr_read_b32 v33, a97\n"
"v_accvgpr_read_b32 v45, a113\n"
"v_accvgpr_read_b32 v34, a98\n"
"v_accvgpr_read_b32 v46, a114\n"
"v_accvgpr_read_b32 v35, a99\n"
"v_accvgpr_read_b32 v47, a115\n"
"v_exp_f32 v36, v36\n"
"v_exp_f32 v48, v48\n"
"v_exp_f32 v37, v37\n"
"v_exp_f32 v49, v49\n"
"v_exp_f32 v38, v38\n"
"v_exp_f32 v50, v50\n"
"v_exp_f32 v39, v39\n"
"v_exp_f32 v51, v51\n"
"v_pk_mul_f32 v[32:33], v[32:33], v[224:225]\n"
"v_pk_mul_f32 v[44:45], v[44:45], v[240:241]\n"
"v_pk_mul_f32 v[34:35], v[34:35], v[226:227]\n"
"v_pk_mul_f32 v[46:47], v[46:47], v[242:243]\n"
"v_pk_add_f32 v[36:37], v[36:37], v[106:107]\n"
"v_pk_add_f32 v[48:49], v[48:49], v[106:107]\n"
"v_pk_add_f32 v[38:39], v[38:39], v[106:107]\n"
"v_pk_add_f32 v[50:51], v[50:51], v[106:107]\n"
"v_rcp_f32 v36, v36\n"
"v_rcp_f32 v48, v48\n"
"v_rcp_f32 v37, v37\n"
"v_rcp_f32 v49, v49\n"
"v_rcp_f32 v38, v38\n"
"v_rcp_f32 v50, v50\n"
"v_rcp_f32 v39, v39\n"
"v_rcp_f32 v51, v51\n"
"v_pk_mul_f32 v[32:33], v[32:33], v[36:37]\n"
"v_pk_mul_f32 v[44:45], v[44:45], v[48:49]\n"
"v_pk_mul_f32 v[34:35], v[34:35], v[38:39]\n"
"v_pk_mul_f32 v[46:47], v[46:47], v[50:51]\n"
"v_cvt_pk_bf16_f32 v88, v32, v33\n"
"v_cvt_pk_bf16_f32 v90, v44, v45\n"
"v_cvt_pk_bf16_f32 v89, v34, v35\n"
"v_cvt_pk_bf16_f32 v91, v46, v47\n"
"buffer_store_dwordx4 v[88:91], v121, s[68:71], s98 offen offset:3072 sc1\n"
"s_add_u32 m0, s84, 0x20c00\n"
"s_add_u32 s77, s85, 0x90080\n"
"buffer_load_dwordx4 v118, s[56:59], s77 offen lds\n"
"v_accvgpr_read_b32 v52, a144\n"
"v_accvgpr_read_b32 v53, a145\n"
"v_accvgpr_read_b32 v54, a146\n"
"v_accvgpr_read_b32 v55, a147\n"
"v_pk_mul_f32 v[60:61], v[52:53], v[104:105]\n"
"v_pk_mul_f32 v[62:63], v[54:55], v[104:105]\n"
"v_accvgpr_read_b32 v56, a128\n"
"v_accvgpr_read_b32 v57, a129\n"
"v_accvgpr_read_b32 v58, a130\n"
"v_accvgpr_read_b32 v59, a131\n"
"v_exp_f32 v60, v60\n"
"v_exp_f32 v61, v61\n"
"v_exp_f32 v62, v62\n"
"v_exp_f32 v63, v63\n"
"v_pk_mul_f32 v[56:57], v[56:57], v[52:53]\n"
"v_pk_mul_f32 v[58:59], v[58:59], v[54:55]\n"
"v_pk_add_f32 v[60:61], v[60:61], v[106:107]\n"
"v_pk_add_f32 v[62:63], v[62:63], v[106:107]\n"
"v_rcp_f32 v60, v60\n"
"v_rcp_f32 v61, v61\n"
"v_rcp_f32 v62, v62\n"
"v_rcp_f32 v63, v63\n"
"v_pk_mul_f32 v[56:57], v[56:57], v[60:61]\n"
"v_pk_mul_f32 v[58:59], v[58:59], v[62:63]\n"
"v_cvt_pk_bf16_f32 v92, v56, v57\n"
"v_cvt_pk_bf16_f32 v93, v58, v59\n"
"s_add_u32 s98, s98, 0x1000\n"
"buffer_store_dwordx2 v[92:93], v121, s[68:71], s98 offen sc1\n"
"s_add_u32 s98, s99, 0x24000\n"
"v_pk_mul_f32 v[72:73], v[132:133], v[104:105]\n"
"v_pk_mul_f32 v[24:25], v[148:149], v[104:105]\n"
"v_pk_mul_f32 v[74:75], v[134:135], v[104:105]\n"
"v_pk_mul_f32 v[26:27], v[150:151], v[104:105]\n"
"v_accvgpr_read_b32 v68, a4\n"
"v_accvgpr_read_b32 v20, a20\n"
"v_accvgpr_read_b32 v69, a5\n"
"v_accvgpr_read_b32 v21, a21\n"
"v_accvgpr_read_b32 v70, a6\n"
"v_accvgpr_read_b32 v22, a22\n"
"v_accvgpr_read_b32 v71, a7\n"
"v_accvgpr_read_b32 v23, a23\n"
"v_exp_f32 v72, v72\n"
"v_exp_f32 v24, v24\n"
"v_exp_f32 v73, v73\n"
"v_exp_f32 v25, v25\n"
"v_exp_f32 v74, v74\n"
"v_exp_f32 v26, v26\n"
"v_exp_f32 v75, v75\n"
"v_exp_f32 v27, v27\n"
"v_pk_mul_f32 v[68:69], v[68:69], v[132:133]\n"
"v_pk_mul_f32 v[20:21], v[20:21], v[148:149]\n"
"v_pk_mul_f32 v[70:71], v[70:71], v[134:135]\n"
"v_pk_mul_f32 v[22:23], v[22:23], v[150:151]\n"
"v_pk_add_f32 v[72:73], v[72:73], v[106:107]\n"
"v_pk_add_f32 v[24:25], v[24:25], v[106:107]\n"
"v_pk_add_f32 v[74:75], v[74:75], v[106:107]\n"
"v_pk_add_f32 v[26:27], v[26:27], v[106:107]\n"
"v_rcp_f32 v72, v72\n"
"v_rcp_f32 v24, v24\n"
"v_rcp_f32 v73, v73\n"
"v_rcp_f32 v25, v25\n"
"v_rcp_f32 v74, v74\n"
"v_rcp_f32 v26, v26\n"
"v_rcp_f32 v75, v75\n"
"v_rcp_f32 v27, v27\n"
"v_pk_mul_f32 v[68:69], v[68:69], v[72:73]\n"
"v_pk_mul_f32 v[20:21], v[20:21], v[24:25]\n"
"v_pk_mul_f32 v[70:71], v[70:71], v[74:75]\n"
"v_pk_mul_f32 v[22:23], v[22:23], v[26:27]\n"
"v_cvt_pk_bf16_f32 v96, v68, v69\n"
"v_cvt_pk_bf16_f32 v98, v20, v21\n"
"v_cvt_pk_bf16_f32 v97, v70, v71\n"
"v_cvt_pk_bf16_f32 v99, v22, v23\n"
"buffer_store_dwordx4 v[96:99], v121, s[68:71], s98 offen sc1\n"
"v_pk_mul_f32 v[36:37], v[164:165], v[104:105]\n"
"v_pk_mul_f32 v[48:49], v[180:181], v[104:105]\n"
"v_pk_mul_f32 v[38:39], v[166:167], v[104:105]\n"
"v_pk_mul_f32 v[50:51], v[182:183], v[104:105]\n"
"v_accvgpr_read_b32 v32, a36\n"
"v_accvgpr_read_b32 v44, a52\n"
"v_accvgpr_read_b32 v33, a37\n"
"v_accvgpr_read_b32 v45, a53\n"
"v_accvgpr_read_b32 v34, a38\n"
"v_accvgpr_read_b32 v46, a54\n"
"v_accvgpr_read_b32 v35, a39\n"
"v_accvgpr_read_b32 v47, a55\n"
"v_exp_f32 v36, v36\n"
"v_exp_f32 v48, v48\n"
"v_exp_f32 v37, v37\n"
"v_exp_f32 v49, v49\n"
"v_exp_f32 v38, v38\n"
"v_exp_f32 v50, v50\n"
"v_exp_f32 v39, v39\n"
"v_exp_f32 v51, v51\n"
"v_pk_mul_f32 v[32:33], v[32:33], v[164:165]\n"
"v_pk_mul_f32 v[44:45], v[44:45], v[180:181]\n"
"v_pk_mul_f32 v[34:35], v[34:35], v[166:167]\n"
"v_pk_mul_f32 v[46:47], v[46:47], v[182:183]\n"
"v_pk_add_f32 v[36:37], v[36:37], v[106:107]\n"
"v_pk_add_f32 v[48:49], v[48:49], v[106:107]\n"
"v_pk_add_f32 v[38:39], v[38:39], v[106:107]\n"
"v_pk_add_f32 v[50:51], v[50:51], v[106:107]\n"
"v_rcp_f32 v36, v36\n"
"v_rcp_f32 v48, v48\n"
"v_rcp_f32 v37, v37\n"
"v_rcp_f32 v49, v49\n"
"v_rcp_f32 v38, v38\n"
"v_rcp_f32 v50, v50\n"
"v_rcp_f32 v39, v39\n"
"v_rcp_f32 v51, v51\n"
"v_pk_mul_f32 v[32:33], v[32:33], v[36:37]\n"
"v_pk_mul_f32 v[44:45], v[44:45], v[48:49]\n"
"v_pk_mul_f32 v[34:35], v[34:35], v[38:39]\n"
"v_pk_mul_f32 v[46:47], v[46:47], v[50:51]\n"
"v_cvt_pk_bf16_f32 v100, v32, v33\n"
"v_cvt_pk_bf16_f32 v102, v44, v45\n"
"v_cvt_pk_bf16_f32 v101, v34, v35\n"
"v_cvt_pk_bf16_f32 v103, v46, v47\n"
"buffer_store_dwordx4 v[100:103], v121, s[68:71], s98 offen offset:1024 sc1\n"
"v_pk_mul_f32 v[60:61], v[196:197], v[104:105]\n"
"v_pk_mul_f32 v[72:73], v[212:213], v[104:105]\n"
"v_pk_mul_f32 v[62:63], v[198:199], v[104:105]\n"
"v_pk_mul_f32 v[74:75], v[214:215], v[104:105]\n"
"v_accvgpr_read_b32 v56, a68\n"
"v_accvgpr_read_b32 v68, a84\n"
"v_accvgpr_read_b32 v57, a69\n"
"v_accvgpr_read_b32 v69, a85\n"
"v_accvgpr_read_b32 v58, a70\n"
"v_accvgpr_read_b32 v70, a86\n"
"v_accvgpr_read_b32 v59, a71\n"
"v_accvgpr_read_b32 v71, a87\n"
"v_exp_f32 v60, v60\n"
"v_exp_f32 v72, v72\n"
"v_exp_f32 v61, v61\n"
"v_exp_f32 v73, v73\n"
"v_exp_f32 v62, v62\n"
"v_exp_f32 v74, v74\n"
"v_exp_f32 v63, v63\n"
"v_exp_f32 v75, v75\n"
"v_pk_mul_f32 v[56:57], v[56:57], v[196:197]\n"
"v_pk_mul_f32 v[68:69], v[68:69], v[212:213]\n"
"v_pk_mul_f32 v[58:59], v[58:59], v[198:199]\n"
"v_pk_mul_f32 v[70:71], v[70:71], v[214:215]\n"
"v_pk_add_f32 v[60:61], v[60:61], v[106:107]\n"
"v_pk_add_f32 v[72:73], v[72:73], v[106:107]\n"
"v_pk_add_f32 v[62:63], v[62:63], v[106:107]\n"
"v_pk_add_f32 v[74:75], v[74:75], v[106:107]\n"
"v_rcp_f32 v60, v60\n"
"v_rcp_f32 v72, v72\n"
"v_rcp_f32 v61, v61\n"
"v_rcp_f32 v73, v73\n"
"v_rcp_f32 v62, v62\n"
"v_rcp_f32 v74, v74\n"
"v_rcp_f32 v63, v63\n"
"v_rcp_f32 v75, v75\n"
"v_pk_mul_f32 v[56:57], v[56:57], v[60:61]\n"
"v_pk_mul_f32 v[68:69], v[68:69], v[72:73]\n"
"v_pk_mul_f32 v[58:59], v[58:59], v[62:63]\n"
"v_pk_mul_f32 v[70:71], v[70:71], v[74:75]\n"
"v_cvt_pk_bf16_f32 v76, v56, v57\n"
"v_cvt_pk_bf16_f32 v78, v68, v69\n"
"v_cvt_pk_bf16_f32 v77, v58, v59\n"
"v_cvt_pk_bf16_f32 v79, v70, v71\n"
"buffer_store_dwordx4 v[76:79], v121, s[68:71], s98 offen offset:2048 sc1\n"
"v_pk_mul_f32 v[24:25], v[228:229], v[104:105]\n"
"v_pk_mul_f32 v[36:37], v[244:245], v[104:105]\n"
"v_pk_mul_f32 v[26:27], v[230:231], v[104:105]\n"
"v_pk_mul_f32 v[38:39], v[246:247], v[104:105]\n"
"v_accvgpr_read_b32 v20, a100\n"
"v_accvgpr_read_b32 v32, a116\n"
"v_accvgpr_read_b32 v21, a101\n"
"v_accvgpr_read_b32 v33, a117\n"
"v_accvgpr_read_b32 v22, a102\n"
"v_accvgpr_read_b32 v34, a118\n"
"v_accvgpr_read_b32 v23, a103\n"
"v_accvgpr_read_b32 v35, a119\n"
"v_exp_f32 v24, v24\n"
"v_exp_f32 v36, v36\n"
"v_exp_f32 v25, v25\n"
"v_exp_f32 v37, v37\n"
"v_exp_f32 v26, v26\n"
"v_exp_f32 v38, v38\n"
"v_exp_f32 v27, v27\n"
"v_exp_f32 v39, v39\n"
"v_pk_mul_f32 v[20:21], v[20:21], v[228:229]\n"
"v_pk_mul_f32 v[32:33], v[32:33], v[244:245]\n"
"v_pk_mul_f32 v[22:23], v[22:23], v[230:231]\n"
"v_pk_mul_f32 v[34:35], v[34:35], v[246:247]\n"
"v_pk_add_f32 v[24:25], v[24:25], v[106:107]\n"
"v_pk_add_f32 v[36:37], v[36:37], v[106:107]\n"
"v_pk_add_f32 v[26:27], v[26:27], v[106:107]\n"
"v_pk_add_f32 v[38:39], v[38:39], v[106:107]\n"
"v_rcp_f32 v24, v24\n"
"v_rcp_f32 v36, v36\n"
"v_rcp_f32 v25, v25\n"
"v_rcp_f32 v37, v37\n"
"v_rcp_f32 v26, v26\n"
"v_rcp_f32 v38, v38\n"
"v_rcp_f32 v27, v27\n"
"v_rcp_f32 v39, v39\n"
"v_pk_mul_f32 v[20:21], v[20:21], v[24:25]\n"
"v_pk_mul_f32 v[32:33], v[32:33], v[36:37]\n"
"v_pk_mul_f32 v[22:23], v[22:23], v[26:27]\n"
"v_pk_mul_f32 v[34:35], v[34:35], v[38:39]\n"
"v_cvt_pk_bf16_f32 v80, v20, v21\n"
"v_cvt_pk_bf16_f32 v82, v32, v33\n"
"v_cvt_pk_bf16_f32 v81, v22, v23\n"
"v_cvt_pk_bf16_f32 v83, v34, v35\n"
"buffer_store_dwordx4 v[80:83], v121, s[68:71], s98 offen offset:3072 sc1\n"
"v_accvgpr_read_b32 v40, a148\n"
"v_accvgpr_read_b32 v41, a149\n"
"v_accvgpr_read_b32 v42, a150\n"
"v_accvgpr_read_b32 v43, a151\n"
"v_pk_mul_f32 v[48:49], v[40:41], v[104:105]\n"
"v_pk_mul_f32 v[50:51], v[42:43], v[104:105]\n"
"v_accvgpr_read_b32 v44, a132\n"
"v_accvgpr_read_b32 v45, a133\n"
"v_accvgpr_read_b32 v46, a134\n"
"v_accvgpr_read_b32 v47, a135\n"
"v_exp_f32 v48, v48\n"
"v_exp_f32 v49, v49\n"
"v_exp_f32 v50, v50\n"
"v_exp_f32 v51, v51\n"
"v_pk_mul_f32 v[44:45], v[44:45], v[40:41]\n"
"v_pk_mul_f32 v[46:47], v[46:47], v[42:43]\n"
"v_pk_add_f32 v[48:49], v[48:49], v[106:107]\n"
"v_pk_add_f32 v[50:51], v[50:51], v[106:107]\n"
"v_rcp_f32 v48, v48\n"
"v_rcp_f32 v49, v49\n"
"v_rcp_f32 v50, v50\n"
"v_rcp_f32 v51, v51\n"
"v_pk_mul_f32 v[44:45], v[44:45], v[48:49]\n"
"v_pk_mul_f32 v[46:47], v[46:47], v[50:51]\n"
"v_cvt_pk_bf16_f32 v84, v44, v45\n"
"v_cvt_pk_bf16_f32 v85, v46, v47\n"
"s_add_u32 s98, s98, 0x1000\n"
"buffer_store_dwordx2 v[84:85], v121, s[68:71], s98 offen sc1\n"
"s_add_u32 s98, s99, 0x48000\n"
"v_pk_mul_f32 v[60:61], v[136:137], v[104:105]\n"
"v_pk_mul_f32 v[72:73], v[152:153], v[104:105]\n"
"v_pk_mul_f32 v[62:63], v[138:139], v[104:105]\n"
"v_pk_mul_f32 v[74:75], v[154:155], v[104:105]\n"
"v_accvgpr_read_b32 v56, a8\n"
"v_accvgpr_read_b32 v68, a24\n"
"v_accvgpr_read_b32 v57, a9\n"
"v_accvgpr_read_b32 v69, a25\n"
"v_accvgpr_read_b32 v58, a10\n"
"v_accvgpr_read_b32 v70, a26\n"
"v_accvgpr_read_b32 v59, a11\n"
"v_accvgpr_read_b32 v71, a27\n"
"v_exp_f32 v60, v60\n"
"v_exp_f32 v72, v72\n"
"v_exp_f32 v61, v61\n"
"v_exp_f32 v73, v73\n"
"v_exp_f32 v62, v62\n"
"v_exp_f32 v74, v74\n"
"v_exp_f32 v63, v63\n"
"v_exp_f32 v75, v75\n"
"v_pk_mul_f32 v[56:57], v[56:57], v[136:137]\n"
"v_pk_mul_f32 v[68:69], v[68:69], v[152:153]\n"
"v_pk_mul_f32 v[58:59], v[58:59], v[138:139]\n"
"v_pk_mul_f32 v[70:71], v[70:71], v[154:155]\n"
"v_pk_add_f32 v[60:61], v[60:61], v[106:107]\n"
"v_pk_add_f32 v[72:73], v[72:73], v[106:107]\n"
"v_pk_add_f32 v[62:63], v[62:63], v[106:107]\n"
"v_pk_add_f32 v[74:75], v[74:75], v[106:107]\n"
"v_rcp_f32 v60, v60\n"
"v_rcp_f32 v72, v72\n"
"v_rcp_f32 v61, v61\n"
"v_rcp_f32 v73, v73\n"
"v_rcp_f32 v62, v62\n"
"v_rcp_f32 v74, v74\n"
"v_rcp_f32 v63, v63\n"
"v_rcp_f32 v75, v75\n"
"v_pk_mul_f32 v[56:57], v[56:57], v[60:61]\n"
"v_pk_mul_f32 v[68:69], v[68:69], v[72:73]\n"
"v_pk_mul_f32 v[58:59], v[58:59], v[62:63]\n"
"v_pk_mul_f32 v[70:71], v[70:71], v[74:75]\n"
"v_cvt_pk_bf16_f32 v88, v56, v57\n"
"v_cvt_pk_bf16_f32 v90, v68, v69\n"
"v_cvt_pk_bf16_f32 v89, v58, v59\n"
"v_cvt_pk_bf16_f32 v91, v70, v71\n"
"buffer_store_dwordx4 v[88:91], v121, s[68:71], s98 offen sc1\n"
"v_pk_mul_f32 v[24:25], v[168:169], v[104:105]\n"
"v_pk_mul_f32 v[36:37], v[184:185], v[104:105]\n"
"v_pk_mul_f32 v[26:27], v[170:171], v[104:105]\n"
"v_pk_mul_f32 v[38:39], v[186:187], v[104:105]\n"
"v_accvgpr_read_b32 v20, a40\n"
"v_accvgpr_read_b32 v32, a56\n"
"v_accvgpr_read_b32 v21, a41\n"
"v_accvgpr_read_b32 v33, a57\n"
"v_accvgpr_read_b32 v22, a42\n"
"v_accvgpr_read_b32 v34, a58\n"
"v_accvgpr_read_b32 v23, a43\n"
"v_accvgpr_read_b32 v35, a59\n"
"v_exp_f32 v24, v24\n"
"v_exp_f32 v36, v36\n"
"v_exp_f32 v25, v25\n"
"v_exp_f32 v37, v37\n"
"v_exp_f32 v26, v26\n"
"v_exp_f32 v38, v38\n"
"v_exp_f32 v27, v27\n"
"v_exp_f32 v39, v39\n"
"v_pk_mul_f32 v[20:21], v[20:21], v[168:169]\n"
"v_pk_mul_f32 v[32:33], v[32:33], v[184:185]\n"
"v_pk_mul_f32 v[22:23], v[22:23], v[170:171]\n"
"v_pk_mul_f32 v[34:35], v[34:35], v[186:187]\n"
"v_pk_add_f32 v[24:25], v[24:25], v[106:107]\n"
"v_pk_add_f32 v[36:37], v[36:37], v[106:107]\n"
"v_pk_add_f32 v[26:27], v[26:27], v[106:107]\n"
"v_pk_add_f32 v[38:39], v[38:39], v[106:107]\n"
"v_rcp_f32 v24, v24\n"
"v_rcp_f32 v36, v36\n"
"v_rcp_f32 v25, v25\n"
"v_rcp_f32 v37, v37\n"
"v_rcp_f32 v26, v26\n"
"v_rcp_f32 v38, v38\n"
"v_rcp_f32 v27, v27\n"
"v_rcp_f32 v39, v39\n"
"v_pk_mul_f32 v[20:21], v[20:21], v[24:25]\n"
"v_pk_mul_f32 v[32:33], v[32:33], v[36:37]\n"
"v_pk_mul_f32 v[22:23], v[22:23], v[26:27]\n"
"v_pk_mul_f32 v[34:35], v[34:35], v[38:39]\n"
"v_cvt_pk_bf16_f32 v92, v20, v21\n"
"v_cvt_pk_bf16_f32 v94, v32, v33\n"
"v_cvt_pk_bf16_f32 v93, v22, v23\n"
"v_cvt_pk_bf16_f32 v95, v34, v35\n"
"buffer_store_dwordx4 v[92:95], v121, s[68:71], s98 offen offset:1024 sc1\n"
"v_pk_mul_f32 v[48:49], v[200:201], v[104:105]\n"
"v_pk_mul_f32 v[60:61], v[216:217], v[104:105]\n"
"v_pk_mul_f32 v[50:51], v[202:203], v[104:105]\n"
"v_pk_mul_f32 v[62:63], v[218:219], v[104:105]\n"
"v_accvgpr_read_b32 v44, a72\n"
"v_accvgpr_read_b32 v56, a88\n"
"v_accvgpr_read_b32 v45, a73\n"
"v_accvgpr_read_b32 v57, a89\n"
"v_accvgpr_read_b32 v46, a74\n"
"v_accvgpr_read_b32 v58, a90\n"
"v_accvgpr_read_b32 v47, a75\n"
"v_accvgpr_read_b32 v59, a91\n"
"v_exp_f32 v48, v48\n"
"v_exp_f32 v60, v60\n"
"v_exp_f32 v49, v49\n"
"v_exp_f32 v61, v61\n"
"v_exp_f32 v50, v50\n"
"v_exp_f32 v62, v62\n"
"v_exp_f32 v51, v51\n"
"v_exp_f32 v63, v63\n"
"v_pk_mul_f32 v[44:45], v[44:45], v[200:201]\n"
"v_pk_mul_f32 v[56:57], v[56:57], v[216:217]\n"
"v_pk_mul_f32 v[46:47], v[46:47], v[202:203]\n"
"v_pk_mul_f32 v[58:59], v[58:59], v[218:219]\n"
"v_pk_add_f32 v[48:49], v[48:49], v[106:107]\n"
"v_pk_add_f32 v[60:61], v[60:61], v[106:107]\n"
"v_pk_add_f32 v[50:51], v[50:51], v[106:107]\n"
"v_pk_add_f32 v[62:63], v[62:63], v[106:107]\n"
"v_rcp_f32 v48, v48\n"
"v_rcp_f32 v60, v60\n"
"v_rcp_f32 v49, v49\n"
"v_rcp_f32 v61, v61\n"
"v_rcp_f32 v50, v50\n"
"v_rcp_f32 v62, v62\n"
"v_rcp_f32 v51, v51\n"
"v_rcp_f32 v63, v63\n"
"v_pk_mul_f32 v[44:45], v[44:45], v[48:49]\n"
"v_pk_mul_f32 v[56:57], v[56:57], v[60:61]\n"
"v_pk_mul_f32 v[46:47], v[46:47], v[50:51]\n"
"v_pk_mul_f32 v[58:59], v[58:59], v[62:63]\n"
"v_cvt_pk_bf16_f32 v96, v44, v45\n"
"v_cvt_pk_bf16_f32 v98, v56, v57\n"
"v_cvt_pk_bf16_f32 v97, v46, v47\n"
"v_cvt_pk_bf16_f32 v99, v58, v59\n"
"buffer_store_dwordx4 v[96:99], v121, s[68:71], s98 offen offset:2048 sc1\n"
"v_pk_mul_f32 v[72:73], v[232:233], v[104:105]\n"
"v_pk_mul_f32 v[24:25], v[248:249], v[104:105]\n"
"v_pk_mul_f32 v[74:75], v[234:235], v[104:105]\n"
"v_pk_mul_f32 v[26:27], v[250:251], v[104:105]\n"
"v_accvgpr_read_b32 v68, a104\n"
"v_accvgpr_read_b32 v20, a120\n"
"v_accvgpr_read_b32 v69, a105\n"
"v_accvgpr_read_b32 v21, a121\n"
"v_accvgpr_read_b32 v70, a106\n"
"v_accvgpr_read_b32 v22, a122\n"
"v_accvgpr_read_b32 v71, a107\n"
"v_accvgpr_read_b32 v23, a123\n"
"v_exp_f32 v72, v72\n"
"v_exp_f32 v24, v24\n"
"v_exp_f32 v73, v73\n"
"v_exp_f32 v25, v25\n"
"v_exp_f32 v74, v74\n"
"v_exp_f32 v26, v26\n"
"v_exp_f32 v75, v75\n"
"v_exp_f32 v27, v27\n"
"v_pk_mul_f32 v[68:69], v[68:69], v[232:233]\n"
"v_pk_mul_f32 v[20:21], v[20:21], v[248:249]\n"
"v_pk_mul_f32 v[70:71], v[70:71], v[234:235]\n"
"v_pk_mul_f32 v[22:23], v[22:23], v[250:251]\n"
"v_pk_add_f32 v[72:73], v[72:73], v[106:107]\n"
"v_pk_add_f32 v[24:25], v[24:25], v[106:107]\n"
"v_pk_add_f32 v[74:75], v[74:75], v[106:107]\n"
"v_pk_add_f32 v[26:27], v[26:27], v[106:107]\n"
"v_rcp_f32 v72, v72\n"
"v_rcp_f32 v24, v24\n"
"v_rcp_f32 v73, v73\n"
"v_rcp_f32 v25, v25\n"
"v_rcp_f32 v74, v74\n"
"v_rcp_f32 v26, v26\n"
"v_rcp_f32 v75, v75\n"
"v_rcp_f32 v27, v27\n"
"v_pk_mul_f32 v[68:69], v[68:69], v[72:73]\n"
"v_pk_mul_f32 v[20:21], v[20:21], v[24:25]\n"
"v_pk_mul_f32 v[70:71], v[70:71], v[74:75]\n"
"v_pk_mul_f32 v[22:23], v[22:23], v[26:27]\n"
"v_cvt_pk_bf16_f32 v100, v68, v69\n"
"v_cvt_pk_bf16_f32 v102, v20, v21\n"
"v_cvt_pk_bf16_f32 v101, v70, v71\n"
"v_cvt_pk_bf16_f32 v103, v22, v23\n"
"buffer_store_dwordx4 v[100:103], v121, s[68:71], s98 offen offset:3072 sc1\n"
"v_accvgpr_read_b32 v28, a152\n"
"v_accvgpr_read_b32 v29, a153\n"
"v_accvgpr_read_b32 v30, a154\n"
"v_accvgpr_read_b32 v31, a155\n"
"v_pk_mul_f32 v[36:37], v[28:29], v[104:105]\n"
"v_pk_mul_f32 v[38:39], v[30:31], v[104:105]\n"
"v_accvgpr_read_b32 v32, a136\n"
"v_accvgpr_read_b32 v33, a137\n"
"v_accvgpr_read_b32 v34, a138\n"
"v_accvgpr_read_b32 v35, a139\n"
"v_exp_f32 v36, v36\n"
"v_exp_f32 v37, v37\n"
"v_exp_f32 v38, v38\n"
"v_exp_f32 v39, v39\n"
"v_pk_mul_f32 v[32:33], v[32:33], v[28:29]\n"
"v_pk_mul_f32 v[34:35], v[34:35], v[30:31]\n"
"v_pk_add_f32 v[36:37], v[36:37], v[106:107]\n"
"v_pk_add_f32 v[38:39], v[38:39], v[106:107]\n"
"v_rcp_f32 v36, v36\n"
"v_rcp_f32 v37, v37\n"
"v_rcp_f32 v38, v38\n"
"v_rcp_f32 v39, v39\n"
"v_pk_mul_f32 v[32:33], v[32:33], v[36:37]\n"
"v_pk_mul_f32 v[34:35], v[34:35], v[38:39]\n"
"v_cvt_pk_bf16_f32 v76, v32, v33\n"
"v_cvt_pk_bf16_f32 v77, v34, v35\n"
"s_add_u32 s98, s98, 0x1000\n"
"buffer_store_dwordx2 v[76:77], v121, s[68:71], s98 offen sc1\n"
"s_add_u32 s98, s99, 0x6c000\n"
"v_pk_mul_f32 v[48:49], v[140:141], v[104:105]\n"
"v_pk_mul_f32 v[60:61], v[156:157], v[104:105]\n"
"v_pk_mul_f32 v[50:51], v[142:143], v[104:105]\n"
"v_pk_mul_f32 v[62:63], v[158:159], v[104:105]\n"
"v_accvgpr_read_b32 v44, a12\n"
"v_accvgpr_read_b32 v56, a28\n"
"v_accvgpr_read_b32 v45, a13\n"
"v_accvgpr_read_b32 v57, a29\n"
"v_accvgpr_read_b32 v46, a14\n"
"v_accvgpr_read_b32 v58, a30\n"
"v_accvgpr_read_b32 v47, a15\n"
"v_accvgpr_read_b32 v59, a31\n"
"v_exp_f32 v48, v48\n"
"v_exp_f32 v60, v60\n"
"v_exp_f32 v49, v49\n"
"v_exp_f32 v61, v61\n"
"v_exp_f32 v50, v50\n"
"v_exp_f32 v62, v62\n"
"v_exp_f32 v51, v51\n"
"v_exp_f32 v63, v63\n"
"v_pk_mul_f32 v[44:45], v[44:45], v[140:141]\n"
"v_pk_mul_f32 v[56:57], v[56:57], v[156:157]\n"
"v_pk_mul_f32 v[46:47], v[46:47], v[142:143]\n"
"v_pk_mul_f32 v[58:59], v[58:59], v[158:159]\n"
"v_pk_add_f32 v[48:49], v[48:49], v[106:107]\n"
"v_pk_add_f32 v[60:61], v[60:61], v[106:107]\n"
"v_pk_add_f32 v[50:51], v[50:51], v[106:107]\n"
"v_pk_add_f32 v[62:63], v[62:63], v[106:107]\n"
"v_rcp_f32 v48, v48\n"
"v_rcp_f32 v60, v60\n"
"v_rcp_f32 v49, v49\n"
"v_rcp_f32 v61, v61\n"
"v_rcp_f32 v50, v50\n"
"v_rcp_f32 v62, v62\n"
"v_rcp_f32 v51, v51\n"
"v_rcp_f32 v63, v63\n"
"v_pk_mul_f32 v[44:45], v[44:45], v[48:49]\n"
"v_pk_mul_f32 v[56:57], v[56:57], v[60:61]\n"
"v_pk_mul_f32 v[46:47], v[46:47], v[50:51]\n"
"v_pk_mul_f32 v[58:59], v[58:59], v[62:63]\n"
"v_cvt_pk_bf16_f32 v80, v44, v45\n"
"v_cvt_pk_bf16_f32 v82, v56, v57\n"
"v_cvt_pk_bf16_f32 v81, v46, v47\n"
"v_cvt_pk_bf16_f32 v83, v58, v59\n"
"buffer_store_dwordx4 v[80:83], v121, s[68:71], s98 offen sc1\n"
"v_pk_mul_f32 v[72:73], v[172:173], v[104:105]\n"
"v_pk_mul_f32 v[24:25], v[188:189], v[104:105]\n"
"v_pk_mul_f32 v[74:75], v[174:175], v[104:105]\n"
"v_pk_mul_f32 v[26:27], v[190:191], v[104:105]\n"
"v_accvgpr_read_b32 v68, a44\n"
"v_accvgpr_read_b32 v20, a60\n"
"v_accvgpr_read_b32 v69, a45\n"
"v_accvgpr_read_b32 v21, a61\n"
"v_accvgpr_read_b32 v70, a46\n"
"v_accvgpr_read_b32 v22, a62\n"
"v_accvgpr_read_b32 v71, a47\n"
"v_accvgpr_read_b32 v23, a63\n"
"v_exp_f32 v72, v72\n"
"v_exp_f32 v24, v24\n"
"v_exp_f32 v73, v73\n"
"v_exp_f32 v25, v25\n"
"v_exp_f32 v74, v74\n"
"v_exp_f32 v26, v26\n"
"v_exp_f32 v75, v75\n"
"v_exp_f32 v27, v27\n"
"v_pk_mul_f32 v[68:69], v[68:69], v[172:173]\n"
"v_pk_mul_f32 v[20:21], v[20:21], v[188:189]\n"
"v_pk_mul_f32 v[70:71], v[70:71], v[174:175]\n"
"v_pk_mul_f32 v[22:23], v[22:23], v[190:191]\n"
"v_pk_add_f32 v[72:73], v[72:73], v[106:107]\n"
"v_pk_add_f32 v[24:25], v[24:25], v[106:107]\n"
"v_pk_add_f32 v[74:75], v[74:75], v[106:107]\n"
"v_pk_add_f32 v[26:27], v[26:27], v[106:107]\n"
"v_rcp_f32 v72, v72\n"
"v_rcp_f32 v24, v24\n"
"v_rcp_f32 v73, v73\n"
"v_rcp_f32 v25, v25\n"
"v_rcp_f32 v74, v74\n"
"v_rcp_f32 v26, v26\n"
"v_rcp_f32 v75, v75\n"
"v_rcp_f32 v27, v27\n"
"v_pk_mul_f32 v[68:69], v[68:69], v[72:73]\n"
"v_pk_mul_f32 v[20:21], v[20:21], v[24:25]\n"
"v_pk_mul_f32 v[70:71], v[70:71], v[74:75]\n"
"v_pk_mul_f32 v[22:23], v[22:23], v[26:27]\n"
"v_cvt_pk_bf16_f32 v84, v68, v69\n"
"v_cvt_pk_bf16_f32 v86, v20, v21\n"
"v_cvt_pk_bf16_f32 v85, v70, v71\n"
"v_cvt_pk_bf16_f32 v87, v22, v23\n"
"buffer_store_dwordx4 v[84:87], v121, s[68:71], s98 offen offset:1024 sc1\n"
"v_pk_mul_f32 v[36:37], v[204:205], v[104:105]\n"
"v_pk_mul_f32 v[48:49], v[220:221], v[104:105]\n"
"v_pk_mul_f32 v[38:39], v[206:207], v[104:105]\n"
"v_pk_mul_f32 v[50:51], v[222:223], v[104:105]\n"
"v_accvgpr_read_b32 v32, a76\n"
"v_accvgpr_read_b32 v44, a92\n"
"v_accvgpr_read_b32 v33, a77\n"
"v_accvgpr_read_b32 v45, a93\n"
"v_accvgpr_read_b32 v34, a78\n"
"v_accvgpr_read_b32 v46, a94\n"
"v_accvgpr_read_b32 v35, a79\n"
"v_accvgpr_read_b32 v47, a95\n"
"v_exp_f32 v36, v36\n"
"v_exp_f32 v48, v48\n"
"v_exp_f32 v37, v37\n"
"v_exp_f32 v49, v49\n"
"v_exp_f32 v38, v38\n"
"v_exp_f32 v50, v50\n"
"v_exp_f32 v39, v39\n"
"v_exp_f32 v51, v51\n"
"v_pk_mul_f32 v[32:33], v[32:33], v[204:205]\n"
"v_pk_mul_f32 v[44:45], v[44:45], v[220:221]\n"
"v_pk_mul_f32 v[34:35], v[34:35], v[206:207]\n"
"v_pk_mul_f32 v[46:47], v[46:47], v[222:223]\n"
"v_pk_add_f32 v[36:37], v[36:37], v[106:107]\n"
"v_pk_add_f32 v[48:49], v[48:49], v[106:107]\n"
"v_pk_add_f32 v[38:39], v[38:39], v[106:107]\n"
"v_pk_add_f32 v[50:51], v[50:51], v[106:107]\n"
"v_rcp_f32 v36, v36\n"
"v_rcp_f32 v48, v48\n"
"v_rcp_f32 v37, v37\n"
"v_rcp_f32 v49, v49\n"
"v_rcp_f32 v38, v38\n"
"v_rcp_f32 v50, v50\n"
"v_rcp_f32 v39, v39\n"
"v_rcp_f32 v51, v51\n"
"v_pk_mul_f32 v[32:33], v[32:33], v[36:37]\n"
"v_pk_mul_f32 v[44:45], v[44:45], v[48:49]\n"
"v_pk_mul_f32 v[34:35], v[34:35], v[38:39]\n"
"v_pk_mul_f32 v[46:47], v[46:47], v[50:51]\n"
"v_cvt_pk_bf16_f32 v88, v32, v33\n"
"v_cvt_pk_bf16_f32 v90, v44, v45\n"
"v_cvt_pk_bf16_f32 v89, v34, v35\n"
"v_cvt_pk_bf16_f32 v91, v46, v47\n"
"buffer_store_dwordx4 v[88:91], v121, s[68:71], s98 offen offset:2048 sc1\n"
"v_pk_mul_f32 v[60:61], v[236:237], v[104:105]\n"
"v_pk_mul_f32 v[72:73], v[252:253], v[104:105]\n"
"v_pk_mul_f32 v[62:63], v[238:239], v[104:105]\n"
"v_pk_mul_f32 v[74:75], v[254:255], v[104:105]\n"
"v_accvgpr_read_b32 v56, a108\n"
"v_accvgpr_read_b32 v68, a124\n"
"v_accvgpr_read_b32 v57, a109\n"
"v_accvgpr_read_b32 v69, a125\n"
"v_accvgpr_read_b32 v58, a110\n"
"v_accvgpr_read_b32 v70, a126\n"
"v_accvgpr_read_b32 v59, a111\n"
"v_accvgpr_read_b32 v71, a127\n"
"v_exp_f32 v60, v60\n"
"v_exp_f32 v72, v72\n"
"v_exp_f32 v61, v61\n"
"v_exp_f32 v73, v73\n"
"v_exp_f32 v62, v62\n"
"v_exp_f32 v74, v74\n"
"v_exp_f32 v63, v63\n"
"v_exp_f32 v75, v75\n"
"v_pk_mul_f32 v[56:57], v[56:57], v[236:237]\n"
"v_pk_mul_f32 v[68:69], v[68:69], v[252:253]\n"
"v_pk_mul_f32 v[58:59], v[58:59], v[238:239]\n"
"v_pk_mul_f32 v[70:71], v[70:71], v[254:255]\n"
"v_pk_add_f32 v[60:61], v[60:61], v[106:107]\n"
"v_pk_add_f32 v[72:73], v[72:73], v[106:107]\n"
"v_pk_add_f32 v[62:63], v[62:63], v[106:107]\n"
"v_pk_add_f32 v[74:75], v[74:75], v[106:107]\n"
"v_rcp_f32 v60, v60\n"
"v_rcp_f32 v72, v72\n"
"v_rcp_f32 v61, v61\n"
"v_rcp_f32 v73, v73\n"
"v_rcp_f32 v62, v62\n"
"v_rcp_f32 v74, v74\n"
"v_rcp_f32 v63, v63\n"
"v_rcp_f32 v75, v75\n"
"v_pk_mul_f32 v[56:57], v[56:57], v[60:61]\n"
"v_pk_mul_f32 v[68:69], v[68:69], v[72:73]\n"
"v_pk_mul_f32 v[58:59], v[58:59], v[62:63]\n"
"v_pk_mul_f32 v[70:71], v[70:71], v[74:75]\n"
"v_cvt_pk_bf16_f32 v92, v56, v57\n"
"v_cvt_pk_bf16_f32 v94, v68, v69\n"
"v_cvt_pk_bf16_f32 v93, v58, v59\n"
"v_cvt_pk_bf16_f32 v95, v70, v71\n"
"buffer_store_dwordx4 v[92:95], v121, s[68:71], s98 offen offset:3072 sc1\n"
"v_accvgpr_read_b32 v16, a156\n"
"v_accvgpr_read_b32 v17, a157\n"
"v_accvgpr_read_b32 v18, a158\n"
"v_accvgpr_read_b32 v19, a159\n"
"v_pk_mul_f32 v[24:25], v[16:17], v[104:105]\n"
"v_pk_mul_f32 v[26:27], v[18:19], v[104:105]\n"
"v_accvgpr_read_b32 v20, a140\n"
"v_accvgpr_read_b32 v21, a141\n"
"v_accvgpr_read_b32 v22, a142\n"
"v_accvgpr_read_b32 v23, a143\n"
"v_exp_f32 v24, v24\n"
"v_exp_f32 v25, v25\n"
"v_exp_f32 v26, v26\n"
"v_exp_f32 v27, v27\n"
"v_pk_mul_f32 v[20:21], v[20:21], v[16:17]\n"
"v_pk_mul_f32 v[22:23], v[22:23], v[18:19]\n"
"v_pk_add_f32 v[24:25], v[24:25], v[106:107]\n"
"v_pk_add_f32 v[26:27], v[26:27], v[106:107]\n"
"v_rcp_f32 v24, v24\n"
"v_rcp_f32 v25, v25\n"
"v_rcp_f32 v26, v26\n"
"v_rcp_f32 v27, v27\n"
"v_pk_mul_f32 v[20:21], v[20:21], v[24:25]\n"
"v_pk_mul_f32 v[22:23], v[22:23], v[26:27]\n"
"v_cvt_pk_bf16_f32 v96, v20, v21\n"
"v_cvt_pk_bf16_f32 v97, v22, v23\n"
"s_add_u32 s98, s98, 0x1000\n"
"buffer_store_dwordx2 v[96:97], v121, s[68:71], s98 offen sc1\n"
"s_branch HJOIN_%=\n"
"HODD_%=:\n"
"s_add_u32 s98, s99, 0\n"
"v_pk_mul_f32 v[36:37], v[128:129], v[104:105]\n"
"v_pk_mul_f32 v[38:39], v[130:131], v[104:105]\n"
"v_accvgpr_read_b32 v32, a0\n"
"v_accvgpr_read_b32 v33, a1\n"
"v_accvgpr_read_b32 v34, a2\n"
"v_accvgpr_read_b32 v35, a3\n"
"v_exp_f32 v36, v36\n"
"v_exp_f32 v37, v37\n"
"v_exp_f32 v38, v38\n"
"v_exp_f32 v39, v39\n"
"v_pk_mul_f32 v[32:33], v[32:33], v[128:129]\n"
"v_pk_mul_f32 v[34:35], v[34:35], v[130:131]\n"
"v_pk_add_f32 v[36:37], v[36:37], v[106:107]\n"
"v_pk_add_f32 v[38:39], v[38:39], v[106:107]\n"
"v_rcp_f32 v36, v36\n"
"v_rcp_f32 v37, v37\n"
"v_rcp_f32 v38, v38\n"
"v_rcp_f32 v39, v39\n"
"v_pk_mul_f32 v[32:33], v[32:33], v[36:37]\n"
"v_pk_mul_f32 v[34:35], v[34:35], v[38:39]\n"
"v_cvt_pk_bf16_f32 v76, v32, v33\n"
"v_cvt_pk_bf16_f32 v77, v34, v35\n"
"buffer_store_dwordx2 v[76:77], v121, s[68:71], s98 offen offset:8 sc1\n"
"s_add_u32 m0, s84, 0x20000\n"
"s_add_u32 s77, s85, 0x80000\n"
"buffer_load_dwordx4 v118, s[56:59], s77 offen lds\n"
"v_pk_mul_f32 v[48:49], v[144:145], v[104:105]\n"
"v_pk_mul_f32 v[60:61], v[160:161], v[104:105]\n"
"v_pk_mul_f32 v[50:51], v[146:147], v[104:105]\n"
"v_pk_mul_f32 v[62:63], v[162:163], v[104:105]\n"
"v_accvgpr_read_b32 v44, a16\n"
"v_accvgpr_read_b32 v56, a32\n"
"v_accvgpr_read_b32 v45, a17\n"
"v_accvgpr_read_b32 v57, a33\n"
"v_accvgpr_read_b32 v46, a18\n"
"v_accvgpr_read_b32 v58, a34\n"
"v_accvgpr_read_b32 v47, a19\n"
"v_accvgpr_read_b32 v59, a35\n"
"v_exp_f32 v48, v48\n"
"v_exp_f32 v60, v60\n"
"v_exp_f32 v49, v49\n"
"v_exp_f32 v61, v61\n"
"v_exp_f32 v50, v50\n"
"v_exp_f32 v62, v62\n"
"v_exp_f32 v51, v51\n"
"v_exp_f32 v63, v63\n"
"v_pk_mul_f32 v[44:45], v[44:45], v[144:145]\n"
"v_pk_mul_f32 v[56:57], v[56:57], v[160:161]\n"
"v_pk_mul_f32 v[46:47], v[46:47], v[146:147]\n"
"v_pk_mul_f32 v[58:59], v[58:59], v[162:163]\n"
"v_pk_add_f32 v[48:49], v[48:49], v[106:107]\n"
"v_pk_add_f32 v[60:61], v[60:61], v[106:107]\n"
"v_pk_add_f32 v[50:51], v[50:51], v[106:107]\n"
"v_pk_add_f32 v[62:63], v[62:63], v[106:107]\n"
"v_rcp_f32 v48, v48\n"
"v_rcp_f32 v60, v60\n"
"v_rcp_f32 v49, v49\n"
"v_rcp_f32 v61, v61\n"
"v_rcp_f32 v50, v50\n"
"v_rcp_f32 v62, v62\n"
"v_rcp_f32 v51, v51\n"
"v_rcp_f32 v63, v63\n"
"v_pk_mul_f32 v[44:45], v[44:45], v[48:49]\n"
"v_pk_mul_f32 v[56:57], v[56:57], v[60:61]\n"
"v_pk_mul_f32 v[46:47], v[46:47], v[50:51]\n"
"v_pk_mul_f32 v[58:59], v[58:59], v[62:63]\n"
"v_cvt_pk_bf16_f32 v80, v44, v45\n"
"v_cvt_pk_bf16_f32 v82, v56, v57\n"
"v_cvt_pk_bf16_f32 v81, v46, v47\n"
"v_cvt_pk_bf16_f32 v83, v58, v59\n"
"buffer_store_dwordx4 v[80:83], v121, s[68:71], s98 offen offset:1024 sc1\n"
"s_add_u32 m0, s84, 0x20400\n"
"s_add_u32 s77, s85, 0x80080\n"
"buffer_load_dwordx4 v118, s[56:59], s77 offen lds\n"
"v_pk_mul_f32 v[72:73], v[176:177], v[104:105]\n"
"v_pk_mul_f32 v[24:25], v[192:193], v[104:105]\n"
"v_pk_mul_f32 v[74:75], v[178:179], v[104:105]\n"
"v_pk_mul_f32 v[26:27], v[194:195], v[104:105]\n"
"v_accvgpr_read_b32 v68, a48\n"
"v_accvgpr_read_b32 v20, a64\n"
"v_accvgpr_read_b32 v69, a49\n"
"v_accvgpr_read_b32 v21, a65\n"
"v_accvgpr_read_b32 v70, a50\n"
"v_accvgpr_read_b32 v22, a66\n"
"v_accvgpr_read_b32 v71, a51\n"
"v_accvgpr_read_b32 v23, a67\n"
"v_exp_f32 v72, v72\n"
"v_exp_f32 v24, v24\n"
"v_exp_f32 v73, v73\n"
"v_exp_f32 v25, v25\n"
"v_exp_f32 v74, v74\n"
"v_exp_f32 v26, v26\n"
"v_exp_f32 v75, v75\n"
"v_exp_f32 v27, v27\n"
"v_pk_mul_f32 v[68:69], v[68:69], v[176:177]\n"
"v_pk_mul_f32 v[20:21], v[20:21], v[192:193]\n"
"v_pk_mul_f32 v[70:71], v[70:71], v[178:179]\n"
"v_pk_mul_f32 v[22:23], v[22:23], v[194:195]\n"
"v_pk_add_f32 v[72:73], v[72:73], v[106:107]\n"
"v_pk_add_f32 v[24:25], v[24:25], v[106:107]\n"
"v_pk_add_f32 v[74:75], v[74:75], v[106:107]\n"
"v_pk_add_f32 v[26:27], v[26:27], v[106:107]\n"
"v_rcp_f32 v72, v72\n"
"v_rcp_f32 v24, v24\n"
"v_rcp_f32 v73, v73\n"
"v_rcp_f32 v25, v25\n"
"v_rcp_f32 v74, v74\n"
"v_rcp_f32 v26, v26\n"
"v_rcp_f32 v75, v75\n"
"v_rcp_f32 v27, v27\n"
"v_pk_mul_f32 v[68:69], v[68:69], v[72:73]\n"
"v_pk_mul_f32 v[20:21], v[20:21], v[24:25]\n"
"v_pk_mul_f32 v[70:71], v[70:71], v[74:75]\n"
"v_pk_mul_f32 v[22:23], v[22:23], v[26:27]\n"
"v_cvt_pk_bf16_f32 v84, v68, v69\n"
"v_cvt_pk_bf16_f32 v86, v20, v21\n"
"v_cvt_pk_bf16_f32 v85, v70, v71\n"
"v_cvt_pk_bf16_f32 v87, v22, v23\n"
"buffer_store_dwordx4 v[84:87], v121, s[68:71], s98 offen offset:2048 sc1\n"
"s_add_u32 m0, s84, 0x20800\n"
"s_add_u32 s77, s85, 0x90000\n"
"buffer_load_dwordx4 v118, s[56:59], s77 offen lds\n"
"v_pk_mul_f32 v[36:37], v[208:209], v[104:105]\n"
"v_pk_mul_f32 v[48:49], v[224:225], v[104:105]\n"
"v_pk_mul_f32 v[38:39], v[210:211], v[104:105]\n"
"v_pk_mul_f32 v[50:51], v[226:227], v[104:105]\n"
"v_accvgpr_read_b32 v32, a80\n"
"v_accvgpr_read_b32 v44, a96\n"
"v_accvgpr_read_b32 v33, a81\n"
"v_accvgpr_read_b32 v45, a97\n"
"v_accvgpr_read_b32 v34, a82\n"
"v_accvgpr_read_b32 v46, a98\n"
"v_accvgpr_read_b32 v35, a83\n"
"v_accvgpr_read_b32 v47, a99\n"
"v_exp_f32 v36, v36\n"
"v_exp_f32 v48, v48\n"
"v_exp_f32 v37, v37\n"
"v_exp_f32 v49, v49\n"
"v_exp_f32 v38, v38\n"
"v_exp_f32 v50, v50\n"
"v_exp_f32 v39, v39\n"
"v_exp_f32 v51, v51\n"
"v_pk_mul_f32 v[32:33], v[32:33], v[208:209]\n"
"v_pk_mul_f32 v[44:45], v[44:45], v[224:225]\n"
"v_pk_mul_f32 v[34:35], v[34:35], v[210:211]\n"
"v_pk_mul_f32 v[46:47], v[46:47], v[226:227]\n"
"v_pk_add_f32 v[36:37], v[36:37], v[106:107]\n"
"v_pk_add_f32 v[48:49], v[48:49], v[106:107]\n"
"v_pk_add_f32 v[38:39], v[38:39], v[106:107]\n"
"v_pk_add_f32 v[50:51], v[50:51], v[106:107]\n"
"v_rcp_f32 v36, v36\n"
"v_rcp_f32 v48, v48\n"
"v_rcp_f32 v37, v37\n"
"v_rcp_f32 v49, v49\n"
"v_rcp_f32 v38, v38\n"
"v_rcp_f32 v50, v50\n"
"v_rcp_f32 v39, v39\n"
"v_rcp_f32 v51, v51\n"
"v_pk_mul_f32 v[32:33], v[32:33], v[36:37]\n"
"v_pk_mul_f32 v[44:45], v[44:45], v[48:49]\n"
"v_pk_mul_f32 v[34:35], v[34:35], v[38:39]\n"
"v_pk_mul_f32 v[46:47], v[46:47], v[50:51]\n"
"v_cvt_pk_bf16_f32 v88, v32, v33\n"
"v_cvt_pk_bf16_f32 v90, v44, v45\n"
"v_cvt_pk_bf16_f32 v89, v34, v35\n"
"v_cvt_pk_bf16_f32 v91, v46, v47\n"
"buffer_store_dwordx4 v[88:91], v121, s[68:71], s98 offen offset:3072 sc1\n"
"s_add_u32 m0, s84, 0x20c00\n"
"s_add_u32 s77, s85, 0x90080\n"
"buffer_load_dwordx4 v118, s[56:59], s77 offen lds\n"
"v_pk_mul_f32 v[60:61], v[240:241], v[104:105]\n"
"v_accvgpr_read_b32 v64, a144\n"
"v_pk_mul_f32 v[62:63], v[242:243], v[104:105]\n"
"v_accvgpr_read_b32 v65, a145\n"
"v_accvgpr_read_b32 v56, a112\n"
"v_accvgpr_read_b32 v66, a146\n"
"v_accvgpr_read_b32 v57, a113\n"
"v_accvgpr_read_b32 v67, a147\n"
"v_accvgpr_read_b32 v58, a114\n"
"v_pk_mul_f32 v[72:73], v[64:65], v[104:105]\n"
"v_accvgpr_read_b32 v59, a115\n"
"v_pk_mul_f32 v[74:75], v[66:67], v[104:105]\n"
"v_exp_f32 v60, v60\n"
"v_accvgpr_read_b32 v68, a128\n"
"v_exp_f32 v61, v61\n"
"v_accvgpr_read_b32 v69, a129\n"
"v_exp_f32 v62, v62\n"
"v_accvgpr_read_b32 v70, a130\n"
"v_exp_f32 v63, v63\n"
"v_accvgpr_read_b32 v71, a131\n"
"v_pk_mul_f32 v[56:57], v[56:57], v[240:241]\n"
"v_exp_f32 v72, v72\n"
"v_pk_mul_f32 v[58:59], v[58:59], v[242:243]\n"
"v_exp_f32 v73, v73\n"
"v_pk_add_f32 v[60:61], v[60:61], v[106:107]\n"
"v_exp_f32 v74, v74\n"
"v_pk_add_f32 v[62:63], v[62:63], v[106:107]\n"
"v_exp_f32 v75, v75\n"
"v_rcp_f32 v60, v60\n"
"v_pk_mul_f32 v[68:69], v[68:69], v[64:65]\n"
"v_rcp_f32 v61, v61\n"
"v_pk_mul_f32 v[70:71], v[70:71], v[66:67]\n"
"v_rcp_f32 v62, v62\n"
"v_pk_add_f32 v[72:73], v[72:73], v[106:107]\n"
"v_rcp_f32 v63, v63\n"
"v_pk_add_f32 v[74:75], v[74:75], v[106:107]\n"
"v_pk_mul_f32 v[56:57], v[56:57], v[60:61]\n"
"v_rcp_f32 v72, v72\n"
"v_pk_mul_f32 v[58:59], v[58:59], v[62:63]\n"
"v_rcp_f32 v73, v73\n"
"v_cvt_pk_bf16_f32 v92, v56, v57\n"
"v_rcp_f32 v74, v74\n"
"v_cvt_pk_bf16_f32 v93, v58, v59\n"
"v_rcp_f32 v75, v75\n"
"v_pk_mul_f32 v[68:69], v[68:69], v[72:73]\n"
"v_pk_mul_f32 v[70:71], v[70:71], v[74:75]\n"
"v_cvt_pk_bf16_f32 v94, v68, v69\n"
"v_cvt_pk_bf16_f32 v95, v70, v71\n"
"s_add_u32 s98, s98, 0x1000\n"
"buffer_store_dwordx4 v[92:95], v121, s[68:71], s98 offen sc1\n"
"s_add_u32 s98, s99, 0x24000\n"
"v_pk_mul_f32 v[24:25], v[132:133], v[104:105]\n"
"v_pk_mul_f32 v[26:27], v[134:135], v[104:105]\n"
"v_accvgpr_read_b32 v20, a4\n"
"v_accvgpr_read_b32 v21, a5\n"
"v_accvgpr_read_b32 v22, a6\n"
"v_accvgpr_read_b32 v23, a7\n"
"v_exp_f32 v24, v24\n"
"v_exp_f32 v25, v25\n"
"v_exp_f32 v26, v26\n"
"v_exp_f32 v27, v27\n"
"v_pk_mul_f32 v[20:21], v[20:21], v[132:133]\n"
"v_pk_mul_f32 v[22:23], v[22:23], v[134:135]\n"
"v_pk_add_f32 v[24:25], v[24:25], v[106:107]\n"
"v_pk_add_f32 v[26:27], v[26:27], v[106:107]\n"
"v_rcp_f32 v24, v24\n"
"v_rcp_f32 v25, v25\n"
"v_rcp_f32 v26, v26\n"
"v_rcp_f32 v27, v27\n"
"v_pk_mul_f32 v[20:21], v[20:21], v[24:25]\n"
"v_pk_mul_f32 v[22:23], v[22:23], v[26:27]\n"
"v_cvt_pk_bf16_f32 v96, v20, v21\n"
"v_cvt_pk_bf16_f32 v97, v22, v23\n"
"buffer_store_dwordx2 v[96:97], v121, s[68:71], s98 offen offset:8 sc1\n"
"v_pk_mul_f32 v[36:37], v[148:149], v[104:105]\n"
"v_pk_mul_f32 v[48:49], v[164:165], v[104:105]\n"
"v_pk_mul_f32 v[38:39], v[150:151], v[104:105]\n"
"v_pk_mul_f32 v[50:51], v[166:167], v[104:105]\n"
"v_accvgpr_read_b32 v32, a20\n"
"v_accvgpr_read_b32 v44, a36\n"
"v_accvgpr_read_b32 v33, a21\n"
"v_accvgpr_read_b32 v45, a37\n"
"v_accvgpr_read_b32 v34, a22\n"
"v_accvgpr_read_b32 v46, a38\n"
"v_accvgpr_read_b32 v35, a23\n"
"v_accvgpr_read_b32 v47, a39\n"
"v_exp_f32 v36, v36\n"
"v_exp_f32 v48, v48\n"
"v_exp_f32 v37, v37\n"
"v_exp_f32 v49, v49\n"
"v_exp_f32 v38, v38\n"
"v_exp_f32 v50, v50\n"
"v_exp_f32 v39, v39\n"
"v_exp_f32 v51, v51\n"
"v_pk_mul_f32 v[32:33], v[32:33], v[148:149]\n"
"v_pk_mul_f32 v[44:45], v[44:45], v[164:165]\n"
"v_pk_mul_f32 v[34:35], v[34:35], v[150:151]\n"
"v_pk_mul_f32 v[46:47], v[46:47], v[166:167]\n"
"v_pk_add_f32 v[36:37], v[36:37], v[106:107]\n"
"v_pk_add_f32 v[48:49], v[48:49], v[106:107]\n"
"v_pk_add_f32 v[38:39], v[38:39], v[106:107]\n"
"v_pk_add_f32 v[50:51], v[50:51], v[106:107]\n"
"v_rcp_f32 v36, v36\n"
"v_rcp_f32 v48, v48\n"
"v_rcp_f32 v37, v37\n"
"v_rcp_f32 v49, v49\n"
"v_rcp_f32 v38, v38\n"
"v_rcp_f32 v50, v50\n"
"v_rcp_f32 v39, v39\n"
"v_rcp_f32 v51, v51\n"
"v_pk_mul_f32 v[32:33], v[32:33], v[36:37]\n"
"v_pk_mul_f32 v[44:45], v[44:45], v[48:49]\n"
"v_pk_mul_f32 v[34:35], v[34:35], v[38:39]\n"
"v_pk_mul_f32 v[46:47], v[46:47], v[50:51]\n"
"v_cvt_pk_bf16_f32 v100, v32, v33\n"
"v_cvt_pk_bf16_f32 v102, v44, v45\n"
"v_cvt_pk_bf16_f32 v101, v34, v35\n"
"v_cvt_pk_bf16_f32 v103, v46, v47\n"
"buffer_store_dwordx4 v[100:103], v121, s[68:71], s98 offen offset:1024 sc1\n"
"v_pk_mul_f32 v[60:61], v[180:181], v[104:105]\n"
"v_pk_mul_f32 v[72:73], v[196:197], v[104:105]\n"
"v_pk_mul_f32 v[62:63], v[182:183], v[104:105]\n"
"v_pk_mul_f32 v[74:75], v[198:199], v[104:105]\n"
"v_accvgpr_read_b32 v56, a52\n"
"v_accvgpr_read_b32 v68, a68\n"
"v_accvgpr_read_b32 v57, a53\n"
"v_accvgpr_read_b32 v69, a69\n"
"v_accvgpr_read_b32 v58, a54\n"
"v_accvgpr_read_b32 v70, a70\n"
"v_accvgpr_read_b32 v59, a55\n"
"v_accvgpr_read_b32 v71, a71\n"
"v_exp_f32 v60, v60\n"
"v_exp_f32 v72, v72\n"
"v_exp_f32 v61, v61\n"
"v_exp_f32 v73, v73\n"
"v_exp_f32 v62, v62\n"
"v_exp_f32 v74, v74\n"
"v_exp_f32 v63, v63\n"
"v_exp_f32 v75, v75\n"
"v_pk_mul_f32 v[56:57], v[56:57], v[180:181]\n"
"v_pk_mul_f32 v[68:69], v[68:69], v[196:197]\n"
"v_pk_mul_f32 v[58:59], v[58:59], v[182:183]\n"
"v_pk_mul_f32 v[70:71], v[70:71], v[198:199]\n"
"v_pk_add_f32 v[60:61], v[60:61], v[106:107]\n"
"v_pk_add_f32 v[72:73], v[72:73], v[106:107]\n"
"v_pk_add_f32 v[62:63], v[62:63], v[106:107]\n"
"v_pk_add_f32 v[74:75], v[74:75], v[106:107]\n"
"v_rcp_f32 v60, v60\n"
"v_rcp_f32 v72, v72\n"
"v_rcp_f32 v61, v61\n"
"v_rcp_f32 v73, v73\n"
"v_rcp_f32 v62, v62\n"
"v_rcp_f32 v74, v74\n"
"v_rcp_f32 v63, v63\n"
"v_rcp_f32 v75, v75\n"
"v_pk_mul_f32 v[56:57], v[56:57], v[60:61]\n"
"v_pk_mul_f32 v[68:69], v[68:69], v[72:73]\n"
"v_pk_mul_f32 v[58:59], v[58:59], v[62:63]\n"
"v_pk_mul_f32 v[70:71], v[70:71], v[74:75]\n"
"v_cvt_pk_bf16_f32 v76, v56, v57\n"
"v_cvt_pk_bf16_f32 v78, v68, v69\n"
"v_cvt_pk_bf16_f32 v77, v58, v59\n"
"v_cvt_pk_bf16_f32 v79, v70, v71\n"
"buffer_store_dwordx4 v[76:79], v121, s[68:71], s98 offen offset:2048 sc1\n"
"v_pk_mul_f32 v[24:25], v[212:213], v[104:105]\n"
"v_pk_mul_f32 v[36:37], v[228:229], v[104:105]\n"
"v_pk_mul_f32 v[26:27], v[214:215], v[104:105]\n"
"v_pk_mul_f32 v[38:39], v[230:231], v[104:105]\n"
"v_accvgpr_read_b32 v20, a84\n"
"v_accvgpr_read_b32 v32, a100\n"
"v_accvgpr_read_b32 v21, a85\n"
"v_accvgpr_read_b32 v33, a101\n"
"v_accvgpr_read_b32 v22, a86\n"
"v_accvgpr_read_b32 v34, a102\n"
"v_accvgpr_read_b32 v23, a87\n"
"v_accvgpr_read_b32 v35, a103\n"
"v_exp_f32 v24, v24\n"
"v_exp_f32 v36, v36\n"
"v_exp_f32 v25, v25\n"
"v_exp_f32 v37, v37\n"
"v_exp_f32 v26, v26\n"
"v_exp_f32 v38, v38\n"
"v_exp_f32 v27, v27\n"
"v_exp_f32 v39, v39\n"
"v_pk_mul_f32 v[20:21], v[20:21], v[212:213]\n"
"v_pk_mul_f32 v[32:33], v[32:33], v[228:229]\n"
"v_pk_mul_f32 v[22:23], v[22:23], v[214:215]\n"
"v_pk_mul_f32 v[34:35], v[34:35], v[230:231]\n"
"v_pk_add_f32 v[24:25], v[24:25], v[106:107]\n"
"v_pk_add_f32 v[36:37], v[36:37], v[106:107]\n"
"v_pk_add_f32 v[26:27], v[26:27], v[106:107]\n"
"v_pk_add_f32 v[38:39], v[38:39], v[106:107]\n"
"v_rcp_f32 v24, v24\n"
"v_rcp_f32 v36, v36\n"
"v_rcp_f32 v25, v25\n"
"v_rcp_f32 v37, v37\n"
"v_rcp_f32 v26, v26\n"
"v_rcp_f32 v38, v38\n"
"v_rcp_f32 v27, v27\n"
"v_rcp_f32 v39, v39\n"
"v_pk_mul_f32 v[20:21], v[20:21], v[24:25]\n"
"v_pk_mul_f32 v[32:33], v[32:33], v[36:37]\n"
"v_pk_mul_f32 v[22:23], v[22:23], v[26:27]\n"
"v_pk_mul_f32 v[34:35], v[34:35], v[38:39]\n"
"v_cvt_pk_bf16_f32 v80, v20, v21\n"
"v_cvt_pk_bf16_f32 v82, v32, v33\n"
"v_cvt_pk_bf16_f32 v81, v22, v23\n"
"v_cvt_pk_bf16_f32 v83, v34, v35\n"
"buffer_store_dwordx4 v[80:83], v121, s[68:71], s98 offen offset:3072 sc1\n"
"v_pk_mul_f32 v[48:49], v[244:245], v[104:105]\n"
"v_accvgpr_read_b32 v52, a148\n"
"v_pk_mul_f32 v[50:51], v[246:247], v[104:105]\n"
"v_accvgpr_read_b32 v53, a149\n"
"v_accvgpr_read_b32 v44, a116\n"
"v_accvgpr_read_b32 v54, a150\n"
"v_accvgpr_read_b32 v45, a117\n"
"v_accvgpr_read_b32 v55, a151\n"
"v_accvgpr_read_b32 v46, a118\n"
"v_pk_mul_f32 v[60:61], v[52:53], v[104:105]\n"
"v_accvgpr_read_b32 v47, a119\n"
"v_pk_mul_f32 v[62:63], v[54:55], v[104:105]\n"
"v_exp_f32 v48, v48\n"
"v_accvgpr_read_b32 v56, a132\n"
"v_exp_f32 v49, v49\n"
"v_accvgpr_read_b32 v57, a133\n"
"v_exp_f32 v50, v50\n"
"v_accvgpr_read_b32 v58, a134\n"
"v_exp_f32 v51, v51\n"
"v_accvgpr_read_b32 v59, a135\n"
"v_pk_mul_f32 v[44:45], v[44:45], v[244:245]\n"
"v_exp_f32 v60, v60\n"
"v_pk_mul_f32 v[46:47], v[46:47], v[246:247]\n"
"v_exp_f32 v61, v61\n"
"v_pk_add_f32 v[48:49], v[48:49], v[106:107]\n"
"v_exp_f32 v62, v62\n"
"v_pk_add_f32 v[50:51], v[50:51], v[106:107]\n"
"v_exp_f32 v63, v63\n"
"v_rcp_f32 v48, v48\n"
"v_pk_mul_f32 v[56:57], v[56:57], v[52:53]\n"
"v_rcp_f32 v49, v49\n"
"v_pk_mul_f32 v[58:59], v[58:59], v[54:55]\n"
"v_rcp_f32 v50, v50\n"
"v_pk_add_f32 v[60:61], v[60:61], v[106:107]\n"
"v_rcp_f32 v51, v51\n"
"v_pk_add_f32 v[62:63], v[62:63], v[106:107]\n"
"v_pk_mul_f32 v[44:45], v[44:45], v[48:49]\n"
"v_rcp_f32 v60, v60\n"
"v_pk_mul_f32 v[46:47], v[46:47], v[50:51]\n"
"v_rcp_f32 v61, v61\n"
"v_cvt_pk_bf16_f32 v84, v44, v45\n"
"v_rcp_f32 v62, v62\n"
"v_cvt_pk_bf16_f32 v85, v46, v47\n"
"v_rcp_f32 v63, v63\n"
"v_pk_mul_f32 v[56:57], v[56:57], v[60:61]\n"
"v_pk_mul_f32 v[58:59], v[58:59], v[62:63]\n"
"v_cvt_pk_bf16_f32 v86, v56, v57\n"
"v_cvt_pk_bf16_f32 v87, v58, v59\n"
"s_add_u32 s98, s98, 0x1000\n"
"buffer_store_dwordx4 v[84:87], v121, s[68:71], s98 offen sc1\n"
"s_add_u32 s98, s99, 0x48000\n"
"v_pk_mul_f32 v[72:73], v[136:137], v[104:105]\n"
"v_pk_mul_f32 v[74:75], v[138:139], v[104:105]\n"
"v_accvgpr_read_b32 v68, a8\n"
"v_accvgpr_read_b32 v69, a9\n"
"v_accvgpr_read_b32 v70, a10\n"
"v_accvgpr_read_b32 v71, a11\n"
"v_exp_f32 v72, v72\n"
"v_exp_f32 v73, v73\n"
"v_exp_f32 v74, v74\n"
"v_exp_f32 v75, v75\n"
"v_pk_mul_f32 v[68:69], v[68:69], v[136:137]\n"
"v_pk_mul_f32 v[70:71], v[70:71], v[138:139]\n"
"v_pk_add_f32 v[72:73], v[72:73], v[106:107]\n"
"v_pk_add_f32 v[74:75], v[74:75], v[106:107]\n"
"v_rcp_f32 v72, v72\n"
"v_rcp_f32 v73, v73\n"
"v_rcp_f32 v74, v74\n"
"v_rcp_f32 v75, v75\n"
"v_pk_mul_f32 v[68:69], v[68:69], v[72:73]\n"
"v_pk_mul_f32 v[70:71], v[70:71], v[74:75]\n"
"v_cvt_pk_bf16_f32 v88, v68, v69\n"
"v_cvt_pk_bf16_f32 v89, v70, v71\n"
"buffer_store_dwordx2 v[88:89], v121, s[68:71], s98 offen offset:8 sc1\n"
"v_pk_mul_f32 v[24:25], v[152:153], v[104:105]\n"
"v_pk_mul_f32 v[36:37], v[168:169], v[104:105]\n"
"v_pk_mul_f32 v[26:27], v[154:155], v[104:105]\n"
"v_pk_mul_f32 v[38:39], v[170:171], v[104:105]\n"
"v_accvgpr_read_b32 v20, a24\n"
"v_accvgpr_read_b32 v32, a40\n"
"v_accvgpr_read_b32 v21, a25\n"
"v_accvgpr_read_b32 v33, a41\n"
"v_accvgpr_read_b32 v22, a26\n"
"v_accvgpr_read_b32 v34, a42\n"
"v_accvgpr_read_b32 v23, a27\n"
"v_accvgpr_read_b32 v35, a43\n"
"v_exp_f32 v24, v24\n"
"v_exp_f32 v36, v36\n"
"v_exp_f32 v25, v25\n"
"v_exp_f32 v37, v37\n"
"v_exp_f32 v26, v26\n"
"v_exp_f32 v38, v38\n"
"v_exp_f32 v27, v27\n"
"v_exp_f32 v39, v39\n"
"v_pk_mul_f32 v[20:21], v[20:21], v[152:153]\n"
"v_pk_mul_f32 v[32:33], v[32:33], v[168:169]\n"
"v_pk_mul_f32 v[22:23], v[22:23], v[154:155]\n"
"v_pk_mul_f32 v[34:35], v[34:35], v[170:171]\n"
"v_pk_add_f32 v[24:25], v[24:25], v[106:107]\n"
"v_pk_add_f32 v[36:37], v[36:37], v[106:107]\n"
"v_pk_add_f32 v[26:27], v[26:27], v[106:107]\n"
"v_pk_add_f32 v[38:39], v[38:39], v[106:107]\n"
"v_rcp_f32 v24, v24\n"
"v_rcp_f32 v36, v36\n"
"v_rcp_f32 v25, v25\n"
"v_rcp_f32 v37, v37\n"
"v_rcp_f32 v26, v26\n"
"v_rcp_f32 v38, v38\n"
"v_rcp_f32 v27, v27\n"
"v_rcp_f32 v39, v39\n"
"v_pk_mul_f32 v[20:21], v[20:21], v[24:25]\n"
"v_pk_mul_f32 v[32:33], v[32:33], v[36:37]\n"
"v_pk_mul_f32 v[22:23], v[22:23], v[26:27]\n"
"v_pk_mul_f32 v[34:35], v[34:35], v[38:39]\n"
"v_cvt_pk_bf16_f32 v92, v20, v21\n"
"v_cvt_pk_bf16_f32 v94, v32, v33\n"
"v_cvt_pk_bf16_f32 v93, v22, v23\n"
"v_cvt_pk_bf16_f32 v95, v34, v35\n"
"buffer_store_dwordx4 v[92:95], v121, s[68:71], s98 offen offset:1024 sc1\n"
"v_pk_mul_f32 v[48:49], v[184:185], v[104:105]\n"
"v_pk_mul_f32 v[60:61], v[200:201], v[104:105]\n"
"v_pk_mul_f32 v[50:51], v[186:187], v[104:105]\n"
"v_pk_mul_f32 v[62:63], v[202:203], v[104:105]\n"
"v_accvgpr_read_b32 v44, a56\n"
"v_accvgpr_read_b32 v56, a72\n"
"v_accvgpr_read_b32 v45, a57\n"
"v_accvgpr_read_b32 v57, a73\n"
"v_accvgpr_read_b32 v46, a58\n"
"v_accvgpr_read_b32 v58, a74\n"
"v_accvgpr_read_b32 v47, a59\n"
"v_accvgpr_read_b32 v59, a75\n"
"v_exp_f32 v48, v48\n"
"v_exp_f32 v60, v60\n"
"v_exp_f32 v49, v49\n"
"v_exp_f32 v61, v61\n"
"v_exp_f32 v50, v50\n"
"v_exp_f32 v62, v62\n"
"v_exp_f32 v51, v51\n"
"v_exp_f32 v63, v63\n"
"v_pk_mul_f32 v[44:45], v[44:45], v[184:185]\n"
"v_pk_mul_f32 v[56:57], v[56:57], v[200:201]\n"
"v_pk_mul_f32 v[46:47], v[46:47], v[186:187]\n"
"v_pk_mul_f32 v[58:59], v[58:59], v[202:203]\n"
"v_pk_add_f32 v[48:49], v[48:49], v[106:107]\n"
"v_pk_add_f32 v[60:61], v[60:61], v[106:107]\n"
"v_pk_add_f32 v[50:51], v[50:51], v[106:107]\n"
"v_pk_add_f32 v[62:63], v[62:63], v[106:107]\n"
"v_rcp_f32 v48, v48\n"
"v_rcp_f32 v60, v60\n"
"v_rcp_f32 v49, v49\n"
"v_rcp_f32 v61, v61\n"
"v_rcp_f32 v50, v50\n"
"v_rcp_f32 v62, v62\n"
"v_rcp_f32 v51, v51\n"
"v_rcp_f32 v63, v63\n"
"v_pk_mul_f32 v[44:45], v[44:45], v[48:49]\n"
"v_pk_mul_f32 v[56:57], v[56:57], v[60:61]\n"
"v_pk_mul_f32 v[46:47], v[46:47], v[50:51]\n"
"v_pk_mul_f32 v[58:59], v[58:59], v[62:63]\n"
"v_cvt_pk_bf16_f32 v96, v44, v45\n"
"v_cvt_pk_bf16_f32 v98, v56, v57\n"
"v_cvt_pk_bf16_f32 v97, v46, v47\n"
"v_cvt_pk_bf16_f32 v99, v58, v59\n"
"buffer_store_dwordx4 v[96:99], v121, s[68:71], s98 offen offset:2048 sc1\n"
"v_pk_mul_f32 v[72:73], v[216:217], v[104:105]\n"
"v_pk_mul_f32 v[24:25], v[232:233], v[104:105]\n"
"v_pk_mul_f32 v[74:75], v[218:219], v[104:105]\n"
"v_pk_mul_f32 v[26:27], v[234:235], v[104:105]\n"
"v_accvgpr_read_b32 v68, a88\n"
"v_accvgpr_read_b32 v20, a104\n"
"v_accvgpr_read_b32 v69, a89\n"
"v_accvgpr_read_b32 v21, a105\n"
"v_accvgpr_read_b32 v70, a90\n"
"v_accvgpr_read_b32 v22, a106\n"
"v_accvgpr_read_b32 v71, a91\n"
"v_accvgpr_read_b32 v23, a107\n"
"v_exp_f32 v72, v72\n"
"v_exp_f32 v24, v24\n"
"v_exp_f32 v73, v73\n"
"v_exp_f32 v25, v25\n"
"v_exp_f32 v74, v74\n"
"v_exp_f32 v26, v26\n"
"v_exp_f32 v75, v75\n"
"v_exp_f32 v27, v27\n"
"v_pk_mul_f32 v[68:69], v[68:69], v[216:217]\n"
"v_pk_mul_f32 v[20:21], v[20:21], v[232:233]\n"
"v_pk_mul_f32 v[70:71], v[70:71], v[218:219]\n"
"v_pk_mul_f32 v[22:23], v[22:23], v[234:235]\n"
"v_pk_add_f32 v[72:73], v[72:73], v[106:107]\n"
"v_pk_add_f32 v[24:25], v[24:25], v[106:107]\n"
"v_pk_add_f32 v[74:75], v[74:75], v[106:107]\n"
"v_pk_add_f32 v[26:27], v[26:27], v[106:107]\n"
"v_rcp_f32 v72, v72\n"
"v_rcp_f32 v24, v24\n"
"v_rcp_f32 v73, v73\n"
"v_rcp_f32 v25, v25\n"
"v_rcp_f32 v74, v74\n"
"v_rcp_f32 v26, v26\n"
"v_rcp_f32 v75, v75\n"
"v_rcp_f32 v27, v27\n"
"v_pk_mul_f32 v[68:69], v[68:69], v[72:73]\n"
"v_pk_mul_f32 v[20:21], v[20:21], v[24:25]\n"
"v_pk_mul_f32 v[70:71], v[70:71], v[74:75]\n"
"v_pk_mul_f32 v[22:23], v[22:23], v[26:27]\n"
"v_cvt_pk_bf16_f32 v100, v68, v69\n"
"v_cvt_pk_bf16_f32 v102, v20, v21\n"
"v_cvt_pk_bf16_f32 v101, v70, v71\n"
"v_cvt_pk_bf16_f32 v103, v22, v23\n"
"buffer_store_dwordx4 v[100:103], v121, s[68:71], s98 offen offset:3072 sc1\n"
"v_pk_mul_f32 v[36:37], v[248:249], v[104:105]\n"
"v_accvgpr_read_b32 v40, a152\n"
"v_pk_mul_f32 v[38:39], v[250:251], v[104:105]\n"
"v_accvgpr_read_b32 v41, a153\n"
"v_accvgpr_read_b32 v32, a120\n"
"v_accvgpr_read_b32 v42, a154\n"
"v_accvgpr_read_b32 v33, a121\n"
"v_accvgpr_read_b32 v43, a155\n"
"v_accvgpr_read_b32 v34, a122\n"
"v_pk_mul_f32 v[48:49], v[40:41], v[104:105]\n"
"v_accvgpr_read_b32 v35, a123\n"
"v_pk_mul_f32 v[50:51], v[42:43], v[104:105]\n"
"v_exp_f32 v36, v36\n"
"v_accvgpr_read_b32 v44, a136\n"
"v_exp_f32 v37, v37\n"
"v_accvgpr_read_b32 v45, a137\n"
"v_exp_f32 v38, v38\n"
"v_accvgpr_read_b32 v46, a138\n"
"v_exp_f32 v39, v39\n"
"v_accvgpr_read_b32 v47, a139\n"
"v_pk_mul_f32 v[32:33], v[32:33], v[248:249]\n"
"v_exp_f32 v48, v48\n"
"v_pk_mul_f32 v[34:35], v[34:35], v[250:251]\n"
"v_exp_f32 v49, v49\n"
"v_pk_add_f32 v[36:37], v[36:37], v[106:107]\n"
"v_exp_f32 v50, v50\n"
"v_pk_add_f32 v[38:39], v[38:39], v[106:107]\n"
"v_exp_f32 v51, v51\n"
"v_rcp_f32 v36, v36\n"
"v_pk_mul_f32 v[44:45], v[44:45], v[40:41]\n"
"v_rcp_f32 v37, v37\n"
"v_pk_mul_f32 v[46:47], v[46:47], v[42:43]\n"
"v_rcp_f32 v38, v38\n"
"v_pk_add_f32 v[48:49], v[48:49], v[106:107]\n"
"v_rcp_f32 v39, v39\n"
"v_pk_add_f32 v[50:51], v[50:51], v[106:107]\n"
"v_pk_mul_f32 v[32:33], v[32:33], v[36:37]\n"
"v_rcp_f32 v48, v48\n"
"v_pk_mul_f32 v[34:35], v[34:35], v[38:39]\n"
"v_rcp_f32 v49, v49\n"
"v_cvt_pk_bf16_f32 v76, v32, v33\n"
"v_rcp_f32 v50, v50\n"
"v_cvt_pk_bf16_f32 v77, v34, v35\n"
"v_rcp_f32 v51, v51\n"
"v_pk_mul_f32 v[44:45], v[44:45], v[48:49]\n"
"v_pk_mul_f32 v[46:47], v[46:47], v[50:51]\n"
"v_cvt_pk_bf16_f32 v78, v44, v45\n"
"v_cvt_pk_bf16_f32 v79, v46, v47\n"
"s_add_u32 s98, s98, 0x1000\n"
"buffer_store_dwordx4 v[76:79], v121, s[68:71], s98 offen sc1\n"
"s_add_u32 s98, s99, 0x6c000\n"
"v_pk_mul_f32 v[60:61], v[140:141], v[104:105]\n"
"v_pk_mul_f32 v[62:63], v[142:143], v[104:105]\n"
"v_accvgpr_read_b32 v56, a12\n"
"v_accvgpr_read_b32 v57, a13\n"
"v_accvgpr_read_b32 v58, a14\n"
"v_accvgpr_read_b32 v59, a15\n"
"v_exp_f32 v60, v60\n"
"v_exp_f32 v61, v61\n"
"v_exp_f32 v62, v62\n"
"v_exp_f32 v63, v63\n"
"v_pk_mul_f32 v[56:57], v[56:57], v[140:141]\n"
"v_pk_mul_f32 v[58:59], v[58:59], v[142:143]\n"
"v_pk_add_f32 v[60:61], v[60:61], v[106:107]\n"
"v_pk_add_f32 v[62:63], v[62:63], v[106:107]\n"
"v_rcp_f32 v60, v60\n"
"v_rcp_f32 v61, v61\n"
"v_rcp_f32 v62, v62\n"
"v_rcp_f32 v63, v63\n"
"v_pk_mul_f32 v[56:57], v[56:57], v[60:61]\n"
"v_pk_mul_f32 v[58:59], v[58:59], v[62:63]\n"
"v_cvt_pk_bf16_f32 v80, v56, v57\n"
"v_cvt_pk_bf16_f32 v81, v58, v59\n"
"buffer_store_dwordx2 v[80:81], v121, s[68:71], s98 offen offset:8 sc1\n"
"v_pk_mul_f32 v[72:73], v[156:157], v[104:105]\n"
"v_pk_mul_f32 v[24:25], v[172:173], v[104:105]\n"
"v_pk_mul_f32 v[74:75], v[158:159], v[104:105]\n"
"v_pk_mul_f32 v[26:27], v[174:175], v[104:105]\n"
"v_accvgpr_read_b32 v68, a28\n"
"v_accvgpr_read_b32 v20, a44\n"
"v_accvgpr_read_b32 v69, a29\n"
"v_accvgpr_read_b32 v21, a45\n"
"v_accvgpr_read_b32 v70, a30\n"
"v_accvgpr_read_b32 v22, a46\n"
"v_accvgpr_read_b32 v71, a31\n"
"v_accvgpr_read_b32 v23, a47\n"
"v_exp_f32 v72, v72\n"
"v_exp_f32 v24, v24\n"
"v_exp_f32 v73, v73\n"
"v_exp_f32 v25, v25\n"
"v_exp_f32 v74, v74\n"
"v_exp_f32 v26, v26\n"
"v_exp_f32 v75, v75\n"
"v_exp_f32 v27, v27\n"
"v_pk_mul_f32 v[68:69], v[68:69], v[156:157]\n"
"v_pk_mul_f32 v[20:21], v[20:21], v[172:173]\n"
"v_pk_mul_f32 v[70:71], v[70:71], v[158:159]\n"
"v_pk_mul_f32 v[22:23], v[22:23], v[174:175]\n"
"v_pk_add_f32 v[72:73], v[72:73], v[106:107]\n"
"v_pk_add_f32 v[24:25], v[24:25], v[106:107]\n"
"v_pk_add_f32 v[74:75], v[74:75], v[106:107]\n"
"v_pk_add_f32 v[26:27], v[26:27], v[106:107]\n"
"v_rcp_f32 v72, v72\n"
"v_rcp_f32 v24, v24\n"
"v_rcp_f32 v73, v73\n"
"v_rcp_f32 v25, v25\n"
"v_rcp_f32 v74, v74\n"
"v_rcp_f32 v26, v26\n"
"v_rcp_f32 v75, v75\n"
"v_rcp_f32 v27, v27\n"
"v_pk_mul_f32 v[68:69], v[68:69], v[72:73]\n"
"v_pk_mul_f32 v[20:21], v[20:21], v[24:25]\n"
"v_pk_mul_f32 v[70:71], v[70:71], v[74:75]\n"
"v_pk_mul_f32 v[22:23], v[22:23], v[26:27]\n"
"v_cvt_pk_bf16_f32 v84, v68, v69\n"
"v_cvt_pk_bf16_f32 v86, v20, v21\n"
"v_cvt_pk_bf16_f32 v85, v70, v71\n"
"v_cvt_pk_bf16_f32 v87, v22, v23\n"
"buffer_store_dwordx4 v[84:87], v121, s[68:71], s98 offen offset:1024 sc1\n"
"v_pk_mul_f32 v[36:37], v[188:189], v[104:105]\n"
"v_pk_mul_f32 v[48:49], v[204:205], v[104:105]\n"
"v_pk_mul_f32 v[38:39], v[190:191], v[104:105]\n"
"v_pk_mul_f32 v[50:51], v[206:207], v[104:105]\n"
"v_accvgpr_read_b32 v32, a60\n"
"v_accvgpr_read_b32 v44, a76\n"
"v_accvgpr_read_b32 v33, a61\n"
"v_accvgpr_read_b32 v45, a77\n"
"v_accvgpr_read_b32 v34, a62\n"
"v_accvgpr_read_b32 v46, a78\n"
"v_accvgpr_read_b32 v35, a63\n"
"v_accvgpr_read_b32 v47, a79\n"
"v_exp_f32 v36, v36\n"
"v_exp_f32 v48, v48\n"
"v_exp_f32 v37, v37\n"
"v_exp_f32 v49, v49\n"
"v_exp_f32 v38, v38\n"
"v_exp_f32 v50, v50\n"
"v_exp_f32 v39, v39\n"
"v_exp_f32 v51, v51\n"
"v_pk_mul_f32 v[32:33], v[32:33], v[188:189]\n"
"v_pk_mul_f32 v[44:45], v[44:45], v[204:205]\n"
"v_pk_mul_f32 v[34:35], v[34:35], v[190:191]\n"
"v_pk_mul_f32 v[46:47], v[46:47], v[206:207]\n"
"v_pk_add_f32 v[36:37], v[36:37], v[106:107]\n"
"v_pk_add_f32 v[48:49], v[48:49], v[106:107]\n"
"v_pk_add_f32 v[38:39], v[38:39], v[106:107]\n"
"v_pk_add_f32 v[50:51], v[50:51], v[106:107]\n"
"v_rcp_f32 v36, v36\n"
"v_rcp_f32 v48, v48\n"
"v_rcp_f32 v37, v37\n"
"v_rcp_f32 v49, v49\n"
"v_rcp_f32 v38, v38\n"
"v_rcp_f32 v50, v50\n"
"v_rcp_f32 v39, v39\n"
"v_rcp_f32 v51, v51\n"
"v_pk_mul_f32 v[32:33], v[32:33], v[36:37]\n"
"v_pk_mul_f32 v[44:45], v[44:45], v[48:49]\n"
"v_pk_mul_f32 v[34:35], v[34:35], v[38:39]\n"
"v_pk_mul_f32 v[46:47], v[46:47], v[50:51]\n"
"v_cvt_pk_bf16_f32 v88, v32, v33\n"
"v_cvt_pk_bf16_f32 v90, v44, v45\n"
"v_cvt_pk_bf16_f32 v89, v34, v35\n"
"v_cvt_pk_bf16_f32 v91, v46, v47\n"
"buffer_store_dwordx4 v[88:91], v121, s[68:71], s98 offen offset:2048 sc1\n"
"v_pk_mul_f32 v[60:61], v[220:221], v[104:105]\n"
"v_pk_mul_f32 v[72:73], v[236:237], v[104:105]\n"
"v_pk_mul_f32 v[62:63], v[222:223], v[104:105]\n"
"v_pk_mul_f32 v[74:75], v[238:239], v[104:105]\n"
"v_accvgpr_read_b32 v56, a92\n"
"v_accvgpr_read_b32 v68, a108\n"
"v_accvgpr_read_b32 v57, a93\n"
"v_accvgpr_read_b32 v69, a109\n"
"v_accvgpr_read_b32 v58, a94\n"
"v_accvgpr_read_b32 v70, a110\n"
"v_accvgpr_read_b32 v59, a95\n"
"v_accvgpr_read_b32 v71, a111\n"
"v_exp_f32 v60, v60\n"
"v_exp_f32 v72, v72\n"
"v_exp_f32 v61, v61\n"
"v_exp_f32 v73, v73\n"
"v_exp_f32 v62, v62\n"
"v_exp_f32 v74, v74\n"
"v_exp_f32 v63, v63\n"
"v_exp_f32 v75, v75\n"
"v_pk_mul_f32 v[56:57], v[56:57], v[220:221]\n"
"v_pk_mul_f32 v[68:69], v[68:69], v[236:237]\n"
"v_pk_mul_f32 v[58:59], v[58:59], v[222:223]\n"
"v_pk_mul_f32 v[70:71], v[70:71], v[238:239]\n"
"v_pk_add_f32 v[60:61], v[60:61], v[106:107]\n"
"v_pk_add_f32 v[72:73], v[72:73], v[106:107]\n"
"v_pk_add_f32 v[62:63], v[62:63], v[106:107]\n"
"v_pk_add_f32 v[74:75], v[74:75], v[106:107]\n"
"v_rcp_f32 v60, v60\n"
"v_rcp_f32 v72, v72\n"
"v_rcp_f32 v61, v61\n"
"v_rcp_f32 v73, v73\n"
"v_rcp_f32 v62, v62\n"
"v_rcp_f32 v74, v74\n"
"v_rcp_f32 v63, v63\n"
"v_rcp_f32 v75, v75\n"
"v_pk_mul_f32 v[56:57], v[56:57], v[60:61]\n"
"v_pk_mul_f32 v[68:69], v[68:69], v[72:73]\n"
"v_pk_mul_f32 v[58:59], v[58:59], v[62:63]\n"
"v_pk_mul_f32 v[70:71], v[70:71], v[74:75]\n"
"v_cvt_pk_bf16_f32 v92, v56, v57\n"
"v_cvt_pk_bf16_f32 v94, v68, v69\n"
"v_cvt_pk_bf16_f32 v93, v58, v59\n"
"v_cvt_pk_bf16_f32 v95, v70, v71\n"
"buffer_store_dwordx4 v[92:95], v121, s[68:71], s98 offen offset:3072 sc1\n"
"v_pk_mul_f32 v[24:25], v[252:253], v[104:105]\n"
"v_accvgpr_read_b32 v28, a156\n"
"v_pk_mul_f32 v[26:27], v[254:255], v[104:105]\n"
"v_accvgpr_read_b32 v29, a157\n"
"v_accvgpr_read_b32 v20, a124\n"
"v_accvgpr_read_b32 v30, a158\n"
"v_accvgpr_read_b32 v21, a125\n"
"v_accvgpr_read_b32 v31, a159\n"
"v_accvgpr_read_b32 v22, a126\n"
"v_pk_mul_f32 v[36:37], v[28:29], v[104:105]\n"
"v_accvgpr_read_b32 v23, a127\n"
"v_pk_mul_f32 v[38:39], v[30:31], v[104:105]\n"
"v_exp_f32 v24, v24\n"
"v_accvgpr_read_b32 v32, a140\n"
"v_exp_f32 v25, v25\n"
"v_accvgpr_read_b32 v33, a141\n"
"v_exp_f32 v26, v26\n"
"v_accvgpr_read_b32 v34, a142\n"
"v_exp_f32 v27, v27\n"
"v_accvgpr_read_b32 v35, a143\n"
"v_pk_mul_f32 v[20:21], v[20:21], v[252:253]\n"
"v_exp_f32 v36, v36\n"
"v_pk_mul_f32 v[22:23], v[22:23], v[254:255]\n"
"v_exp_f32 v37, v37\n"
"v_pk_add_f32 v[24:25], v[24:25], v[106:107]\n"
"v_exp_f32 v38, v38\n"
"v_pk_add_f32 v[26:27], v[26:27], v[106:107]\n"
"v_exp_f32 v39, v39\n"
"v_rcp_f32 v24, v24\n"
"v_pk_mul_f32 v[32:33], v[32:33], v[28:29]\n"
"v_rcp_f32 v25, v25\n"
"v_pk_mul_f32 v[34:35], v[34:35], v[30:31]\n"
"v_rcp_f32 v26, v26\n"
"v_pk_add_f32 v[36:37], v[36:37], v[106:107]\n"
"v_rcp_f32 v27, v27\n"
"v_pk_add_f32 v[38:39], v[38:39], v[106:107]\n"
"v_pk_mul_f32 v[20:21], v[20:21], v[24:25]\n"
"v_rcp_f32 v36, v36\n"
"v_pk_mul_f32 v[22:23], v[22:23], v[26:27]\n"
"v_rcp_f32 v37, v37\n"
"v_cvt_pk_bf16_f32 v96, v20, v21\n"
"v_rcp_f32 v38, v38\n"
"v_cvt_pk_bf16_f32 v97, v22, v23\n"
"v_rcp_f32 v39, v39\n"
"v_pk_mul_f32 v[32:33], v[32:33], v[36:37]\n"
"v_pk_mul_f32 v[34:35], v[34:35], v[38:39]\n"
"v_cvt_pk_bf16_f32 v98, v32, v33\n"
"v_cvt_pk_bf16_f32 v99, v34, v35\n"
"s_add_u32 s98, s98, 0x1000\n"
"buffer_store_dwordx4 v[96:99], v121, s[68:71], s98 offen sc1\n"
"HJOIN_%=:\n"
"s_waitcnt vmcnt(30)\n"
"s_lshl_b32 s94, s72, 12\n"
"s_mov_b32 s95, s94\n"
"s_lshl_b32 s76, s91, 7\n"
"s_lshl_b32 s77, s74, 2\n"
"s_add_u32 s76, s76, s77\n"
"s_lshl_b32 s97, s76, 10\n"
"s_nop 0\n"
"s_barrier\n"
"s_and_b32 s99, s72, 1\n"
"s_mul_i32 s99, s99, 0x1400\n"
"ds_read_b64_tr_b16 v[80:81], v119 offset:49152\n"
"ds_read_b64_tr_b16 v[82:83], v119 offset:53248\n"
"ds_read_b64_tr_b16 v[84:85], v119 offset:57344\n"
"ds_read_b64_tr_b16 v[86:87], v119 offset:61440\n"
"ds_read_b64_tr_b16 v[88:89], v120 offset:49152\n"
"ds_read_b64_tr_b16 v[90:91], v120 offset:53248\n"
"ds_read_b64_tr_b16 v[92:93], v120 offset:57344\n"
"ds_read_b64_tr_b16 v[94:95], v120 offset:61440\n"
"s_waitcnt lgkmcnt(6)\n"
"v_mfma_f32_16x16x32_bf16 v[128:131], v[80:83], a[160:163], 0\n"
"s_waitcnt lgkmcnt(4)\n"
"v_mfma_f32_16x16x32_bf16 v[128:131], v[84:87], a[164:167], v[128:131]\n"
"s_add_u32 s76, s90, 0x1800\n"
"ds_read_b64_tr_b16 v[96:97], v119 offset:49664\n"
"v_mfma_f32_16x16x32_bf16 v[132:135], v[84:87], a[172:175], 0\n"
"buffer_load_dwordx4 a[0:3], v121, s[64:67], s76 offen sc1\n"
"v_mfma_f32_16x16x32_bf16 v[132:135], v[80:83], a[168:171], v[132:135]\n"
"ds_read_b64_tr_b16 v[98:99], v119 offset:53760\n"
"v_lshrrev_b32 v122, 4, v5\n"
"v_mfma_f32_16x16x32_bf16 v[136:139], v[80:83], a[176:179], 0\n"
"v_and_b32 v123, 15, v5\n"
"v_mfma_f32_16x16x32_bf16 v[136:139], v[84:87], a[180:183], v[136:139]\n"
"s_add_u32 s76, s90, 0x1c00\n"
"ds_read_b64_tr_b16 v[100:101], v119 offset:57856\n"
"v_add_u32 v124, 0, v122\n"
"v_mfma_f32_16x16x32_bf16 v[140:143], v[84:87], a[188:191], 0\n"
"buffer_load_dwordx4 a[4:7], v121, s[64:67], s76 offen sc1\n"
"v_xor_b32 v110, v123, v124\n"
"v_mfma_f32_16x16x32_bf16 v[140:143], v[80:83], a[184:187], v[140:143]\n"
"ds_read_b64_tr_b16 v[102:103], v119 offset:61952\n"
"v_lshlrev_b32 v110, 4, v110\n"
"s_waitcnt lgkmcnt(6)\n"
"v_mfma_f32_16x16x32_bf16 v[144:147], v[88:91], a[160:163], 0\n"
"v_lshl_add_u32 v110, v124, 8, v110\n"
"s_waitcnt lgkmcnt(4)\n"
"v_mfma_f32_16x16x32_bf16 v[144:147], v[92:95], a[164:167], v[144:147]\n"
"ds_read_b64_tr_b16 v[80:81], v120 offset:49664\n"
"v_add_u32 v124, 4, v122\n"
"v_mfma_f32_16x16x32_bf16 v[148:151], v[92:95], a[172:175], 0\n"
"v_xor_b32 v111, v123, v124\n"
"v_mfma_f32_16x16x32_bf16 v[148:151], v[88:91], a[168:171], v[148:151]\n"
"s_add_u32 s76, s90, 0x6000\n"
"ds_read_b64_tr_b16 v[82:83], v120 offset:53760\n"
"v_lshlrev_b32 v111, 4, v111\n"
"v_mfma_f32_16x16x32_bf16 v[152:155], v[88:91], a[176:179], 0\n"
"buffer_load_dwordx4 a[8:11], v121, s[64:67], s76 offen sc1\n"
"v_lshl_add_u32 v111, v124, 8, v111\n"
"v_mfma_f32_16x16x32_bf16 v[152:155], v[92:95], a[180:183], v[152:155]\n"
"ds_read_b64_tr_b16 v[84:85], v120 offset:57856\n"
"v_add_u32 v124, 8, v122\n"
"v_mfma_f32_16x16x32_bf16 v[156:159], v[92:95], a[188:191], 0\n"
"v_xor_b32 v112, v123, v124\n"
"v_mfma_f32_16x16x32_bf16 v[156:159], v[88:91], a[184:187], v[156:159]\n"
"s_add_u32 s76, s90, 0x6400\n"
"ds_read_b64_tr_b16 v[86:87], v120 offset:61952\n"
"v_lshlrev_b32 v112, 4, v112\n"
"s_waitcnt lgkmcnt(6)\n"
"v_mfma_f32_16x16x32_bf16 v[160:163], v[96:99], a[160:163], 0\n"
"buffer_load_dwordx4 a[12:15], v121, s[64:67], s76 offen sc1\n"
"v_lshl_add_u32 v112, v124, 8, v112\n"
"s_waitcnt lgkmcnt(4)\n"
"v_mfma_f32_16x16x32_bf16 v[160:163], v[100:103], a[164:167], v[160:163]\n"
"ds_read_b64_tr_b16 v[88:89], v119 offset:50176\n"
"v_add_u32 v124, 12, v122\n"
"v_mfma_f32_16x16x32_bf16 v[164:167], v[100:103], a[172:175], 0\n"
"v_xor_b32 v113, v123, v124\n"
"v_mfma_f32_16x16x32_bf16 v[164:167], v[96:99], a[168:171], v[164:167]\n"
"ds_read_b64_tr_b16 v[90:91], v119 offset:54272\n"
"v_lshlrev_b32 v113, 4, v113\n"
"v_mfma_f32_16x16x32_bf16 v[168:171], v[96:99], a[176:179], 0\n"
"v_lshl_add_u32 v113, v124, 8, v113\n"
"v_mfma_f32_16x16x32_bf16 v[168:171], v[100:103], a[180:183], v[168:171]\n"
"s_add_u32 s76, s90, 0xa800\n"
"ds_read_b64_tr_b16 v[92:93], v119 offset:58368\n"
"v_bfe_u32 v122, v5, 2, 3\n"
"v_mfma_f32_16x16x32_bf16 v[172:175], v[100:103], a[188:191], 0\n"
"buffer_load_dwordx4 a[16:19], v121, s[64:67], s76 offen sc1\n"
"v_lshrrev_b32 v123, 2, v122\n"
"v_mfma_f32_16x16x32_bf16 v[172:175], v[96:99], a[184:187], v[172:175]\n"
"ds_read_b64_tr_b16 v[94:95], v119 offset:62464\n"
"v_lshlrev_b32 v123, 1, v123\n"
"s_waitcnt lgkmcnt(6)\n"
"v_mfma_f32_16x16x32_bf16 v[176:179], v[80:83], a[160:163], 0\n"
"v_and_b32 v124, 3, v5\n"
"s_waitcnt lgkmcnt(4)\n"
"v_mfma_f32_16x16x32_bf16 v[176:179], v[84:87], a[164:167], v[176:179]\n"
"s_add_u32 s76, s90, 0xac00\n"
"ds_read_b64_tr_b16 v[96:97], v120 offset:50176\n"
"v_xor_b32 v123, v123, v124\n"
"v_mfma_f32_16x16x32_bf16 v[180:183], v[84:87], a[172:175], 0\n"
"buffer_load_dwordx4 a[20:23], v121, s[64:67], s76 offen sc1\n"
"v_lshlrev_b32 v123, 4, v123\n"
"v_mfma_f32_16x16x32_bf16 v[180:183], v[80:83], a[168:171], v[180:183]\n"
"ds_read_b64_tr_b16 v[98:99], v120 offset:54272\n"
"v_lshl_add_u32 v123, v122, 8, v123\n"
"v_mfma_f32_16x16x32_bf16 v[184:187], v[80:83], a[176:179], 0\n"
"v_lshrrev_b32 v124, 5, v5\n"
"v_mfma_f32_16x16x32_bf16 v[184:187], v[84:87], a[180:183], v[184:187]\n"
"ds_read_b64_tr_b16 v[100:101], v120 offset:58368\n"
"v_lshl_add_u32 v114, v124, 6, v123\n"
"v_mfma_f32_16x16x32_bf16 v[188:191], v[84:87], a[188:191], 0\n"
"v_add_u32 v115, 0x80, v114\n"
"v_mfma_f32_16x16x32_bf16 v[188:191], v[80:83], a[184:187], v[188:191]\n"
"s_add_u32 s76, s90, 0xf000\n"
"ds_read_b64_tr_b16 v[102:103], v120 offset:62464\n"
"v_add_u32 v116, 0x800, v114\n"
"s_waitcnt lgkmcnt(6)\n"
"v_mfma_f32_16x16x32_bf16 v[192:195], v[88:91], a[160:163], 0\n"
"buffer_load_dwordx4 a[24:27], v121, s[64:67], s76 offen sc1\n"
"v_add_u32 v117, 0x880, v114\n"
"s_waitcnt lgkmcnt(4)\n"
"v_mfma_f32_16x16x32_bf16 v[192:195], v[92:95], a[164:167], v[192:195]\n"
"ds_read_b64_tr_b16 v[80:81], v119 offset:50688\n"
"v_and_b32 v122, 15, v5\n"
"v_mfma_f32_16x16x32_bf16 v[196:199], v[92:95], a[172:175], 0\n"
"v_lshrrev_b32 v123, 4, v5\n"
"v_mfma_f32_16x16x32_bf16 v[196:199], v[88:91], a[168:171], v[196:199]\n"
"s_add_u32 s76, s90, 0xf400\n"
"ds_read_b64_tr_b16 v[82:83], v119 offset:54784\n"
"v_add_u32 v124, 0, v123\n"
"v_mfma_f32_16x16x32_bf16 v[200:203], v[88:91], a[176:179], 0\n"
"buffer_load_dwordx4 a[28:31], v121, s[64:67], s76 offen sc1\n"
"v_xor_b32 v124, v124, v122\n"
"v_mfma_f32_16x16x32_bf16 v[200:203], v[92:95], a[180:183], v[200:203]\n"
"ds_read_b64_tr_b16 v[84:85], v119 offset:58880\n"
"v_lshlrev_b32 v124, 4, v124\n"
"v_mfma_f32_16x16x32_bf16 v[204:207], v[92:95], a[188:191], 0\n"
"v_lshl_add_u32 v104, v122, 8, v124\n"
"v_mfma_f32_16x16x32_bf16 v[204:207], v[88:91], a[184:187], v[204:207]\n"
"ds_read_b64_tr_b16 v[86:87], v119 offset:62976\n"
"v_add_u32 v124, 4, v123\n"
"s_waitcnt lgkmcnt(6)\n"
"v_mfma_f32_16x16x32_bf16 v[208:211], v[96:99], a[160:163], 0\n"
"v_xor_b32 v124, v124, v122\n"
"s_waitcnt lgkmcnt(4)\n"
"v_mfma_f32_16x16x32_bf16 v[208:211], v[100:103], a[164:167], v[208:211]\n"
"s_add_u32 s77, s85, 0x100000\n"
"s_add_u32 m0, s84, 0x18000\n"
"ds_read_b64_tr_b16 v[88:89], v120 offset:50688\n"
"v_lshlrev_b32 v124, 4, v124\n"
"v_mfma_f32_16x16x32_bf16 v[212:215], v[100:103], a[172:175], 0\n"
"buffer_load_dwordx4 v118, s[56:59], s77 offen lds\n"
"v_lshl_add_u32 v105, v122, 8, v124\n"
"v_mfma_f32_16x16x32_bf16 v[212:215], v[96:99], a[168:171], v[212:215]\n"
"ds_read_b64_tr_b16 v[90:91], v120 offset:54784\n"
"v_add_u32 v124, 8, v123\n"
"v_mfma_f32_16x16x32_bf16 v[216:219], v[96:99], a[176:179], 0\n"
"v_xor_b32 v124, v124, v122\n"
"v_mfma_f32_16x16x32_bf16 v[216:219], v[100:103], a[180:183], v[216:219]\n"
"s_add_u32 s77, s85, 0x100080\n"
"s_add_u32 m0, s84, 0x18400\n"
"ds_read_b64_tr_b16 v[92:93], v120 offset:58880\n"
"v_lshlrev_b32 v124, 4, v124\n"
"v_mfma_f32_16x16x32_bf16 v[220:223], v[100:103], a[188:191], 0\n"
"buffer_load_dwordx4 v118, s[56:59], s77 offen lds\n"
"v_lshl_add_u32 v106, v122, 8, v124\n"
"v_mfma_f32_16x16x32_bf16 v[220:223], v[96:99], a[184:187], v[220:223]\n"
"s_waitcnt vmcnt(26)\n"
"s_barrier\n"
"ds_read_b64_tr_b16 v[94:95], v120 offset:62976\n"
"v_add_u32 v124, 12, v123\n"
"s_waitcnt lgkmcnt(6)\n"
"v_mfma_f32_16x16x32_bf16 v[224:227], v[80:83], a[160:163], 0\n"
"v_xor_b32 v124, v124, v122\n"
"s_waitcnt lgkmcnt(4)\n"
"v_mfma_f32_16x16x32_bf16 v[224:227], v[84:87], a[164:167], v[224:227]\n"
"ds_read_b64_tr_b16 v[96:97], v119 offset:32768\n"
"v_lshlrev_b32 v124, 4, v124\n"
"v_mfma_f32_16x16x32_bf16 v[228:231], v[84:87], a[172:175], 0\n"
"v_lshl_add_u32 v107, v122, 8, v124\n"
"v_mfma_f32_16x16x32_bf16 v[228:231], v[80:83], a[168:171], v[228:231]\n"
"s_add_u32 s77, s85, 0x110000\n"
"s_add_u32 m0, s84, 0x18800\n"
"ds_read_b64_tr_b16 v[98:99], v119 offset:36864\n"
"v_lshrrev_b32 v122, 5, v5\n"
"v_mfma_f32_16x16x32_bf16 v[232:235], v[80:83], a[176:179], 0\n"
"buffer_load_dwordx4 v118, s[56:59], s77 offen lds\n"
"v_lshlrev_b32 v108, 11, v122\n"
"v_mfma_f32_16x16x32_bf16 v[232:235], v[84:87], a[180:183], v[232:235]\n"
"ds_read_b64_tr_b16 v[100:101], v119 offset:40960\n"
"v_bfe_u32 v122, v5, 4, 1\n"
"v_mfma_f32_16x16x32_bf16 v[236:239], v[84:87], a[188:191], 0\n"
"v_lshl_add_u32 v108, v122, 8, v108\n"
"v_mfma_f32_16x16x32_bf16 v[236:239], v[80:83], a[184:187], v[236:239]\n"
"s_add_u32 s77, s85, 0x110080\n"
"s_add_u32 m0, s84, 0x18c00\n"
"ds_read_b64_tr_b16 v[102:103], v119 offset:45056\n"
"v_xor_b32 v122, 0, v122\n"
"s_waitcnt lgkmcnt(6)\n"
"v_mfma_f32_16x16x32_bf16 v[240:243], v[88:91], a[160:163], 0\n"
"buffer_load_dwordx4 v118, s[56:59], s77 offen lds\n"
"v_lshl_add_u32 v108, v122, 5, v108\n"
"s_waitcnt lgkmcnt(4)\n"
"v_mfma_f32_16x16x32_bf16 v[240:243], v[92:95], a[164:167], v[240:243]\n"
"ds_read_b64_tr_b16 v[80:81], v120 offset:32768\n"
"v_bfe_u32 v122, v5, 2, 2\n"
"v_mfma_f32_16x16x32_bf16 v[244:247], v[92:95], a[172:175], 0\n"
"v_lshl_add_u32 v108, v122, 6, v108\n"
"v_mfma_f32_16x16x32_bf16 v[244:247], v[88:91], a[168:171], v[244:247]\n"
"ds_read_b64_tr_b16 v[82:83], v120 offset:36864\n"
"v_and_b32 v122, 3, v5\n"
"v_mfma_f32_16x16x32_bf16 v[248:251], v[88:91], a[176:179], 0\n"
"v_lshl_add_u32 v108, v122, 3, v108\n"
"v_mfma_f32_16x16x32_bf16 v[248:251], v[92:95], a[180:183], v[248:251]\n"
"ds_read_b64_tr_b16 v[84:85], v120 offset:40960\n"
"v_add_u32 v108, 0x10000, v108\n"
"v_mfma_f32_16x16x32_bf16 v[252:255], v[92:95], a[188:191], 0\n"
"v_lshrrev_b32 v122, 5, v5\n"
"v_mfma_f32_16x16x32_bf16 v[252:255], v[88:91], a[184:187], v[252:255]\n"
"ds_read_b64_tr_b16 v[86:87], v120 offset:45056\n"
"v_lshlrev_b32 v109, 11, v122\n"
"s_waitcnt lgkmcnt(6)\n"
"v_mfma_f32_16x16x32_bf16 v[128:131], v[96:99], a[192:195], v[128:131]\n"
"s_waitcnt lgkmcnt(4)\n"
"v_mfma_f32_16x16x32_bf16 v[128:131], v[100:103], a[196:199], v[128:131]\n"
"s_add_u32 s76, s90, 0x2000\n"
"ds_read_b64_tr_b16 v[88:89], v119 offset:33280\n"
"v_mfma_f32_16x16x32_bf16 v[132:135], v[100:103], a[204:207], v[132:135]\n"
"buffer_load_dwordx4 a[160:163], v121, s[64:67], s76 offen sc1\n"
"v_mfma_f32_16x16x32_bf16 v[132:135], v[96:99], a[200:203], v[132:135]\n"
"ds_read_b64_tr_b16 v[90:91], v119 offset:37376\n"
"v_bfe_u32 v122, v5, 4, 1\n"
"v_mfma_f32_16x16x32_bf16 v[136:139], v[96:99], a[208:211], v[136:139]\n"
"v_lshl_add_u32 v109, v122, 8, v109\n"
"v_mfma_f32_16x16x32_bf16 v[136:139], v[100:103], a[212:215], v[136:139]\n"
"s_add_u32 s76, s90, 0x2400\n"
"ds_read_b64_tr_b16 v[92:93], v119 offset:41472\n"
"v_xor_b32 v122, 1, v122\n"
"v_mfma_f32_16x16x32_bf16 v[140:143], v[100:103], a[220:223], v[140:143]\n"
"buffer_load_dwordx4 a[164:167], v121, s[64:67], s76 offen sc1\n"
"v_lshl_add_u32 v109, v122, 5, v109\n"
"v_mfma_f32_16x16x32_bf16 v[140:143], v[96:99], a[216:219], v[140:143]\n"
"ds_read_b64_tr_b16 v[94:95], v119 offset:45568\n"
"v_bfe_u32 v122, v5, 2, 2\n"
"s_waitcnt lgkmcnt(6)\n"
"v_mfma_f32_16x16x32_bf16 v[144:147], v[80:83], a[192:195], v[144:147]\n"
"v_lshl_add_u32 v109, v122, 6, v109\n"
"s_waitcnt lgkmcnt(4)\n"
"v_mfma_f32_16x16x32_bf16 v[144:147], v[84:87], a[196:199], v[144:147]\n"
"ds_read_b64_tr_b16 v[96:97], v120 offset:33280\n"
"v_and_b32 v122, 3, v5\n"
"v_mfma_f32_16x16x32_bf16 v[148:151], v[84:87], a[204:207], v[148:151]\n"
"v_lshl_add_u32 v109, v122, 3, v109\n"
"v_mfma_f32_16x16x32_bf16 v[148:151], v[80:83], a[200:203], v[148:151]\n"
"s_add_u32 s76, s90, 0x6800\n"
"ds_read_b64_tr_b16 v[98:99], v120 offset:37376\n"
"v_add_u32 v109, 0x10000, v109\n"
"v_mfma_f32_16x16x32_bf16 v[152:155], v[80:83], a[208:211], v[152:155]\n"
"buffer_load_dwordx4 a[168:171], v121, s[64:67], s76 offen sc1\n"
"v_mov_b32 v125, 0xe38f\n"
"v_mfma_f32_16x16x32_bf16 v[152:155], v[84:87], a[212:215], v[152:155]\n"
"ds_read_b64_tr_b16 v[100:101], v120 offset:41472\n"
"v_mov_b32 v126, 0x480\n"
"v_mfma_f32_16x16x32_bf16 v[156:159], v[84:87], a[220:223], v[156:159]\n"
"v_lshlrev_b32 v122, 4, v5\n"
"v_mfma_f32_16x16x32_bf16 v[156:159], v[80:83], a[216:219], v[156:159]\n"
"s_add_u32 s76, s90, 0x6c00\n"
"ds_read_b64_tr_b16 v[102:103], v120 offset:45568\n"
"v_add_u32 v122, s99, v122\n"
"s_waitcnt lgkmcnt(6)\n"
"v_mfma_f32_16x16x32_bf16 v[160:163], v[88:91], a[192:195], v[160:163]\n"
"buffer_load_dwordx4 a[172:175], v121, s[64:67], s76 offen sc1\n"
"v_add_u32 v122, 0, v122\n"
"s_waitcnt lgkmcnt(4)\n"
"v_mfma_f32_16x16x32_bf16 v[160:163], v[92:95], a[196:199], v[160:163]\n"
"ds_read_b64_tr_b16 v[80:81], v119 offset:33792\n"
"v_mul_lo_u32 v123, v125, v122\n"
"v_mfma_f32_16x16x32_bf16 v[164:167], v[92:95], a[204:207], v[164:167]\n"
"v_lshrrev_b32 v123, 24, v123\n"
"v_mfma_f32_16x16x32_bf16 v[164:167], v[88:91], a[200:203], v[164:167]\n"
"ds_read_b64_tr_b16 v[82:83], v119 offset:37888\n"
"v_mul_u32_u24 v124, 0x120, v123\n"
"v_mfma_f32_16x16x32_bf16 v[168:171], v[88:91], a[208:211], v[168:171]\n"
"v_sub_u32 v122, v122, v124\n"
"v_mfma_f32_16x16x32_bf16 v[168:171], v[92:95], a[212:215], v[168:171]\n"
"s_add_u32 s76, s90, 0xb000\n"
"ds_read_b64_tr_b16 v[84:85], v119 offset:41984\n"
"v_mul_lo_u32 v123, v126, v123\n"
"v_mfma_f32_16x16x32_bf16 v[172:175], v[92:95], a[220:223], v[172:175]\n"
"buffer_load_dwordx4 a[176:179], v121, s[64:67], s76 offen sc1\n"
"v_add_u32 v6, v122, v123\n"
"v_mfma_f32_16x16x32_bf16 v[172:175], v[88:91], a[216:219], v[172:175]\n"
"ds_read_b64_tr_b16 v[86:87], v119 offset:46080\n"
"v_lshlrev_b32 v122, 4, v5\n"
"s_waitcnt lgkmcnt(6)\n"
"v_mfma_f32_16x16x32_bf16 v[176:179], v[96:99], a[192:195], v[176:179]\n"
"v_add_u32 v122, s99, v122\n"
"s_waitcnt lgkmcnt(4)\n"
"v_mfma_f32_16x16x32_bf16 v[176:179], v[100:103], a[196:199], v[176:179]\n"
"s_add_u32 s76, s90, 0xb400\n"
"ds_read_b64_tr_b16 v[88:89], v120 offset:33792\n"
"v_add_u32 v122, 0x400, v122\n"
"v_mfma_f32_16x16x32_bf16 v[180:183], v[100:103], a[204:207], v[180:183]\n"
"buffer_load_dwordx4 a[180:183], v121, s[64:67], s76 offen sc1\n"
"v_mul_lo_u32 v123, v125, v122\n"
"v_mfma_f32_16x16x32_bf16 v[180:183], v[96:99], a[200:203], v[180:183]\n"
"ds_read_b64_tr_b16 v[90:91], v120 offset:37888\n"
"v_lshrrev_b32 v123, 24, v123\n"
"v_mfma_f32_16x16x32_bf16 v[184:187], v[96:99], a[208:211], v[184:187]\n"
"v_mul_u32_u24 v124, 0x120, v123\n"
"v_mfma_f32_16x16x32_bf16 v[184:187], v[100:103], a[212:215], v[184:187]\n"
"ds_read_b64_tr_b16 v[92:93], v120 offset:41984\n"
"v_sub_u32 v122, v122, v124\n"
"v_mfma_f32_16x16x32_bf16 v[188:191], v[100:103], a[220:223], v[188:191]\n"
"v_mul_lo_u32 v123, v126, v123\n"
"v_mfma_f32_16x16x32_bf16 v[188:191], v[96:99], a[216:219], v[188:191]\n"
"s_add_u32 s76, s90, 0xf800\n"
"ds_read_b64_tr_b16 v[94:95], v120 offset:46080\n"
"v_add_u32 v7, v122, v123\n"
"s_waitcnt lgkmcnt(6)\n"
"v_mfma_f32_16x16x32_bf16 v[192:195], v[80:83], a[192:195], v[192:195]\n"
"buffer_load_dwordx4 a[184:187], v121, s[64:67], s76 offen sc1\n"
"v_lshlrev_b32 v122, 4, v5\n"
"s_waitcnt lgkmcnt(4)\n"
"v_mfma_f32_16x16x32_bf16 v[192:195], v[84:87], a[196:199], v[192:195]\n"
"ds_read_b64_tr_b16 v[96:97], v119 offset:34304\n"
"v_add_u32 v122, s99, v122\n"
"v_mfma_f32_16x16x32_bf16 v[196:199], v[84:87], a[204:207], v[196:199]\n"
"v_add_u32 v122, 0x800, v122\n"
"v_mfma_f32_16x16x32_bf16 v[196:199], v[80:83], a[200:203], v[196:199]\n"
"s_add_u32 s76, s90, 0xfc00\n"
"ds_read_b64_tr_b16 v[98:99], v119 offset:38400\n"
"v_mul_lo_u32 v123, v125, v122\n"
"v_mfma_f32_16x16x32_bf16 v[200:203], v[80:83], a[208:211], v[200:203]\n"
"buffer_load_dwordx4 a[188:191], v121, s[64:67], s76 offen sc1\n"
"v_lshrrev_b32 v123, 24, v123\n"
"v_mfma_f32_16x16x32_bf16 v[200:203], v[84:87], a[212:215], v[200:203]\n"
"ds_read_b64_tr_b16 v[100:101], v119 offset:42496\n"
"v_mul_u32_u24 v124, 0x120, v123\n"
"v_mfma_f32_16x16x32_bf16 v[204:207], v[84:87], a[220:223], v[204:207]\n"
"v_sub_u32 v122, v122, v124\n"
"v_mfma_f32_16x16x32_bf16 v[204:207], v[80:83], a[216:219], v[204:207]\n"
"ds_read_b64_tr_b16 v[102:103], v119 offset:46592\n"
"v_mul_lo_u32 v123, v126, v123\n"
"s_waitcnt lgkmcnt(6)\n"
"v_mfma_f32_16x16x32_bf16 v[208:211], v[88:91], a[192:195], v[208:211]\n"
"v_add_u32 v8, v122, v123\n"
"s_waitcnt lgkmcnt(4)\n"
"v_mfma_f32_16x16x32_bf16 v[208:211], v[92:95], a[196:199], v[208:211]\n"
"s_add_u32 s77, s85, 0x180000\n"
"s_add_u32 m0, s84, 0x1c000\n"
"ds_read_b64_tr_b16 v[80:81], v120 offset:34304\n"
"v_lshlrev_b32 v122, 4, v5\n"
"v_mfma_f32_16x16x32_bf16 v[212:215], v[92:95], a[204:207], v[212:215]\n"
"buffer_load_dwordx4 v118, s[56:59], s77 offen lds\n"
"v_add_u32 v122, s99, v122\n"
"v_mfma_f32_16x16x32_bf16 v[212:215], v[88:91], a[200:203], v[212:215]\n"
"ds_read_b64_tr_b16 v[82:83], v120 offset:38400\n"
"v_add_u32 v122, 0xc00, v122\n"
"v_mfma_f32_16x16x32_bf16 v[216:219], v[88:91], a[208:211], v[216:219]\n"
"v_mul_lo_u32 v123, v125, v122\n"
"v_mfma_f32_16x16x32_bf16 v[216:219], v[92:95], a[212:215], v[216:219]\n"
"s_add_u32 s77, s85, 0x180080\n"
"s_add_u32 m0, s84, 0x1c400\n"
"ds_read_b64_tr_b16 v[84:85], v120 offset:42496\n"
"v_lshrrev_b32 v123, 24, v123\n"
"v_mfma_f32_16x16x32_bf16 v[220:223], v[92:95], a[220:223], v[220:223]\n"
"buffer_load_dwordx4 v118, s[56:59], s77 offen lds\n"
"v_mul_u32_u24 v124, 0x120, v123\n"
"v_mfma_f32_16x16x32_bf16 v[220:223], v[88:91], a[216:219], v[220:223]\n"
"s_waitcnt vmcnt(10)\n"
"s_barrier\n"
"s_cmp_lg_u32 s72, 0\n"
"s_cbranch_scc1 NOSIGH_%=\n"
"s_lshl_b32 s76, s75, 5\n"
"s_add_u32 s76, s76, s74\n"
"s_lshl_b32 s76, s76, 3\n"
"s_add_u32 s76, s76, 0x1800\n"
"v_mov_b32 v16, s76\n"
"v_mov_b32 v18, s88\n"
"v_mov_b32 v19, s89\n"
"s_mov_b64 exec, 1\n"
"global_store_dwordx2 v16, v[18:19], s[92:93] sc1\n"
"s_mov_b64 exec, -1\n"
"NOSIGH_%=:\n"
"ds_read_b64_tr_b16 v[86:87], v120 offset:46592\n"
"v_sub_u32 v122, v122, v124\n"
"s_waitcnt lgkmcnt(6)\n"
"v_mfma_f32_16x16x32_bf16 v[224:227], v[96:99], a[192:195], v[224:227]\n"
"v_mul_lo_u32 v123, v126, v123\n"
"s_waitcnt lgkmcnt(4)\n"
"v_mfma_f32_16x16x32_bf16 v[224:227], v[100:103], a[196:199], v[224:227]\n"
"ds_read_b64_tr_b16 v[88:89], v119\n"
"v_add_u32 v9, v122, v123\n"
"v_mfma_f32_16x16x32_bf16 v[228:231], v[100:103], a[204:207], v[228:231]\n"
"v_lshlrev_b32 v122, 4, v5\n"
"v_mfma_f32_16x16x32_bf16 v[228:231], v[96:99], a[200:203], v[228:231]\n"
"s_add_u32 s77, s85, 0x190000\n"
"s_add_u32 m0, s84, 0x1c800\n"
"ds_read_b64_tr_b16 v[90:91], v119 offset:4096\n"
"v_add_u32 v122, s99, v122\n"
"v_mfma_f32_16x16x32_bf16 v[232:235], v[96:99], a[208:211], v[232:235]\n"
"buffer_load_dwordx4 v118, s[56:59], s77 offen lds\n"
"v_add_u32 v122, 0x1000, v122\n"
"v_mfma_f32_16x16x32_bf16 v[232:235], v[100:103], a[212:215], v[232:235]\n"
"ds_read_b64_tr_b16 v[92:93], v119 offset:8192\n"
"v_mul_lo_u32 v123, v125, v122\n"
"v_mfma_f32_16x16x32_bf16 v[236:239], v[100:103], a[220:223], v[236:239]\n"
"v_lshrrev_b32 v123, 24, v123\n"
"v_mfma_f32_16x16x32_bf16 v[236:239], v[96:99], a[216:219], v[236:239]\n"
"s_add_u32 s77, s85, 0x190080\n"
"s_add_u32 m0, s84, 0x1cc00\n"
"ds_read_b64_tr_b16 v[94:95], v119 offset:12288\n"
"v_mul_u32_u24 v124, 0x120, v123\n"
"s_waitcnt lgkmcnt(6)\n"
"v_mfma_f32_16x16x32_bf16 v[240:243], v[80:83], a[192:195], v[240:243]\n"
"buffer_load_dwordx4 v118, s[56:59], s77 offen lds\n"
"v_sub_u32 v122, v122, v124\n"
"s_waitcnt lgkmcnt(4)\n"
"v_mfma_f32_16x16x32_bf16 v[240:243], v[84:87], a[196:199], v[240:243]\n"
"ds_read_b64_tr_b16 v[96:97], v120\n"
"v_mul_lo_u32 v123, v126, v123\n"
"v_mfma_f32_16x16x32_bf16 v[244:247], v[84:87], a[204:207], v[244:247]\n"
"v_add_u32 v10, v122, v123\n"
"v_mfma_f32_16x16x32_bf16 v[244:247], v[80:83], a[200:203], v[244:247]\n"
"ds_read_b64_tr_b16 v[98:99], v120 offset:4096\n"
"v_lshrrev_b32 v122, 2, v5\n"
"v_mfma_f32_16x16x32_bf16 v[248:251], v[80:83], a[208:211], v[248:251]\n"
"v_mul_u32_u24 v122, 0x120, v122\n"
"v_mfma_f32_16x16x32_bf16 v[248:251], v[84:87], a[212:215], v[248:251]\n"
"ds_read_b64_tr_b16 v[100:101], v120 offset:8192\n"
"v_and_b32 v123, 3, v5\n"
"v_mfma_f32_16x16x32_bf16 v[252:255], v[84:87], a[220:223], v[252:255]\n"
"v_lshl_add_u32 v126, v123, 3, v122\n"
"v_mfma_f32_16x16x32_bf16 v[252:255], v[80:83], a[216:219], v[252:255]\n"
"ds_read_b64_tr_b16 v[102:103], v120 offset:12288\n"
"v_and_b32 v122, 15, v5\n"
"s_waitcnt lgkmcnt(6)\n"
"v_mfma_f32_16x16x32_bf16 v[128:131], v[88:91], a[224:227], v[128:131]\n"
"s_waitcnt lgkmcnt(4)\n"
"v_mfma_f32_16x16x32_bf16 v[128:131], v[92:95], a[228:231], v[128:131]\n"
"s_add_u32 s76, s90, 0x2800\n"
"ds_read_b64_tr_b16 v[80:81], v119 offset:512\n"
"v_mfma_f32_16x16x32_bf16 v[132:135], v[92:95], a[236:239], v[132:135]\n"
"buffer_load_dwordx4 a[192:195], v121, s[64:67], s76 offen sc1\n"
"v_mfma_f32_16x16x32_bf16 v[132:135], v[88:91], a[232:235], v[132:135]\n"
"ds_read_b64_tr_b16 v[82:83], v119 offset:4608\n"
"v_mul_u32_u24 v127, 0x480, v122\n"
"v_mfma_f32_16x16x32_bf16 v[136:139], v[88:91], a[240:243], v[136:139]\n"
"v_lshrrev_b32 v122, 4, v5\n"
"v_mfma_f32_16x16x32_bf16 v[136:139], v[92:95], a[244:247], v[136:139]\n"
"s_add_u32 s76, s90, 0x2c00\n"
"ds_read_b64_tr_b16 v[84:85], v119 offset:8704\n"
"v_lshl_add_u32 v127, v122, 3, v127\n"
"v_mfma_f32_16x16x32_bf16 v[140:143], v[92:95], a[252:255], v[140:143]\n"
"buffer_load_dwordx4 a[196:199], v121, s[64:67], s76 offen sc1\n"
"v_and_b32 v122, 1, v5\n"
"v_mfma_f32_16x16x32_bf16 v[140:143], v[88:91], a[248:251], v[140:143]\n"
"ds_read_b64_tr_b16 v[86:87], v119 offset:12800\n"
"v_lshlrev_b32 v122, 4, v122\n"
"s_waitcnt lgkmcnt(6)\n"
"v_mfma_f32_16x16x32_bf16 v[144:147], v[96:99], a[224:227], v[144:147]\n"
"v_mov_b32 v123, 0x3f80\n"
"s_waitcnt lgkmcnt(4)\n"
"v_mfma_f32_16x16x32_bf16 v[144:147], v[100:103], a[228:231], v[144:147]\n"
"ds_read_b64_tr_b16 v[88:89], v120 offset:512\n"
"v_lshlrev_b32 v123, v122, v123\n"
"v_mfma_f32_16x16x32_bf16 v[148:151], v[100:103], a[236:239], v[148:151]\n"
"v_and_b32 v122, 2, v5\n"
"v_mfma_f32_16x16x32_bf16 v[148:151], v[96:99], a[232:235], v[148:151]\n"
"s_add_u32 s76, s90, 0x7000\n"
"ds_read_b64_tr_b16 v[90:91], v120 offset:4608\n"
"v_cmp_eq_u32 vcc, 0, v122\n"
"v_mfma_f32_16x16x32_bf16 v[152:155], v[96:99], a[240:243], v[152:155]\n"
"buffer_load_dwordx4 a[200:203], v121, s[64:67], s76 offen sc1\n"
"v_cndmask_b32 v12, 0, v123\n"
"v_mfma_f32_16x16x32_bf16 v[152:155], v[100:103], a[244:247], v[152:155]\n"
"ds_read_b64_tr_b16 v[92:93], v120 offset:8704\n"
"v_sub_u32 v13, v123, v12\n"
"v_mfma_f32_16x16x32_bf16 v[156:159], v[100:103], a[252:255], v[156:159]\n"
"v_mfma_f32_16x16x32_bf16 v[156:159], v[96:99], a[248:251], v[156:159]\n"
"s_add_u32 s76, s90, 0x7400\n"
"ds_read_b64_tr_b16 v[94:95], v120 offset:12800\n"
"s_waitcnt lgkmcnt(6)\n"
"v_mfma_f32_16x16x32_bf16 v[160:163], v[80:83], a[224:227], v[160:163]\n"
"buffer_load_dwordx4 a[204:207], v121, s[64:67], s76 offen sc1\n"
"s_waitcnt lgkmcnt(4)\n"
"v_mfma_f32_16x16x32_bf16 v[160:163], v[84:87], a[228:231], v[160:163]\n"
"ds_read_b64_tr_b16 v[96:97], v119 offset:1024\n"
"v_mfma_f32_16x16x32_bf16 v[164:167], v[84:87], a[236:239], v[164:167]\n"
"v_mfma_f32_16x16x32_bf16 v[164:167], v[80:83], a[232:235], v[164:167]\n"
"ds_read_b64_tr_b16 v[98:99], v119 offset:5120\n"
"v_mfma_f32_16x16x32_bf16 v[168:171], v[80:83], a[240:243], v[168:171]\n"
"v_mfma_f32_16x16x32_bf16 v[168:171], v[84:87], a[244:247], v[168:171]\n"
"s_add_u32 s76, s90, 0xb800\n"
"ds_read_b64_tr_b16 v[100:101], v119 offset:9216\n"
"v_mfma_f32_16x16x32_bf16 v[172:175], v[84:87], a[252:255], v[172:175]\n"
"buffer_load_dwordx4 a[208:211], v121, s[64:67], s76 offen sc1\n"
"v_mfma_f32_16x16x32_bf16 v[172:175], v[80:83], a[248:251], v[172:175]\n"
"ds_read_b64_tr_b16 v[102:103], v119 offset:13312\n"
"s_waitcnt lgkmcnt(6)\n"
"v_mfma_f32_16x16x32_bf16 v[176:179], v[88:91], a[224:227], v[176:179]\n"
"s_waitcnt lgkmcnt(4)\n"
"v_mfma_f32_16x16x32_bf16 v[176:179], v[92:95], a[228:231], v[176:179]\n"
"s_add_u32 s76, s90, 0xbc00\n"
"ds_read_b64_tr_b16 v[80:81], v120 offset:1024\n"
"v_mfma_f32_16x16x32_bf16 v[180:183], v[92:95], a[236:239], v[180:183]\n"
"buffer_load_dwordx4 a[212:215], v121, s[64:67], s76 offen sc1\n"
"v_mfma_f32_16x16x32_bf16 v[180:183], v[88:91], a[232:235], v[180:183]\n"
"ds_read_b64_tr_b16 v[82:83], v120 offset:5120\n"
"v_mfma_f32_16x16x32_bf16 v[184:187], v[88:91], a[240:243], v[184:187]\n"
"v_mfma_f32_16x16x32_bf16 v[184:187], v[92:95], a[244:247], v[184:187]\n"
"ds_read_b64_tr_b16 v[84:85], v120 offset:9216\n"
"v_mfma_f32_16x16x32_bf16 v[188:191], v[92:95], a[252:255], v[188:191]\n"
"v_mfma_f32_16x16x32_bf16 v[188:191], v[88:91], a[248:251], v[188:191]\n"
"s_add_u32 s76, s90, 0x10000\n"
"ds_read_b64_tr_b16 v[86:87], v120 offset:13312\n"
"s_waitcnt lgkmcnt(6)\n"
"v_mfma_f32_16x16x32_bf16 v[192:195], v[96:99], a[224:227], v[192:195]\n"
"buffer_load_dwordx4 a[216:219], v121, s[64:67], s76 offen sc1\n"
"s_waitcnt lgkmcnt(4)\n"
"v_mfma_f32_16x16x32_bf16 v[192:195], v[100:103], a[228:231], v[192:195]\n"
"ds_read_b64_tr_b16 v[88:89], v119 offset:1536\n"
"v_mfma_f32_16x16x32_bf16 v[196:199], v[100:103], a[236:239], v[196:199]\n"
"v_mfma_f32_16x16x32_bf16 v[196:199], v[96:99], a[232:235], v[196:199]\n"
"s_add_u32 s76, s90, 0x10400\n"
"ds_read_b64_tr_b16 v[90:91], v119 offset:5632\n"
"v_mfma_f32_16x16x32_bf16 v[200:203], v[96:99], a[240:243], v[200:203]\n"
"buffer_load_dwordx4 a[220:223], v121, s[64:67], s76 offen sc1\n"
"v_mfma_f32_16x16x32_bf16 v[200:203], v[100:103], a[244:247], v[200:203]\n"
"ds_read_b64_tr_b16 v[92:93], v119 offset:9728\n"
"v_mfma_f32_16x16x32_bf16 v[204:207], v[100:103], a[252:255], v[204:207]\n"
"v_mfma_f32_16x16x32_bf16 v[204:207], v[96:99], a[248:251], v[204:207]\n"
"ds_read_b64_tr_b16 v[94:95], v119 offset:13824\n"
"s_waitcnt lgkmcnt(6)\n"
"v_mfma_f32_16x16x32_bf16 v[208:211], v[80:83], a[224:227], v[208:211]\n"
"s_waitcnt lgkmcnt(4)\n"
"v_mfma_f32_16x16x32_bf16 v[208:211], v[84:87], a[228:231], v[208:211]\n"
"s_add_u32 s77, s85, 0x200000\n"
"s_add_u32 m0, s84, 0x24000\n"
"ds_read_b64_tr_b16 v[96:97], v120 offset:1536\n"
"v_mfma_f32_16x16x32_bf16 v[212:215], v[84:87], a[236:239], v[212:215]\n"
"buffer_load_dwordx4 v118, s[56:59], s77 offen lds\n"
"v_mfma_f32_16x16x32_bf16 v[212:215], v[80:83], a[232:235], v[212:215]\n"
"ds_read_b64_tr_b16 v[98:99], v120 offset:5632\n"
"v_mfma_f32_16x16x32_bf16 v[216:219], v[80:83], a[240:243], v[216:219]\n"
"v_mfma_f32_16x16x32_bf16 v[216:219], v[84:87], a[244:247], v[216:219]\n"
"s_add_u32 s77, s85, 0x200080\n"
"s_add_u32 m0, s84, 0x24400\n"
"ds_read_b64_tr_b16 v[100:101], v120 offset:9728\n"
"v_mfma_f32_16x16x32_bf16 v[220:223], v[84:87], a[252:255], v[220:223]\n"
"buffer_load_dwordx4 v118, s[56:59], s77 offen lds\n"
"v_mfma_f32_16x16x32_bf16 v[220:223], v[80:83], a[248:251], v[220:223]\n"
"s_waitcnt vmcnt(10)\n"
"s_barrier\n"
"ds_read_b64_tr_b16 v[102:103], v120 offset:13824\n"
"s_waitcnt lgkmcnt(6)\n"
"v_mfma_f32_16x16x32_bf16 v[224:227], v[88:91], a[224:227], v[224:227]\n"
"s_waitcnt lgkmcnt(4)\n"
"v_mfma_f32_16x16x32_bf16 v[224:227], v[92:95], a[228:231], v[224:227]\n"
"ds_read_b64_tr_b16 v[80:81], v119 offset:16384\n"
"v_mfma_f32_16x16x32_bf16 v[228:231], v[92:95], a[236:239], v[228:231]\n"
"v_mfma_f32_16x16x32_bf16 v[228:231], v[88:91], a[232:235], v[228:231]\n"
"s_add_u32 s77, s85, 0x210000\n"
"s_add_u32 m0, s84, 0x24800\n"
"ds_read_b64_tr_b16 v[82:83], v119 offset:20480\n"
"v_mfma_f32_16x16x32_bf16 v[232:235], v[88:91], a[240:243], v[232:235]\n"
"buffer_load_dwordx4 v118, s[56:59], s77 offen lds\n"
"v_mfma_f32_16x16x32_bf16 v[232:235], v[92:95], a[244:247], v[232:235]\n"
"ds_read_b64_tr_b16 v[84:85], v119 offset:24576\n"
"v_mfma_f32_16x16x32_bf16 v[236:239], v[92:95], a[252:255], v[236:239]\n"
"v_mfma_f32_16x16x32_bf16 v[236:239], v[88:91], a[248:251], v[236:239]\n"
"s_add_u32 s77, s85, 0x210080\n"
"s_add_u32 m0, s84, 0x24c00\n"
"ds_read_b64_tr_b16 v[86:87], v119 offset:28672\n"
"s_waitcnt lgkmcnt(6)\n"
"v_mfma_f32_16x16x32_bf16 v[240:243], v[96:99], a[224:227], v[240:243]\n"
"buffer_load_dwordx4 v118, s[56:59], s77 offen lds\n"
"s_waitcnt lgkmcnt(4)\n"
"v_mfma_f32_16x16x32_bf16 v[240:243], v[100:103], a[228:231], v[240:243]\n"
"ds_read_b64_tr_b16 v[88:89], v120 offset:16384\n"
"v_mfma_f32_16x16x32_bf16 v[244:247], v[100:103], a[236:239], v[244:247]\n"
"v_mfma_f32_16x16x32_bf16 v[244:247], v[96:99], a[232:235], v[244:247]\n"
"ds_read_b64_tr_b16 v[90:91], v120 offset:20480\n"
"v_mfma_f32_16x16x32_bf16 v[248:251], v[96:99], a[240:243], v[248:251]\n"
"v_mfma_f32_16x16x32_bf16 v[248:251], v[100:103], a[244:247], v[248:251]\n"
"ds_read_b64_tr_b16 v[92:93], v120 offset:24576\n"
"v_mfma_f32_16x16x32_bf16 v[252:255], v[100:103], a[252:255], v[252:255]\n"
"v_mfma_f32_16x16x32_bf16 v[252:255], v[96:99], a[248:251], v[252:255]\n"
"ds_read_b64_tr_b16 v[94:95], v120 offset:28672\n"
"s_waitcnt lgkmcnt(6)\n"
"v_mfma_f32_16x16x32_bf16 v[128:131], v[80:83], a[0:3], v[128:131]\n"
"s_waitcnt lgkmcnt(4)\n"
"v_mfma_f32_16x16x32_bf16 v[128:131], v[84:87], a[4:7], v[128:131]\n"
"s_add_u32 s76, s90, 0x3000\n"
"ds_read_b64_tr_b16 v[96:97], v119 offset:16896\n"
"v_mfma_f32_16x16x32_bf16 v[132:135], v[84:87], a[12:15], v[132:135]\n"
"buffer_load_dwordx4 a[224:227], v121, s[64:67], s76 offen sc1\n"
"v_mfma_f32_16x16x32_bf16 v[132:135], v[80:83], a[8:11], v[132:135]\n"
"ds_read_b64_tr_b16 v[98:99], v119 offset:20992\n"
"v_mfma_f32_16x16x32_bf16 v[136:139], v[80:83], a[16:19], v[136:139]\n"
"v_mfma_f32_16x16x32_bf16 v[136:139], v[84:87], a[20:23], v[136:139]\n"
"s_add_u32 s76, s90, 0x3400\n"
"ds_read_b64_tr_b16 v[100:101], v119 offset:25088\n"
"v_mfma_f32_16x16x32_bf16 v[140:143], v[84:87], a[28:31], v[140:143]\n"
"buffer_load_dwordx4 a[228:231], v121, s[64:67], s76 offen sc1\n"
"v_mfma_f32_16x16x32_bf16 v[140:143], v[80:83], a[24:27], v[140:143]\n"
"ds_read_b64_tr_b16 v[102:103], v119 offset:29184\n"
"s_waitcnt lgkmcnt(6)\n"
"v_mfma_f32_16x16x32_bf16 v[144:147], v[88:91], a[0:3], v[144:147]\n"
"s_waitcnt lgkmcnt(4)\n"
"v_mfma_f32_16x16x32_bf16 v[144:147], v[92:95], a[4:7], v[144:147]\n"
"ds_read_b64_tr_b16 v[80:81], v120 offset:16896\n"
"v_mfma_f32_16x16x32_bf16 v[148:151], v[92:95], a[12:15], v[148:151]\n"
"v_mfma_f32_16x16x32_bf16 v[148:151], v[88:91], a[8:11], v[148:151]\n"
"s_add_u32 s76, s90, 0x7800\n"
"ds_read_b64_tr_b16 v[82:83], v120 offset:20992\n"
"v_mfma_f32_16x16x32_bf16 v[152:155], v[88:91], a[16:19], v[152:155]\n"
"buffer_load_dwordx4 a[232:235], v121, s[64:67], s76 offen sc1\n"
"v_mfma_f32_16x16x32_bf16 v[152:155], v[92:95], a[20:23], v[152:155]\n"
"ds_read_b64_tr_b16 v[84:85], v120 offset:25088\n"
"v_mfma_f32_16x16x32_bf16 v[156:159], v[92:95], a[28:31], v[156:159]\n"
"v_mfma_f32_16x16x32_bf16 v[156:159], v[88:91], a[24:27], v[156:159]\n"
"s_add_u32 s76, s90, 0x7c00\n"
"ds_read_b64_tr_b16 v[86:87], v120 offset:29184\n"
"s_waitcnt lgkmcnt(6)\n"
"v_mfma_f32_16x16x32_bf16 v[160:163], v[96:99], a[0:3], v[160:163]\n"
"buffer_load_dwordx4 a[236:239], v121, s[64:67], s76 offen sc1\n"
"s_waitcnt lgkmcnt(4)\n"
"v_mfma_f32_16x16x32_bf16 v[160:163], v[100:103], a[4:7], v[160:163]\n"
"ds_read_b64_tr_b16 v[88:89], v119 offset:17408\n"
"v_mfma_f32_16x16x32_bf16 v[164:167], v[100:103], a[12:15], v[164:167]\n"
"v_mfma_f32_16x16x32_bf16 v[164:167], v[96:99], a[8:11], v[164:167]\n"
"ds_read_b64_tr_b16 v[90:91], v119 offset:21504\n"
"v_mfma_f32_16x16x32_bf16 v[168:171], v[96:99], a[16:19], v[168:171]\n"
"v_mfma_f32_16x16x32_bf16 v[168:171], v[100:103], a[20:23], v[168:171]\n"
"s_add_u32 s76, s90, 0xc000\n"
"ds_read_b64_tr_b16 v[92:93], v119 offset:25600\n"
"v_mfma_f32_16x16x32_bf16 v[172:175], v[100:103], a[28:31], v[172:175]\n"
"buffer_load_dwordx4 a[240:243], v121, s[64:67], s76 offen sc1\n"
"v_mfma_f32_16x16x32_bf16 v[172:175], v[96:99], a[24:27], v[172:175]\n"
"ds_read_b64_tr_b16 v[94:95], v119 offset:29696\n"
"s_waitcnt lgkmcnt(6)\n"
"v_mfma_f32_16x16x32_bf16 v[176:179], v[80:83], a[0:3], v[176:179]\n"
"s_waitcnt lgkmcnt(4)\n"
"v_mfma_f32_16x16x32_bf16 v[176:179], v[84:87], a[4:7], v[176:179]\n"
"s_add_u32 s76, s90, 0xc400\n"
"ds_read_b64_tr_b16 v[96:97], v120 offset:17408\n"
"v_mfma_f32_16x16x32_bf16 v[180:183], v[84:87], a[12:15], v[180:183]\n"
"buffer_load_dwordx4 a[244:247], v121, s[64:67], s76 offen sc1\n"
"v_mfma_f32_16x16x32_bf16 v[180:183], v[80:83], a[8:11], v[180:183]\n"
"ds_read_b64_tr_b16 v[98:99], v120 offset:21504\n"
"v_mfma_f32_16x16x32_bf16 v[184:187], v[80:83], a[16:19], v[184:187]\n"
"v_mfma_f32_16x16x32_bf16 v[184:187], v[84:87], a[20:23], v[184:187]\n"
"ds_read_b64_tr_b16 v[100:101], v120 offset:25600\n"
"v_mfma_f32_16x16x32_bf16 v[188:191], v[84:87], a[28:31], v[188:191]\n"
"v_mfma_f32_16x16x32_bf16 v[188:191], v[80:83], a[24:27], v[188:191]\n"
"s_add_u32 s76, s90, 0x10800\n"
"ds_read_b64_tr_b16 v[102:103], v120 offset:29696\n"
"s_waitcnt lgkmcnt(6)\n"
"v_mfma_f32_16x16x32_bf16 v[192:195], v[88:91], a[0:3], v[192:195]\n"
"buffer_load_dwordx4 a[248:251], v121, s[64:67], s76 offen sc1\n"
"s_waitcnt lgkmcnt(4)\n"
"v_mfma_f32_16x16x32_bf16 v[192:195], v[92:95], a[4:7], v[192:195]\n"
"ds_read_b64_tr_b16 v[80:81], v119 offset:17920\n"
"v_mfma_f32_16x16x32_bf16 v[196:199], v[92:95], a[12:15], v[196:199]\n"
"v_mfma_f32_16x16x32_bf16 v[196:199], v[88:91], a[8:11], v[196:199]\n"
"s_add_u32 s76, s90, 0x10c00\n"
"ds_read_b64_tr_b16 v[82:83], v119 offset:22016\n"
"v_mfma_f32_16x16x32_bf16 v[200:203], v[88:91], a[16:19], v[200:203]\n"
"buffer_load_dwordx4 a[252:255], v121, s[64:67], s76 offen sc1\n"
"v_mfma_f32_16x16x32_bf16 v[200:203], v[92:95], a[20:23], v[200:203]\n"
"ds_read_b64_tr_b16 v[84:85], v119 offset:26112\n"
"v_mfma_f32_16x16x32_bf16 v[204:207], v[92:95], a[28:31], v[204:207]\n"
"v_mfma_f32_16x16x32_bf16 v[204:207], v[88:91], a[24:27], v[204:207]\n"
"ds_read_b64_tr_b16 v[86:87], v119 offset:30208\n"
"s_waitcnt lgkmcnt(6)\n"
"v_mfma_f32_16x16x32_bf16 v[208:211], v[96:99], a[0:3], v[208:211]\n"
"s_waitcnt lgkmcnt(4)\n"
"v_mfma_f32_16x16x32_bf16 v[208:211], v[100:103], a[4:7], v[208:211]\n"
"s_add_u32 s77, s85, 0x280000\n"
"s_add_u32 m0, s84, 0x20000\n"
"ds_read_b64_tr_b16 v[88:89], v120 offset:17920\n"
"v_mfma_f32_16x16x32_bf16 v[212:215], v[100:103], a[12:15], v[212:215]\n"
"buffer_load_dwordx4 v118, s[56:59], s77 offen lds\n"
"v_mfma_f32_16x16x32_bf16 v[212:215], v[96:99], a[8:11], v[212:215]\n"
"ds_read_b64_tr_b16 v[90:91], v120 offset:22016\n"
"v_mfma_f32_16x16x32_bf16 v[216:219], v[96:99], a[16:19], v[216:219]\n"
"v_mfma_f32_16x16x32_bf16 v[216:219], v[100:103], a[20:23], v[216:219]\n"
"s_add_u32 s77, s85, 0x280080\n"
"s_add_u32 m0, s84, 0x20400\n"
"ds_read_b64_tr_b16 v[92:93], v120 offset:26112\n"
"v_mfma_f32_16x16x32_bf16 v[220:223], v[100:103], a[28:31], v[220:223]\n"
"buffer_load_dwordx4 v118, s[56:59], s77 offen lds\n"
"v_mfma_f32_16x16x32_bf16 v[220:223], v[96:99], a[24:27], v[220:223]\n"
"s_waitcnt vmcnt(10)\n"
"s_barrier\n"
"ds_read_b64_tr_b16 v[94:95], v120 offset:30208\n"
"s_waitcnt lgkmcnt(6)\n"
"v_mfma_f32_16x16x32_bf16 v[224:227], v[80:83], a[0:3], v[224:227]\n"
"s_waitcnt lgkmcnt(4)\n"
"v_mfma_f32_16x16x32_bf16 v[224:227], v[84:87], a[4:7], v[224:227]\n"
"ds_read_b64_tr_b16 v[96:97], v119 offset:49152\n"
"v_mfma_f32_16x16x32_bf16 v[228:231], v[84:87], a[12:15], v[228:231]\n"
"v_mfma_f32_16x16x32_bf16 v[228:231], v[80:83], a[8:11], v[228:231]\n"
"s_add_u32 s77, s85, 0x290000\n"
"s_add_u32 m0, s84, 0x20800\n"
"ds_read_b64_tr_b16 v[98:99], v119 offset:53248\n"
"v_mfma_f32_16x16x32_bf16 v[232:235], v[80:83], a[16:19], v[232:235]\n"
"buffer_load_dwordx4 v118, s[56:59], s77 offen lds\n"
"v_mfma_f32_16x16x32_bf16 v[232:235], v[84:87], a[20:23], v[232:235]\n"
"ds_read_b64_tr_b16 v[100:101], v119 offset:57344\n"
"v_mfma_f32_16x16x32_bf16 v[236:239], v[84:87], a[28:31], v[236:239]\n"
"v_mfma_f32_16x16x32_bf16 v[236:239], v[80:83], a[24:27], v[236:239]\n"
"s_add_u32 s77, s85, 0x290080\n"
"s_add_u32 m0, s84, 0x20c00\n"
"ds_read_b64_tr_b16 v[102:103], v119 offset:61440\n"
"s_waitcnt lgkmcnt(6)\n"
"v_mfma_f32_16x16x32_bf16 v[240:243], v[88:91], a[0:3], v[240:243]\n"
"buffer_load_dwordx4 v118, s[56:59], s77 offen lds\n"
"s_waitcnt lgkmcnt(4)\n"
"v_mfma_f32_16x16x32_bf16 v[240:243], v[92:95], a[4:7], v[240:243]\n"
"ds_read_b64_tr_b16 v[80:81], v120 offset:49152\n"
"v_mfma_f32_16x16x32_bf16 v[244:247], v[92:95], a[12:15], v[244:247]\n"
"v_mfma_f32_16x16x32_bf16 v[244:247], v[88:91], a[8:11], v[244:247]\n"
"ds_read_b64_tr_b16 v[82:83], v120 offset:53248\n"
"v_mfma_f32_16x16x32_bf16 v[248:251], v[88:91], a[16:19], v[248:251]\n"
"v_mfma_f32_16x16x32_bf16 v[248:251], v[92:95], a[20:23], v[248:251]\n"
"ds_read_b64_tr_b16 v[84:85], v120 offset:57344\n"
"v_mfma_f32_16x16x32_bf16 v[252:255], v[92:95], a[28:31], v[252:255]\n"
"v_mfma_f32_16x16x32_bf16 v[252:255], v[88:91], a[24:27], v[252:255]\n"
"ds_read_b64_tr_b16 v[86:87], v120 offset:61440\n"
"s_waitcnt lgkmcnt(6)\n"
"v_mfma_f32_16x16x32_bf16 v[128:131], v[96:99], a[160:163], v[128:131]\n"
"s_waitcnt lgkmcnt(4)\n"
"v_mfma_f32_16x16x32_bf16 v[128:131], v[100:103], a[164:167], v[128:131]\n"
"s_add_u32 s76, s90, 0x3800\n"
"ds_read_b64_tr_b16 v[88:89], v119 offset:49664\n"
"v_mfma_f32_16x16x32_bf16 v[132:135], v[100:103], a[172:175], v[132:135]\n"
"buffer_load_dwordx4 a[0:3], v121, s[64:67], s76 offen sc1\n"
"v_mfma_f32_16x16x32_bf16 v[132:135], v[96:99], a[168:171], v[132:135]\n"
"ds_read_b64_tr_b16 v[90:91], v119 offset:53760\n"
"v_mfma_f32_16x16x32_bf16 v[136:139], v[96:99], a[176:179], v[136:139]\n"
"v_mfma_f32_16x16x32_bf16 v[136:139], v[100:103], a[180:183], v[136:139]\n"
"s_add_u32 s76, s90, 0x3c00\n"
"ds_read_b64_tr_b16 v[92:93], v119 offset:57856\n"
"v_mfma_f32_16x16x32_bf16 v[140:143], v[100:103], a[188:191], v[140:143]\n"
"buffer_load_dwordx4 a[4:7], v121, s[64:67], s76 offen sc1\n"
"v_mfma_f32_16x16x32_bf16 v[140:143], v[96:99], a[184:187], v[140:143]\n"
"ds_read_b64_tr_b16 v[94:95], v119 offset:61952\n"
"s_waitcnt lgkmcnt(6)\n"
"v_mfma_f32_16x16x32_bf16 v[144:147], v[80:83], a[160:163], v[144:147]\n"
"s_waitcnt lgkmcnt(4)\n"
"v_mfma_f32_16x16x32_bf16 v[144:147], v[84:87], a[164:167], v[144:147]\n"
"ds_read_b64_tr_b16 v[96:97], v120 offset:49664\n"
"v_mfma_f32_16x16x32_bf16 v[148:151], v[84:87], a[172:175], v[148:151]\n"
"v_mfma_f32_16x16x32_bf16 v[148:151], v[80:83], a[168:171], v[148:151]\n"
"s_add_u32 s76, s90, 0x8000\n"
"ds_read_b64_tr_b16 v[98:99], v120 offset:53760\n"
"v_mfma_f32_16x16x32_bf16 v[152:155], v[80:83], a[176:179], v[152:155]\n"
"buffer_load_dwordx4 a[8:11], v121, s[64:67], s76 offen sc1\n"
"v_mfma_f32_16x16x32_bf16 v[152:155], v[84:87], a[180:183], v[152:155]\n"
"ds_read_b64_tr_b16 v[100:101], v120 offset:57856\n"
"v_mfma_f32_16x16x32_bf16 v[156:159], v[84:87], a[188:191], v[156:159]\n"
"v_mfma_f32_16x16x32_bf16 v[156:159], v[80:83], a[184:187], v[156:159]\n"
"s_add_u32 s76, s90, 0x8400\n"
"ds_read_b64_tr_b16 v[102:103], v120 offset:61952\n"
"s_waitcnt lgkmcnt(6)\n"
"v_mfma_f32_16x16x32_bf16 v[160:163], v[88:91], a[160:163], v[160:163]\n"
"buffer_load_dwordx4 a[12:15], v121, s[64:67], s76 offen sc1\n"
"s_waitcnt lgkmcnt(4)\n"
"v_mfma_f32_16x16x32_bf16 v[160:163], v[92:95], a[164:167], v[160:163]\n"
"ds_read_b64_tr_b16 v[80:81], v119 offset:50176\n"
"v_mfma_f32_16x16x32_bf16 v[164:167], v[92:95], a[172:175], v[164:167]\n"
"v_mfma_f32_16x16x32_bf16 v[164:167], v[88:91], a[168:171], v[164:167]\n"
"ds_read_b64_tr_b16 v[82:83], v119 offset:54272\n"
"v_mfma_f32_16x16x32_bf16 v[168:171], v[88:91], a[176:179], v[168:171]\n"
"v_mfma_f32_16x16x32_bf16 v[168:171], v[92:95], a[180:183], v[168:171]\n"
"s_add_u32 s76, s90, 0xc800\n"
"ds_read_b64_tr_b16 v[84:85], v119 offset:58368\n"
"v_mfma_f32_16x16x32_bf16 v[172:175], v[92:95], a[188:191], v[172:175]\n"
"buffer_load_dwordx4 a[16:19], v121, s[64:67], s76 offen sc1\n"
"v_mfma_f32_16x16x32_bf16 v[172:175], v[88:91], a[184:187], v[172:175]\n"
"ds_read_b64_tr_b16 v[86:87], v119 offset:62464\n"
"s_waitcnt lgkmcnt(6)\n"
"v_mfma_f32_16x16x32_bf16 v[176:179], v[96:99], a[160:163], v[176:179]\n"
"s_waitcnt lgkmcnt(4)\n"
"v_mfma_f32_16x16x32_bf16 v[176:179], v[100:103], a[164:167], v[176:179]\n"
"s_add_u32 s76, s90, 0xcc00\n"
"ds_read_b64_tr_b16 v[88:89], v120 offset:50176\n"
"v_mfma_f32_16x16x32_bf16 v[180:183], v[100:103], a[172:175], v[180:183]\n"
"buffer_load_dwordx4 a[20:23], v121, s[64:67], s76 offen sc1\n"
"v_mfma_f32_16x16x32_bf16 v[180:183], v[96:99], a[168:171], v[180:183]\n"
"ds_read_b64_tr_b16 v[90:91], v120 offset:54272\n"
"v_mfma_f32_16x16x32_bf16 v[184:187], v[96:99], a[176:179], v[184:187]\n"
"v_mfma_f32_16x16x32_bf16 v[184:187], v[100:103], a[180:183], v[184:187]\n"
"ds_read_b64_tr_b16 v[92:93], v120 offset:58368\n"
"v_mfma_f32_16x16x32_bf16 v[188:191], v[100:103], a[188:191], v[188:191]\n"
"v_mfma_f32_16x16x32_bf16 v[188:191], v[96:99], a[184:187], v[188:191]\n"
"s_add_u32 s76, s90, 0x11000\n"
"ds_read_b64_tr_b16 v[94:95], v120 offset:62464\n"
"s_waitcnt lgkmcnt(6)\n"
"v_mfma_f32_16x16x32_bf16 v[192:195], v[80:83], a[160:163], v[192:195]\n"
"buffer_load_dwordx4 a[24:27], v121, s[64:67], s76 offen sc1\n"
"s_waitcnt lgkmcnt(4)\n"
"v_mfma_f32_16x16x32_bf16 v[192:195], v[84:87], a[164:167], v[192:195]\n"
"ds_read_b64_tr_b16 v[96:97], v119 offset:50688\n"
"v_mfma_f32_16x16x32_bf16 v[196:199], v[84:87], a[172:175], v[196:199]\n"
"v_mfma_f32_16x16x32_bf16 v[196:199], v[80:83], a[168:171], v[196:199]\n"
"s_add_u32 s76, s90, 0x11400\n"
"ds_read_b64_tr_b16 v[98:99], v119 offset:54784\n"
"v_mfma_f32_16x16x32_bf16 v[200:203], v[80:83], a[176:179], v[200:203]\n"
"buffer_load_dwordx4 a[28:31], v121, s[64:67], s76 offen sc1\n"
"v_mfma_f32_16x16x32_bf16 v[200:203], v[84:87], a[180:183], v[200:203]\n"
"ds_read_b64_tr_b16 v[100:101], v119 offset:58880\n"
"v_mfma_f32_16x16x32_bf16 v[204:207], v[84:87], a[188:191], v[204:207]\n"
"v_mfma_f32_16x16x32_bf16 v[204:207], v[80:83], a[184:187], v[204:207]\n"
"ds_read_b64_tr_b16 v[102:103], v119 offset:62976\n"
"s_waitcnt lgkmcnt(6)\n"
"v_mfma_f32_16x16x32_bf16 v[208:211], v[88:91], a[160:163], v[208:211]\n"
"s_waitcnt lgkmcnt(4)\n"
"v_mfma_f32_16x16x32_bf16 v[208:211], v[92:95], a[164:167], v[208:211]\n"
"s_add_u32 s77, s85, 0x300000\n"
"s_add_u32 m0, s84, 0x18000\n"
"ds_read_b64_tr_b16 v[80:81], v120 offset:50688\n"
"v_mfma_f32_16x16x32_bf16 v[212:215], v[92:95], a[172:175], v[212:215]\n"
"buffer_load_dwordx4 v118, s[56:59], s77 offen lds\n"
"v_mfma_f32_16x16x32_bf16 v[212:215], v[88:91], a[168:171], v[212:215]\n"
"ds_read_b64_tr_b16 v[82:83], v120 offset:54784\n"
"v_mfma_f32_16x16x32_bf16 v[216:219], v[88:91], a[176:179], v[216:219]\n"
"v_mfma_f32_16x16x32_bf16 v[216:219], v[92:95], a[180:183], v[216:219]\n"
"s_add_u32 s77, s85, 0x300080\n"
"s_add_u32 m0, s84, 0x18400\n"
"ds_read_b64_tr_b16 v[84:85], v120 offset:58880\n"
"v_mfma_f32_16x16x32_bf16 v[220:223], v[92:95], a[188:191], v[220:223]\n"
"buffer_load_dwordx4 v118, s[56:59], s77 offen lds\n"
"v_mfma_f32_16x16x32_bf16 v[220:223], v[88:91], a[184:187], v[220:223]\n"
"s_waitcnt vmcnt(10)\n"
"s_barrier\n"
"ds_read_b64_tr_b16 v[86:87], v120 offset:62976\n"
"s_waitcnt lgkmcnt(6)\n"
"v_mfma_f32_16x16x32_bf16 v[224:227], v[96:99], a[160:163], v[224:227]\n"
"s_waitcnt lgkmcnt(4)\n"
"v_mfma_f32_16x16x32_bf16 v[224:227], v[100:103], a[164:167], v[224:227]\n"
"ds_read_b64_tr_b16 v[88:89], v119 offset:32768\n"
"v_mfma_f32_16x16x32_bf16 v[228:231], v[100:103], a[172:175], v[228:231]\n"
"v_mfma_f32_16x16x32_bf16 v[228:231], v[96:99], a[168:171], v[228:231]\n"
"s_add_u32 s77, s85, 0x310000\n"
"s_add_u32 m0, s84, 0x18800\n"
"ds_read_b64_tr_b16 v[90:91], v119 offset:36864\n"
"v_mfma_f32_16x16x32_bf16 v[232:235], v[96:99], a[176:179], v[232:235]\n"
"buffer_load_dwordx4 v118, s[56:59], s77 offen lds\n"
"v_mfma_f32_16x16x32_bf16 v[232:235], v[100:103], a[180:183], v[232:235]\n"
"ds_read_b64_tr_b16 v[92:93], v119 offset:40960\n"
"v_mfma_f32_16x16x32_bf16 v[236:239], v[100:103], a[188:191], v[236:239]\n"
"v_mfma_f32_16x16x32_bf16 v[236:239], v[96:99], a[184:187], v[236:239]\n"
"s_add_u32 s77, s85, 0x310080\n"
"s_add_u32 m0, s84, 0x18c00\n"
"ds_read_b64_tr_b16 v[94:95], v119 offset:45056\n"
"s_waitcnt lgkmcnt(6)\n"
"v_mfma_f32_16x16x32_bf16 v[240:243], v[80:83], a[160:163], v[240:243]\n"
"buffer_load_dwordx4 v118, s[56:59], s77 offen lds\n"
"s_waitcnt lgkmcnt(4)\n"
"v_mfma_f32_16x16x32_bf16 v[240:243], v[84:87], a[164:167], v[240:243]\n"
"ds_read_b64_tr_b16 v[96:97], v120 offset:32768\n"
"v_mfma_f32_16x16x32_bf16 v[244:247], v[84:87], a[172:175], v[244:247]\n"
"v_mfma_f32_16x16x32_bf16 v[244:247], v[80:83], a[168:171], v[244:247]\n"
"ds_read_b64_tr_b16 v[98:99], v120 offset:36864\n"
"v_mfma_f32_16x16x32_bf16 v[248:251], v[80:83], a[176:179], v[248:251]\n"
"v_mfma_f32_16x16x32_bf16 v[248:251], v[84:87], a[180:183], v[248:251]\n"
"ds_read_b64_tr_b16 v[100:101], v120 offset:40960\n"
"v_mfma_f32_16x16x32_bf16 v[252:255], v[84:87], a[188:191], v[252:255]\n"
"v_mfma_f32_16x16x32_bf16 v[252:255], v[80:83], a[184:187], v[252:255]\n"
"ds_read_b64_tr_b16 v[102:103], v120 offset:45056\n"
"s_waitcnt lgkmcnt(6)\n"
"v_mfma_f32_16x16x32_bf16 v[128:131], v[88:91], a[192:195], v[128:131]\n"
"s_waitcnt lgkmcnt(4)\n"
"v_mfma_f32_16x16x32_bf16 v[128:131], v[92:95], a[196:199], v[128:131]\n"
"s_add_u32 s76, s90, 0x4000\n"
"ds_read_b64_tr_b16 v[80:81], v119 offset:33280\n"
"v_mfma_f32_16x16x32_bf16 v[132:135], v[92:95], a[204:207], v[132:135]\n"
"buffer_load_dwordx4 a[160:163], v121, s[64:67], s76 offen sc1\n"
"v_mfma_f32_16x16x32_bf16 v[132:135], v[88:91], a[200:203], v[132:135]\n"
"ds_read_b64_tr_b16 v[82:83], v119 offset:37376\n"
"v_mfma_f32_16x16x32_bf16 v[136:139], v[88:91], a[208:211], v[136:139]\n"
"v_mfma_f32_16x16x32_bf16 v[136:139], v[92:95], a[212:215], v[136:139]\n"
"s_add_u32 s76, s90, 0x4400\n"
"ds_read_b64_tr_b16 v[84:85], v119 offset:41472\n"
"v_mfma_f32_16x16x32_bf16 v[140:143], v[92:95], a[220:223], v[140:143]\n"
"buffer_load_dwordx4 a[164:167], v121, s[64:67], s76 offen sc1\n"
"v_mfma_f32_16x16x32_bf16 v[140:143], v[88:91], a[216:219], v[140:143]\n"
"ds_read_b64_tr_b16 v[86:87], v119 offset:45568\n"
"s_waitcnt lgkmcnt(6)\n"
"v_mfma_f32_16x16x32_bf16 v[144:147], v[96:99], a[192:195], v[144:147]\n"
"s_waitcnt lgkmcnt(4)\n"
"v_mfma_f32_16x16x32_bf16 v[144:147], v[100:103], a[196:199], v[144:147]\n"
"ds_read_b64_tr_b16 v[88:89], v120 offset:33280\n"
"v_mfma_f32_16x16x32_bf16 v[148:151], v[100:103], a[204:207], v[148:151]\n"
"v_mfma_f32_16x16x32_bf16 v[148:151], v[96:99], a[200:203], v[148:151]\n"
"s_add_u32 s76, s90, 0x8800\n"
"ds_read_b64_tr_b16 v[90:91], v120 offset:37376\n"
"v_mfma_f32_16x16x32_bf16 v[152:155], v[96:99], a[208:211], v[152:155]\n"
"buffer_load_dwordx4 a[168:171], v121, s[64:67], s76 offen sc1\n"
"v_mfma_f32_16x16x32_bf16 v[152:155], v[100:103], a[212:215], v[152:155]\n"
"ds_read_b64_tr_b16 v[92:93], v120 offset:41472\n"
"v_mfma_f32_16x16x32_bf16 v[156:159], v[100:103], a[220:223], v[156:159]\n"
"v_mfma_f32_16x16x32_bf16 v[156:159], v[96:99], a[216:219], v[156:159]\n"
"s_add_u32 s76, s90, 0x8c00\n"
"ds_read_b64_tr_b16 v[94:95], v120 offset:45568\n"
"s_waitcnt lgkmcnt(6)\n"
"v_mfma_f32_16x16x32_bf16 v[160:163], v[80:83], a[192:195], v[160:163]\n"
"buffer_load_dwordx4 a[172:175], v121, s[64:67], s76 offen sc1\n"
"s_waitcnt lgkmcnt(4)\n"
"v_mfma_f32_16x16x32_bf16 v[160:163], v[84:87], a[196:199], v[160:163]\n"
"ds_read_b64_tr_b16 v[96:97], v119 offset:33792\n"
"v_mfma_f32_16x16x32_bf16 v[164:167], v[84:87], a[204:207], v[164:167]\n"
"v_mfma_f32_16x16x32_bf16 v[164:167], v[80:83], a[200:203], v[164:167]\n"
"ds_read_b64_tr_b16 v[98:99], v119 offset:37888\n"
"v_mfma_f32_16x16x32_bf16 v[168:171], v[80:83], a[208:211], v[168:171]\n"
"v_mfma_f32_16x16x32_bf16 v[168:171], v[84:87], a[212:215], v[168:171]\n"
"s_add_u32 s76, s90, 0xd000\n"
"ds_read_b64_tr_b16 v[100:101], v119 offset:41984\n"
"v_mfma_f32_16x16x32_bf16 v[172:175], v[84:87], a[220:223], v[172:175]\n"
"buffer_load_dwordx4 a[176:179], v121, s[64:67], s76 offen sc1\n"
"v_mfma_f32_16x16x32_bf16 v[172:175], v[80:83], a[216:219], v[172:175]\n"
"ds_read_b64_tr_b16 v[102:103], v119 offset:46080\n"
"s_waitcnt lgkmcnt(6)\n"
"v_mfma_f32_16x16x32_bf16 v[176:179], v[88:91], a[192:195], v[176:179]\n"
"s_waitcnt lgkmcnt(4)\n"
"v_mfma_f32_16x16x32_bf16 v[176:179], v[92:95], a[196:199], v[176:179]\n"
"s_add_u32 s76, s90, 0xd400\n"
"ds_read_b64_tr_b16 v[80:81], v120 offset:33792\n"
"v_mfma_f32_16x16x32_bf16 v[180:183], v[92:95], a[204:207], v[180:183]\n"
"buffer_load_dwordx4 a[180:183], v121, s[64:67], s76 offen sc1\n"
"v_mfma_f32_16x16x32_bf16 v[180:183], v[88:91], a[200:203], v[180:183]\n"
"ds_read_b64_tr_b16 v[82:83], v120 offset:37888\n"
"v_mfma_f32_16x16x32_bf16 v[184:187], v[88:91], a[208:211], v[184:187]\n"
"v_mfma_f32_16x16x32_bf16 v[184:187], v[92:95], a[212:215], v[184:187]\n"
"ds_read_b64_tr_b16 v[84:85], v120 offset:41984\n"
"v_mfma_f32_16x16x32_bf16 v[188:191], v[92:95], a[220:223], v[188:191]\n"
"v_mfma_f32_16x16x32_bf16 v[188:191], v[88:91], a[216:219], v[188:191]\n"
"s_add_u32 s76, s90, 0x11800\n"
"ds_read_b64_tr_b16 v[86:87], v120 offset:46080\n"
"s_waitcnt lgkmcnt(6)\n"
"v_mfma_f32_16x16x32_bf16 v[192:195], v[96:99], a[192:195], v[192:195]\n"
"buffer_load_dwordx4 a[184:187], v121, s[64:67], s76 offen sc1\n"
"s_waitcnt lgkmcnt(4)\n"
"v_mfma_f32_16x16x32_bf16 v[192:195], v[100:103], a[196:199], v[192:195]\n"
"ds_read_b64_tr_b16 v[88:89], v119 offset:34304\n"
"v_mfma_f32_16x16x32_bf16 v[196:199], v[100:103], a[204:207], v[196:199]\n"
"v_mfma_f32_16x16x32_bf16 v[196:199], v[96:99], a[200:203], v[196:199]\n"
"s_add_u32 s76, s90, 0x11c00\n"
"ds_read_b64_tr_b16 v[90:91], v119 offset:38400\n"
"v_mfma_f32_16x16x32_bf16 v[200:203], v[96:99], a[208:211], v[200:203]\n"
"buffer_load_dwordx4 a[188:191], v121, s[64:67], s76 offen sc1\n"
"v_mfma_f32_16x16x32_bf16 v[200:203], v[100:103], a[212:215], v[200:203]\n"
"ds_read_b64_tr_b16 v[92:93], v119 offset:42496\n"
"v_mfma_f32_16x16x32_bf16 v[204:207], v[100:103], a[220:223], v[204:207]\n"
"v_mfma_f32_16x16x32_bf16 v[204:207], v[96:99], a[216:219], v[204:207]\n"
"ds_read_b64_tr_b16 v[94:95], v119 offset:46592\n"
"s_waitcnt lgkmcnt(6)\n"
"v_mfma_f32_16x16x32_bf16 v[208:211], v[80:83], a[192:195], v[208:211]\n"
"s_waitcnt lgkmcnt(4)\n"
"v_mfma_f32_16x16x32_bf16 v[208:211], v[84:87], a[196:199], v[208:211]\n"
"s_add_u32 s77, s85, 0x380000\n"
"s_add_u32 m0, s84, 0x1c000\n"
"ds_read_b64_tr_b16 v[96:97], v120 offset:34304\n"
"v_mfma_f32_16x16x32_bf16 v[212:215], v[84:87], a[204:207], v[212:215]\n"
"buffer_load_dwordx4 v118, s[56:59], s77 offen lds\n"
"v_mfma_f32_16x16x32_bf16 v[212:215], v[80:83], a[200:203], v[212:215]\n"
"ds_read_b64_tr_b16 v[98:99], v120 offset:38400\n"
"v_mfma_f32_16x16x32_bf16 v[216:219], v[80:83], a[208:211], v[216:219]\n"
"v_mfma_f32_16x16x32_bf16 v[216:219], v[84:87], a[212:215], v[216:219]\n"
"s_add_u32 s77, s85, 0x380080\n"
"s_add_u32 m0, s84, 0x1c400\n"
"ds_read_b64_tr_b16 v[100:101], v120 offset:42496\n"
"v_mfma_f32_16x16x32_bf16 v[220:223], v[84:87], a[220:223], v[220:223]\n"
"buffer_load_dwordx4 v118, s[56:59], s77 offen lds\n"
"v_mfma_f32_16x16x32_bf16 v[220:223], v[80:83], a[216:219], v[220:223]\n"
"s_waitcnt vmcnt(10)\n"
"s_barrier\n"
"ds_read_b64_tr_b16 v[102:103], v120 offset:46592\n"
"s_waitcnt lgkmcnt(6)\n"
"v_mfma_f32_16x16x32_bf16 v[224:227], v[88:91], a[192:195], v[224:227]\n"
"s_waitcnt lgkmcnt(4)\n"
"v_mfma_f32_16x16x32_bf16 v[224:227], v[92:95], a[196:199], v[224:227]\n"
"ds_read_b64_tr_b16 v[80:81], v119\n"
"v_mfma_f32_16x16x32_bf16 v[228:231], v[92:95], a[204:207], v[228:231]\n"
"v_mfma_f32_16x16x32_bf16 v[228:231], v[88:91], a[200:203], v[228:231]\n"
"s_add_u32 s77, s85, 0x390000\n"
"s_add_u32 m0, s84, 0x1c800\n"
"ds_read_b64_tr_b16 v[82:83], v119 offset:4096\n"
"v_mfma_f32_16x16x32_bf16 v[232:235], v[88:91], a[208:211], v[232:235]\n"
"buffer_load_dwordx4 v118, s[56:59], s77 offen lds\n"
"v_mfma_f32_16x16x32_bf16 v[232:235], v[92:95], a[212:215], v[232:235]\n"
"ds_read_b64_tr_b16 v[84:85], v119 offset:8192\n"
"v_mfma_f32_16x16x32_bf16 v[236:239], v[92:95], a[220:223], v[236:239]\n"
"v_mfma_f32_16x16x32_bf16 v[236:239], v[88:91], a[216:219], v[236:239]\n"
"s_add_u32 s77, s85, 0x390080\n"
"s_add_u32 m0, s84, 0x1cc00\n"
"ds_read_b64_tr_b16 v[86:87], v119 offset:12288\n"
"s_waitcnt lgkmcnt(6)\n"
"v_mfma_f32_16x16x32_bf16 v[240:243], v[96:99], a[192:195], v[240:243]\n"
"buffer_load_dwordx4 v118, s[56:59], s77 offen lds\n"
"s_waitcnt lgkmcnt(4)\n"
"v_mfma_f32_16x16x32_bf16 v[240:243], v[100:103], a[196:199], v[240:243]\n"
"ds_read_b64_tr_b16 v[88:89], v120\n"
"v_mfma_f32_16x16x32_bf16 v[244:247], v[100:103], a[204:207], v[244:247]\n"
"v_mfma_f32_16x16x32_bf16 v[244:247], v[96:99], a[200:203], v[244:247]\n"
"ds_read_b64_tr_b16 v[90:91], v120 offset:4096\n"
"v_mfma_f32_16x16x32_bf16 v[248:251], v[96:99], a[208:211], v[248:251]\n"
"v_mfma_f32_16x16x32_bf16 v[248:251], v[100:103], a[212:215], v[248:251]\n"
"ds_read_b64_tr_b16 v[92:93], v120 offset:8192\n"
"v_mfma_f32_16x16x32_bf16 v[252:255], v[100:103], a[220:223], v[252:255]\n"
"v_mfma_f32_16x16x32_bf16 v[252:255], v[96:99], a[216:219], v[252:255]\n"
"ds_read_b64_tr_b16 v[94:95], v120 offset:12288\n"
"s_waitcnt lgkmcnt(6)\n"
"v_mfma_f32_16x16x32_bf16 v[128:131], v[80:83], a[224:227], v[128:131]\n"
"s_waitcnt lgkmcnt(4)\n"
"v_mfma_f32_16x16x32_bf16 v[128:131], v[84:87], a[228:231], v[128:131]\n"
"s_add_u32 s77, s85, 0x400000\n"
"s_add_u32 m0, s84, 0x24000\n"
"ds_read_b64_tr_b16 v[96:97], v119 offset:512\n"
"v_mfma_f32_16x16x32_bf16 v[132:135], v[84:87], a[236:239], v[132:135]\n"
"buffer_load_dwordx4 v118, s[56:59], s77 offen lds\n"
"v_mfma_f32_16x16x32_bf16 v[132:135], v[80:83], a[232:235], v[132:135]\n"
"ds_read_b64_tr_b16 v[98:99], v119 offset:4608\n"
"v_mfma_f32_16x16x32_bf16 v[136:139], v[80:83], a[240:243], v[136:139]\n"
"v_mfma_f32_16x16x32_bf16 v[136:139], v[84:87], a[244:247], v[136:139]\n"
"s_add_u32 s77, s85, 0x400080\n"
"s_add_u32 m0, s84, 0x24400\n"
"ds_read_b64_tr_b16 v[100:101], v119 offset:8704\n"
"v_mfma_f32_16x16x32_bf16 v[140:143], v[84:87], a[252:255], v[140:143]\n"
"buffer_load_dwordx4 v118, s[56:59], s77 offen lds\n"
"v_mfma_f32_16x16x32_bf16 v[140:143], v[80:83], a[248:251], v[140:143]\n"
"ds_read_b64_tr_b16 v[102:103], v119 offset:12800\n"
"s_waitcnt lgkmcnt(6)\n"
"v_mfma_f32_16x16x32_bf16 v[144:147], v[88:91], a[224:227], v[144:147]\n"
"s_waitcnt lgkmcnt(4)\n"
"v_mfma_f32_16x16x32_bf16 v[144:147], v[92:95], a[228:231], v[144:147]\n"
"ds_read_b64_tr_b16 v[80:81], v120 offset:512\n"
"v_mfma_f32_16x16x32_bf16 v[148:151], v[92:95], a[236:239], v[148:151]\n"
"v_mfma_f32_16x16x32_bf16 v[148:151], v[88:91], a[232:235], v[148:151]\n"
"s_add_u32 s77, s85, 0x410000\n"
"s_add_u32 m0, s84, 0x24800\n"
"ds_read_b64_tr_b16 v[82:83], v120 offset:4608\n"
"v_mfma_f32_16x16x32_bf16 v[152:155], v[88:91], a[240:243], v[152:155]\n"
"buffer_load_dwordx4 v118, s[56:59], s77 offen lds\n"
"v_mfma_f32_16x16x32_bf16 v[152:155], v[92:95], a[244:247], v[152:155]\n"
"ds_read_b64_tr_b16 v[84:85], v120 offset:8704\n"
"v_mfma_f32_16x16x32_bf16 v[156:159], v[92:95], a[252:255], v[156:159]\n"
"v_mfma_f32_16x16x32_bf16 v[156:159], v[88:91], a[248:251], v[156:159]\n"
"s_add_u32 s77, s85, 0x410080\n"
"s_add_u32 m0, s84, 0x24c00\n"
"ds_read_b64_tr_b16 v[86:87], v120 offset:12800\n"
"s_waitcnt lgkmcnt(6)\n"
"v_mfma_f32_16x16x32_bf16 v[160:163], v[96:99], a[224:227], v[160:163]\n"
"buffer_load_dwordx4 v118, s[56:59], s77 offen lds\n"
"s_waitcnt lgkmcnt(4)\n"
"v_mfma_f32_16x16x32_bf16 v[160:163], v[100:103], a[228:231], v[160:163]\n"
"ds_read_b64_tr_b16 v[88:89], v119 offset:1024\n"
"v_mfma_f32_16x16x32_bf16 v[164:167], v[100:103], a[236:239], v[164:167]\n"
"v_mfma_f32_16x16x32_bf16 v[164:167], v[96:99], a[232:235], v[164:167]\n"
"ds_read_b64_tr_b16 v[90:91], v119 offset:5120\n"
"v_mfma_f32_16x16x32_bf16 v[168:171], v[96:99], a[240:243], v[168:171]\n"
"v_mfma_f32_16x16x32_bf16 v[168:171], v[100:103], a[244:247], v[168:171]\n"
"s_add_u32 s77, s94, 0\n"
"s_add_u32 m0, s78, 0\n"
"ds_read_b64_tr_b16 v[92:93], v119 offset:9216\n"
"v_mfma_f32_16x16x32_bf16 v[172:175], v[100:103], a[252:255], v[172:175]\n"
"buffer_load_dwordx4 v110, s[40:43], s77 offen sc1 lds\n"
"v_mfma_f32_16x16x32_bf16 v[172:175], v[96:99], a[248:251], v[172:175]\n"
"ds_read_b64_tr_b16 v[94:95], v119 offset:13312\n"
"s_waitcnt lgkmcnt(6)\n"
"v_mfma_f32_16x16x32_bf16 v[176:179], v[80:83], a[224:227], v[176:179]\n"
"s_waitcnt lgkmcnt(4)\n"
"v_mfma_f32_16x16x32_bf16 v[176:179], v[84:87], a[228:231], v[176:179]\n"
"s_add_u32 s77, s94, 0\n"
"s_add_u32 m0, s78, 0x400\n"
"ds_read_b64_tr_b16 v[96:97], v120 offset:1024\n"
"v_mfma_f32_16x16x32_bf16 v[180:183], v[84:87], a[236:239], v[180:183]\n"
"buffer_load_dwordx4 v111, s[40:43], s77 offen sc1 lds\n"
"v_mfma_f32_16x16x32_bf16 v[180:183], v[80:83], a[232:235], v[180:183]\n"
"ds_read_b64_tr_b16 v[98:99], v120 offset:5120\n"
"v_mfma_f32_16x16x32_bf16 v[184:187], v[80:83], a[240:243], v[184:187]\n"
"v_mfma_f32_16x16x32_bf16 v[184:187], v[84:87], a[244:247], v[184:187]\n"
"ds_read_b64_tr_b16 v[100:101], v120 offset:9216\n"
"v_mfma_f32_16x16x32_bf16 v[188:191], v[84:87], a[252:255], v[188:191]\n"
"v_mfma_f32_16x16x32_bf16 v[188:191], v[80:83], a[248:251], v[188:191]\n"
"s_add_u32 s77, s94, 0\n"
"s_add_u32 m0, s78, 0x800\n"
"ds_read_b64_tr_b16 v[102:103], v120 offset:13312\n"
"s_waitcnt lgkmcnt(6)\n"
"v_mfma_f32_16x16x32_bf16 v[192:195], v[88:91], a[224:227], v[192:195]\n"
"buffer_load_dwordx4 v112, s[40:43], s77 offen sc1 lds\n"
"s_waitcnt lgkmcnt(4)\n"
"v_mfma_f32_16x16x32_bf16 v[192:195], v[92:95], a[228:231], v[192:195]\n"
"ds_read_b64_tr_b16 v[80:81], v119 offset:1536\n"
"v_mfma_f32_16x16x32_bf16 v[196:199], v[92:95], a[236:239], v[196:199]\n"
"v_mfma_f32_16x16x32_bf16 v[196:199], v[88:91], a[232:235], v[196:199]\n"
"s_add_u32 s77, s94, 0\n"
"s_add_u32 m0, s78, 0xc00\n"
"ds_read_b64_tr_b16 v[82:83], v119 offset:5632\n"
"v_mfma_f32_16x16x32_bf16 v[200:203], v[88:91], a[240:243], v[200:203]\n"
"buffer_load_dwordx4 v113, s[40:43], s77 offen sc1 lds\n"
"v_mfma_f32_16x16x32_bf16 v[200:203], v[92:95], a[244:247], v[200:203]\n"
"ds_read_b64_tr_b16 v[84:85], v119 offset:9728\n"
"v_mfma_f32_16x16x32_bf16 v[204:207], v[92:95], a[252:255], v[204:207]\n"
"v_mfma_f32_16x16x32_bf16 v[204:207], v[88:91], a[248:251], v[204:207]\n"
"ds_read_b64_tr_b16 v[86:87], v119 offset:13824\n"
"s_waitcnt lgkmcnt(6)\n"
"v_mfma_f32_16x16x32_bf16 v[208:211], v[96:99], a[224:227], v[208:211]\n"
"s_waitcnt lgkmcnt(4)\n"
"v_mfma_f32_16x16x32_bf16 v[208:211], v[100:103], a[228:231], v[208:211]\n"
"s_add_u32 s77, s95, 0\n"
"s_add_u32 m0, s78, 0x10000\n"
"ds_read_b64_tr_b16 v[88:89], v120 offset:1536\n"
"v_mfma_f32_16x16x32_bf16 v[212:215], v[100:103], a[236:239], v[212:215]\n"
"buffer_load_dwordx4 v114, s[44:47], s77 offen sc1 lds\n"
"v_mfma_f32_16x16x32_bf16 v[212:215], v[96:99], a[232:235], v[212:215]\n"
"ds_read_b64_tr_b16 v[90:91], v120 offset:5632\n"
"v_mfma_f32_16x16x32_bf16 v[216:219], v[96:99], a[240:243], v[216:219]\n"
"v_mfma_f32_16x16x32_bf16 v[216:219], v[100:103], a[244:247], v[216:219]\n"
"s_add_u32 s77, s95, 0\n"
"s_add_u32 m0, s78, 0x10400\n"
"ds_read_b64_tr_b16 v[92:93], v120 offset:9728\n"
"v_mfma_f32_16x16x32_bf16 v[220:223], v[100:103], a[252:255], v[220:223]\n"
"buffer_load_dwordx4 v115, s[44:47], s77 offen sc1 lds\n"
"v_mfma_f32_16x16x32_bf16 v[220:223], v[96:99], a[248:251], v[220:223]\n"
"s_waitcnt vmcnt(10)\n"
"s_barrier\n"
"ds_read_b64_tr_b16 v[94:95], v120 offset:13824\n"
"s_waitcnt lgkmcnt(6)\n"
"v_mfma_f32_16x16x32_bf16 v[224:227], v[80:83], a[224:227], v[224:227]\n"
"s_waitcnt lgkmcnt(4)\n"
"v_mfma_f32_16x16x32_bf16 v[224:227], v[84:87], a[228:231], v[224:227]\n"
"ds_read_b64_tr_b16 v[96:97], v119 offset:16384\n"
"v_mfma_f32_16x16x32_bf16 v[228:231], v[84:87], a[236:239], v[228:231]\n"
"v_mfma_f32_16x16x32_bf16 v[228:231], v[80:83], a[232:235], v[228:231]\n"
"s_add_u32 s77, s95, 0\n"
"s_add_u32 m0, s78, 0x10800\n"
"ds_read_b64_tr_b16 v[98:99], v119 offset:20480\n"
"v_mfma_f32_16x16x32_bf16 v[232:235], v[80:83], a[240:243], v[232:235]\n"
"buffer_load_dwordx4 v116, s[44:47], s77 offen sc1 lds\n"
"v_mfma_f32_16x16x32_bf16 v[232:235], v[84:87], a[244:247], v[232:235]\n"
"ds_read_b64_tr_b16 v[100:101], v119 offset:24576\n"
"v_mfma_f32_16x16x32_bf16 v[236:239], v[84:87], a[252:255], v[236:239]\n"
"v_mfma_f32_16x16x32_bf16 v[236:239], v[80:83], a[248:251], v[236:239]\n"
"s_add_u32 s77, s95, 0\n"
"s_add_u32 m0, s78, 0x10c00\n"
"ds_read_b64_tr_b16 v[102:103], v119 offset:28672\n"
"s_waitcnt lgkmcnt(6)\n"
"v_mfma_f32_16x16x32_bf16 v[240:243], v[88:91], a[224:227], v[240:243]\n"
"buffer_load_dwordx4 v117, s[44:47], s77 offen sc1 lds\n"
"s_waitcnt lgkmcnt(4)\n"
"v_mfma_f32_16x16x32_bf16 v[240:243], v[92:95], a[228:231], v[240:243]\n"
"ds_read_b64_tr_b16 v[80:81], v120 offset:16384\n"
"v_mfma_f32_16x16x32_bf16 v[244:247], v[92:95], a[236:239], v[244:247]\n"
"v_mfma_f32_16x16x32_bf16 v[244:247], v[88:91], a[232:235], v[244:247]\n"
"ds_read_b64_tr_b16 v[82:83], v120 offset:20480\n"
"v_mfma_f32_16x16x32_bf16 v[248:251], v[88:91], a[240:243], v[248:251]\n"
"v_mfma_f32_16x16x32_bf16 v[248:251], v[92:95], a[244:247], v[248:251]\n"
"ds_read_b64_tr_b16 v[84:85], v120 offset:24576\n"
"v_mfma_f32_16x16x32_bf16 v[252:255], v[92:95], a[252:255], v[252:255]\n"
"v_mfma_f32_16x16x32_bf16 v[252:255], v[88:91], a[248:251], v[252:255]\n"
"ds_read_b64_tr_b16 v[86:87], v120 offset:28672\n"
"s_waitcnt lgkmcnt(6)\n"
"v_mfma_f32_16x16x32_bf16 v[128:131], v[96:99], a[0:3], v[128:131]\n"
"s_waitcnt lgkmcnt(4)\n"
"v_mfma_f32_16x16x32_bf16 v[128:131], v[100:103], a[4:7], v[128:131]\n"
"s_add_u32 s77, s94, 0x4000\n"
"s_add_u32 m0, s78, 0x4000\n"
"ds_read_b64_tr_b16 v[88:89], v119 offset:16896\n"
"v_mfma_f32_16x16x32_bf16 v[132:135], v[100:103], a[12:15], v[132:135]\n"
"buffer_load_dwordx4 v110, s[40:43], s77 offen sc1 lds\n"
"v_mfma_f32_16x16x32_bf16 v[132:135], v[96:99], a[8:11], v[132:135]\n"
"ds_read_b64_tr_b16 v[90:91], v119 offset:20992\n"
"v_mfma_f32_16x16x32_bf16 v[136:139], v[96:99], a[16:19], v[136:139]\n"
"v_mfma_f32_16x16x32_bf16 v[136:139], v[100:103], a[20:23], v[136:139]\n"
"ds_read_b64_tr_b16 v[92:93], v119 offset:25088\n"
"v_mfma_f32_16x16x32_bf16 v[140:143], v[100:103], a[28:31], v[140:143]\n"
"v_mfma_f32_16x16x32_bf16 v[140:143], v[96:99], a[24:27], v[140:143]\n"
"s_add_u32 s77, s94, 0x4000\n"
"s_add_u32 m0, s78, 0x4400\n"
"ds_read_b64_tr_b16 v[94:95], v119 offset:29184\n"
"s_waitcnt lgkmcnt(6)\n"
"v_mfma_f32_16x16x32_bf16 v[144:147], v[80:83], a[0:3], v[144:147]\n"
"buffer_load_dwordx4 v111, s[40:43], s77 offen sc1 lds\n"
"s_waitcnt lgkmcnt(4)\n"
"v_mfma_f32_16x16x32_bf16 v[144:147], v[84:87], a[4:7], v[144:147]\n"
"ds_read_b64_tr_b16 v[96:97], v120 offset:16896\n"
"v_mfma_f32_16x16x32_bf16 v[148:151], v[84:87], a[12:15], v[148:151]\n"
"v_mfma_f32_16x16x32_bf16 v[148:151], v[80:83], a[8:11], v[148:151]\n"
"ds_read_b64_tr_b16 v[98:99], v120 offset:20992\n"
"v_mfma_f32_16x16x32_bf16 v[152:155], v[80:83], a[16:19], v[152:155]\n"
"v_mfma_f32_16x16x32_bf16 v[152:155], v[84:87], a[20:23], v[152:155]\n"
"ds_read_b64_tr_b16 v[100:101], v120 offset:25088\n"
"v_mfma_f32_16x16x32_bf16 v[156:159], v[84:87], a[28:31], v[156:159]\n"
"v_mfma_f32_16x16x32_bf16 v[156:159], v[80:83], a[24:27], v[156:159]\n"
"s_add_u32 s77, s94, 0x4000\n"
"s_add_u32 m0, s78, 0x4800\n"
"ds_read_b64_tr_b16 v[102:103], v120 offset:29184\n"
"s_waitcnt lgkmcnt(6)\n"
"v_mfma_f32_16x16x32_bf16 v[160:163], v[88:91], a[0:3], v[160:163]\n"
"buffer_load_dwordx4 v112, s[40:43], s77 offen sc1 lds\n"
"s_waitcnt lgkmcnt(4)\n"
"v_mfma_f32_16x16x32_bf16 v[160:163], v[92:95], a[4:7], v[160:163]\n"
"ds_read_b64_tr_b16 v[80:81], v119 offset:17408\n"
"v_mfma_f32_16x16x32_bf16 v[164:167], v[92:95], a[12:15], v[164:167]\n"
"v_mfma_f32_16x16x32_bf16 v[164:167], v[88:91], a[8:11], v[164:167]\n"
"ds_read_b64_tr_b16 v[82:83], v119 offset:21504\n"
"v_mfma_f32_16x16x32_bf16 v[168:171], v[88:91], a[16:19], v[168:171]\n"
"v_mfma_f32_16x16x32_bf16 v[168:171], v[92:95], a[20:23], v[168:171]\n"
"ds_read_b64_tr_b16 v[84:85], v119 offset:25600\n"
"v_mfma_f32_16x16x32_bf16 v[172:175], v[92:95], a[28:31], v[172:175]\n"
"v_mfma_f32_16x16x32_bf16 v[172:175], v[88:91], a[24:27], v[172:175]\n"
"s_add_u32 s77, s94, 0x4000\n"
"s_add_u32 m0, s78, 0x4c00\n"
"ds_read_b64_tr_b16 v[86:87], v119 offset:29696\n"
"s_waitcnt lgkmcnt(6)\n"
"v_mfma_f32_16x16x32_bf16 v[176:179], v[96:99], a[0:3], v[176:179]\n"
"buffer_load_dwordx4 v113, s[40:43], s77 offen sc1 lds\n"
"s_waitcnt lgkmcnt(4)\n"
"v_mfma_f32_16x16x32_bf16 v[176:179], v[100:103], a[4:7], v[176:179]\n"
"ds_read_b64_tr_b16 v[88:89], v120 offset:17408\n"
"v_mfma_f32_16x16x32_bf16 v[180:183], v[100:103], a[12:15], v[180:183]\n"
"v_mfma_f32_16x16x32_bf16 v[180:183], v[96:99], a[8:11], v[180:183]\n"
"ds_read_b64_tr_b16 v[90:91], v120 offset:21504\n"
"v_mfma_f32_16x16x32_bf16 v[184:187], v[96:99], a[16:19], v[184:187]\n"
"v_mfma_f32_16x16x32_bf16 v[184:187], v[100:103], a[20:23], v[184:187]\n"
"ds_read_b64_tr_b16 v[92:93], v120 offset:25600\n"
"v_mfma_f32_16x16x32_bf16 v[188:191], v[100:103], a[28:31], v[188:191]\n"
"v_mfma_f32_16x16x32_bf16 v[188:191], v[96:99], a[24:27], v[188:191]\n"
"s_add_u32 s77, s95, 0x4000\n"
"s_add_u32 m0, s78, 0x14000\n"
"ds_read_b64_tr_b16 v[94:95], v120 offset:29696\n"
"s_waitcnt lgkmcnt(6)\n"
"v_mfma_f32_16x16x32_bf16 v[192:195], v[80:83], a[0:3], v[192:195]\n"
"buffer_load_dwordx4 v114, s[44:47], s77 offen sc1 lds\n"
"s_waitcnt lgkmcnt(4)\n"
"v_mfma_f32_16x16x32_bf16 v[192:195], v[84:87], a[4:7], v[192:195]\n"
"ds_read_b64_tr_b16 v[96:97], v119 offset:17920\n"
"v_mfma_f32_16x16x32_bf16 v[196:199], v[84:87], a[12:15], v[196:199]\n"
"v_mfma_f32_16x16x32_bf16 v[196:199], v[80:83], a[8:11], v[196:199]\n"
"ds_read_b64_tr_b16 v[98:99], v119 offset:22016\n"
"v_mfma_f32_16x16x32_bf16 v[200:203], v[80:83], a[16:19], v[200:203]\n"
"v_mfma_f32_16x16x32_bf16 v[200:203], v[84:87], a[20:23], v[200:203]\n"
"s_add_u32 s77, s95, 0x4000\n"
"s_add_u32 m0, s78, 0x14400\n"
"ds_read_b64_tr_b16 v[100:101], v119 offset:26112\n"
"v_mfma_f32_16x16x32_bf16 v[204:207], v[84:87], a[28:31], v[204:207]\n"
"buffer_load_dwordx4 v115, s[44:47], s77 offen sc1 lds\n"
"v_mfma_f32_16x16x32_bf16 v[204:207], v[80:83], a[24:27], v[204:207]\n"
"ds_read_b64_tr_b16 v[102:103], v119 offset:30208\n"
"s_waitcnt lgkmcnt(6)\n"
"v_mfma_f32_16x16x32_bf16 v[208:211], v[88:91], a[0:3], v[208:211]\n"
"s_waitcnt lgkmcnt(4)\n"
"v_mfma_f32_16x16x32_bf16 v[208:211], v[92:95], a[4:7], v[208:211]\n"
"ds_read_b64_tr_b16 v[80:81], v120 offset:17920\n"
"v_mfma_f32_16x16x32_bf16 v[212:215], v[92:95], a[12:15], v[212:215]\n"
"v_mfma_f32_16x16x32_bf16 v[212:215], v[88:91], a[8:11], v[212:215]\n"
"ds_read_b64_tr_b16 v[82:83], v120 offset:22016\n"
"v_mfma_f32_16x16x32_bf16 v[216:219], v[88:91], a[16:19], v[216:219]\n"
"v_mfma_f32_16x16x32_bf16 v[216:219], v[92:95], a[20:23], v[216:219]\n"
"s_add_u32 s77, s95, 0x4000\n"
"s_add_u32 m0, s78, 0x14800\n"
"ds_read_b64_tr_b16 v[84:85], v120 offset:26112\n"
"v_mfma_f32_16x16x32_bf16 v[220:223], v[92:95], a[28:31], v[220:223]\n"
"buffer_load_dwordx4 v116, s[44:47], s77 offen sc1 lds\n"
"v_mfma_f32_16x16x32_bf16 v[220:223], v[88:91], a[24:27], v[220:223]\n"
"s_waitcnt vmcnt(15)\n"
"s_barrier\n"
"ds_read_b64_tr_b16 v[86:87], v120 offset:30208\n"
"s_waitcnt lgkmcnt(6)\n"
"v_mfma_f32_16x16x32_bf16 v[224:227], v[96:99], a[0:3], v[224:227]\n"
"s_waitcnt lgkmcnt(4)\n"
"v_mfma_f32_16x16x32_bf16 v[224:227], v[100:103], a[4:7], v[224:227]\n"
"ds_read_b64_tr_b16 v[88:89], v119 offset:49152\n"
"v_mfma_f32_16x16x32_bf16 v[228:231], v[100:103], a[12:15], v[228:231]\n"
"v_mfma_f32_16x16x32_bf16 v[228:231], v[96:99], a[8:11], v[228:231]\n"
"ds_read_b64_tr_b16 v[90:91], v119 offset:53248\n"
"v_mfma_f32_16x16x32_bf16 v[232:235], v[96:99], a[16:19], v[232:235]\n"
"v_mfma_f32_16x16x32_bf16 v[232:235], v[100:103], a[20:23], v[232:235]\n"
"s_add_u32 s77, s95, 0x4000\n"
"s_add_u32 m0, s78, 0x14c00\n"
"ds_read_b64_tr_b16 v[92:93], v119 offset:57344\n"
"v_mfma_f32_16x16x32_bf16 v[236:239], v[100:103], a[28:31], v[236:239]\n"
"buffer_load_dwordx4 v117, s[44:47], s77 offen sc1 lds\n"
"v_mfma_f32_16x16x32_bf16 v[236:239], v[96:99], a[24:27], v[236:239]\n"
"ds_read_b64_tr_b16 v[94:95], v119 offset:61440\n"
"s_waitcnt lgkmcnt(6)\n"
"v_mfma_f32_16x16x32_bf16 v[240:243], v[80:83], a[0:3], v[240:243]\n"
"s_waitcnt lgkmcnt(4)\n"
"v_mfma_f32_16x16x32_bf16 v[240:243], v[84:87], a[4:7], v[240:243]\n"
"ds_read_b64_tr_b16 v[96:97], v120 offset:49152\n"
"v_mfma_f32_16x16x32_bf16 v[244:247], v[84:87], a[12:15], v[244:247]\n"
"v_mfma_f32_16x16x32_bf16 v[244:247], v[80:83], a[8:11], v[244:247]\n"
"ds_read_b64_tr_b16 v[98:99], v120 offset:53248\n"
"v_mfma_f32_16x16x32_bf16 v[248:251], v[80:83], a[16:19], v[248:251]\n"
"v_mfma_f32_16x16x32_bf16 v[248:251], v[84:87], a[20:23], v[248:251]\n"
"ds_read_b64_tr_b16 v[100:101], v120 offset:57344\n"
"v_mfma_f32_16x16x32_bf16 v[252:255], v[84:87], a[28:31], v[252:255]\n"
"v_mfma_f32_16x16x32_bf16 v[252:255], v[80:83], a[24:27], v[252:255]\n"
"ds_read_b64_tr_b16 v[102:103], v120 offset:61440\n"
"s_waitcnt lgkmcnt(6)\n"
"v_mfma_f32_16x16x32_bf16 v[128:131], v[88:91], a[160:163], v[128:131]\n"
"s_waitcnt lgkmcnt(4)\n"
"v_mfma_f32_16x16x32_bf16 v[128:131], v[92:95], a[164:167], v[128:131]\n"
"s_add_u32 s77, s94, 0x8000\n"
"s_add_u32 m0, s78, 0x8000\n"
"ds_read_b64_tr_b16 v[80:81], v119 offset:49664\n"
"v_mfma_f32_16x16x32_bf16 v[132:135], v[92:95], a[172:175], v[132:135]\n"
"buffer_load_dwordx4 v110, s[40:43], s77 offen sc1 lds\n"
"v_mfma_f32_16x16x32_bf16 v[132:135], v[88:91], a[168:171], v[132:135]\n"
"ds_read_b64_tr_b16 v[82:83], v119 offset:53760\n"
"v_mfma_f32_16x16x32_bf16 v[136:139], v[88:91], a[176:179], v[136:139]\n"
"v_mfma_f32_16x16x32_bf16 v[136:139], v[92:95], a[180:183], v[136:139]\n"
"ds_read_b64_tr_b16 v[84:85], v119 offset:57856\n"
"v_mfma_f32_16x16x32_bf16 v[140:143], v[92:95], a[188:191], v[140:143]\n"
"v_mfma_f32_16x16x32_bf16 v[140:143], v[88:91], a[184:187], v[140:143]\n"
"s_add_u32 s77, s94, 0x8000\n"
"s_add_u32 m0, s78, 0x8400\n"
"ds_read_b64_tr_b16 v[86:87], v119 offset:61952\n"
"s_waitcnt lgkmcnt(6)\n"
"v_mfma_f32_16x16x32_bf16 v[144:147], v[96:99], a[160:163], v[144:147]\n"
"buffer_load_dwordx4 v111, s[40:43], s77 offen sc1 lds\n"
"s_waitcnt lgkmcnt(4)\n"
"v_mfma_f32_16x16x32_bf16 v[144:147], v[100:103], a[164:167], v[144:147]\n"
"ds_read_b64_tr_b16 v[88:89], v120 offset:49664\n"
"v_mfma_f32_16x16x32_bf16 v[148:151], v[100:103], a[172:175], v[148:151]\n"
"v_mfma_f32_16x16x32_bf16 v[148:151], v[96:99], a[168:171], v[148:151]\n"
"ds_read_b64_tr_b16 v[90:91], v120 offset:53760\n"
"v_mfma_f32_16x16x32_bf16 v[152:155], v[96:99], a[176:179], v[152:155]\n"
"v_mfma_f32_16x16x32_bf16 v[152:155], v[100:103], a[180:183], v[152:155]\n"
"ds_read_b64_tr_b16 v[92:93], v120 offset:57856\n"
"v_mfma_f32_16x16x32_bf16 v[156:159], v[100:103], a[188:191], v[156:159]\n"
"v_mfma_f32_16x16x32_bf16 v[156:159], v[96:99], a[184:187], v[156:159]\n"
"s_add_u32 s77, s94, 0x8000\n"
"s_add_u32 m0, s78, 0x8800\n"
"ds_read_b64_tr_b16 v[94:95], v120 offset:61952\n"
"s_waitcnt lgkmcnt(6)\n"
"v_mfma_f32_16x16x32_bf16 v[160:163], v[80:83], a[160:163], v[160:163]\n"
"buffer_load_dwordx4 v112, s[40:43], s77 offen sc1 lds\n"
"s_waitcnt lgkmcnt(4)\n"
"v_mfma_f32_16x16x32_bf16 v[160:163], v[84:87], a[164:167], v[160:163]\n"
"ds_read_b64_tr_b16 v[96:97], v119 offset:50176\n"
"v_mfma_f32_16x16x32_bf16 v[164:167], v[84:87], a[172:175], v[164:167]\n"
"v_mfma_f32_16x16x32_bf16 v[164:167], v[80:83], a[168:171], v[164:167]\n"
"ds_read_b64_tr_b16 v[98:99], v119 offset:54272\n"
"v_mfma_f32_16x16x32_bf16 v[168:171], v[80:83], a[176:179], v[168:171]\n"
"v_mfma_f32_16x16x32_bf16 v[168:171], v[84:87], a[180:183], v[168:171]\n"
"ds_read_b64_tr_b16 v[100:101], v119 offset:58368\n"
"v_mfma_f32_16x16x32_bf16 v[172:175], v[84:87], a[188:191], v[172:175]\n"
"v_mfma_f32_16x16x32_bf16 v[172:175], v[80:83], a[184:187], v[172:175]\n"
"s_add_u32 s77, s94, 0x8000\n"
"s_add_u32 m0, s78, 0x8c00\n"
"ds_read_b64_tr_b16 v[102:103], v119 offset:62464\n"
"s_waitcnt lgkmcnt(6)\n"
"v_mfma_f32_16x16x32_bf16 v[176:179], v[88:91], a[160:163], v[176:179]\n"
"buffer_load_dwordx4 v113, s[40:43], s77 offen sc1 lds\n"
"s_waitcnt lgkmcnt(4)\n"
"v_mfma_f32_16x16x32_bf16 v[176:179], v[92:95], a[164:167], v[176:179]\n"
"ds_read_b64_tr_b16 v[80:81], v120 offset:50176\n"
"v_mfma_f32_16x16x32_bf16 v[180:183], v[92:95], a[172:175], v[180:183]\n"
"v_mfma_f32_16x16x32_bf16 v[180:183], v[88:91], a[168:171], v[180:183]\n"
"ds_read_b64_tr_b16 v[82:83], v120 offset:54272\n"
"v_mfma_f32_16x16x32_bf16 v[184:187], v[88:91], a[176:179], v[184:187]\n"
"v_mfma_f32_16x16x32_bf16 v[184:187], v[92:95], a[180:183], v[184:187]\n"
"ds_read_b64_tr_b16 v[84:85], v120 offset:58368\n"
"v_mfma_f32_16x16x32_bf16 v[188:191], v[92:95], a[188:191], v[188:191]\n"
"v_mfma_f32_16x16x32_bf16 v[188:191], v[88:91], a[184:187], v[188:191]\n"
"s_add_u32 s77, s94, 0xc000\n"
"s_add_u32 m0, s78, 0xc000\n"
"ds_read_b64_tr_b16 v[86:87], v120 offset:62464\n"
"s_waitcnt lgkmcnt(6)\n"
"v_mfma_f32_16x16x32_bf16 v[192:195], v[96:99], a[160:163], v[192:195]\n"
"buffer_load_dwordx4 v110, s[40:43], s77 offen sc1 lds\n"
"s_waitcnt lgkmcnt(4)\n"
"v_mfma_f32_16x16x32_bf16 v[192:195], v[100:103], a[164:167], v[192:195]\n"
"ds_read_b64_tr_b16 v[88:89], v119 offset:50688\n"
"v_mfma_f32_16x16x32_bf16 v[196:199], v[100:103], a[172:175], v[196:199]\n"
"v_mfma_f32_16x16x32_bf16 v[196:199], v[96:99], a[168:171], v[196:199]\n"
"ds_read_b64_tr_b16 v[90:91], v119 offset:54784\n"
"v_mfma_f32_16x16x32_bf16 v[200:203], v[96:99], a[176:179], v[200:203]\n"
"v_mfma_f32_16x16x32_bf16 v[200:203], v[100:103], a[180:183], v[200:203]\n"
"s_add_u32 s77, s94, 0xc000\n"
"s_add_u32 m0, s78, 0xc400\n"
"ds_read_b64_tr_b16 v[92:93], v119 offset:58880\n"
"v_mfma_f32_16x16x32_bf16 v[204:207], v[100:103], a[188:191], v[204:207]\n"
"buffer_load_dwordx4 v111, s[40:43], s77 offen sc1 lds\n"
"v_mfma_f32_16x16x32_bf16 v[204:207], v[96:99], a[184:187], v[204:207]\n"
"ds_read_b64_tr_b16 v[94:95], v119 offset:62976\n"
"s_waitcnt lgkmcnt(6)\n"
"v_mfma_f32_16x16x32_bf16 v[208:211], v[80:83], a[160:163], v[208:211]\n"
"s_waitcnt lgkmcnt(4)\n"
"v_mfma_f32_16x16x32_bf16 v[208:211], v[84:87], a[164:167], v[208:211]\n"
"ds_read_b64_tr_b16 v[96:97], v120 offset:50688\n"
"v_mfma_f32_16x16x32_bf16 v[212:215], v[84:87], a[172:175], v[212:215]\n"
"v_mfma_f32_16x16x32_bf16 v[212:215], v[80:83], a[168:171], v[212:215]\n"
"ds_read_b64_tr_b16 v[98:99], v120 offset:54784\n"
"v_mfma_f32_16x16x32_bf16 v[216:219], v[80:83], a[176:179], v[216:219]\n"
"v_mfma_f32_16x16x32_bf16 v[216:219], v[84:87], a[180:183], v[216:219]\n"
"s_add_u32 s77, s94, 0xc000\n"
"s_add_u32 m0, s78, 0xc800\n"
"ds_read_b64_tr_b16 v[100:101], v120 offset:58880\n"
"v_mfma_f32_16x16x32_bf16 v[220:223], v[84:87], a[188:191], v[220:223]\n"
"buffer_load_dwordx4 v112, s[40:43], s77 offen sc1 lds\n"
"v_mfma_f32_16x16x32_bf16 v[220:223], v[80:83], a[184:187], v[220:223]\n"
"ds_read_b64_tr_b16 v[102:103], v120 offset:62976\n"
"s_waitcnt lgkmcnt(6)\n"
"v_mfma_f32_16x16x32_bf16 v[224:227], v[88:91], a[160:163], v[224:227]\n"
"s_waitcnt lgkmcnt(4)\n"
"v_mfma_f32_16x16x32_bf16 v[224:227], v[92:95], a[164:167], v[224:227]\n"
"v_mfma_f32_16x16x32_bf16 v[228:231], v[92:95], a[172:175], v[228:231]\n"
"v_mfma_f32_16x16x32_bf16 v[228:231], v[88:91], a[168:171], v[228:231]\n"
"v_mfma_f32_16x16x32_bf16 v[232:235], v[88:91], a[176:179], v[232:235]\n"
"v_mfma_f32_16x16x32_bf16 v[232:235], v[92:95], a[180:183], v[232:235]\n"
"s_add_u32 s77, s94, 0xc000\n"
"s_add_u32 m0, s78, 0xcc00\n"
"v_mfma_f32_16x16x32_bf16 v[236:239], v[92:95], a[188:191], v[236:239]\n"
"buffer_load_dwordx4 v113, s[40:43], s77 offen sc1 lds\n"
"v_mfma_f32_16x16x32_bf16 v[236:239], v[88:91], a[184:187], v[236:239]\n"
"s_waitcnt lgkmcnt(2)\n"
"v_mfma_f32_16x16x32_bf16 v[240:243], v[96:99], a[160:163], v[240:243]\n"
"s_waitcnt lgkmcnt(0)\n"
"v_mfma_f32_16x16x32_bf16 v[240:243], v[100:103], a[164:167], v[240:243]\n"
"v_mfma_f32_16x16x32_bf16 v[244:247], v[100:103], a[172:175], v[244:247]\n"
"v_mfma_f32_16x16x32_bf16 v[244:247], v[96:99], a[168:171], v[244:247]\n"
"v_mfma_f32_16x16x32_bf16 v[248:251], v[96:99], a[176:179], v[248:251]\n"
"v_mfma_f32_16x16x32_bf16 v[248:251], v[100:103], a[180:183], v[248:251]\n"
"v_mfma_f32_16x16x32_bf16 v[252:255], v[100:103], a[188:191], v[252:255]\n"
"v_mfma_f32_16x16x32_bf16 v[252:255], v[96:99], a[184:187], v[252:255]\n"
"s_waitcnt vmcnt(16) lgkmcnt(0)\n"
"s_barrier\n"
"ds_read_b128 a[192:195], v104\n"
"ds_read_b128 a[196:199], v105\n"
"ds_read_b128 a[200:203], v106\n"
"ds_read_b128 a[204:207], v107\n"
"ds_read_b128 a[208:211], v104 offset:4096\n"
"ds_read_b128 a[212:215], v105 offset:4096\n"
"ds_read_b128 a[216:219], v106 offset:4096\n"
"ds_read_b128 a[220:223], v107 offset:4096\n"
"ds_read_b128 a[224:227], v104 offset:8192\n"
"ds_read_b128 a[228:231], v105 offset:8192\n"
"ds_read_b128 a[232:235], v106 offset:8192\n"
"ds_read_b128 a[236:239], v107 offset:8192\n"
"ds_read_b128 a[240:243], v104 offset:12288\n"
"ds_read_b128 a[244:247], v105 offset:12288\n"
"ds_read_b128 a[248:251], v106 offset:12288\n"
"ds_read_b128 a[252:255], v107 offset:12288\n"
"v_mov_b32 v100, 0x3e0293ee\n"
"v_mov_b32 v101, 0x3e0293ee\n"
"ds_read_b64_tr_b16 v[16:17], v108\n"
"ds_read_b64_tr_b16 v[18:19], v108 offset:4096\n"
"ds_read_b64_tr_b16 v[20:21], v108 offset:8192\n"
"ds_read_b64_tr_b16 v[22:23], v108 offset:12288\n"
"v_pk_mul_f32 v[128:129], v[128:129], v[100:101]\n"
"v_pk_mul_f32 v[160:161], v[160:161], v[100:101]\n"
"v_pk_mul_f32 v[130:131], v[130:131], v[100:101]\n"
"v_pk_mul_f32 v[162:163], v[162:163], v[100:101]\n"
"v_pk_mul_f32 v[144:145], v[144:145], v[100:101]\n"
"v_pk_mul_f32 v[176:177], v[176:177], v[100:101]\n"
"v_pk_mul_f32 v[146:147], v[146:147], v[100:101]\n"
"v_pk_mul_f32 v[178:179], v[178:179], v[100:101]\n"
"v_cvt_pk_bf16_f32 v80, v128, v129\n"
"v_cvt_pk_bf16_f32 v84, v160, v161\n"
"v_cvt_pk_bf16_f32 v81, v130, v131\n"
"v_cvt_pk_bf16_f32 v85, v162, v163\n"
"v_cvt_pk_bf16_f32 v82, v144, v145\n"
"v_cvt_pk_bf16_f32 v86, v176, v177\n"
"v_cvt_pk_bf16_f32 v83, v146, v147\n"
"v_cvt_pk_bf16_f32 v87, v178, v179\n"
"v_permlane32_swap_b32 v80, v82\n"
"v_permlane32_swap_b32 v81, v83\n"
"v_permlane32_swap_b32 v84, v86\n"
"v_permlane32_swap_b32 v85, v87\n"
"v_permlane16_swap_b32 v80, v82\n"
"v_permlane16_swap_b32 v81, v83\n"
"v_permlane16_swap_b32 v84, v86\n"
"v_permlane16_swap_b32 v85, v87\n"
"v_accvgpr_write_b32 a128, v80\n"
"v_accvgpr_write_b32 a129, v81\n"
"v_accvgpr_write_b32 a130, v82\n"
"v_accvgpr_write_b32 a131, v83\n"
"v_accvgpr_write_b32 a132, v84\n"
"v_accvgpr_write_b32 a133, v85\n"
"v_accvgpr_write_b32 a134, v86\n"
"v_accvgpr_write_b32 a135, v87\n"
"ds_read_b64_tr_b16 v[24:25], v109\n"
"ds_read_b64_tr_b16 v[26:27], v109 offset:4096\n"
"ds_read_b64_tr_b16 v[28:29], v109 offset:8192\n"
"ds_read_b64_tr_b16 v[30:31], v109 offset:12288\n"
"v_pk_mul_f32 v[192:193], v[192:193], v[100:101]\n"
"v_pk_mul_f32 v[224:225], v[224:225], v[100:101]\n"
"v_pk_mul_f32 v[194:195], v[194:195], v[100:101]\n"
"v_pk_mul_f32 v[226:227], v[226:227], v[100:101]\n"
"v_pk_mul_f32 v[208:209], v[208:209], v[100:101]\n"
"v_pk_mul_f32 v[240:241], v[240:241], v[100:101]\n"
"v_pk_mul_f32 v[210:211], v[210:211], v[100:101]\n"
"v_pk_mul_f32 v[242:243], v[242:243], v[100:101]\n"
"v_cvt_pk_bf16_f32 v88, v192, v193\n"
"v_cvt_pk_bf16_f32 v92, v224, v225\n"
"v_cvt_pk_bf16_f32 v89, v194, v195\n"
"v_cvt_pk_bf16_f32 v93, v226, v227\n"
"v_cvt_pk_bf16_f32 v90, v208, v209\n"
"v_cvt_pk_bf16_f32 v94, v240, v241\n"
"v_cvt_pk_bf16_f32 v91, v210, v211\n"
"v_cvt_pk_bf16_f32 v95, v242, v243\n"
"v_permlane32_swap_b32 v88, v90\n"
"v_permlane32_swap_b32 v89, v91\n"
"v_permlane32_swap_b32 v92, v94\n"
"v_permlane32_swap_b32 v93, v95\n"
"v_permlane16_swap_b32 v88, v90\n"
"v_permlane16_swap_b32 v89, v91\n"
"v_permlane16_swap_b32 v92, v94\n"
"v_permlane16_swap_b32 v93, v95\n"
"v_accvgpr_write_b32 a136, v88\n"
"v_accvgpr_write_b32 a137, v89\n"
"v_accvgpr_write_b32 a138, v90\n"
"v_accvgpr_write_b32 a139, v91\n"
"v_accvgpr_write_b32 a140, v92\n"
"v_accvgpr_write_b32 a141, v93\n"
"v_accvgpr_write_b32 a142, v94\n"
"v_accvgpr_write_b32 a143, v95\n"
"ds_read_b64_tr_b16 v[32:33], v108 offset:512\n"
"ds_read_b64_tr_b16 v[34:35], v108 offset:4608\n"
"ds_read_b64_tr_b16 v[36:37], v108 offset:8704\n"
"ds_read_b64_tr_b16 v[38:39], v108 offset:12800\n"
"v_pk_mul_f32 v[132:133], v[132:133], v[100:101]\n"
"v_pk_mul_f32 v[164:165], v[164:165], v[100:101]\n"
"v_pk_mul_f32 v[134:135], v[134:135], v[100:101]\n"
"v_pk_mul_f32 v[166:167], v[166:167], v[100:101]\n"
"v_pk_mul_f32 v[148:149], v[148:149], v[100:101]\n"
"v_pk_mul_f32 v[180:181], v[180:181], v[100:101]\n"
"v_pk_mul_f32 v[150:151], v[150:151], v[100:101]\n"
"v_pk_mul_f32 v[182:183], v[182:183], v[100:101]\n"
"v_cvt_pk_bf16_f32 v80, v132, v133\n"
"v_cvt_pk_bf16_f32 v84, v164, v165\n"
"v_cvt_pk_bf16_f32 v81, v134, v135\n"
"v_cvt_pk_bf16_f32 v85, v166, v167\n"
"v_cvt_pk_bf16_f32 v82, v148, v149\n"
"v_cvt_pk_bf16_f32 v86, v180, v181\n"
"v_cvt_pk_bf16_f32 v83, v150, v151\n"
"v_cvt_pk_bf16_f32 v87, v182, v183\n"
"v_permlane32_swap_b32 v80, v82\n"
"v_permlane32_swap_b32 v81, v83\n"
"v_permlane32_swap_b32 v84, v86\n"
"v_permlane32_swap_b32 v85, v87\n"
"v_permlane16_swap_b32 v80, v82\n"
"v_permlane16_swap_b32 v81, v83\n"
"v_permlane16_swap_b32 v84, v86\n"
"v_permlane16_swap_b32 v85, v87\n"
"v_accvgpr_write_b32 a144, v80\n"
"v_accvgpr_write_b32 a145, v81\n"
"v_accvgpr_write_b32 a146, v82\n"
"v_accvgpr_write_b32 a147, v83\n"
"v_accvgpr_write_b32 a148, v84\n"
"v_accvgpr_write_b32 a149, v85\n"
"v_accvgpr_write_b32 a150, v86\n"
"v_accvgpr_write_b32 a151, v87\n"
"ds_read_b64_tr_b16 v[40:41], v109 offset:512\n"
"ds_read_b64_tr_b16 v[42:43], v109 offset:4608\n"
"ds_read_b64_tr_b16 v[44:45], v109 offset:8704\n"
"ds_read_b64_tr_b16 v[46:47], v109 offset:12800\n"
"v_pk_mul_f32 v[196:197], v[196:197], v[100:101]\n"
"v_pk_mul_f32 v[228:229], v[228:229], v[100:101]\n"
"v_pk_mul_f32 v[198:199], v[198:199], v[100:101]\n"
"v_pk_mul_f32 v[230:231], v[230:231], v[100:101]\n"
"v_pk_mul_f32 v[212:213], v[212:213], v[100:101]\n"
"v_pk_mul_f32 v[244:245], v[244:245], v[100:101]\n"
"v_pk_mul_f32 v[214:215], v[214:215], v[100:101]\n"
"v_pk_mul_f32 v[246:247], v[246:247], v[100:101]\n"
"v_cvt_pk_bf16_f32 v88, v196, v197\n"
"v_cvt_pk_bf16_f32 v92, v228, v229\n"
"v_cvt_pk_bf16_f32 v89, v198, v199\n"
"v_cvt_pk_bf16_f32 v93, v230, v231\n"
"v_cvt_pk_bf16_f32 v90, v212, v213\n"
"v_cvt_pk_bf16_f32 v94, v244, v245\n"
"v_cvt_pk_bf16_f32 v91, v214, v215\n"
"v_cvt_pk_bf16_f32 v95, v246, v247\n"
"v_permlane32_swap_b32 v88, v90\n"
"v_permlane32_swap_b32 v89, v91\n"
"v_permlane32_swap_b32 v92, v94\n"
"v_permlane32_swap_b32 v93, v95\n"
"v_permlane16_swap_b32 v88, v90\n"
"v_permlane16_swap_b32 v89, v91\n"
"v_permlane16_swap_b32 v92, v94\n"
"v_permlane16_swap_b32 v93, v95\n"
"v_accvgpr_write_b32 a152, v88\n"
"v_accvgpr_write_b32 a153, v89\n"
"v_accvgpr_write_b32 a154, v90\n"
"v_accvgpr_write_b32 a155, v91\n"
"v_accvgpr_write_b32 a156, v92\n"
"v_accvgpr_write_b32 a157, v93\n"
"v_accvgpr_write_b32 a158, v94\n"
"v_accvgpr_write_b32 a159, v95\n"
"ds_read_b64_tr_b16 v[48:49], v108 offset:1024\n"
"ds_read_b64_tr_b16 v[50:51], v108 offset:5120\n"
"ds_read_b64_tr_b16 v[52:53], v108 offset:9216\n"
"ds_read_b64_tr_b16 v[54:55], v108 offset:13312\n"
"v_pk_mul_f32 v[136:137], v[136:137], v[100:101]\n"
"v_pk_mul_f32 v[168:169], v[168:169], v[100:101]\n"
"v_pk_mul_f32 v[138:139], v[138:139], v[100:101]\n"
"v_pk_mul_f32 v[170:171], v[170:171], v[100:101]\n"
"v_pk_mul_f32 v[152:153], v[152:153], v[100:101]\n"
"v_pk_mul_f32 v[184:185], v[184:185], v[100:101]\n"
"v_pk_mul_f32 v[154:155], v[154:155], v[100:101]\n"
"v_pk_mul_f32 v[186:187], v[186:187], v[100:101]\n"
"v_cvt_pk_bf16_f32 v80, v136, v137\n"
"v_cvt_pk_bf16_f32 v84, v168, v169\n"
"v_cvt_pk_bf16_f32 v81, v138, v139\n"
"v_cvt_pk_bf16_f32 v85, v170, v171\n"
"v_cvt_pk_bf16_f32 v82, v152, v153\n"
"v_cvt_pk_bf16_f32 v86, v184, v185\n"
"v_cvt_pk_bf16_f32 v83, v154, v155\n"
"v_cvt_pk_bf16_f32 v87, v186, v187\n"
"v_permlane32_swap_b32 v80, v82\n"
"v_permlane32_swap_b32 v81, v83\n"
"v_permlane32_swap_b32 v84, v86\n"
"v_permlane32_swap_b32 v85, v87\n"
"v_permlane16_swap_b32 v80, v82\n"
"v_permlane16_swap_b32 v81, v83\n"
"v_permlane16_swap_b32 v84, v86\n"
"v_permlane16_swap_b32 v85, v87\n"
"v_accvgpr_write_b32 a160, v80\n"
"v_accvgpr_write_b32 a161, v81\n"
"v_accvgpr_write_b32 a162, v82\n"
"v_accvgpr_write_b32 a163, v83\n"
"v_accvgpr_write_b32 a164, v84\n"
"v_accvgpr_write_b32 a165, v85\n"
"v_accvgpr_write_b32 a166, v86\n"
"v_accvgpr_write_b32 a167, v87\n"
"ds_read_b64_tr_b16 v[56:57], v109 offset:1024\n"
"ds_read_b64_tr_b16 v[58:59], v109 offset:5120\n"
"ds_read_b64_tr_b16 v[60:61], v109 offset:9216\n"
"ds_read_b64_tr_b16 v[62:63], v109 offset:13312\n"
"v_pk_mul_f32 v[200:201], v[200:201], v[100:101]\n"
"v_pk_mul_f32 v[232:233], v[232:233], v[100:101]\n"
"v_pk_mul_f32 v[202:203], v[202:203], v[100:101]\n"
"v_pk_mul_f32 v[234:235], v[234:235], v[100:101]\n"
"v_pk_mul_f32 v[216:217], v[216:217], v[100:101]\n"
"v_pk_mul_f32 v[248:249], v[248:249], v[100:101]\n"
"v_pk_mul_f32 v[218:219], v[218:219], v[100:101]\n"
"v_pk_mul_f32 v[250:251], v[250:251], v[100:101]\n"
"v_cvt_pk_bf16_f32 v88, v200, v201\n"
"v_cvt_pk_bf16_f32 v92, v232, v233\n"
"v_cvt_pk_bf16_f32 v89, v202, v203\n"
"v_cvt_pk_bf16_f32 v93, v234, v235\n"
"v_cvt_pk_bf16_f32 v90, v216, v217\n"
"v_cvt_pk_bf16_f32 v94, v248, v249\n"
"v_cvt_pk_bf16_f32 v91, v218, v219\n"
"v_cvt_pk_bf16_f32 v95, v250, v251\n"
"v_permlane32_swap_b32 v88, v90\n"
"v_permlane32_swap_b32 v89, v91\n"
"v_permlane32_swap_b32 v92, v94\n"
"v_permlane32_swap_b32 v93, v95\n"
"v_permlane16_swap_b32 v88, v90\n"
"v_permlane16_swap_b32 v89, v91\n"
"v_permlane16_swap_b32 v92, v94\n"
"v_permlane16_swap_b32 v93, v95\n"
"v_accvgpr_write_b32 a168, v88\n"
"v_accvgpr_write_b32 a169, v89\n"
"v_accvgpr_write_b32 a170, v90\n"
"v_accvgpr_write_b32 a171, v91\n"
"v_accvgpr_write_b32 a172, v92\n"
"v_accvgpr_write_b32 a173, v93\n"
"v_accvgpr_write_b32 a174, v94\n"
"v_accvgpr_write_b32 a175, v95\n"
"ds_read_b64_tr_b16 v[64:65], v108 offset:1536\n"
"ds_read_b64_tr_b16 v[66:67], v108 offset:5632\n"
"ds_read_b64_tr_b16 v[68:69], v108 offset:9728\n"
"ds_read_b64_tr_b16 v[70:71], v108 offset:13824\n"
"v_pk_mul_f32 v[140:141], v[140:141], v[100:101]\n"
"v_pk_mul_f32 v[172:173], v[172:173], v[100:101]\n"
"v_pk_mul_f32 v[142:143], v[142:143], v[100:101]\n"
"v_pk_mul_f32 v[174:175], v[174:175], v[100:101]\n"
"v_pk_mul_f32 v[156:157], v[156:157], v[100:101]\n"
"v_pk_mul_f32 v[188:189], v[188:189], v[100:101]\n"
"v_pk_mul_f32 v[158:159], v[158:159], v[100:101]\n"
"v_pk_mul_f32 v[190:191], v[190:191], v[100:101]\n"
"v_cvt_pk_bf16_f32 v80, v140, v141\n"
"v_cvt_pk_bf16_f32 v84, v172, v173\n"
"v_cvt_pk_bf16_f32 v81, v142, v143\n"
"v_cvt_pk_bf16_f32 v85, v174, v175\n"
"v_cvt_pk_bf16_f32 v82, v156, v157\n"
"v_cvt_pk_bf16_f32 v86, v188, v189\n"
"v_cvt_pk_bf16_f32 v83, v158, v159\n"
"v_cvt_pk_bf16_f32 v87, v190, v191\n"
"v_permlane32_swap_b32 v80, v82\n"
"v_permlane32_swap_b32 v81, v83\n"
"v_permlane32_swap_b32 v84, v86\n"
"v_permlane32_swap_b32 v85, v87\n"
"v_permlane16_swap_b32 v80, v82\n"
"v_permlane16_swap_b32 v81, v83\n"
"v_permlane16_swap_b32 v84, v86\n"
"v_permlane16_swap_b32 v85, v87\n"
"v_accvgpr_write_b32 a176, v80\n"
"v_accvgpr_write_b32 a177, v81\n"
"v_accvgpr_write_b32 a178, v82\n"
"v_accvgpr_write_b32 a179, v83\n"
"v_accvgpr_write_b32 a180, v84\n"
"v_accvgpr_write_b32 a181, v85\n"
"v_accvgpr_write_b32 a182, v86\n"
"v_accvgpr_write_b32 a183, v87\n"
"ds_read_b64_tr_b16 v[72:73], v109 offset:1536\n"
"ds_read_b64_tr_b16 v[74:75], v109 offset:5632\n"
"ds_read_b64_tr_b16 v[76:77], v109 offset:9728\n"
"ds_read_b64_tr_b16 v[78:79], v109 offset:13824\n"
"v_pk_mul_f32 v[204:205], v[204:205], v[100:101]\n"
"v_pk_mul_f32 v[236:237], v[236:237], v[100:101]\n"
"v_pk_mul_f32 v[206:207], v[206:207], v[100:101]\n"
"v_pk_mul_f32 v[238:239], v[238:239], v[100:101]\n"
"v_pk_mul_f32 v[220:221], v[220:221], v[100:101]\n"
"v_pk_mul_f32 v[252:253], v[252:253], v[100:101]\n"
"v_pk_mul_f32 v[222:223], v[222:223], v[100:101]\n"
"v_pk_mul_f32 v[254:255], v[254:255], v[100:101]\n"
"v_cvt_pk_bf16_f32 v88, v204, v205\n"
"v_cvt_pk_bf16_f32 v92, v236, v237\n"
"v_cvt_pk_bf16_f32 v89, v206, v207\n"
"v_cvt_pk_bf16_f32 v93, v238, v239\n"
"v_cvt_pk_bf16_f32 v90, v220, v221\n"
"v_cvt_pk_bf16_f32 v94, v252, v253\n"
"v_cvt_pk_bf16_f32 v91, v222, v223\n"
"v_cvt_pk_bf16_f32 v95, v254, v255\n"
"v_permlane32_swap_b32 v88, v90\n"
"v_permlane32_swap_b32 v89, v91\n"
"v_permlane32_swap_b32 v92, v94\n"
"v_permlane32_swap_b32 v93, v95\n"
"v_permlane16_swap_b32 v88, v90\n"
"v_permlane16_swap_b32 v89, v91\n"
"v_permlane16_swap_b32 v92, v94\n"
"v_permlane16_swap_b32 v93, v95\n"
"v_accvgpr_write_b32 a184, v88\n"
"v_accvgpr_write_b32 a185, v89\n"
"v_accvgpr_write_b32 a186, v90\n"
"v_accvgpr_write_b32 a187, v91\n"
"v_accvgpr_write_b32 a188, v92\n"
"v_accvgpr_write_b32 a189, v93\n"
"v_accvgpr_write_b32 a190, v94\n"
"v_accvgpr_write_b32 a191, v95\n"
"s_add_u32 s94, s94, 0x10000\n"
"s_add_u32 s95, s95, 0x10000\n"
"s_waitcnt lgkmcnt(15)\n"
"v_mfma_f32_16x16x32_bf16 v[192:195], a[192:195], a[128:131], 0\n"
"v_mfma_f32_16x16x32_bf16 v[192:195], a[196:199], a[132:135], v[192:195]\n"
"v_mfma_f32_16x16x32_bf16 v[192:195], a[200:203], a[136:139], v[192:195]\n"
"v_mfma_f32_16x16x32_bf16 v[192:195], a[204:207], a[140:143], v[192:195]\n"
"v_mfma_f32_16x16x32_bf16 v[196:199], a[192:195], a[144:147], 0\n"
"v_mfma_f32_16x16x32_bf16 v[196:199], a[196:199], a[148:151], v[196:199]\n"
"v_mfma_f32_16x16x32_bf16 v[196:199], a[200:203], a[152:155], v[196:199]\n"
"v_mfma_f32_16x16x32_bf16 v[196:199], a[204:207], a[156:159], v[196:199]\n"
"v_mfma_f32_16x16x32_bf16 v[200:203], a[192:195], a[160:163], 0\n"
"v_mfma_f32_16x16x32_bf16 v[200:203], a[196:199], a[164:167], v[200:203]\n"
"v_mfma_f32_16x16x32_bf16 v[200:203], a[200:203], a[168:171], v[200:203]\n"
"v_mfma_f32_16x16x32_bf16 v[200:203], a[204:207], a[172:175], v[200:203]\n"
"v_mfma_f32_16x16x32_bf16 v[204:207], a[192:195], a[176:179], 0\n"
"v_mfma_f32_16x16x32_bf16 v[204:207], a[196:199], a[180:183], v[204:207]\n"
"v_mfma_f32_16x16x32_bf16 v[204:207], a[200:203], a[184:187], v[204:207]\n"
"v_mfma_f32_16x16x32_bf16 v[204:207], a[204:207], a[188:191], v[204:207]\n"
"v_mov_b32 v96, 0x3f803f80\n"
"v_mov_b32 v97, 0x3f803f80\n"
"v_mov_b32 v98, 0x3f803f80\n"
"v_mov_b32 v99, 0x3f803f80\n"
"v_max3_f32 v224, v192, v193, v194\n"
"v_max_f32 v224, v224, v195\n"
"v_max3_f32 v226, v196, v197, v198\n"
"v_max_f32 v226, v226, v199\n"
"v_max3_f32 v228, v200, v201, v202\n"
"v_max_f32 v228, v228, v203\n"
"v_max3_f32 v230, v204, v205, v206\n"
"v_max_f32 v230, v230, v207\n"
"v_mov_b32 v225, v224\n"
"v_mov_b32 v227, v226\n"
"v_mov_b32 v229, v228\n"
"v_mov_b32 v231, v230\n"
"v_permlane32_swap_b32 v224, v225\n"
"v_permlane32_swap_b32 v226, v227\n"
"v_permlane32_swap_b32 v228, v229\n"
"v_permlane32_swap_b32 v230, v231\n"
"v_max_f32 v224, v224, v225\n"
"v_max_f32 v226, v226, v227\n"
"v_max_f32 v228, v228, v229\n"
"v_max_f32 v230, v230, v231\n"
"v_mov_b32 v225, v224\n"
"v_mov_b32 v227, v226\n"
"v_mov_b32 v229, v228\n"
"v_mov_b32 v231, v230\n"
"v_permlane16_swap_b32 v224, v225\n"
"v_permlane16_swap_b32 v226, v227\n"
"v_permlane16_swap_b32 v228, v229\n"
"v_permlane16_swap_b32 v230, v231\n"
"v_max_f32 v224, v224, v225\n"
"v_max_f32 v226, v226, v227\n"
"v_max_f32 v228, v228, v229\n"
"v_max_f32 v230, v230, v231\n"
"v_sub_f32 v240, 0, v224\n"
"v_sub_f32 v241, 0, v224\n"
"v_sub_f32 v242, 0, v224\n"
"v_sub_f32 v243, 0, v224\n"
"v_sub_f32 v244, 0, v226\n"
"v_sub_f32 v245, 0, v226\n"
"v_sub_f32 v246, 0, v226\n"
"v_sub_f32 v247, 0, v226\n"
"v_sub_f32 v248, 0, v228\n"
"v_sub_f32 v249, 0, v228\n"
"v_sub_f32 v250, 0, v228\n"
"v_sub_f32 v251, 0, v228\n"
"v_sub_f32 v252, 0, v230\n"
"v_sub_f32 v253, 0, v230\n"
"v_sub_f32 v254, 0, v230\n"
"v_sub_f32 v255, 0, v230\n"
"ATT_RESTART_%=:\n"
"v_mfma_f32_16x16x32_bf16 v[192:195], a[192:195], a[128:131], v[240:243]\n"
"v_mfma_f32_16x16x32_bf16 v[192:195], a[196:199], a[132:135], v[192:195]\n"
"v_mfma_f32_16x16x32_bf16 v[192:195], a[200:203], a[136:139], v[192:195]\n"
"v_mfma_f32_16x16x32_bf16 v[192:195], a[204:207], a[140:143], v[192:195]\n"
"v_mfma_f32_16x16x32_bf16 v[196:199], a[220:223], a[140:143], v[240:243]\n"
"v_mfma_f32_16x16x32_bf16 v[196:199], a[216:219], a[136:139], v[196:199]\n"
"v_mfma_f32_16x16x32_bf16 v[196:199], a[212:215], a[132:135], v[196:199]\n"
"v_mfma_f32_16x16x32_bf16 v[196:199], a[208:211], a[128:131], v[196:199]\n"
"v_mfma_f32_16x16x32_bf16 v[200:203], a[224:227], a[128:131], v[240:243]\n"
"s_waitcnt vmcnt(8) lgkmcnt(0)\n"
"s_barrier\n"
"s_nop 0\n"
"v_exp_f32 v192, v192\n"
"v_mfma_f32_16x16x32_bf16 v[200:203], a[228:231], a[132:135], v[200:203]\n"
"v_mfma_f32_16x16x32_bf16 v[200:203], a[232:235], a[136:139], v[200:203]\n"
"s_sub_u32 s77, s95, 0x8000\n"
"s_add_u32 m0, s78, 0x18000\n"
"v_exp_f32 v193, v193\n"
"v_mfma_f32_16x16x32_bf16 v[200:203], a[236:239], a[140:143], v[200:203]\n"
"buffer_load_dwordx4 v114, s[44:47], s77 offen sc1 lds\n"
"v_cvt_pk_bf16_f32 v224, v192, v193\n"
"v_mfma_f32_16x16x32_bf16 v[204:207], a[252:255], a[140:143], v[240:243]\n"
"s_sub_u32 s77, s95, 0x8000\n"
"s_add_u32 m0, s78, 0x18400\n"
"v_exp_f32 v194, v194\n"
"v_mfma_f32_16x16x32_bf16 v[204:207], a[248:251], a[136:139], v[204:207]\n"
"buffer_load_dwordx4 v115, s[44:47], s77 offen sc1 lds\n"
"v_mfma_f32_16x16x32_bf16 v[204:207], a[244:247], a[132:135], v[204:207]\n"
"s_sub_u32 s77, s95, 0x8000\n"
"s_add_u32 m0, s78, 0x18800\n"
"v_exp_f32 v195, v195\n"
"v_mfma_f32_16x16x32_bf16 v[204:207], a[240:243], a[128:131], v[204:207]\n"
"buffer_load_dwordx4 v116, s[44:47], s77 offen sc1 lds\n"
"v_cvt_pk_bf16_f32 v225, v194, v195\n"
"s_sub_u32 s77, s95, 0x8000\n"
"s_add_u32 m0, s78, 0x18c00\n"
"v_exp_f32 v196, v196\n"
"buffer_load_dwordx4 v117, s[44:47], s77 offen sc1 lds\n"
"s_sub_u32 s77, s95, 0x4000\n"
"s_add_u32 m0, s78, 0x1c000\n"
"v_exp_f32 v197, v197\n"
"buffer_load_dwordx4 v114, s[44:47], s77 offen sc1 lds\n"
"v_cvt_pk_bf16_f32 v226, v196, v197\n"
"s_sub_u32 s77, s95, 0x4000\n"
"s_add_u32 m0, s78, 0x1c400\n"
"v_exp_f32 v198, v198\n"
"buffer_load_dwordx4 v115, s[44:47], s77 offen sc1 lds\n"
"s_sub_u32 s77, s95, 0x4000\n"
"s_add_u32 m0, s78, 0x1c800\n"
"v_exp_f32 v199, v199\n"
"buffer_load_dwordx4 v116, s[44:47], s77 offen sc1 lds\n"
"v_cvt_pk_bf16_f32 v227, v198, v199\n"
"s_sub_u32 s77, s95, 0x4000\n"
"s_add_u32 m0, s78, 0x1cc00\n"
"v_exp_f32 v200, v200\n"
"buffer_load_dwordx4 v117, s[44:47], s77 offen sc1 lds\n"
"s_add_u32 m0, s78, 0x10000\n"
"v_exp_f32 v201, v201\n"
"buffer_load_dwordx4 v114, s[44:47], s95 offen sc1 lds\n"
"v_cvt_pk_bf16_f32 v228, v200, v201\n"
"s_add_u32 m0, s78, 0x10400\n"
"v_exp_f32 v202, v202\n"
"buffer_load_dwordx4 v115, s[44:47], s95 offen sc1 lds\n"
"s_add_u32 m0, s78, 0x10800\n"
"v_exp_f32 v203, v203\n"
"buffer_load_dwordx4 v116, s[44:47], s95 offen sc1 lds\n"
"v_cvt_pk_bf16_f32 v229, v202, v203\n"
"s_add_u32 m0, s78, 0x10c00\n"
"v_exp_f32 v204, v204\n"
"v_mfma_f32_16x16x32_bf16 v[208:211], a[192:195], a[144:147], v[244:247]\n"
"buffer_load_dwordx4 v117, s[44:47], s95 offen sc1 lds\n"
"v_mfma_f32_16x16x32_bf16 v[208:211], a[196:199], a[148:151], v[208:211]\n"
"s_add_u32 m0, s78, 0\n"
"v_exp_f32 v205, v205\n"
"v_mfma_f32_16x16x32_bf16 v[208:211], a[200:203], a[152:155], v[208:211]\n"
"buffer_load_dwordx4 v110, s[40:43], s94 offen sc1 lds\n"
"v_cvt_pk_bf16_f32 v230, v204, v205\n"
"v_mfma_f32_16x16x32_bf16 v[208:211], a[204:207], a[156:159], v[208:211]\n"
"s_add_u32 m0, s78, 0x400\n"
"v_exp_f32 v206, v206\n"
"v_mfma_f32_16x16x32_bf16 v[212:215], a[220:223], a[156:159], v[244:247]\n"
"buffer_load_dwordx4 v111, s[40:43], s94 offen sc1 lds\n"
"v_mfma_f32_16x16x32_bf16 v[212:215], a[216:219], a[152:155], v[212:215]\n"
"s_add_u32 m0, s78, 0x800\n"
"v_exp_f32 v207, v207\n"
"v_mfma_f32_16x16x32_bf16 v[212:215], a[212:215], a[148:151], v[212:215]\n"
"buffer_load_dwordx4 v112, s[40:43], s94 offen sc1 lds\n"
"v_cvt_pk_bf16_f32 v231, v206, v207\n"
"v_mfma_f32_16x16x32_bf16 v[212:215], a[208:211], a[144:147], v[212:215]\n"
"v_mfma_f32_16x16x32_bf16 v[216:219], a[224:227], a[144:147], v[244:247]\n"
"s_add_u32 m0, s78, 0xc00\n"
"v_exp_f32 v208, v208\n"
"v_mfma_f32_16x16x32_bf16 v[216:219], a[228:231], a[148:151], v[216:219]\n"
"buffer_load_dwordx4 v113, s[40:43], s94 offen sc1 lds\n"
"v_mfma_f32_16x16x32_bf16 v[216:219], a[232:235], a[152:155], v[216:219]\n"
"v_exp_f32 v209, v209\n"
"v_mfma_f32_16x16x32_bf16 v[216:219], a[236:239], a[156:159], v[216:219]\n"
"s_add_u32 s94, s94, 0x4000\n"
"ds_read_b64_tr_b16 v[128:129], v108 offset:16384\n"
"v_cvt_pk_bf16_f32 v232, v208, v209\n"
"v_mfma_f32_16x16x32_bf16 v[220:223], a[252:255], a[156:159], v[244:247]\n"
"s_add_u32 s95, s95, 0x4000\n"
"v_exp_f32 v210, v210\n"
"v_mfma_f32_16x16x32_bf16 v[220:223], a[248:251], a[152:155], v[220:223]\n"
"ds_read_b64_tr_b16 v[130:131], v108 offset:20480\n"
"v_mfma_f32_16x16x32_bf16 v[220:223], a[244:247], a[148:151], v[220:223]\n"
"v_exp_f32 v211, v211\n"
"v_mfma_f32_16x16x32_bf16 v[220:223], a[240:243], a[144:147], v[220:223]\n"
"ds_read_b64_tr_b16 v[132:133], v108 offset:24576\n"
"v_cvt_pk_bf16_f32 v233, v210, v211\n"
"v_mfma_f32_16x16x32_bf16 a[0:3], v[16:19], v[224:227], 0\n"
"v_exp_f32 v212, v212\n"
"v_mfma_f32_16x16x32_bf16 a[0:3], v[20:23], v[228:231], a[0:3]\n"
"ds_read_b64_tr_b16 v[134:135], v108 offset:28672\n"
"v_mfma_f32_16x16x32_bf16 a[16:19], v[28:31], v[228:231], 0\n"
"v_exp_f32 v213, v213\n"
"v_mfma_f32_16x16x32_bf16 a[16:19], v[24:27], v[224:227], a[16:19]\n"
"ds_read_b64_tr_b16 v[136:137], v109 offset:16384\n"
"v_cvt_pk_bf16_f32 v234, v212, v213\n"
"v_mfma_f32_16x16x32_bf16 a[32:35], v[32:35], v[224:227], 0\n"
"v_exp_f32 v214, v214\n"
"v_mfma_f32_16x16x32_bf16 a[32:35], v[36:39], v[228:231], a[32:35]\n"
"ds_read_b64_tr_b16 v[138:139], v109 offset:20480\n"
"v_mfma_f32_16x16x32_bf16 a[48:51], v[44:47], v[228:231], 0\n"
"v_exp_f32 v215, v215\n"
"v_mfma_f32_16x16x32_bf16 a[48:51], v[40:43], v[224:227], a[48:51]\n"
"ds_read_b64_tr_b16 v[140:141], v109 offset:24576\n"
"v_cvt_pk_bf16_f32 v235, v214, v215\n"
"v_mfma_f32_16x16x32_bf16 a[64:67], v[48:51], v[224:227], 0\n"
"ds_read_b64_tr_b16 v[142:143], v109 offset:28672\n"
"v_mfma_f32_16x16x32_bf16 a[64:67], v[52:55], v[228:231], a[64:67]\n"
"v_exp_f32 v216, v216\n"
"v_mfma_f32_16x16x32_bf16 a[80:83], v[60:63], v[228:231], 0\n"
"ds_read_b64_tr_b16 v[144:145], v108 offset:16896\n"
"v_mfma_f32_16x16x32_bf16 a[80:83], v[56:59], v[224:227], a[80:83]\n"
"v_exp_f32 v217, v217\n"
"v_mfma_f32_16x16x32_bf16 a[96:99], v[64:67], v[224:227], 0\n"
"ds_read_b64_tr_b16 v[146:147], v108 offset:20992\n"
"v_cvt_pk_bf16_f32 v236, v216, v217\n"
"v_mfma_f32_16x16x32_bf16 a[96:99], v[68:71], v[228:231], a[96:99]\n"
"v_exp_f32 v218, v218\n"
"v_mfma_f32_16x16x32_bf16 a[112:115], v[76:79], v[228:231], 0\n"
"ds_read_b64_tr_b16 v[148:149], v108 offset:25088\n"
"v_mfma_f32_16x16x32_bf16 a[112:115], v[72:75], v[224:227], a[112:115]\n"
"v_exp_f32 v219, v219\n"
"v_mfma_f32_16x16x32_bf16 v[80:83], v[96:99], v[224:227], 0\n"
"ds_read_b64_tr_b16 v[150:151], v108 offset:29184\n"
"v_cvt_pk_bf16_f32 v237, v218, v219\n"
"v_mfma_f32_16x16x32_bf16 v[80:83], v[96:99], v[228:231], v[80:83]\n"
"v_exp_f32 v220, v220\n"
"v_mfma_f32_16x16x32_bf16 v[192:195], a[192:195], a[160:163], v[248:251]\n"
"ds_read_b64_tr_b16 v[152:153], v109 offset:16896\n"
"v_mfma_f32_16x16x32_bf16 v[192:195], a[196:199], a[164:167], v[192:195]\n"
"v_exp_f32 v221, v221\n"
"v_mfma_f32_16x16x32_bf16 v[192:195], a[200:203], a[168:171], v[192:195]\n"
"ds_read_b64_tr_b16 v[154:155], v109 offset:20992\n"
"v_cvt_pk_bf16_f32 v238, v220, v221\n"
"v_mfma_f32_16x16x32_bf16 v[192:195], a[204:207], a[172:175], v[192:195]\n"
"v_exp_f32 v222, v222\n"
"v_mfma_f32_16x16x32_bf16 v[196:199], a[220:223], a[172:175], v[248:251]\n"
"ds_read_b64_tr_b16 v[156:157], v109 offset:25088\n"
"v_mfma_f32_16x16x32_bf16 v[196:199], a[216:219], a[168:171], v[196:199]\n"
"v_exp_f32 v223, v223\n"
"v_mfma_f32_16x16x32_bf16 v[196:199], a[212:215], a[164:167], v[196:199]\n"
"ds_read_b64_tr_b16 v[158:159], v109 offset:29184\n"
"v_cvt_pk_bf16_f32 v239, v222, v223\n"
"v_mfma_f32_16x16x32_bf16 v[196:199], a[208:211], a[160:163], v[196:199]\n"
"ds_read_b64_tr_b16 v[160:161], v108 offset:17408\n"
"v_mfma_f32_16x16x32_bf16 v[200:203], a[224:227], a[160:163], v[248:251]\n"
"v_exp_f32 v192, v192\n"
"v_mfma_f32_16x16x32_bf16 v[200:203], a[228:231], a[164:167], v[200:203]\n"
"ds_read_b64_tr_b16 v[162:163], v108 offset:21504\n"
"v_mfma_f32_16x16x32_bf16 v[200:203], a[232:235], a[168:171], v[200:203]\n"
"v_exp_f32 v193, v193\n"
"v_mfma_f32_16x16x32_bf16 v[200:203], a[236:239], a[172:175], v[200:203]\n"
"ds_read_b64_tr_b16 v[164:165], v108 offset:25600\n"
"v_cvt_pk_bf16_f32 v224, v192, v193\n"
"v_mfma_f32_16x16x32_bf16 v[204:207], a[252:255], a[172:175], v[248:251]\n"
"v_exp_f32 v194, v194\n"
"v_mfma_f32_16x16x32_bf16 v[204:207], a[248:251], a[168:171], v[204:207]\n"
"ds_read_b64_tr_b16 v[166:167], v108 offset:29696\n"
"v_mfma_f32_16x16x32_bf16 v[204:207], a[244:247], a[164:167], v[204:207]\n"
"v_exp_f32 v195, v195\n"
"v_mfma_f32_16x16x32_bf16 v[204:207], a[240:243], a[160:163], v[204:207]\n"
"ds_read_b64_tr_b16 v[168:169], v109 offset:17408\n"
"v_cvt_pk_bf16_f32 v225, v194, v195\n"
"v_mfma_f32_16x16x32_bf16 a[4:7], v[16:19], v[232:235], 0\n"
"v_exp_f32 v196, v196\n"
"v_mfma_f32_16x16x32_bf16 a[4:7], v[20:23], v[236:239], a[4:7]\n"
"ds_read_b64_tr_b16 v[170:171], v109 offset:21504\n"
"v_mfma_f32_16x16x32_bf16 a[20:23], v[28:31], v[236:239], 0\n"
"v_exp_f32 v197, v197\n"
"v_mfma_f32_16x16x32_bf16 a[20:23], v[24:27], v[232:235], a[20:23]\n"
"ds_read_b64_tr_b16 v[172:173], v109 offset:25600\n"
"v_cvt_pk_bf16_f32 v226, v196, v197\n"
"v_mfma_f32_16x16x32_bf16 a[36:39], v[32:35], v[232:235], 0\n"
"v_exp_f32 v198, v198\n"
"v_mfma_f32_16x16x32_bf16 a[36:39], v[36:39], v[236:239], a[36:39]\n"
"ds_read_b64_tr_b16 v[174:175], v109 offset:29696\n"
"v_mfma_f32_16x16x32_bf16 a[52:55], v[44:47], v[236:239], 0\n"
"v_exp_f32 v199, v199\n"
"v_mfma_f32_16x16x32_bf16 a[52:55], v[40:43], v[232:235], a[52:55]\n"
"ds_read_b64_tr_b16 v[176:177], v108 offset:17920\n"
"v_cvt_pk_bf16_f32 v227, v198, v199\n"
"v_mfma_f32_16x16x32_bf16 a[68:71], v[48:51], v[232:235], 0\n"
"ds_read_b64_tr_b16 v[178:179], v108 offset:22016\n"
"v_mfma_f32_16x16x32_bf16 a[68:71], v[52:55], v[236:239], a[68:71]\n"
"v_exp_f32 v200, v200\n"
"v_mfma_f32_16x16x32_bf16 a[84:87], v[60:63], v[236:239], 0\n"
"ds_read_b64_tr_b16 v[180:181], v108 offset:26112\n"
"v_mfma_f32_16x16x32_bf16 a[84:87], v[56:59], v[232:235], a[84:87]\n"
"v_exp_f32 v201, v201\n"
"v_mfma_f32_16x16x32_bf16 a[100:103], v[64:67], v[232:235], 0\n"
"ds_read_b64_tr_b16 v[182:183], v108 offset:30208\n"
"v_cvt_pk_bf16_f32 v228, v200, v201\n"
"v_mfma_f32_16x16x32_bf16 a[100:103], v[68:71], v[236:239], a[100:103]\n"
"v_exp_f32 v202, v202\n"
"v_mfma_f32_16x16x32_bf16 a[116:119], v[76:79], v[236:239], 0\n"
"ds_read_b64_tr_b16 v[184:185], v109 offset:17920\n"
"v_mfma_f32_16x16x32_bf16 a[116:119], v[72:75], v[232:235], a[116:119]\n"
"v_exp_f32 v203, v203\n"
"v_mfma_f32_16x16x32_bf16 v[84:87], v[96:99], v[232:235], 0\n"
"ds_read_b64_tr_b16 v[186:187], v109 offset:22016\n"
"v_cvt_pk_bf16_f32 v229, v202, v203\n"
"v_mfma_f32_16x16x32_bf16 v[84:87], v[96:99], v[236:239], v[84:87]\n"
"v_exp_f32 v204, v204\n"
"v_mfma_f32_16x16x32_bf16 v[208:211], a[192:195], a[176:179], v[252:255]\n"
"ds_read_b64_tr_b16 v[188:189], v109 offset:26112\n"
"v_mfma_f32_16x16x32_bf16 v[208:211], a[196:199], a[180:183], v[208:211]\n"
"v_exp_f32 v205, v205\n"
"v_mfma_f32_16x16x32_bf16 v[208:211], a[200:203], a[184:187], v[208:211]\n"
"ds_read_b64_tr_b16 v[190:191], v109 offset:30208\n"
"v_cvt_pk_bf16_f32 v230, v204, v205\n"
"v_mfma_f32_16x16x32_bf16 v[208:211], a[204:207], a[188:191], v[208:211]\n"
"v_exp_f32 v206, v206\n"
"v_mfma_f32_16x16x32_bf16 v[212:215], a[220:223], a[188:191], v[252:255]\n"
"ds_read_b128 a[192:195], v104 offset:16384\n"
"v_mfma_f32_16x16x32_bf16 v[212:215], a[216:219], a[184:187], v[212:215]\n"
"v_exp_f32 v207, v207\n"
"v_mfma_f32_16x16x32_bf16 v[212:215], a[212:215], a[180:183], v[212:215]\n"
"ds_read_b128 a[196:199], v105 offset:16384\n"
"v_cvt_pk_bf16_f32 v231, v206, v207\n"
"v_mfma_f32_16x16x32_bf16 v[212:215], a[208:211], a[176:179], v[212:215]\n"
"ds_read_b128 a[200:203], v106 offset:16384\n"
"v_mfma_f32_16x16x32_bf16 v[216:219], a[224:227], a[176:179], v[252:255]\n"
"v_exp_f32 v208, v208\n"
"v_mfma_f32_16x16x32_bf16 v[216:219], a[228:231], a[180:183], v[216:219]\n"
"ds_read_b128 a[204:207], v107 offset:16384\n"
"v_mfma_f32_16x16x32_bf16 v[216:219], a[232:235], a[184:187], v[216:219]\n"
"v_exp_f32 v209, v209\n"
"v_mfma_f32_16x16x32_bf16 v[216:219], a[236:239], a[188:191], v[216:219]\n"
"ds_read_b128 a[208:211], v104 offset:20480\n"
"v_cvt_pk_bf16_f32 v232, v208, v209\n"
"v_mfma_f32_16x16x32_bf16 v[220:223], a[252:255], a[188:191], v[252:255]\n"
"v_exp_f32 v210, v210\n"
"v_mfma_f32_16x16x32_bf16 v[220:223], a[248:251], a[184:187], v[220:223]\n"
"ds_read_b128 a[212:215], v105 offset:20480\n"
"v_mfma_f32_16x16x32_bf16 v[220:223], a[244:247], a[180:183], v[220:223]\n"
"v_exp_f32 v211, v211\n"
"v_mfma_f32_16x16x32_bf16 v[220:223], a[240:243], a[176:179], v[220:223]\n"
"ds_read_b128 a[216:219], v106 offset:20480\n"
"v_cvt_pk_bf16_f32 v233, v210, v211\n"
"v_mfma_f32_16x16x32_bf16 a[8:11], v[16:19], v[224:227], 0\n"
"v_exp_f32 v212, v212\n"
"v_mfma_f32_16x16x32_bf16 a[8:11], v[20:23], v[228:231], a[8:11]\n"
"ds_read_b128 a[220:223], v107 offset:20480\n"
"v_mfma_f32_16x16x32_bf16 a[24:27], v[28:31], v[228:231], 0\n"
"v_exp_f32 v213, v213\n"
"v_mfma_f32_16x16x32_bf16 a[24:27], v[24:27], v[224:227], a[24:27]\n"
"ds_read_b128 a[224:227], v104 offset:24576\n"
"v_cvt_pk_bf16_f32 v234, v212, v213\n"
"v_mfma_f32_16x16x32_bf16 a[40:43], v[32:35], v[224:227], 0\n"
"v_exp_f32 v214, v214\n"
"v_mfma_f32_16x16x32_bf16 a[40:43], v[36:39], v[228:231], a[40:43]\n"
"ds_read_b128 a[228:231], v105 offset:24576\n"
"v_mfma_f32_16x16x32_bf16 a[56:59], v[44:47], v[228:231], 0\n"
"v_exp_f32 v215, v215\n"
"v_mfma_f32_16x16x32_bf16 a[56:59], v[40:43], v[224:227], a[56:59]\n"
"ds_read_b128 a[232:235], v106 offset:24576\n"
"v_cvt_pk_bf16_f32 v235, v214, v215\n"
"v_mfma_f32_16x16x32_bf16 a[72:75], v[48:51], v[224:227], 0\n"
"ds_read_b128 a[236:239], v107 offset:24576\n"
"v_mfma_f32_16x16x32_bf16 a[72:75], v[52:55], v[228:231], a[72:75]\n"
"v_exp_f32 v216, v216\n"
"v_mfma_f32_16x16x32_bf16 a[88:91], v[60:63], v[228:231], 0\n"
"ds_read_b128 a[240:243], v104 offset:28672\n"
"v_mfma_f32_16x16x32_bf16 a[88:91], v[56:59], v[224:227], a[88:91]\n"
"v_exp_f32 v217, v217\n"
"v_mfma_f32_16x16x32_bf16 a[104:107], v[64:67], v[224:227], 0\n"
"ds_read_b128 a[244:247], v105 offset:28672\n"
"v_cvt_pk_bf16_f32 v236, v216, v217\n"
"v_mfma_f32_16x16x32_bf16 a[104:107], v[68:71], v[228:231], a[104:107]\n"
"v_exp_f32 v218, v218\n"
"v_mfma_f32_16x16x32_bf16 a[120:123], v[76:79], v[228:231], 0\n"
"ds_read_b128 a[248:251], v106 offset:28672\n"
"v_mfma_f32_16x16x32_bf16 a[120:123], v[72:75], v[224:227], a[120:123]\n"
"v_exp_f32 v219, v219\n"
"v_mfma_f32_16x16x32_bf16 v[88:91], v[96:99], v[224:227], 0\n"
"ds_read_b128 a[252:255], v107 offset:28672\n"
"v_cvt_pk_bf16_f32 v237, v218, v219\n"
"v_mfma_f32_16x16x32_bf16 v[88:91], v[96:99], v[228:231], v[88:91]\n"
"s_waitcnt lgkmcnt(4)\n"
"v_exp_f32 v220, v220\n"
"v_mfma_f32_16x16x32_bf16 v[192:195], a[192:195], a[128:131], v[240:243]\n"
"v_mfma_f32_16x16x32_bf16 v[192:195], a[196:199], a[132:135], v[192:195]\n"
"v_exp_f32 v221, v221\n"
"v_mfma_f32_16x16x32_bf16 v[192:195], a[200:203], a[136:139], v[192:195]\n"
"v_cvt_pk_bf16_f32 v238, v220, v221\n"
"v_mfma_f32_16x16x32_bf16 v[192:195], a[204:207], a[140:143], v[192:195]\n"
"v_exp_f32 v222, v222\n"
"v_mfma_f32_16x16x32_bf16 v[196:199], a[220:223], a[140:143], v[240:243]\n"
"v_mfma_f32_16x16x32_bf16 v[196:199], a[216:219], a[136:139], v[196:199]\n"
"v_exp_f32 v223, v223\n"
"v_mfma_f32_16x16x32_bf16 v[196:199], a[212:215], a[132:135], v[196:199]\n"
"v_cvt_pk_bf16_f32 v239, v222, v223\n"
"v_mfma_f32_16x16x32_bf16 v[196:199], a[208:211], a[128:131], v[196:199]\n"
"v_mfma_f32_16x16x32_bf16 v[200:203], a[224:227], a[128:131], v[240:243]\n"
"s_waitcnt vmcnt(12) lgkmcnt(0)\n"
"s_barrier\n"
"s_mov_b64 s[98:99], %7\n"
"s_mov_b64 s[76:77], %4\n"
"s_cmp_lg_u32 s73, 0\n"
"s_cselect_b32 s56, s76, s98\n"
"s_cselect_b32 s57, s77, s99\n"
"s_and_b32 s57, s57, 0xffff\n"
"s_cmp_lg_u32 s73, 0\n"
"s_mov_b32 s98, 0x144000\n"
"s_cmov_b32 s98, 0x120000\n"
"s_mul_i32 s99, s81, s98\n"
"s_add_u32 s58, s99, s98\n"
"s_mov_b32 s59, 0x20000\n"
"s_lshl_b32 s76, s83, 2\n"
"s_add_u32 s76, s76, s72\n"
"s_lshl_b32 s76, s76, 14\n"
"s_add_u32 s99, s99, s76\n"
"v_lshlrev_b32 v124, 7, v5\n"
"v_exp_f32 v192, v192\n"
"v_mfma_f32_16x16x32_bf16 v[200:203], a[228:231], a[132:135], v[200:203]\n"
"v_mfma_f32_16x16x32_bf16 v[200:203], a[232:235], a[136:139], v[200:203]\n"
"s_add_u32 m0, s78, 0x14000\n"
"v_exp_f32 v193, v193\n"
"v_mfma_f32_16x16x32_bf16 v[200:203], a[236:239], a[140:143], v[200:203]\n"
"buffer_load_dwordx4 v114, s[44:47], s95 offen sc1 lds\n"
"v_cvt_pk_bf16_f32 v224, v192, v193\n"
"v_mfma_f32_16x16x32_bf16 v[204:207], a[252:255], a[140:143], v[240:243]\n"
"v_exp_f32 v194, v194\n"
"v_mfma_f32_16x16x32_bf16 v[204:207], a[248:251], a[136:139], v[204:207]\n"
"v_mfma_f32_16x16x32_bf16 v[204:207], a[244:247], a[132:135], v[204:207]\n"
"s_add_u32 m0, s78, 0x14400\n"
"v_exp_f32 v195, v195\n"
"v_mfma_f32_16x16x32_bf16 v[204:207], a[240:243], a[128:131], v[204:207]\n"
"buffer_load_dwordx4 v115, s[44:47], s95 offen sc1 lds\n"
"v_cvt_pk_bf16_f32 v225, v194, v195\n"
"v_mfma_f32_16x16x32_bf16 a[12:15], v[16:19], v[232:235], 0\n"
"v_exp_f32 v196, v196\n"
"v_mfma_f32_16x16x32_bf16 a[12:15], v[20:23], v[236:239], a[12:15]\n"
"v_mfma_f32_16x16x32_bf16 a[28:31], v[28:31], v[236:239], 0\n"
"s_add_u32 m0, s78, 0x14800\n"
"v_exp_f32 v197, v197\n"
"v_mfma_f32_16x16x32_bf16 a[28:31], v[24:27], v[232:235], a[28:31]\n"
"buffer_load_dwordx4 v116, s[44:47], s95 offen sc1 lds\n"
"v_cvt_pk_bf16_f32 v226, v196, v197\n"
"v_mfma_f32_16x16x32_bf16 a[44:47], v[32:35], v[232:235], 0\n"
"v_exp_f32 v198, v198\n"
"v_mfma_f32_16x16x32_bf16 a[44:47], v[36:39], v[236:239], a[44:47]\n"
"v_mfma_f32_16x16x32_bf16 a[60:63], v[44:47], v[236:239], 0\n"
"s_add_u32 m0, s78, 0x14c00\n"
"v_exp_f32 v199, v199\n"
"v_mfma_f32_16x16x32_bf16 a[60:63], v[40:43], v[232:235], a[60:63]\n"
"buffer_load_dwordx4 v117, s[44:47], s95 offen sc1 lds\n"
"v_cvt_pk_bf16_f32 v227, v198, v199\n"
"v_mfma_f32_16x16x32_bf16 a[76:79], v[48:51], v[232:235], 0\n"
"v_mfma_f32_16x16x32_bf16 a[76:79], v[52:55], v[236:239], a[76:79]\n"
"s_add_u32 m0, s78, 0x4000\n"
"v_exp_f32 v200, v200\n"
"v_mfma_f32_16x16x32_bf16 a[92:95], v[60:63], v[236:239], 0\n"
"buffer_load_dwordx4 v110, s[40:43], s94 offen sc1 lds\n"
"v_mfma_f32_16x16x32_bf16 a[92:95], v[56:59], v[232:235], a[92:95]\n"
"v_exp_f32 v201, v201\n"
"v_mfma_f32_16x16x32_bf16 a[108:111], v[64:67], v[232:235], 0\n"
"v_cvt_pk_bf16_f32 v228, v200, v201\n"
"v_mfma_f32_16x16x32_bf16 a[108:111], v[68:71], v[236:239], a[108:111]\n"
"s_add_u32 m0, s78, 0x4400\n"
"v_exp_f32 v202, v202\n"
"v_mfma_f32_16x16x32_bf16 a[124:127], v[76:79], v[236:239], 0\n"
"buffer_load_dwordx4 v111, s[40:43], s94 offen sc1 lds\n"
"v_mfma_f32_16x16x32_bf16 a[124:127], v[72:75], v[232:235], a[124:127]\n"
"v_exp_f32 v203, v203\n"
"v_mfma_f32_16x16x32_bf16 v[92:95], v[96:99], v[232:235], 0\n"
"v_cvt_pk_bf16_f32 v229, v202, v203\n"
"v_mfma_f32_16x16x32_bf16 v[92:95], v[96:99], v[236:239], v[92:95]\n"
"s_add_u32 m0, s78, 0x4800\n"
"v_exp_f32 v204, v204\n"
"v_mfma_f32_16x16x32_bf16 v[208:211], a[192:195], a[144:147], v[244:247]\n"
"buffer_load_dwordx4 v112, s[40:43], s94 offen sc1 lds\n"
"v_mfma_f32_16x16x32_bf16 v[208:211], a[196:199], a[148:151], v[208:211]\n"
"v_exp_f32 v205, v205\n"
"v_mfma_f32_16x16x32_bf16 v[208:211], a[200:203], a[152:155], v[208:211]\n"
"v_cvt_pk_bf16_f32 v230, v204, v205\n"
"v_mfma_f32_16x16x32_bf16 v[208:211], a[204:207], a[156:159], v[208:211]\n"
"s_add_u32 m0, s78, 0x4c00\n"
"v_exp_f32 v206, v206\n"
"v_mfma_f32_16x16x32_bf16 v[212:215], a[220:223], a[156:159], v[244:247]\n"
"buffer_load_dwordx4 v113, s[40:43], s94 offen sc1 lds\n"
"v_mfma_f32_16x16x32_bf16 v[212:215], a[216:219], a[152:155], v[212:215]\n"
"v_exp_f32 v207, v207\n"
"v_mfma_f32_16x16x32_bf16 v[212:215], a[212:215], a[148:151], v[212:215]\n"
"v_cvt_pk_bf16_f32 v231, v206, v207\n"
"v_mfma_f32_16x16x32_bf16 v[212:215], a[208:211], a[144:147], v[212:215]\n"
"s_add_u32 s94, s94, 0x4000\n"
"v_mfma_f32_16x16x32_bf16 v[216:219], a[224:227], a[144:147], v[244:247]\n"
"s_add_u32 s95, s95, 0x4000\n"
"v_exp_f32 v208, v208\n"
"v_mfma_f32_16x16x32_bf16 v[216:219], a[228:231], a[148:151], v[216:219]\n"
"buffer_load_dword v11, v124, s[56:59], s99 offen\n"
"v_mfma_f32_16x16x32_bf16 v[216:219], a[232:235], a[152:155], v[216:219]\n"
"v_exp_f32 v209, v209\n"
"v_mfma_f32_16x16x32_bf16 v[216:219], a[236:239], a[156:159], v[216:219]\n"
"ds_read_b64_tr_b16 v[16:17], v108 offset:32768\n"
"v_cvt_pk_bf16_f32 v232, v208, v209\n"
"v_mfma_f32_16x16x32_bf16 v[220:223], a[252:255], a[156:159], v[244:247]\n"
"v_exp_f32 v210, v210\n"
"v_mfma_f32_16x16x32_bf16 v[220:223], a[248:251], a[152:155], v[220:223]\n"
"ds_read_b64_tr_b16 v[18:19], v108 offset:36864\n"
"v_mfma_f32_16x16x32_bf16 v[220:223], a[244:247], a[148:151], v[220:223]\n"
"v_exp_f32 v211, v211\n"
"v_mfma_f32_16x16x32_bf16 v[220:223], a[240:243], a[144:147], v[220:223]\n"
"ds_read_b64_tr_b16 v[20:21], v108 offset:40960\n"
"v_cvt_pk_bf16_f32 v233, v210, v211\n"
"v_mfma_f32_16x16x32_bf16 a[0:3], v[128:131], v[224:227], a[0:3]\n"
"v_exp_f32 v212, v212\n"
"v_mfma_f32_16x16x32_bf16 a[0:3], v[132:135], v[228:231], a[0:3]\n"
"ds_read_b64_tr_b16 v[22:23], v108 offset:45056\n"
"v_mfma_f32_16x16x32_bf16 a[16:19], v[140:143], v[228:231], a[16:19]\n"
"v_exp_f32 v213, v213\n"
"v_mfma_f32_16x16x32_bf16 a[16:19], v[136:139], v[224:227], a[16:19]\n"
"ds_read_b64_tr_b16 v[24:25], v109 offset:32768\n"
"v_cvt_pk_bf16_f32 v234, v212, v213\n"
"v_mfma_f32_16x16x32_bf16 a[32:35], v[144:147], v[224:227], a[32:35]\n"
"v_exp_f32 v214, v214\n"
"v_mfma_f32_16x16x32_bf16 a[32:35], v[148:151], v[228:231], a[32:35]\n"
"ds_read_b64_tr_b16 v[26:27], v109 offset:36864\n"
"v_mfma_f32_16x16x32_bf16 a[48:51], v[156:159], v[228:231], a[48:51]\n"
"v_exp_f32 v215, v215\n"
"v_mfma_f32_16x16x32_bf16 a[48:51], v[152:155], v[224:227], a[48:51]\n"
"ds_read_b64_tr_b16 v[28:29], v109 offset:40960\n"
"v_cvt_pk_bf16_f32 v235, v214, v215\n"
"v_mfma_f32_16x16x32_bf16 a[64:67], v[160:163], v[224:227], a[64:67]\n"
"ds_read_b64_tr_b16 v[30:31], v109 offset:45056\n"
"v_mfma_f32_16x16x32_bf16 a[64:67], v[164:167], v[228:231], a[64:67]\n"
"v_exp_f32 v216, v216\n"
"v_mfma_f32_16x16x32_bf16 a[80:83], v[172:175], v[228:231], a[80:83]\n"
"ds_read_b64_tr_b16 v[32:33], v108 offset:33280\n"
"v_mfma_f32_16x16x32_bf16 a[80:83], v[168:171], v[224:227], a[80:83]\n"
"v_exp_f32 v217, v217\n"
"v_mfma_f32_16x16x32_bf16 a[96:99], v[176:179], v[224:227], a[96:99]\n"
"ds_read_b64_tr_b16 v[34:35], v108 offset:37376\n"
"v_cvt_pk_bf16_f32 v236, v216, v217\n"
"v_mfma_f32_16x16x32_bf16 a[96:99], v[180:183], v[228:231], a[96:99]\n"
"v_exp_f32 v218, v218\n"
"v_mfma_f32_16x16x32_bf16 a[112:115], v[188:191], v[228:231], a[112:115]\n"
"ds_read_b64_tr_b16 v[36:37], v108 offset:41472\n"
"v_mfma_f32_16x16x32_bf16 a[112:115], v[184:187], v[224:227], a[112:115]\n"
"v_exp_f32 v219, v219\n"
"v_mfma_f32_16x16x32_bf16 v[80:83], v[96:99], v[224:227], v[80:83]\n"
"ds_read_b64_tr_b16 v[38:39], v108 offset:45568\n"
"v_cvt_pk_bf16_f32 v237, v218, v219\n"
"v_mfma_f32_16x16x32_bf16 v[80:83], v[96:99], v[228:231], v[80:83]\n"
"v_exp_f32 v220, v220\n"
"v_mfma_f32_16x16x32_bf16 v[192:195], a[192:195], a[160:163], v[248:251]\n"
"ds_read_b64_tr_b16 v[40:41], v109 offset:33280\n"
"v_mfma_f32_16x16x32_bf16 v[192:195], a[196:199], a[164:167], v[192:195]\n"
"v_exp_f32 v221, v221\n"
"v_mfma_f32_16x16x32_bf16 v[192:195], a[200:203], a[168:171], v[192:195]\n"
"ds_read_b64_tr_b16 v[42:43], v109 offset:37376\n"
"v_cvt_pk_bf16_f32 v238, v220, v221\n"
"v_mfma_f32_16x16x32_bf16 v[192:195], a[204:207], a[172:175], v[192:195]\n"
"v_exp_f32 v222, v222\n"
"v_mfma_f32_16x16x32_bf16 v[196:199], a[220:223], a[172:175], v[248:251]\n"
"ds_read_b64_tr_b16 v[44:45], v109 offset:41472\n"
"v_mfma_f32_16x16x32_bf16 v[196:199], a[216:219], a[168:171], v[196:199]\n"
"v_exp_f32 v223, v223\n"
"v_mfma_f32_16x16x32_bf16 v[196:199], a[212:215], a[164:167], v[196:199]\n"
"ds_read_b64_tr_b16 v[46:47], v109 offset:45568\n"
"v_cvt_pk_bf16_f32 v239, v222, v223\n"
"v_mfma_f32_16x16x32_bf16 v[196:199], a[208:211], a[160:163], v[196:199]\n"
"ds_read_b64_tr_b16 v[48:49], v108 offset:33792\n"
"v_mfma_f32_16x16x32_bf16 v[200:203], a[224:227], a[160:163], v[248:251]\n"
"v_exp_f32 v192, v192\n"
"v_mfma_f32_16x16x32_bf16 v[200:203], a[228:231], a[164:167], v[200:203]\n"
"ds_read_b64_tr_b16 v[50:51], v108 offset:37888\n"
"v_mfma_f32_16x16x32_bf16 v[200:203], a[232:235], a[168:171], v[200:203]\n"
"v_exp_f32 v193, v193\n"
"v_mfma_f32_16x16x32_bf16 v[200:203], a[236:239], a[172:175], v[200:203]\n"
"ds_read_b64_tr_b16 v[52:53], v108 offset:41984\n"
"v_cvt_pk_bf16_f32 v224, v192, v193\n"
"v_mfma_f32_16x16x32_bf16 v[204:207], a[252:255], a[172:175], v[248:251]\n"
"v_exp_f32 v194, v194\n"
"v_mfma_f32_16x16x32_bf16 v[204:207], a[248:251], a[168:171], v[204:207]\n"
"ds_read_b64_tr_b16 v[54:55], v108 offset:46080\n"
"v_mfma_f32_16x16x32_bf16 v[204:207], a[244:247], a[164:167], v[204:207]\n"
"v_exp_f32 v195, v195\n"
"v_mfma_f32_16x16x32_bf16 v[204:207], a[240:243], a[160:163], v[204:207]\n"
"ds_read_b64_tr_b16 v[56:57], v109 offset:33792\n"
"v_cvt_pk_bf16_f32 v225, v194, v195\n"
"v_mfma_f32_16x16x32_bf16 a[4:7], v[128:131], v[232:235], a[4:7]\n"
"v_exp_f32 v196, v196\n"
"v_mfma_f32_16x16x32_bf16 a[4:7], v[132:135], v[236:239], a[4:7]\n"
"ds_read_b64_tr_b16 v[58:59], v109 offset:37888\n"
"v_mfma_f32_16x16x32_bf16 a[20:23], v[140:143], v[236:239], a[20:23]\n"
"v_exp_f32 v197, v197\n"
"v_mfma_f32_16x16x32_bf16 a[20:23], v[136:139], v[232:235], a[20:23]\n"
"ds_read_b64_tr_b16 v[60:61], v109 offset:41984\n"
"v_cvt_pk_bf16_f32 v226, v196, v197\n"
"v_mfma_f32_16x16x32_bf16 a[36:39], v[144:147], v[232:235], a[36:39]\n"
"v_exp_f32 v198, v198\n"
"v_mfma_f32_16x16x32_bf16 a[36:39], v[148:151], v[236:239], a[36:39]\n"
"ds_read_b64_tr_b16 v[62:63], v109 offset:46080\n"
"v_mfma_f32_16x16x32_bf16 a[52:55], v[156:159], v[236:239], a[52:55]\n"
"v_exp_f32 v199, v199\n"
"v_mfma_f32_16x16x32_bf16 a[52:55], v[152:155], v[232:235], a[52:55]\n"
"ds_read_b64_tr_b16 v[64:65], v108 offset:34304\n"
"v_cvt_pk_bf16_f32 v227, v198, v199\n"
"v_mfma_f32_16x16x32_bf16 a[68:71], v[160:163], v[232:235], a[68:71]\n"
"ds_read_b64_tr_b16 v[66:67], v108 offset:38400\n"
"v_mfma_f32_16x16x32_bf16 a[68:71], v[164:167], v[236:239], a[68:71]\n"
"v_exp_f32 v200, v200\n"
"v_mfma_f32_16x16x32_bf16 a[84:87], v[172:175], v[236:239], a[84:87]\n"
"ds_read_b64_tr_b16 v[68:69], v108 offset:42496\n"
"v_mfma_f32_16x16x32_bf16 a[84:87], v[168:171], v[232:235], a[84:87]\n"
"v_exp_f32 v201, v201\n"
"v_mfma_f32_16x16x32_bf16 a[100:103], v[176:179], v[232:235], a[100:103]\n"
"ds_read_b64_tr_b16 v[70:71], v108 offset:46592\n"
"v_cvt_pk_bf16_f32 v228, v200, v201\n"
"v_mfma_f32_16x16x32_bf16 a[100:103], v[180:183], v[236:239], a[100:103]\n"
"v_exp_f32 v202, v202\n"
"v_mfma_f32_16x16x32_bf16 a[116:119], v[188:191], v[236:239], a[116:119]\n"
"ds_read_b64_tr_b16 v[72:73], v109 offset:34304\n"
"v_mfma_f32_16x16x32_bf16 a[116:119], v[184:187], v[232:235], a[116:119]\n"
"v_exp_f32 v203, v203\n"
"v_mfma_f32_16x16x32_bf16 v[84:87], v[96:99], v[232:235], v[84:87]\n"
"ds_read_b64_tr_b16 v[74:75], v109 offset:38400\n"
"v_cvt_pk_bf16_f32 v229, v202, v203\n"
"v_mfma_f32_16x16x32_bf16 v[84:87], v[96:99], v[236:239], v[84:87]\n"
"v_exp_f32 v204, v204\n"
"v_mfma_f32_16x16x32_bf16 v[208:211], a[192:195], a[176:179], v[252:255]\n"
"ds_read_b64_tr_b16 v[76:77], v109 offset:42496\n"
"v_mfma_f32_16x16x32_bf16 v[208:211], a[196:199], a[180:183], v[208:211]\n"
"v_exp_f32 v205, v205\n"
"v_mfma_f32_16x16x32_bf16 v[208:211], a[200:203], a[184:187], v[208:211]\n"
"ds_read_b64_tr_b16 v[78:79], v109 offset:46592\n"
"v_cvt_pk_bf16_f32 v230, v204, v205\n"
"v_mfma_f32_16x16x32_bf16 v[208:211], a[204:207], a[188:191], v[208:211]\n"
"v_exp_f32 v206, v206\n"
"v_mfma_f32_16x16x32_bf16 v[212:215], a[220:223], a[188:191], v[252:255]\n"
"ds_read_b128 a[192:195], v104 offset:32768\n"
"v_mfma_f32_16x16x32_bf16 v[212:215], a[216:219], a[184:187], v[212:215]\n"
"v_exp_f32 v207, v207\n"
"v_mfma_f32_16x16x32_bf16 v[212:215], a[212:215], a[180:183], v[212:215]\n"
"ds_read_b128 a[196:199], v105 offset:32768\n"
"v_cvt_pk_bf16_f32 v231, v206, v207\n"
"v_mfma_f32_16x16x32_bf16 v[212:215], a[208:211], a[176:179], v[212:215]\n"
"ds_read_b128 a[200:203], v106 offset:32768\n"
"v_mfma_f32_16x16x32_bf16 v[216:219], a[224:227], a[176:179], v[252:255]\n"
"v_exp_f32 v208, v208\n"
"v_mfma_f32_16x16x32_bf16 v[216:219], a[228:231], a[180:183], v[216:219]\n"
"ds_read_b128 a[204:207], v107 offset:32768\n"
"v_mfma_f32_16x16x32_bf16 v[216:219], a[232:235], a[184:187], v[216:219]\n"
"v_exp_f32 v209, v209\n"
"v_mfma_f32_16x16x32_bf16 v[216:219], a[236:239], a[188:191], v[216:219]\n"
"ds_read_b128 a[208:211], v104 offset:36864\n"
"v_cvt_pk_bf16_f32 v232, v208, v209\n"
"v_mfma_f32_16x16x32_bf16 v[220:223], a[252:255], a[188:191], v[252:255]\n"
"v_exp_f32 v210, v210\n"
"v_mfma_f32_16x16x32_bf16 v[220:223], a[248:251], a[184:187], v[220:223]\n"
"ds_read_b128 a[212:215], v105 offset:36864\n"
"v_mfma_f32_16x16x32_bf16 v[220:223], a[244:247], a[180:183], v[220:223]\n"
"v_exp_f32 v211, v211\n"
"v_mfma_f32_16x16x32_bf16 v[220:223], a[240:243], a[176:179], v[220:223]\n"
"ds_read_b128 a[216:219], v106 offset:36864\n"
"v_cvt_pk_bf16_f32 v233, v210, v211\n"
"v_mfma_f32_16x16x32_bf16 a[8:11], v[128:131], v[224:227], a[8:11]\n"
"v_exp_f32 v212, v212\n"
"v_mfma_f32_16x16x32_bf16 a[8:11], v[132:135], v[228:231], a[8:11]\n"
"ds_read_b128 a[220:223], v107 offset:36864\n"
"v_mfma_f32_16x16x32_bf16 a[24:27], v[140:143], v[228:231], a[24:27]\n"
"v_exp_f32 v213, v213\n"
"v_mfma_f32_16x16x32_bf16 a[24:27], v[136:139], v[224:227], a[24:27]\n"
"ds_read_b128 a[224:227], v104 offset:40960\n"
"v_cvt_pk_bf16_f32 v234, v212, v213\n"
"v_mfma_f32_16x16x32_bf16 a[40:43], v[144:147], v[224:227], a[40:43]\n"
"v_exp_f32 v214, v214\n"
"v_mfma_f32_16x16x32_bf16 a[40:43], v[148:151], v[228:231], a[40:43]\n"
"ds_read_b128 a[228:231], v105 offset:40960\n"
"v_mfma_f32_16x16x32_bf16 a[56:59], v[156:159], v[228:231], a[56:59]\n"
"v_exp_f32 v215, v215\n"
"v_mfma_f32_16x16x32_bf16 a[56:59], v[152:155], v[224:227], a[56:59]\n"
"ds_read_b128 a[232:235], v106 offset:40960\n"
"v_cvt_pk_bf16_f32 v235, v214, v215\n"
"v_mfma_f32_16x16x32_bf16 a[72:75], v[160:163], v[224:227], a[72:75]\n"
"ds_read_b128 a[236:239], v107 offset:40960\n"
"v_mfma_f32_16x16x32_bf16 a[72:75], v[164:167], v[228:231], a[72:75]\n"
"v_exp_f32 v216, v216\n"
"v_mfma_f32_16x16x32_bf16 a[88:91], v[172:175], v[228:231], a[88:91]\n"
"ds_read_b128 a[240:243], v104 offset:45056\n"
"v_mfma_f32_16x16x32_bf16 a[88:91], v[168:171], v[224:227], a[88:91]\n"
"v_exp_f32 v217, v217\n"
"v_mfma_f32_16x16x32_bf16 a[104:107], v[176:179], v[224:227], a[104:107]\n"
"ds_read_b128 a[244:247], v105 offset:45056\n"
"v_cvt_pk_bf16_f32 v236, v216, v217\n"
"v_mfma_f32_16x16x32_bf16 a[104:107], v[180:183], v[228:231], a[104:107]\n"
"v_exp_f32 v218, v218\n"
"v_mfma_f32_16x16x32_bf16 a[120:123], v[188:191], v[228:231], a[120:123]\n"
"ds_read_b128 a[248:251], v106 offset:45056\n"
"v_mfma_f32_16x16x32_bf16 a[120:123], v[184:187], v[224:227], a[120:123]\n"
"v_exp_f32 v219, v219\n"
"v_mfma_f32_16x16x32_bf16 v[88:91], v[96:99], v[224:227], v[88:91]\n"
"ds_read_b128 a[252:255], v107 offset:45056\n"
"v_cvt_pk_bf16_f32 v237, v218, v219\n"
"v_mfma_f32_16x16x32_bf16 v[88:91], v[96:99], v[228:231], v[88:91]\n"
"s_waitcnt lgkmcnt(4)\n"
"v_exp_f32 v220, v220\n"
"v_mfma_f32_16x16x32_bf16 v[192:195], a[192:195], a[128:131], v[240:243]\n"
"v_mfma_f32_16x16x32_bf16 v[192:195], a[196:199], a[132:135], v[192:195]\n"
"v_exp_f32 v221, v221\n"
"v_mfma_f32_16x16x32_bf16 v[192:195], a[200:203], a[136:139], v[192:195]\n"
"v_cvt_pk_bf16_f32 v238, v220, v221\n"
"v_mfma_f32_16x16x32_bf16 v[192:195], a[204:207], a[140:143], v[192:195]\n"
"v_exp_f32 v222, v222\n"
"v_mfma_f32_16x16x32_bf16 v[196:199], a[220:223], a[140:143], v[240:243]\n"
"v_mfma_f32_16x16x32_bf16 v[196:199], a[216:219], a[136:139], v[196:199]\n"
"v_exp_f32 v223, v223\n"
"v_mfma_f32_16x16x32_bf16 v[196:199], a[212:215], a[132:135], v[196:199]\n"
"v_cvt_pk_bf16_f32 v239, v222, v223\n"
"v_mfma_f32_16x16x32_bf16 v[196:199], a[208:211], a[128:131], v[196:199]\n"
"v_mfma_f32_16x16x32_bf16 v[200:203], a[224:227], a[128:131], v[240:243]\n"
"s_waitcnt vmcnt(17) lgkmcnt(0)\n"
"s_barrier\n"
"v_exp_f32 v192, v192\n"
"v_mfma_f32_16x16x32_bf16 v[200:203], a[228:231], a[132:135], v[200:203]\n"
"v_mfma_f32_16x16x32_bf16 v[200:203], a[232:235], a[136:139], v[200:203]\n"
"s_add_u32 m0, s78, 0x18000\n"
"v_exp_f32 v193, v193\n"
"v_mfma_f32_16x16x32_bf16 v[200:203], a[236:239], a[140:143], v[200:203]\n"
"buffer_load_dwordx4 v114, s[44:47], s95 offen sc1 lds\n"
"v_cvt_pk_bf16_f32 v224, v192, v193\n"
"v_mfma_f32_16x16x32_bf16 v[204:207], a[252:255], a[140:143], v[240:243]\n"
"v_exp_f32 v194, v194\n"
"v_mfma_f32_16x16x32_bf16 v[204:207], a[248:251], a[136:139], v[204:207]\n"
"v_mfma_f32_16x16x32_bf16 v[204:207], a[244:247], a[132:135], v[204:207]\n"
"s_add_u32 m0, s78, 0x18400\n"
"v_exp_f32 v195, v195\n"
"v_mfma_f32_16x16x32_bf16 v[204:207], a[240:243], a[128:131], v[204:207]\n"
"buffer_load_dwordx4 v115, s[44:47], s95 offen sc1 lds\n"
"v_cvt_pk_bf16_f32 v225, v194, v195\n"
"v_mfma_f32_16x16x32_bf16 a[12:15], v[128:131], v[232:235], a[12:15]\n"
"v_exp_f32 v196, v196\n"
"v_mfma_f32_16x16x32_bf16 a[12:15], v[132:135], v[236:239], a[12:15]\n"
"v_mfma_f32_16x16x32_bf16 a[28:31], v[140:143], v[236:239], a[28:31]\n"
"s_add_u32 m0, s78, 0x18800\n"
"v_exp_f32 v197, v197\n"
"v_mfma_f32_16x16x32_bf16 a[28:31], v[136:139], v[232:235], a[28:31]\n"
"buffer_load_dwordx4 v116, s[44:47], s95 offen sc1 lds\n"
"v_cvt_pk_bf16_f32 v226, v196, v197\n"
"v_mfma_f32_16x16x32_bf16 a[44:47], v[144:147], v[232:235], a[44:47]\n"
"v_exp_f32 v198, v198\n"
"v_mfma_f32_16x16x32_bf16 a[44:47], v[148:151], v[236:239], a[44:47]\n"
"v_mfma_f32_16x16x32_bf16 a[60:63], v[156:159], v[236:239], a[60:63]\n"
"s_add_u32 m0, s78, 0x18c00\n"
"v_exp_f32 v199, v199\n"
"v_mfma_f32_16x16x32_bf16 a[60:63], v[152:155], v[232:235], a[60:63]\n"
"buffer_load_dwordx4 v117, s[44:47], s95 offen sc1 lds\n"
"v_cvt_pk_bf16_f32 v227, v198, v199\n"
"v_mfma_f32_16x16x32_bf16 a[76:79], v[160:163], v[232:235], a[76:79]\n"
"v_mfma_f32_16x16x32_bf16 a[76:79], v[164:167], v[236:239], a[76:79]\n"
"s_add_u32 m0, s78, 0x8000\n"
"v_exp_f32 v200, v200\n"
"v_mfma_f32_16x16x32_bf16 a[92:95], v[172:175], v[236:239], a[92:95]\n"
"buffer_load_dwordx4 v110, s[40:43], s94 offen sc1 lds\n"
"v_mfma_f32_16x16x32_bf16 a[92:95], v[168:171], v[232:235], a[92:95]\n"
"v_exp_f32 v201, v201\n"
"v_mfma_f32_16x16x32_bf16 a[108:111], v[176:179], v[232:235], a[108:111]\n"
"v_cvt_pk_bf16_f32 v228, v200, v201\n"
"v_mfma_f32_16x16x32_bf16 a[108:111], v[180:183], v[236:239], a[108:111]\n"
"s_add_u32 m0, s78, 0x8400\n"
"v_exp_f32 v202, v202\n"
"v_mfma_f32_16x16x32_bf16 a[124:127], v[188:191], v[236:239], a[124:127]\n"
"buffer_load_dwordx4 v111, s[40:43], s94 offen sc1 lds\n"
"v_mfma_f32_16x16x32_bf16 a[124:127], v[184:187], v[232:235], a[124:127]\n"
"v_exp_f32 v203, v203\n"
"v_mfma_f32_16x16x32_bf16 v[92:95], v[96:99], v[232:235], v[92:95]\n"
"v_cvt_pk_bf16_f32 v229, v202, v203\n"
"v_mfma_f32_16x16x32_bf16 v[92:95], v[96:99], v[236:239], v[92:95]\n"
"s_add_u32 m0, s78, 0x8800\n"
"v_exp_f32 v204, v204\n"
"v_mfma_f32_16x16x32_bf16 v[208:211], a[192:195], a[144:147], v[244:247]\n"
"buffer_load_dwordx4 v112, s[40:43], s94 offen sc1 lds\n"
"v_mfma_f32_16x16x32_bf16 v[208:211], a[196:199], a[148:151], v[208:211]\n"
"v_exp_f32 v205, v205\n"
"v_mfma_f32_16x16x32_bf16 v[208:211], a[200:203], a[152:155], v[208:211]\n"
"v_cvt_pk_bf16_f32 v230, v204, v205\n"
"v_mfma_f32_16x16x32_bf16 v[208:211], a[204:207], a[156:159], v[208:211]\n"
"s_add_u32 m0, s78, 0x8c00\n"
"v_exp_f32 v206, v206\n"
"v_mfma_f32_16x16x32_bf16 v[212:215], a[220:223], a[156:159], v[244:247]\n"
"buffer_load_dwordx4 v113, s[40:43], s94 offen sc1 lds\n"
"v_mfma_f32_16x16x32_bf16 v[212:215], a[216:219], a[152:155], v[212:215]\n"
"v_exp_f32 v207, v207\n"
"v_mfma_f32_16x16x32_bf16 v[212:215], a[212:215], a[148:151], v[212:215]\n"
"v_cvt_pk_bf16_f32 v231, v206, v207\n"
"v_mfma_f32_16x16x32_bf16 v[212:215], a[208:211], a[144:147], v[212:215]\n"
"s_add_u32 s94, s94, 0x4000\n"
"v_mfma_f32_16x16x32_bf16 v[216:219], a[224:227], a[144:147], v[244:247]\n"
"s_add_u32 s95, s95, 0x4000\n"
"s_add_u32 s99, s99, 0x2000\n"
"v_exp_f32 v208, v208\n"
"v_mfma_f32_16x16x32_bf16 v[216:219], a[228:231], a[148:151], v[216:219]\n"
"buffer_load_dword v11, v124, s[56:59], s99 offen\n"
"v_mfma_f32_16x16x32_bf16 v[216:219], a[232:235], a[152:155], v[216:219]\n"
"v_exp_f32 v209, v209\n"
"v_mfma_f32_16x16x32_bf16 v[216:219], a[236:239], a[156:159], v[216:219]\n"
"ds_read_b64_tr_b16 v[128:129], v108 offset:49152\n"
"v_cvt_pk_bf16_f32 v232, v208, v209\n"
"v_mfma_f32_16x16x32_bf16 v[220:223], a[252:255], a[156:159], v[244:247]\n"
"v_exp_f32 v210, v210\n"
"v_mfma_f32_16x16x32_bf16 v[220:223], a[248:251], a[152:155], v[220:223]\n"
"ds_read_b64_tr_b16 v[130:131], v108 offset:53248\n"
"v_mfma_f32_16x16x32_bf16 v[220:223], a[244:247], a[148:151], v[220:223]\n"
"v_exp_f32 v211, v211\n"
"v_mfma_f32_16x16x32_bf16 v[220:223], a[240:243], a[144:147], v[220:223]\n"
"ds_read_b64_tr_b16 v[132:133], v108 offset:57344\n"
"v_cvt_pk_bf16_f32 v233, v210, v211\n"
"v_mfma_f32_16x16x32_bf16 a[0:3], v[16:19], v[224:227], a[0:3]\n"
"v_exp_f32 v212, v212\n"
"v_mfma_f32_16x16x32_bf16 a[0:3], v[20:23], v[228:231], a[0:3]\n"
"ds_read_b64_tr_b16 v[134:135], v108 offset:61440\n"
"v_mfma_f32_16x16x32_bf16 a[16:19], v[28:31], v[228:231], a[16:19]\n"
"v_exp_f32 v213, v213\n"
"v_mfma_f32_16x16x32_bf16 a[16:19], v[24:27], v[224:227], a[16:19]\n"
"ds_read_b64_tr_b16 v[136:137], v109 offset:49152\n"
"v_cvt_pk_bf16_f32 v234, v212, v213\n"
"v_mfma_f32_16x16x32_bf16 a[32:35], v[32:35], v[224:227], a[32:35]\n"
"v_exp_f32 v214, v214\n"
"v_mfma_f32_16x16x32_bf16 a[32:35], v[36:39], v[228:231], a[32:35]\n"
"ds_read_b64_tr_b16 v[138:139], v109 offset:53248\n"
"v_mfma_f32_16x16x32_bf16 a[48:51], v[44:47], v[228:231], a[48:51]\n"
"v_exp_f32 v215, v215\n"
"v_mfma_f32_16x16x32_bf16 a[48:51], v[40:43], v[224:227], a[48:51]\n"
"ds_read_b64_tr_b16 v[140:141], v109 offset:57344\n"
"v_cvt_pk_bf16_f32 v235, v214, v215\n"
"v_mfma_f32_16x16x32_bf16 a[64:67], v[48:51], v[224:227], a[64:67]\n"
"ds_read_b64_tr_b16 v[142:143], v109 offset:61440\n"
"v_mfma_f32_16x16x32_bf16 a[64:67], v[52:55], v[228:231], a[64:67]\n"
"v_exp_f32 v216, v216\n"
"v_mfma_f32_16x16x32_bf16 a[80:83], v[60:63], v[228:231], a[80:83]\n"
"ds_read_b64_tr_b16 v[144:145], v108 offset:49664\n"
"v_mfma_f32_16x16x32_bf16 a[80:83], v[56:59], v[224:227], a[80:83]\n"
"v_exp_f32 v217, v217\n"
"v_mfma_f32_16x16x32_bf16 a[96:99], v[64:67], v[224:227], a[96:99]\n"
"ds_read_b64_tr_b16 v[146:147], v108 offset:53760\n"
"v_cvt_pk_bf16_f32 v236, v216, v217\n"
"v_mfma_f32_16x16x32_bf16 a[96:99], v[68:71], v[228:231], a[96:99]\n"
"v_exp_f32 v218, v218\n"
"v_mfma_f32_16x16x32_bf16 a[112:115], v[76:79], v[228:231], a[112:115]\n"
"ds_read_b64_tr_b16 v[148:149], v108 offset:57856\n"
"v_mfma_f32_16x16x32_bf16 a[112:115], v[72:75], v[224:227], a[112:115]\n"
"v_exp_f32 v219, v219\n"
"v_mfma_f32_16x16x32_bf16 v[80:83], v[96:99], v[224:227], v[80:83]\n"
"ds_read_b64_tr_b16 v[150:151], v108 offset:61952\n"
"v_cvt_pk_bf16_f32 v237, v218, v219\n"
"v_mfma_f32_16x16x32_bf16 v[80:83], v[96:99], v[228:231], v[80:83]\n"
"v_exp_f32 v220, v220\n"
"v_mfma_f32_16x16x32_bf16 v[192:195], a[192:195], a[160:163], v[248:251]\n"
"ds_read_b64_tr_b16 v[152:153], v109 offset:49664\n"
"v_mfma_f32_16x16x32_bf16 v[192:195], a[196:199], a[164:167], v[192:195]\n"
"v_exp_f32 v221, v221\n"
"v_mfma_f32_16x16x32_bf16 v[192:195], a[200:203], a[168:171], v[192:195]\n"
"ds_read_b64_tr_b16 v[154:155], v109 offset:53760\n"
"v_cvt_pk_bf16_f32 v238, v220, v221\n"
"v_mfma_f32_16x16x32_bf16 v[192:195], a[204:207], a[172:175], v[192:195]\n"
"v_exp_f32 v222, v222\n"
"v_mfma_f32_16x16x32_bf16 v[196:199], a[220:223], a[172:175], v[248:251]\n"
"ds_read_b64_tr_b16 v[156:157], v109 offset:57856\n"
"v_mfma_f32_16x16x32_bf16 v[196:199], a[216:219], a[168:171], v[196:199]\n"
"v_exp_f32 v223, v223\n"
"v_mfma_f32_16x16x32_bf16 v[196:199], a[212:215], a[164:167], v[196:199]\n"
"ds_read_b64_tr_b16 v[158:159], v109 offset:61952\n"
"v_cvt_pk_bf16_f32 v239, v222, v223\n"
"v_mfma_f32_16x16x32_bf16 v[196:199], a[208:211], a[160:163], v[196:199]\n"
"ds_read_b64_tr_b16 v[160:161], v108 offset:50176\n"
"v_mfma_f32_16x16x32_bf16 v[200:203], a[224:227], a[160:163], v[248:251]\n"
"v_exp_f32 v192, v192\n"
"v_mfma_f32_16x16x32_bf16 v[200:203], a[228:231], a[164:167], v[200:203]\n"
"ds_read_b64_tr_b16 v[162:163], v108 offset:54272\n"
"v_mfma_f32_16x16x32_bf16 v[200:203], a[232:235], a[168:171], v[200:203]\n"
"v_exp_f32 v193, v193\n"
"v_mfma_f32_16x16x32_bf16 v[200:203], a[236:239], a[172:175], v[200:203]\n"
"ds_read_b64_tr_b16 v[164:165], v108 offset:58368\n"
"v_cvt_pk_bf16_f32 v224, v192, v193\n"
"v_mfma_f32_16x16x32_bf16 v[204:207], a[252:255], a[172:175], v[248:251]\n"
"v_exp_f32 v194, v194\n"
"v_mfma_f32_16x16x32_bf16 v[204:207], a[248:251], a[168:171], v[204:207]\n"
"ds_read_b64_tr_b16 v[166:167], v108 offset:62464\n"
"v_mfma_f32_16x16x32_bf16 v[204:207], a[244:247], a[164:167], v[204:207]\n"
"v_exp_f32 v195, v195\n"
"v_mfma_f32_16x16x32_bf16 v[204:207], a[240:243], a[160:163], v[204:207]\n"
"ds_read_b64_tr_b16 v[168:169], v109 offset:50176\n"
"v_cvt_pk_bf16_f32 v225, v194, v195\n"
"v_mfma_f32_16x16x32_bf16 a[4:7], v[16:19], v[232:235], a[4:7]\n"
"v_exp_f32 v196, v196\n"
"v_mfma_f32_16x16x32_bf16 a[4:7], v[20:23], v[236:239], a[4:7]\n"
"ds_read_b64_tr_b16 v[170:171], v109 offset:54272\n"
"v_mfma_f32_16x16x32_bf16 a[20:23], v[28:31], v[236:239], a[20:23]\n"
"v_exp_f32 v197, v197\n"
"v_mfma_f32_16x16x32_bf16 a[20:23], v[24:27], v[232:235], a[20:23]\n"
"ds_read_b64_tr_b16 v[172:173], v109 offset:58368\n"
"v_cvt_pk_bf16_f32 v226, v196, v197\n"
"v_mfma_f32_16x16x32_bf16 a[36:39], v[32:35], v[232:235], a[36:39]\n"
"v_exp_f32 v198, v198\n"
"v_mfma_f32_16x16x32_bf16 a[36:39], v[36:39], v[236:239], a[36:39]\n"
"ds_read_b64_tr_b16 v[174:175], v109 offset:62464\n"
"v_mfma_f32_16x16x32_bf16 a[52:55], v[44:47], v[236:239], a[52:55]\n"
"v_exp_f32 v199, v199\n"
"v_mfma_f32_16x16x32_bf16 a[52:55], v[40:43], v[232:235], a[52:55]\n"
"ds_read_b64_tr_b16 v[176:177], v108 offset:50688\n"
"v_cvt_pk_bf16_f32 v227, v198, v199\n"
"v_mfma_f32_16x16x32_bf16 a[68:71], v[48:51], v[232:235], a[68:71]\n"
"ds_read_b64_tr_b16 v[178:179], v108 offset:54784\n"
"v_mfma_f32_16x16x32_bf16 a[68:71], v[52:55], v[236:239], a[68:71]\n"
"v_exp_f32 v200, v200\n"
"v_mfma_f32_16x16x32_bf16 a[84:87], v[60:63], v[236:239], a[84:87]\n"
"ds_read_b64_tr_b16 v[180:181], v108 offset:58880\n"
"v_mfma_f32_16x16x32_bf16 a[84:87], v[56:59], v[232:235], a[84:87]\n"
"v_exp_f32 v201, v201\n"
"v_mfma_f32_16x16x32_bf16 a[100:103], v[64:67], v[232:235], a[100:103]\n"
"ds_read_b64_tr_b16 v[182:183], v108 offset:62976\n"
"v_cvt_pk_bf16_f32 v228, v200, v201\n"
"v_mfma_f32_16x16x32_bf16 a[100:103], v[68:71], v[236:239], a[100:103]\n"
"v_exp_f32 v202, v202\n"
"v_mfma_f32_16x16x32_bf16 a[116:119], v[76:79], v[236:239], a[116:119]\n"
"ds_read_b64_tr_b16 v[184:185], v109 offset:50688\n"
"v_mfma_f32_16x16x32_bf16 a[116:119], v[72:75], v[232:235], a[116:119]\n"
"v_exp_f32 v203, v203\n"
"v_mfma_f32_16x16x32_bf16 v[84:87], v[96:99], v[232:235], v[84:87]\n"
"ds_read_b64_tr_b16 v[186:187], v109 offset:54784\n"
"v_cvt_pk_bf16_f32 v229, v202, v203\n"
"v_mfma_f32_16x16x32_bf16 v[84:87], v[96:99], v[236:239], v[84:87]\n"
"v_exp_f32 v204, v204\n"
"v_mfma_f32_16x16x32_bf16 v[208:211], a[192:195], a[176:179], v[252:255]\n"
"ds_read_b64_tr_b16 v[188:189], v109 offset:58880\n"
"v_mfma_f32_16x16x32_bf16 v[208:211], a[196:199], a[180:183], v[208:211]\n"
"v_exp_f32 v205, v205\n"
"v_mfma_f32_16x16x32_bf16 v[208:211], a[200:203], a[184:187], v[208:211]\n"
"ds_read_b64_tr_b16 v[190:191], v109 offset:62976\n"
"v_cvt_pk_bf16_f32 v230, v204, v205\n"
"v_mfma_f32_16x16x32_bf16 v[208:211], a[204:207], a[188:191], v[208:211]\n"
"v_exp_f32 v206, v206\n"
"v_mfma_f32_16x16x32_bf16 v[212:215], a[220:223], a[188:191], v[252:255]\n"
"ds_read_b128 a[192:195], v104 offset:49152\n"
"v_mfma_f32_16x16x32_bf16 v[212:215], a[216:219], a[184:187], v[212:215]\n"
"v_exp_f32 v207, v207\n"
"v_mfma_f32_16x16x32_bf16 v[212:215], a[212:215], a[180:183], v[212:215]\n"
"ds_read_b128 a[196:199], v105 offset:49152\n"
"v_cvt_pk_bf16_f32 v231, v206, v207\n"
"v_mfma_f32_16x16x32_bf16 v[212:215], a[208:211], a[176:179], v[212:215]\n"
"ds_read_b128 a[200:203], v106 offset:49152\n"
"v_mfma_f32_16x16x32_bf16 v[216:219], a[224:227], a[176:179], v[252:255]\n"
"v_exp_f32 v208, v208\n"
"v_mfma_f32_16x16x32_bf16 v[216:219], a[228:231], a[180:183], v[216:219]\n"
"ds_read_b128 a[204:207], v107 offset:49152\n"
"v_mfma_f32_16x16x32_bf16 v[216:219], a[232:235], a[184:187], v[216:219]\n"
"v_exp_f32 v209, v209\n"
"v_mfma_f32_16x16x32_bf16 v[216:219], a[236:239], a[188:191], v[216:219]\n"
"ds_read_b128 a[208:211], v104 offset:53248\n"
"v_cvt_pk_bf16_f32 v232, v208, v209\n"
"v_mfma_f32_16x16x32_bf16 v[220:223], a[252:255], a[188:191], v[252:255]\n"
"v_exp_f32 v210, v210\n"
"v_mfma_f32_16x16x32_bf16 v[220:223], a[248:251], a[184:187], v[220:223]\n"
"ds_read_b128 a[212:215], v105 offset:53248\n"
"v_mfma_f32_16x16x32_bf16 v[220:223], a[244:247], a[180:183], v[220:223]\n"
"v_exp_f32 v211, v211\n"
"v_mfma_f32_16x16x32_bf16 v[220:223], a[240:243], a[176:179], v[220:223]\n"
"ds_read_b128 a[216:219], v106 offset:53248\n"
"v_cvt_pk_bf16_f32 v233, v210, v211\n"
"v_mfma_f32_16x16x32_bf16 a[8:11], v[16:19], v[224:227], a[8:11]\n"
"v_exp_f32 v212, v212\n"
"v_mfma_f32_16x16x32_bf16 a[8:11], v[20:23], v[228:231], a[8:11]\n"
"ds_read_b128 a[220:223], v107 offset:53248\n"
"v_mfma_f32_16x16x32_bf16 a[24:27], v[28:31], v[228:231], a[24:27]\n"
"v_exp_f32 v213, v213\n"
"v_mfma_f32_16x16x32_bf16 a[24:27], v[24:27], v[224:227], a[24:27]\n"
"ds_read_b128 a[224:227], v104 offset:57344\n"
"v_cvt_pk_bf16_f32 v234, v212, v213\n"
"v_mfma_f32_16x16x32_bf16 a[40:43], v[32:35], v[224:227], a[40:43]\n"
"v_exp_f32 v214, v214\n"
"v_mfma_f32_16x16x32_bf16 a[40:43], v[36:39], v[228:231], a[40:43]\n"
"ds_read_b128 a[228:231], v105 offset:57344\n"
"v_mfma_f32_16x16x32_bf16 a[56:59], v[44:47], v[228:231], a[56:59]\n"
"v_exp_f32 v215, v215\n"
"v_mfma_f32_16x16x32_bf16 a[56:59], v[40:43], v[224:227], a[56:59]\n"
"ds_read_b128 a[232:235], v106 offset:57344\n"
"v_cvt_pk_bf16_f32 v235, v214, v215\n"
"v_mfma_f32_16x16x32_bf16 a[72:75], v[48:51], v[224:227], a[72:75]\n"
"ds_read_b128 a[236:239], v107 offset:57344\n"
"v_mfma_f32_16x16x32_bf16 a[72:75], v[52:55], v[228:231], a[72:75]\n"
"v_exp_f32 v216, v216\n"
"v_mfma_f32_16x16x32_bf16 a[88:91], v[60:63], v[228:231], a[88:91]\n"
"ds_read_b128 a[240:243], v104 offset:61440\n"
"v_mfma_f32_16x16x32_bf16 a[88:91], v[56:59], v[224:227], a[88:91]\n"
"v_exp_f32 v217, v217\n"
"v_mfma_f32_16x16x32_bf16 a[104:107], v[64:67], v[224:227], a[104:107]\n"
"ds_read_b128 a[244:247], v105 offset:61440\n"
"v_cvt_pk_bf16_f32 v236, v216, v217\n"
"v_mfma_f32_16x16x32_bf16 a[104:107], v[68:71], v[228:231], a[104:107]\n"
"v_exp_f32 v218, v218\n"
"v_mfma_f32_16x16x32_bf16 a[120:123], v[76:79], v[228:231], a[120:123]\n"
"ds_read_b128 a[248:251], v106 offset:61440\n"
"v_mfma_f32_16x16x32_bf16 a[120:123], v[72:75], v[224:227], a[120:123]\n"
"v_exp_f32 v219, v219\n"
"v_mfma_f32_16x16x32_bf16 v[88:91], v[96:99], v[224:227], v[88:91]\n"
"ds_read_b128 a[252:255], v107 offset:61440\n"
"v_cvt_pk_bf16_f32 v237, v218, v219\n"
"v_mfma_f32_16x16x32_bf16 v[88:91], v[96:99], v[228:231], v[88:91]\n"
"s_waitcnt lgkmcnt(4)\n"
"v_exp_f32 v220, v220\n"
"v_mfma_f32_16x16x32_bf16 v[192:195], a[192:195], a[128:131], v[240:243]\n"
"v_mfma_f32_16x16x32_bf16 v[192:195], a[196:199], a[132:135], v[192:195]\n"
"v_exp_f32 v221, v221\n"
"v_mfma_f32_16x16x32_bf16 v[192:195], a[200:203], a[136:139], v[192:195]\n"
"v_cvt_pk_bf16_f32 v238, v220, v221\n"
"v_mfma_f32_16x16x32_bf16 v[192:195], a[204:207], a[140:143], v[192:195]\n"
"v_exp_f32 v222, v222\n"
"v_mfma_f32_16x16x32_bf16 v[196:199], a[220:223], a[140:143], v[240:243]\n"
"v_mfma_f32_16x16x32_bf16 v[196:199], a[216:219], a[136:139], v[196:199]\n"
"v_exp_f32 v223, v223\n"
"v_mfma_f32_16x16x32_bf16 v[196:199], a[212:215], a[132:135], v[196:199]\n"
"v_cvt_pk_bf16_f32 v239, v222, v223\n"
"v_mfma_f32_16x16x32_bf16 v[196:199], a[208:211], a[128:131], v[196:199]\n"
"v_mfma_f32_16x16x32_bf16 v[200:203], a[224:227], a[128:131], v[240:243]\n"
"s_waitcnt vmcnt(18) lgkmcnt(0)\n"
"s_barrier\n"
"v_mov_b32 v122, 0x27ff0\n"
"v_mov_b32 v123, 0\n"
"ds_write_b32 v122, v123\n"
"v_exp_f32 v192, v192\n"
"v_mfma_f32_16x16x32_bf16 v[200:203], a[228:231], a[132:135], v[200:203]\n"
"v_mfma_f32_16x16x32_bf16 v[200:203], a[232:235], a[136:139], v[200:203]\n"
"s_add_u32 m0, s78, 0x1c000\n"
"v_exp_f32 v193, v193\n"
"v_mfma_f32_16x16x32_bf16 v[200:203], a[236:239], a[140:143], v[200:203]\n"
"buffer_load_dwordx4 v114, s[44:47], s95 offen sc1 lds\n"
"v_cvt_pk_bf16_f32 v224, v192, v193\n"
"v_mfma_f32_16x16x32_bf16 v[204:207], a[252:255], a[140:143], v[240:243]\n"
"v_exp_f32 v194, v194\n"
"v_mfma_f32_16x16x32_bf16 v[204:207], a[248:251], a[136:139], v[204:207]\n"
"v_mfma_f32_16x16x32_bf16 v[204:207], a[244:247], a[132:135], v[204:207]\n"
"s_add_u32 m0, s78, 0x1c400\n"
"v_exp_f32 v195, v195\n"
"v_mfma_f32_16x16x32_bf16 v[204:207], a[240:243], a[128:131], v[204:207]\n"
"buffer_load_dwordx4 v115, s[44:47], s95 offen sc1 lds\n"
"v_cvt_pk_bf16_f32 v225, v194, v195\n"
"v_mfma_f32_16x16x32_bf16 a[12:15], v[16:19], v[232:235], a[12:15]\n"
"v_exp_f32 v196, v196\n"
"v_mfma_f32_16x16x32_bf16 a[12:15], v[20:23], v[236:239], a[12:15]\n"
"v_mfma_f32_16x16x32_bf16 a[28:31], v[28:31], v[236:239], a[28:31]\n"
"s_add_u32 m0, s78, 0x1c800\n"
"v_exp_f32 v197, v197\n"
"v_mfma_f32_16x16x32_bf16 a[28:31], v[24:27], v[232:235], a[28:31]\n"
"buffer_load_dwordx4 v116, s[44:47], s95 offen sc1 lds\n"
"v_cvt_pk_bf16_f32 v226, v196, v197\n"
"v_mfma_f32_16x16x32_bf16 a[44:47], v[32:35], v[232:235], a[44:47]\n"
"v_exp_f32 v198, v198\n"
"v_mfma_f32_16x16x32_bf16 a[44:47], v[36:39], v[236:239], a[44:47]\n"
"v_mfma_f32_16x16x32_bf16 a[60:63], v[44:47], v[236:239], a[60:63]\n"
"s_add_u32 m0, s78, 0x1cc00\n"
"v_exp_f32 v199, v199\n"
"v_mfma_f32_16x16x32_bf16 a[60:63], v[40:43], v[232:235], a[60:63]\n"
"buffer_load_dwordx4 v117, s[44:47], s95 offen sc1 lds\n"
"v_cvt_pk_bf16_f32 v227, v198, v199\n"
"v_mfma_f32_16x16x32_bf16 a[76:79], v[48:51], v[232:235], a[76:79]\n"
"v_mfma_f32_16x16x32_bf16 a[76:79], v[52:55], v[236:239], a[76:79]\n"
"s_add_u32 m0, s78, 0xc000\n"
"v_exp_f32 v200, v200\n"
"v_mfma_f32_16x16x32_bf16 a[92:95], v[60:63], v[236:239], a[92:95]\n"
"buffer_load_dwordx4 v110, s[40:43], s94 offen sc1 lds\n"
"v_mfma_f32_16x16x32_bf16 a[92:95], v[56:59], v[232:235], a[92:95]\n"
"v_exp_f32 v201, v201\n"
"v_mfma_f32_16x16x32_bf16 a[108:111], v[64:67], v[232:235], a[108:111]\n"
"v_cvt_pk_bf16_f32 v228, v200, v201\n"
"v_mfma_f32_16x16x32_bf16 a[108:111], v[68:71], v[236:239], a[108:111]\n"
"s_add_u32 m0, s78, 0xc400\n"
"v_exp_f32 v202, v202\n"
"v_mfma_f32_16x16x32_bf16 a[124:127], v[76:79], v[236:239], a[124:127]\n"
"buffer_load_dwordx4 v111, s[40:43], s94 offen sc1 lds\n"
"v_mfma_f32_16x16x32_bf16 a[124:127], v[72:75], v[232:235], a[124:127]\n"
"v_exp_f32 v203, v203\n"
"v_mfma_f32_16x16x32_bf16 v[92:95], v[96:99], v[232:235], v[92:95]\n"
"v_cvt_pk_bf16_f32 v229, v202, v203\n"
"v_mfma_f32_16x16x32_bf16 v[92:95], v[96:99], v[236:239], v[92:95]\n"
"s_add_u32 m0, s78, 0xc800\n"
"v_exp_f32 v204, v204\n"
"v_mfma_f32_16x16x32_bf16 v[208:211], a[192:195], a[144:147], v[244:247]\n"
"buffer_load_dwordx4 v112, s[40:43], s94 offen sc1 lds\n"
"v_mfma_f32_16x16x32_bf16 v[208:211], a[196:199], a[148:151], v[208:211]\n"
"v_exp_f32 v205, v205\n"
"v_mfma_f32_16x16x32_bf16 v[208:211], a[200:203], a[152:155], v[208:211]\n"
"v_cvt_pk_bf16_f32 v230, v204, v205\n"
"v_mfma_f32_16x16x32_bf16 v[208:211], a[204:207], a[156:159], v[208:211]\n"
"s_add_u32 m0, s78, 0xcc00\n"
"v_exp_f32 v206, v206\n"
"v_mfma_f32_16x16x32_bf16 v[212:215], a[220:223], a[156:159], v[244:247]\n"
"buffer_load_dwordx4 v113, s[40:43], s94 offen sc1 lds\n"
"v_mfma_f32_16x16x32_bf16 v[212:215], a[216:219], a[152:155], v[212:215]\n"
"v_exp_f32 v207, v207\n"
"v_mfma_f32_16x16x32_bf16 v[212:215], a[212:215], a[148:151], v[212:215]\n"
"v_cvt_pk_bf16_f32 v231, v206, v207\n"
"v_mfma_f32_16x16x32_bf16 v[212:215], a[208:211], a[144:147], v[212:215]\n"
"s_add_u32 s94, s94, 0x4000\n"
"ds_read_b64_tr_b16 v[16:17], v108\n"
"v_mfma_f32_16x16x32_bf16 v[216:219], a[224:227], a[144:147], v[244:247]\n"
"s_add_u32 s95, s95, 0x4000\n"
"v_exp_f32 v208, v208\n"
"v_mfma_f32_16x16x32_bf16 v[216:219], a[228:231], a[148:151], v[216:219]\n"
"ds_read_b64_tr_b16 v[18:19], v108 offset:4096\n"
"v_mfma_f32_16x16x32_bf16 v[216:219], a[232:235], a[152:155], v[216:219]\n"
"v_exp_f32 v209, v209\n"
"v_mfma_f32_16x16x32_bf16 v[216:219], a[236:239], a[156:159], v[216:219]\n"
"ds_read_b64_tr_b16 v[20:21], v108 offset:8192\n"
"v_cvt_pk_bf16_f32 v232, v208, v209\n"
"v_mfma_f32_16x16x32_bf16 v[220:223], a[252:255], a[156:159], v[244:247]\n"
"v_exp_f32 v210, v210\n"
"v_mfma_f32_16x16x32_bf16 v[220:223], a[248:251], a[152:155], v[220:223]\n"
"ds_read_b64_tr_b16 v[22:23], v108 offset:12288\n"
"v_mfma_f32_16x16x32_bf16 v[220:223], a[244:247], a[148:151], v[220:223]\n"
"v_exp_f32 v211, v211\n"
"v_mfma_f32_16x16x32_bf16 v[220:223], a[240:243], a[144:147], v[220:223]\n"
"ds_read_b64_tr_b16 v[24:25], v109\n"
"v_cvt_pk_bf16_f32 v233, v210, v211\n"
"v_mfma_f32_16x16x32_bf16 a[0:3], v[128:131], v[224:227], a[0:3]\n"
"v_exp_f32 v212, v212\n"
"v_mfma_f32_16x16x32_bf16 a[0:3], v[132:135], v[228:231], a[0:3]\n"
"ds_read_b64_tr_b16 v[26:27], v109 offset:4096\n"
"v_mfma_f32_16x16x32_bf16 a[16:19], v[140:143], v[228:231], a[16:19]\n"
"v_exp_f32 v213, v213\n"
"v_mfma_f32_16x16x32_bf16 a[16:19], v[136:139], v[224:227], a[16:19]\n"
"ds_read_b64_tr_b16 v[28:29], v109 offset:8192\n"
"v_cvt_pk_bf16_f32 v234, v212, v213\n"
"v_mfma_f32_16x16x32_bf16 a[32:35], v[144:147], v[224:227], a[32:35]\n"
"v_exp_f32 v214, v214\n"
"v_mfma_f32_16x16x32_bf16 a[32:35], v[148:151], v[228:231], a[32:35]\n"
"ds_read_b64_tr_b16 v[30:31], v109 offset:12288\n"
"v_mfma_f32_16x16x32_bf16 a[48:51], v[156:159], v[228:231], a[48:51]\n"
"v_exp_f32 v215, v215\n"
"v_mfma_f32_16x16x32_bf16 a[48:51], v[152:155], v[224:227], a[48:51]\n"
"ds_read_b64_tr_b16 v[32:33], v108 offset:512\n"
"v_cvt_pk_bf16_f32 v235, v214, v215\n"
"v_mfma_f32_16x16x32_bf16 a[64:67], v[160:163], v[224:227], a[64:67]\n"
"ds_read_b64_tr_b16 v[34:35], v108 offset:4608\n"
"v_mfma_f32_16x16x32_bf16 a[64:67], v[164:167], v[228:231], a[64:67]\n"
"v_exp_f32 v216, v216\n"
"v_mfma_f32_16x16x32_bf16 a[80:83], v[172:175], v[228:231], a[80:83]\n"
"ds_read_b64_tr_b16 v[36:37], v108 offset:8704\n"
"v_mfma_f32_16x16x32_bf16 a[80:83], v[168:171], v[224:227], a[80:83]\n"
"v_exp_f32 v217, v217\n"
"v_mfma_f32_16x16x32_bf16 a[96:99], v[176:179], v[224:227], a[96:99]\n"
"ds_read_b64_tr_b16 v[38:39], v108 offset:12800\n"
"v_cvt_pk_bf16_f32 v236, v216, v217\n"
"v_mfma_f32_16x16x32_bf16 a[96:99], v[180:183], v[228:231], a[96:99]\n"
"v_exp_f32 v218, v218\n"
"v_mfma_f32_16x16x32_bf16 a[112:115], v[188:191], v[228:231], a[112:115]\n"
"ds_read_b64_tr_b16 v[40:41], v109 offset:512\n"
"v_mfma_f32_16x16x32_bf16 a[112:115], v[184:187], v[224:227], a[112:115]\n"
"v_exp_f32 v219, v219\n"
"v_mfma_f32_16x16x32_bf16 v[80:83], v[96:99], v[224:227], v[80:83]\n"
"ds_read_b64_tr_b16 v[42:43], v109 offset:4608\n"
"v_cvt_pk_bf16_f32 v237, v218, v219\n"
"v_mfma_f32_16x16x32_bf16 v[80:83], v[96:99], v[228:231], v[80:83]\n"
"v_exp_f32 v220, v220\n"
"v_mfma_f32_16x16x32_bf16 v[192:195], a[192:195], a[160:163], v[248:251]\n"
"ds_read_b64_tr_b16 v[44:45], v109 offset:8704\n"
"v_mfma_f32_16x16x32_bf16 v[192:195], a[196:199], a[164:167], v[192:195]\n"
"v_exp_f32 v221, v221\n"
"v_mfma_f32_16x16x32_bf16 v[192:195], a[200:203], a[168:171], v[192:195]\n"
"ds_read_b64_tr_b16 v[46:47], v109 offset:12800\n"
"v_cvt_pk_bf16_f32 v238, v220, v221\n"
"v_mfma_f32_16x16x32_bf16 v[192:195], a[204:207], a[172:175], v[192:195]\n"
"v_exp_f32 v222, v222\n"
"v_mfma_f32_16x16x32_bf16 v[196:199], a[220:223], a[172:175], v[248:251]\n"
"ds_read_b64_tr_b16 v[48:49], v108 offset:1024\n"
"v_mfma_f32_16x16x32_bf16 v[196:199], a[216:219], a[168:171], v[196:199]\n"
"v_exp_f32 v223, v223\n"
"v_mfma_f32_16x16x32_bf16 v[196:199], a[212:215], a[164:167], v[196:199]\n"
"ds_read_b64_tr_b16 v[50:51], v108 offset:5120\n"
"v_cvt_pk_bf16_f32 v239, v222, v223\n"
"v_mfma_f32_16x16x32_bf16 v[196:199], a[208:211], a[160:163], v[196:199]\n"
"ds_read_b64_tr_b16 v[52:53], v108 offset:9216\n"
"v_mfma_f32_16x16x32_bf16 v[200:203], a[224:227], a[160:163], v[248:251]\n"
"v_exp_f32 v192, v192\n"
"v_mfma_f32_16x16x32_bf16 v[200:203], a[228:231], a[164:167], v[200:203]\n"
"ds_read_b64_tr_b16 v[54:55], v108 offset:13312\n"
"v_mfma_f32_16x16x32_bf16 v[200:203], a[232:235], a[168:171], v[200:203]\n"
"v_exp_f32 v193, v193\n"
"v_mfma_f32_16x16x32_bf16 v[200:203], a[236:239], a[172:175], v[200:203]\n"
"ds_read_b64_tr_b16 v[56:57], v109 offset:1024\n"
"v_cvt_pk_bf16_f32 v224, v192, v193\n"
"v_mfma_f32_16x16x32_bf16 v[204:207], a[252:255], a[172:175], v[248:251]\n"
"v_exp_f32 v194, v194\n"
"v_mfma_f32_16x16x32_bf16 v[204:207], a[248:251], a[168:171], v[204:207]\n"
"ds_read_b64_tr_b16 v[58:59], v109 offset:5120\n"
"v_mfma_f32_16x16x32_bf16 v[204:207], a[244:247], a[164:167], v[204:207]\n"
"v_exp_f32 v195, v195\n"
"v_mfma_f32_16x16x32_bf16 v[204:207], a[240:243], a[160:163], v[204:207]\n"
"ds_read_b64_tr_b16 v[60:61], v109 offset:9216\n"
"v_cvt_pk_bf16_f32 v225, v194, v195\n"
"v_mfma_f32_16x16x32_bf16 a[4:7], v[128:131], v[232:235], a[4:7]\n"
"v_exp_f32 v196, v196\n"
"v_mfma_f32_16x16x32_bf16 a[4:7], v[132:135], v[236:239], a[4:7]\n"
"ds_read_b64_tr_b16 v[62:63], v109 offset:13312\n"
"v_mfma_f32_16x16x32_bf16 a[20:23], v[140:143], v[236:239], a[20:23]\n"
"v_exp_f32 v197, v197\n"
"v_mfma_f32_16x16x32_bf16 a[20:23], v[136:139], v[232:235], a[20:23]\n"
"ds_read_b64_tr_b16 v[64:65], v108 offset:1536\n"
"v_cvt_pk_bf16_f32 v226, v196, v197\n"
"v_mfma_f32_16x16x32_bf16 a[36:39], v[144:147], v[232:235], a[36:39]\n"
"v_exp_f32 v198, v198\n"
"v_mfma_f32_16x16x32_bf16 a[36:39], v[148:151], v[236:239], a[36:39]\n"
"ds_read_b64_tr_b16 v[66:67], v108 offset:5632\n"
"v_mfma_f32_16x16x32_bf16 a[52:55], v[156:159], v[236:239], a[52:55]\n"
"v_exp_f32 v199, v199\n"
"v_mfma_f32_16x16x32_bf16 a[52:55], v[152:155], v[232:235], a[52:55]\n"
"ds_read_b64_tr_b16 v[68:69], v108 offset:9728\n"
"v_cvt_pk_bf16_f32 v227, v198, v199\n"
"v_mfma_f32_16x16x32_bf16 a[68:71], v[160:163], v[232:235], a[68:71]\n"
"ds_read_b64_tr_b16 v[70:71], v108 offset:13824\n"
"v_mfma_f32_16x16x32_bf16 a[68:71], v[164:167], v[236:239], a[68:71]\n"
"v_exp_f32 v200, v200\n"
"v_mfma_f32_16x16x32_bf16 a[84:87], v[172:175], v[236:239], a[84:87]\n"
"ds_read_b64_tr_b16 v[72:73], v109 offset:1536\n"
"v_mfma_f32_16x16x32_bf16 a[84:87], v[168:171], v[232:235], a[84:87]\n"
"v_exp_f32 v201, v201\n"
"v_mfma_f32_16x16x32_bf16 a[100:103], v[176:179], v[232:235], a[100:103]\n"
"ds_read_b64_tr_b16 v[74:75], v109 offset:5632\n"
"v_cvt_pk_bf16_f32 v228, v200, v201\n"
"v_mfma_f32_16x16x32_bf16 a[100:103], v[180:183], v[236:239], a[100:103]\n"
"v_exp_f32 v202, v202\n"
"v_mfma_f32_16x16x32_bf16 a[116:119], v[188:191], v[236:239], a[116:119]\n"
"ds_read_b64_tr_b16 v[76:77], v109 offset:9728\n"
"v_mfma_f32_16x16x32_bf16 a[116:119], v[184:187], v[232:235], a[116:119]\n"
"v_exp_f32 v203, v203\n"
"v_mfma_f32_16x16x32_bf16 v[84:87], v[96:99], v[232:235], v[84:87]\n"
"ds_read_b64_tr_b16 v[78:79], v109 offset:13824\n"
"v_cvt_pk_bf16_f32 v229, v202, v203\n"
"v_mfma_f32_16x16x32_bf16 v[84:87], v[96:99], v[236:239], v[84:87]\n"
"v_exp_f32 v204, v204\n"
"v_mfma_f32_16x16x32_bf16 v[208:211], a[192:195], a[176:179], v[252:255]\n"
"v_mfma_f32_16x16x32_bf16 v[208:211], a[196:199], a[180:183], v[208:211]\n"
"v_exp_f32 v205, v205\n"
"v_mfma_f32_16x16x32_bf16 v[208:211], a[200:203], a[184:187], v[208:211]\n"
"ds_read_b128 a[192:195], v104\n"
"v_cvt_pk_bf16_f32 v230, v204, v205\n"
"v_mfma_f32_16x16x32_bf16 v[208:211], a[204:207], a[188:191], v[208:211]\n"
"v_exp_f32 v206, v206\n"
"v_mfma_f32_16x16x32_bf16 v[212:215], a[220:223], a[188:191], v[252:255]\n"
"ds_read_b128 a[196:199], v105\n"
"v_mfma_f32_16x16x32_bf16 v[212:215], a[216:219], a[184:187], v[212:215]\n"
"v_exp_f32 v207, v207\n"
"v_mfma_f32_16x16x32_bf16 v[212:215], a[212:215], a[180:183], v[212:215]\n"
"ds_read_b128 a[200:203], v106\n"
"v_cvt_pk_bf16_f32 v231, v206, v207\n"
"v_mfma_f32_16x16x32_bf16 v[212:215], a[208:211], a[176:179], v[212:215]\n"
"ds_read_b128 a[204:207], v107\n"
"v_mfma_f32_16x16x32_bf16 v[216:219], a[224:227], a[176:179], v[252:255]\n"
"v_exp_f32 v208, v208\n"
"v_mfma_f32_16x16x32_bf16 v[216:219], a[228:231], a[180:183], v[216:219]\n"
"ds_read_b128 a[208:211], v104 offset:4096\n"
"v_mfma_f32_16x16x32_bf16 v[216:219], a[232:235], a[184:187], v[216:219]\n"
"v_exp_f32 v209, v209\n"
"v_mfma_f32_16x16x32_bf16 v[216:219], a[236:239], a[188:191], v[216:219]\n"
"ds_read_b128 a[212:215], v105 offset:4096\n"
"v_cvt_pk_bf16_f32 v232, v208, v209\n"
"v_mfma_f32_16x16x32_bf16 v[220:223], a[252:255], a[188:191], v[252:255]\n"
"v_exp_f32 v210, v210\n"
"v_mfma_f32_16x16x32_bf16 v[220:223], a[248:251], a[184:187], v[220:223]\n"
"ds_read_b128 a[216:219], v106 offset:4096\n"
"v_mfma_f32_16x16x32_bf16 v[220:223], a[244:247], a[180:183], v[220:223]\n"
"v_exp_f32 v211, v211\n"
"v_mfma_f32_16x16x32_bf16 v[220:223], a[240:243], a[176:179], v[220:223]\n"
"ds_read_b128 a[220:223], v107 offset:4096\n"
"v_cvt_pk_bf16_f32 v233, v210, v211\n"
"v_mfma_f32_16x16x32_bf16 a[8:11], v[128:131], v[224:227], a[8:11]\n"
"v_exp_f32 v212, v212\n"
"v_mfma_f32_16x16x32_bf16 a[8:11], v[132:135], v[228:231], a[8:11]\n"
"ds_read_b128 a[224:227], v104 offset:8192\n"
"v_mfma_f32_16x16x32_bf16 a[24:27], v[140:143], v[228:231], a[24:27]\n"
"v_exp_f32 v213, v213\n"
"v_mfma_f32_16x16x32_bf16 a[24:27], v[136:139], v[224:227], a[24:27]\n"
"ds_read_b128 a[228:231], v105 offset:8192\n"
"v_cvt_pk_bf16_f32 v234, v212, v213\n"
"v_mfma_f32_16x16x32_bf16 a[40:43], v[144:147], v[224:227], a[40:43]\n"
"v_exp_f32 v214, v214\n"
"v_mfma_f32_16x16x32_bf16 a[40:43], v[148:151], v[228:231], a[40:43]\n"
"ds_read_b128 a[232:235], v106 offset:8192\n"
"v_mfma_f32_16x16x32_bf16 a[56:59], v[156:159], v[228:231], a[56:59]\n"
"v_exp_f32 v215, v215\n"
"v_mfma_f32_16x16x32_bf16 a[56:59], v[152:155], v[224:227], a[56:59]\n"
"ds_read_b128 a[236:239], v107 offset:8192\n"
"v_cvt_pk_bf16_f32 v235, v214, v215\n"
"v_mfma_f32_16x16x32_bf16 a[72:75], v[160:163], v[224:227], a[72:75]\n"
"ds_read_b128 a[240:243], v104 offset:12288\n"
"v_mfma_f32_16x16x32_bf16 a[72:75], v[164:167], v[228:231], a[72:75]\n"
"v_exp_f32 v216, v216\n"
"v_mfma_f32_16x16x32_bf16 a[88:91], v[172:175], v[228:231], a[88:91]\n"
"ds_read_b128 a[244:247], v105 offset:12288\n"
"v_mfma_f32_16x16x32_bf16 a[88:91], v[168:171], v[224:227], a[88:91]\n"
"v_exp_f32 v217, v217\n"
"v_mfma_f32_16x16x32_bf16 a[104:107], v[176:179], v[224:227], a[104:107]\n"
"ds_read_b128 a[248:251], v106 offset:12288\n"
"v_cvt_pk_bf16_f32 v236, v216, v217\n"
"v_mfma_f32_16x16x32_bf16 a[104:107], v[180:183], v[228:231], a[104:107]\n"
"v_exp_f32 v218, v218\n"
"v_mfma_f32_16x16x32_bf16 a[120:123], v[188:191], v[228:231], a[120:123]\n"
"ds_read_b128 a[252:255], v107 offset:12288\n"
"v_mfma_f32_16x16x32_bf16 a[120:123], v[184:187], v[224:227], a[120:123]\n"
"v_exp_f32 v219, v219\n"
"v_mfma_f32_16x16x32_bf16 v[88:91], v[96:99], v[224:227], v[88:91]\n"
"v_cvt_pk_bf16_f32 v237, v218, v219\n"
"v_mfma_f32_16x16x32_bf16 v[88:91], v[96:99], v[228:231], v[88:91]\n"
"s_waitcnt lgkmcnt(4)\n"
"v_exp_f32 v220, v220\n"
"s_mov_b32 s96, 6\n"
"ALOOP_%=:\n"
"v_mfma_f32_16x16x32_bf16 v[192:195], a[192:195], a[128:131], v[240:243]\n"
"v_mfma_f32_16x16x32_bf16 v[192:195], a[196:199], a[132:135], v[192:195]\n"
"v_exp_f32 v221, v221\n"
"v_mfma_f32_16x16x32_bf16 v[192:195], a[200:203], a[136:139], v[192:195]\n"
"v_cvt_pk_bf16_f32 v238, v220, v221\n"
"v_mfma_f32_16x16x32_bf16 v[192:195], a[204:207], a[140:143], v[192:195]\n"
"v_exp_f32 v222, v222\n"
"v_mfma_f32_16x16x32_bf16 v[196:199], a[220:223], a[140:143], v[240:243]\n"
"v_mfma_f32_16x16x32_bf16 v[196:199], a[216:219], a[136:139], v[196:199]\n"
"v_exp_f32 v223, v223\n"
"v_mfma_f32_16x16x32_bf16 v[196:199], a[212:215], a[132:135], v[196:199]\n"
"v_cvt_pk_bf16_f32 v239, v222, v223\n"
"v_mfma_f32_16x16x32_bf16 v[196:199], a[208:211], a[128:131], v[196:199]\n"
"v_mfma_f32_16x16x32_bf16 v[200:203], a[224:227], a[128:131], v[240:243]\n"
"s_waitcnt vmcnt(16) lgkmcnt(0)\n"
"s_barrier\n"
"v_exp_f32 v192, v192\n"
"v_mfma_f32_16x16x32_bf16 v[200:203], a[228:231], a[132:135], v[200:203]\n"
"v_mfma_f32_16x16x32_bf16 v[200:203], a[232:235], a[136:139], v[200:203]\n"
"s_add_u32 m0, s78, 0x10000\n"
"v_exp_f32 v193, v193\n"
"v_mfma_f32_16x16x32_bf16 v[200:203], a[236:239], a[140:143], v[200:203]\n"
"buffer_load_dwordx4 v114, s[44:47], s95 offen sc1 lds\n"
"v_cvt_pk_bf16_f32 v224, v192, v193\n"
"v_mfma_f32_16x16x32_bf16 v[204:207], a[252:255], a[140:143], v[240:243]\n"
"v_exp_f32 v194, v194\n"
"v_mfma_f32_16x16x32_bf16 v[204:207], a[248:251], a[136:139], v[204:207]\n"
"v_mfma_f32_16x16x32_bf16 v[204:207], a[244:247], a[132:135], v[204:207]\n"
"s_add_u32 m0, s78, 0x10400\n"
"v_exp_f32 v195, v195\n"
"v_mfma_f32_16x16x32_bf16 v[204:207], a[240:243], a[128:131], v[204:207]\n"
"buffer_load_dwordx4 v115, s[44:47], s95 offen sc1 lds\n"
"v_cvt_pk_bf16_f32 v225, v194, v195\n"
"v_mfma_f32_16x16x32_bf16 a[12:15], v[128:131], v[232:235], a[12:15]\n"
"v_exp_f32 v196, v196\n"
"v_mfma_f32_16x16x32_bf16 a[12:15], v[132:135], v[236:239], a[12:15]\n"
"v_mfma_f32_16x16x32_bf16 a[28:31], v[140:143], v[236:239], a[28:31]\n"
"s_add_u32 m0, s78, 0x10800\n"
"v_exp_f32 v197, v197\n"
"v_mfma_f32_16x16x32_bf16 a[28:31], v[136:139], v[232:235], a[28:31]\n"
"buffer_load_dwordx4 v116, s[44:47], s95 offen sc1 lds\n"
"v_cvt_pk_bf16_f32 v226, v196, v197\n"
"v_mfma_f32_16x16x32_bf16 a[44:47], v[144:147], v[232:235], a[44:47]\n"
"v_exp_f32 v198, v198\n"
"v_mfma_f32_16x16x32_bf16 a[44:47], v[148:151], v[236:239], a[44:47]\n"
"v_mfma_f32_16x16x32_bf16 a[60:63], v[156:159], v[236:239], a[60:63]\n"
"s_add_u32 m0, s78, 0x10c00\n"
"v_exp_f32 v199, v199\n"
"v_mfma_f32_16x16x32_bf16 a[60:63], v[152:155], v[232:235], a[60:63]\n"
"buffer_load_dwordx4 v117, s[44:47], s95 offen sc1 lds\n"
"v_cvt_pk_bf16_f32 v227, v198, v199\n"
"v_mfma_f32_16x16x32_bf16 a[76:79], v[160:163], v[232:235], a[76:79]\n"
"v_mfma_f32_16x16x32_bf16 a[76:79], v[164:167], v[236:239], a[76:79]\n"
"s_add_u32 m0, s78, 0\n"
"v_exp_f32 v200, v200\n"
"v_mfma_f32_16x16x32_bf16 a[92:95], v[172:175], v[236:239], a[92:95]\n"
"buffer_load_dwordx4 v110, s[40:43], s94 offen sc1 lds\n"
"v_mfma_f32_16x16x32_bf16 a[92:95], v[168:171], v[232:235], a[92:95]\n"
"v_exp_f32 v201, v201\n"
"v_mfma_f32_16x16x32_bf16 a[108:111], v[176:179], v[232:235], a[108:111]\n"
"v_cvt_pk_bf16_f32 v228, v200, v201\n"
"v_mfma_f32_16x16x32_bf16 a[108:111], v[180:183], v[236:239], a[108:111]\n"
"s_add_u32 m0, s78, 0x400\n"
"v_exp_f32 v202, v202\n"
"v_mfma_f32_16x16x32_bf16 a[124:127], v[188:191], v[236:239], a[124:127]\n"
"buffer_load_dwordx4 v111, s[40:43], s94 offen sc1 lds\n"
"v_mfma_f32_16x16x32_bf16 a[124:127], v[184:187], v[232:235], a[124:127]\n"
"v_exp_f32 v203, v203\n"
"v_mfma_f32_16x16x32_bf16 v[92:95], v[96:99], v[232:235], v[92:95]\n"
"v_cvt_pk_bf16_f32 v229, v202, v203\n"
"v_mfma_f32_16x16x32_bf16 v[92:95], v[96:99], v[236:239], v[92:95]\n"
"s_add_u32 m0, s78, 0x800\n"
"v_exp_f32 v204, v204\n"
"v_mfma_f32_16x16x32_bf16 v[208:211], a[192:195], a[144:147], v[244:247]\n"
"buffer_load_dwordx4 v112, s[40:43], s94 offen sc1 lds\n"
"v_mfma_f32_16x16x32_bf16 v[208:211], a[196:199], a[148:151], v[208:211]\n"
"v_exp_f32 v205, v205\n"
"v_mfma_f32_16x16x32_bf16 v[208:211], a[200:203], a[152:155], v[208:211]\n"
"v_cvt_pk_bf16_f32 v230, v204, v205\n"
"v_mfma_f32_16x16x32_bf16 v[208:211], a[204:207], a[156:159], v[208:211]\n"
"s_add_u32 m0, s78, 0xc00\n"
"v_exp_f32 v206, v206\n"
"v_mfma_f32_16x16x32_bf16 v[212:215], a[220:223], a[156:159], v[244:247]\n"
"buffer_load_dwordx4 v113, s[40:43], s94 offen sc1 lds\n"
"v_mfma_f32_16x16x32_bf16 v[212:215], a[216:219], a[152:155], v[212:215]\n"
"v_exp_f32 v207, v207\n"
"v_mfma_f32_16x16x32_bf16 v[212:215], a[212:215], a[148:151], v[212:215]\n"
"v_cvt_pk_bf16_f32 v231, v206, v207\n"
"v_mfma_f32_16x16x32_bf16 v[212:215], a[208:211], a[144:147], v[212:215]\n"
"s_add_u32 s94, s94, 0x4000\n"
"ds_read_b64_tr_b16 v[128:129], v108 offset:16384\n"
"v_mfma_f32_16x16x32_bf16 v[216:219], a[224:227], a[144:147], v[244:247]\n"
"s_add_u32 s95, s95, 0x4000\n"
"v_exp_f32 v208, v208\n"
"v_mfma_f32_16x16x32_bf16 v[216:219], a[228:231], a[148:151], v[216:219]\n"
"ds_read_b64_tr_b16 v[130:131], v108 offset:20480\n"
"v_mfma_f32_16x16x32_bf16 v[216:219], a[232:235], a[152:155], v[216:219]\n"
"v_exp_f32 v209, v209\n"
"v_mfma_f32_16x16x32_bf16 v[216:219], a[236:239], a[156:159], v[216:219]\n"
"ds_read_b64_tr_b16 v[132:133], v108 offset:24576\n"
"v_cvt_pk_bf16_f32 v232, v208, v209\n"
"v_mfma_f32_16x16x32_bf16 v[220:223], a[252:255], a[156:159], v[244:247]\n"
"v_exp_f32 v210, v210\n"
"v_mfma_f32_16x16x32_bf16 v[220:223], a[248:251], a[152:155], v[220:223]\n"
"ds_read_b64_tr_b16 v[134:135], v108 offset:28672\n"
"v_mfma_f32_16x16x32_bf16 v[220:223], a[244:247], a[148:151], v[220:223]\n"
"v_exp_f32 v211, v211\n"
"v_mfma_f32_16x16x32_bf16 v[220:223], a[240:243], a[144:147], v[220:223]\n"
"ds_read_b64_tr_b16 v[136:137], v109 offset:16384\n"
"v_cvt_pk_bf16_f32 v233, v210, v211\n"
"v_mfma_f32_16x16x32_bf16 a[0:3], v[16:19], v[224:227], a[0:3]\n"
"v_exp_f32 v212, v212\n"
"v_mfma_f32_16x16x32_bf16 a[0:3], v[20:23], v[228:231], a[0:3]\n"
"ds_read_b64_tr_b16 v[138:139], v109 offset:20480\n"
"v_mfma_f32_16x16x32_bf16 a[16:19], v[28:31], v[228:231], a[16:19]\n"
"v_exp_f32 v213, v213\n"
"v_mfma_f32_16x16x32_bf16 a[16:19], v[24:27], v[224:227], a[16:19]\n"
"ds_read_b64_tr_b16 v[140:141], v109 offset:24576\n"
"v_cvt_pk_bf16_f32 v234, v212, v213\n"
"v_mfma_f32_16x16x32_bf16 a[32:35], v[32:35], v[224:227], a[32:35]\n"
"v_exp_f32 v214, v214\n"
"v_mfma_f32_16x16x32_bf16 a[32:35], v[36:39], v[228:231], a[32:35]\n"
"ds_read_b64_tr_b16 v[142:143], v109 offset:28672\n"
"v_mfma_f32_16x16x32_bf16 a[48:51], v[44:47], v[228:231], a[48:51]\n"
"v_exp_f32 v215, v215\n"
"v_mfma_f32_16x16x32_bf16 a[48:51], v[40:43], v[224:227], a[48:51]\n"
"ds_read_b64_tr_b16 v[144:145], v108 offset:16896\n"
"v_cvt_pk_bf16_f32 v235, v214, v215\n"
"v_mfma_f32_16x16x32_bf16 a[64:67], v[48:51], v[224:227], a[64:67]\n"
"ds_read_b64_tr_b16 v[146:147], v108 offset:20992\n"
"v_mfma_f32_16x16x32_bf16 a[64:67], v[52:55], v[228:231], a[64:67]\n"
"v_exp_f32 v216, v216\n"
"v_mfma_f32_16x16x32_bf16 a[80:83], v[60:63], v[228:231], a[80:83]\n"
"ds_read_b64_tr_b16 v[148:149], v108 offset:25088\n"
"v_mfma_f32_16x16x32_bf16 a[80:83], v[56:59], v[224:227], a[80:83]\n"
"v_exp_f32 v217, v217\n"
"v_mfma_f32_16x16x32_bf16 a[96:99], v[64:67], v[224:227], a[96:99]\n"
"ds_read_b64_tr_b16 v[150:151], v108 offset:29184\n"
"v_cvt_pk_bf16_f32 v236, v216, v217\n"
"v_mfma_f32_16x16x32_bf16 a[96:99], v[68:71], v[228:231], a[96:99]\n"
"v_exp_f32 v218, v218\n"
"v_mfma_f32_16x16x32_bf16 a[112:115], v[76:79], v[228:231], a[112:115]\n"
"ds_read_b64_tr_b16 v[152:153], v109 offset:16896\n"
"v_mfma_f32_16x16x32_bf16 a[112:115], v[72:75], v[224:227], a[112:115]\n"
"v_exp_f32 v219, v219\n"
"v_mfma_f32_16x16x32_bf16 v[80:83], v[96:99], v[224:227], v[80:83]\n"
"ds_read_b64_tr_b16 v[154:155], v109 offset:20992\n"
"v_cvt_pk_bf16_f32 v237, v218, v219\n"
"v_mfma_f32_16x16x32_bf16 v[80:83], v[96:99], v[228:231], v[80:83]\n"
"v_exp_f32 v220, v220\n"
"v_mfma_f32_16x16x32_bf16 v[192:195], a[192:195], a[160:163], v[248:251]\n"
"ds_read_b64_tr_b16 v[156:157], v109 offset:25088\n"
"v_mfma_f32_16x16x32_bf16 v[192:195], a[196:199], a[164:167], v[192:195]\n"
"v_exp_f32 v221, v221\n"
"v_mfma_f32_16x16x32_bf16 v[192:195], a[200:203], a[168:171], v[192:195]\n"
"ds_read_b64_tr_b16 v[158:159], v109 offset:29184\n"
"v_cvt_pk_bf16_f32 v238, v220, v221\n"
"v_mfma_f32_16x16x32_bf16 v[192:195], a[204:207], a[172:175], v[192:195]\n"
"v_exp_f32 v222, v222\n"
"v_mfma_f32_16x16x32_bf16 v[196:199], a[220:223], a[172:175], v[248:251]\n"
"ds_read_b64_tr_b16 v[160:161], v108 offset:17408\n"
"v_mfma_f32_16x16x32_bf16 v[196:199], a[216:219], a[168:171], v[196:199]\n"
"v_exp_f32 v223, v223\n"
"v_mfma_f32_16x16x32_bf16 v[196:199], a[212:215], a[164:167], v[196:199]\n"
"ds_read_b64_tr_b16 v[162:163], v108 offset:21504\n"
"v_cvt_pk_bf16_f32 v239, v222, v223\n"
"v_mfma_f32_16x16x32_bf16 v[196:199], a[208:211], a[160:163], v[196:199]\n"
"ds_read_b64_tr_b16 v[164:165], v108 offset:25600\n"
"v_mfma_f32_16x16x32_bf16 v[200:203], a[224:227], a[160:163], v[248:251]\n"
"v_exp_f32 v192, v192\n"
"v_mfma_f32_16x16x32_bf16 v[200:203], a[228:231], a[164:167], v[200:203]\n"
"ds_read_b64_tr_b16 v[166:167], v108 offset:29696\n"
"v_mfma_f32_16x16x32_bf16 v[200:203], a[232:235], a[168:171], v[200:203]\n"
"v_exp_f32 v193, v193\n"
"v_mfma_f32_16x16x32_bf16 v[200:203], a[236:239], a[172:175], v[200:203]\n"
"ds_read_b64_tr_b16 v[168:169], v109 offset:17408\n"
"v_cvt_pk_bf16_f32 v224, v192, v193\n"
"v_mfma_f32_16x16x32_bf16 v[204:207], a[252:255], a[172:175], v[248:251]\n"
"v_exp_f32 v194, v194\n"
"v_mfma_f32_16x16x32_bf16 v[204:207], a[248:251], a[168:171], v[204:207]\n"
"ds_read_b64_tr_b16 v[170:171], v109 offset:21504\n"
"v_mfma_f32_16x16x32_bf16 v[204:207], a[244:247], a[164:167], v[204:207]\n"
"v_exp_f32 v195, v195\n"
"v_mfma_f32_16x16x32_bf16 v[204:207], a[240:243], a[160:163], v[204:207]\n"
"ds_read_b64_tr_b16 v[172:173], v109 offset:25600\n"
"v_cvt_pk_bf16_f32 v225, v194, v195\n"
"v_mfma_f32_16x16x32_bf16 a[4:7], v[16:19], v[232:235], a[4:7]\n"
"v_exp_f32 v196, v196\n"
"v_mfma_f32_16x16x32_bf16 a[4:7], v[20:23], v[236:239], a[4:7]\n"
"ds_read_b64_tr_b16 v[174:175], v109 offset:29696\n"
"v_mfma_f32_16x16x32_bf16 a[20:23], v[28:31], v[236:239], a[20:23]\n"
"v_exp_f32 v197, v197\n"
"v_mfma_f32_16x16x32_bf16 a[20:23], v[24:27], v[232:235], a[20:23]\n"
"ds_read_b64_tr_b16 v[176:177], v108 offset:17920\n"
"v_cvt_pk_bf16_f32 v226, v196, v197\n"
"v_mfma_f32_16x16x32_bf16 a[36:39], v[32:35], v[232:235], a[36:39]\n"
"v_exp_f32 v198, v198\n"
"v_mfma_f32_16x16x32_bf16 a[36:39], v[36:39], v[236:239], a[36:39]\n"
"ds_read_b64_tr_b16 v[178:179], v108 offset:22016\n"
"v_mfma_f32_16x16x32_bf16 a[52:55], v[44:47], v[236:239], a[52:55]\n"
"v_exp_f32 v199, v199\n"
"v_mfma_f32_16x16x32_bf16 a[52:55], v[40:43], v[232:235], a[52:55]\n"
"ds_read_b64_tr_b16 v[180:181], v108 offset:26112\n"
"v_cvt_pk_bf16_f32 v227, v198, v199\n"
"v_mfma_f32_16x16x32_bf16 a[68:71], v[48:51], v[232:235], a[68:71]\n"
"ds_read_b64_tr_b16 v[182:183], v108 offset:30208\n"
"v_mfma_f32_16x16x32_bf16 a[68:71], v[52:55], v[236:239], a[68:71]\n"
"v_exp_f32 v200, v200\n"
"v_mfma_f32_16x16x32_bf16 a[84:87], v[60:63], v[236:239], a[84:87]\n"
"ds_read_b64_tr_b16 v[184:185], v109 offset:17920\n"
"v_mfma_f32_16x16x32_bf16 a[84:87], v[56:59], v[232:235], a[84:87]\n"
"v_exp_f32 v201, v201\n"
"v_mfma_f32_16x16x32_bf16 a[100:103], v[64:67], v[232:235], a[100:103]\n"
"ds_read_b64_tr_b16 v[186:187], v109 offset:22016\n"
"v_cvt_pk_bf16_f32 v228, v200, v201\n"
"v_mfma_f32_16x16x32_bf16 a[100:103], v[68:71], v[236:239], a[100:103]\n"
"v_exp_f32 v202, v202\n"
"v_mfma_f32_16x16x32_bf16 a[116:119], v[76:79], v[236:239], a[116:119]\n"
"ds_read_b64_tr_b16 v[188:189], v109 offset:26112\n"
"v_mfma_f32_16x16x32_bf16 a[116:119], v[72:75], v[232:235], a[116:119]\n"
"v_exp_f32 v203, v203\n"
"v_mfma_f32_16x16x32_bf16 v[84:87], v[96:99], v[232:235], v[84:87]\n"
"ds_read_b64_tr_b16 v[190:191], v109 offset:30208\n"
"v_cvt_pk_bf16_f32 v229, v202, v203\n"
"v_mfma_f32_16x16x32_bf16 v[84:87], v[96:99], v[236:239], v[84:87]\n"
"v_exp_f32 v204, v204\n"
"v_mfma_f32_16x16x32_bf16 v[208:211], a[192:195], a[176:179], v[252:255]\n"
"v_mfma_f32_16x16x32_bf16 v[208:211], a[196:199], a[180:183], v[208:211]\n"
"v_exp_f32 v205, v205\n"
"v_mfma_f32_16x16x32_bf16 v[208:211], a[200:203], a[184:187], v[208:211]\n"
"ds_read_b128 a[192:195], v104 offset:16384\n"
"v_cvt_pk_bf16_f32 v230, v204, v205\n"
"v_mfma_f32_16x16x32_bf16 v[208:211], a[204:207], a[188:191], v[208:211]\n"
"v_exp_f32 v206, v206\n"
"v_mfma_f32_16x16x32_bf16 v[212:215], a[220:223], a[188:191], v[252:255]\n"
"ds_read_b128 a[196:199], v105 offset:16384\n"
"v_mfma_f32_16x16x32_bf16 v[212:215], a[216:219], a[184:187], v[212:215]\n"
"v_exp_f32 v207, v207\n"
"v_mfma_f32_16x16x32_bf16 v[212:215], a[212:215], a[180:183], v[212:215]\n"
"ds_read_b128 a[200:203], v106 offset:16384\n"
"v_cvt_pk_bf16_f32 v231, v206, v207\n"
"v_mfma_f32_16x16x32_bf16 v[212:215], a[208:211], a[176:179], v[212:215]\n"
"ds_read_b128 a[204:207], v107 offset:16384\n"
"v_mfma_f32_16x16x32_bf16 v[216:219], a[224:227], a[176:179], v[252:255]\n"
"v_exp_f32 v208, v208\n"
"v_mfma_f32_16x16x32_bf16 v[216:219], a[228:231], a[180:183], v[216:219]\n"
"ds_read_b128 a[208:211], v104 offset:20480\n"
"v_mfma_f32_16x16x32_bf16 v[216:219], a[232:235], a[184:187], v[216:219]\n"
"v_exp_f32 v209, v209\n"
"v_mfma_f32_16x16x32_bf16 v[216:219], a[236:239], a[188:191], v[216:219]\n"
"ds_read_b128 a[212:215], v105 offset:20480\n"
"v_cvt_pk_bf16_f32 v232, v208, v209\n"
"v_mfma_f32_16x16x32_bf16 v[220:223], a[252:255], a[188:191], v[252:255]\n"
"v_exp_f32 v210, v210\n"
"v_mfma_f32_16x16x32_bf16 v[220:223], a[248:251], a[184:187], v[220:223]\n"
"ds_read_b128 a[216:219], v106 offset:20480\n"
"v_mfma_f32_16x16x32_bf16 v[220:223], a[244:247], a[180:183], v[220:223]\n"
"v_exp_f32 v211, v211\n"
"v_mfma_f32_16x16x32_bf16 v[220:223], a[240:243], a[176:179], v[220:223]\n"
"ds_read_b128 a[220:223], v107 offset:20480\n"
"v_cvt_pk_bf16_f32 v233, v210, v211\n"
"v_mfma_f32_16x16x32_bf16 a[8:11], v[16:19], v[224:227], a[8:11]\n"
"v_exp_f32 v212, v212\n"
"v_mfma_f32_16x16x32_bf16 a[8:11], v[20:23], v[228:231], a[8:11]\n"
"ds_read_b128 a[224:227], v104 offset:24576\n"
"v_mfma_f32_16x16x32_bf16 a[24:27], v[28:31], v[228:231], a[24:27]\n"
"v_exp_f32 v213, v213\n"
"v_mfma_f32_16x16x32_bf16 a[24:27], v[24:27], v[224:227], a[24:27]\n"
"ds_read_b128 a[228:231], v105 offset:24576\n"
"v_cvt_pk_bf16_f32 v234, v212, v213\n"
"v_mfma_f32_16x16x32_bf16 a[40:43], v[32:35], v[224:227], a[40:43]\n"
"v_exp_f32 v214, v214\n"
"v_mfma_f32_16x16x32_bf16 a[40:43], v[36:39], v[228:231], a[40:43]\n"
"ds_read_b128 a[232:235], v106 offset:24576\n"
"v_mfma_f32_16x16x32_bf16 a[56:59], v[44:47], v[228:231], a[56:59]\n"
"v_exp_f32 v215, v215\n"
"v_mfma_f32_16x16x32_bf16 a[56:59], v[40:43], v[224:227], a[56:59]\n"
"ds_read_b128 a[236:239], v107 offset:24576\n"
"v_cvt_pk_bf16_f32 v235, v214, v215\n"
"v_mfma_f32_16x16x32_bf16 a[72:75], v[48:51], v[224:227], a[72:75]\n"
"ds_read_b128 a[240:243], v104 offset:28672\n"
"v_mfma_f32_16x16x32_bf16 a[72:75], v[52:55], v[228:231], a[72:75]\n"
"v_exp_f32 v216, v216\n"
"v_mfma_f32_16x16x32_bf16 a[88:91], v[60:63], v[228:231], a[88:91]\n"
"ds_read_b128 a[244:247], v105 offset:28672\n"
"v_mfma_f32_16x16x32_bf16 a[88:91], v[56:59], v[224:227], a[88:91]\n"
"v_exp_f32 v217, v217\n"
"v_mfma_f32_16x16x32_bf16 a[104:107], v[64:67], v[224:227], a[104:107]\n"
"ds_read_b128 a[248:251], v106 offset:28672\n"
"v_cvt_pk_bf16_f32 v236, v216, v217\n"
"v_mfma_f32_16x16x32_bf16 a[104:107], v[68:71], v[228:231], a[104:107]\n"
"v_exp_f32 v218, v218\n"
"v_mfma_f32_16x16x32_bf16 a[120:123], v[76:79], v[228:231], a[120:123]\n"
"ds_read_b128 a[252:255], v107 offset:28672\n"
"v_mfma_f32_16x16x32_bf16 a[120:123], v[72:75], v[224:227], a[120:123]\n"
"v_exp_f32 v219, v219\n"
"v_mfma_f32_16x16x32_bf16 v[88:91], v[96:99], v[224:227], v[88:91]\n"
"v_cvt_pk_bf16_f32 v237, v218, v219\n"
"v_mfma_f32_16x16x32_bf16 v[88:91], v[96:99], v[228:231], v[88:91]\n"
"s_waitcnt lgkmcnt(4)\n"
"v_exp_f32 v220, v220\n"
"v_mfma_f32_16x16x32_bf16 v[192:195], a[192:195], a[128:131], v[240:243]\n"
"v_mfma_f32_16x16x32_bf16 v[192:195], a[196:199], a[132:135], v[192:195]\n"
"v_exp_f32 v221, v221\n"
"v_mfma_f32_16x16x32_bf16 v[192:195], a[200:203], a[136:139], v[192:195]\n"
"v_cvt_pk_bf16_f32 v238, v220, v221\n"
"v_mfma_f32_16x16x32_bf16 v[192:195], a[204:207], a[140:143], v[192:195]\n"
"v_exp_f32 v222, v222\n"
"v_mfma_f32_16x16x32_bf16 v[196:199], a[220:223], a[140:143], v[240:243]\n"
"v_mfma_f32_16x16x32_bf16 v[196:199], a[216:219], a[136:139], v[196:199]\n"
"v_exp_f32 v223, v223\n"
"v_mfma_f32_16x16x32_bf16 v[196:199], a[212:215], a[132:135], v[196:199]\n"
"v_cvt_pk_bf16_f32 v239, v222, v223\n"
"v_mfma_f32_16x16x32_bf16 v[196:199], a[208:211], a[128:131], v[196:199]\n"
"v_mfma_f32_16x16x32_bf16 v[200:203], a[224:227], a[128:131], v[240:243]\n"
"s_waitcnt vmcnt(16) lgkmcnt(0)\n"
"s_barrier\n"
"v_exp_f32 v192, v192\n"
"v_mfma_f32_16x16x32_bf16 v[200:203], a[228:231], a[132:135], v[200:203]\n"
"v_mfma_f32_16x16x32_bf16 v[200:203], a[232:235], a[136:139], v[200:203]\n"
"s_add_u32 m0, s78, 0x14000\n"
"v_exp_f32 v193, v193\n"
"v_mfma_f32_16x16x32_bf16 v[200:203], a[236:239], a[140:143], v[200:203]\n"
"buffer_load_dwordx4 v114, s[44:47], s95 offen sc1 lds\n"
"v_cvt_pk_bf16_f32 v224, v192, v193\n"
"v_mfma_f32_16x16x32_bf16 v[204:207], a[252:255], a[140:143], v[240:243]\n"
"v_exp_f32 v194, v194\n"
"v_mfma_f32_16x16x32_bf16 v[204:207], a[248:251], a[136:139], v[204:207]\n"
"v_mfma_f32_16x16x32_bf16 v[204:207], a[244:247], a[132:135], v[204:207]\n"
"s_add_u32 m0, s78, 0x14400\n"
"v_exp_f32 v195, v195\n"
"v_mfma_f32_16x16x32_bf16 v[204:207], a[240:243], a[128:131], v[204:207]\n"
"buffer_load_dwordx4 v115, s[44:47], s95 offen sc1 lds\n"
"v_cvt_pk_bf16_f32 v225, v194, v195\n"
"v_mfma_f32_16x16x32_bf16 a[12:15], v[16:19], v[232:235], a[12:15]\n"
"v_exp_f32 v196, v196\n"
"v_mfma_f32_16x16x32_bf16 a[12:15], v[20:23], v[236:239], a[12:15]\n"
"v_mfma_f32_16x16x32_bf16 a[28:31], v[28:31], v[236:239], a[28:31]\n"
"s_add_u32 m0, s78, 0x14800\n"
"v_exp_f32 v197, v197\n"
"v_mfma_f32_16x16x32_bf16 a[28:31], v[24:27], v[232:235], a[28:31]\n"
"buffer_load_dwordx4 v116, s[44:47], s95 offen sc1 lds\n"
"v_cvt_pk_bf16_f32 v226, v196, v197\n"
"v_mfma_f32_16x16x32_bf16 a[44:47], v[32:35], v[232:235], a[44:47]\n"
"v_exp_f32 v198, v198\n"
"v_mfma_f32_16x16x32_bf16 a[44:47], v[36:39], v[236:239], a[44:47]\n"
"v_mfma_f32_16x16x32_bf16 a[60:63], v[44:47], v[236:239], a[60:63]\n"
"s_add_u32 m0, s78, 0x14c00\n"
"v_exp_f32 v199, v199\n"
"v_mfma_f32_16x16x32_bf16 a[60:63], v[40:43], v[232:235], a[60:63]\n"
"buffer_load_dwordx4 v117, s[44:47], s95 offen sc1 lds\n"
"v_cvt_pk_bf16_f32 v227, v198, v199\n"
"v_mfma_f32_16x16x32_bf16 a[76:79], v[48:51], v[232:235], a[76:79]\n"
"v_mfma_f32_16x16x32_bf16 a[76:79], v[52:55], v[236:239], a[76:79]\n"
"s_add_u32 m0, s78, 0x4000\n"
"v_exp_f32 v200, v200\n"
"v_mfma_f32_16x16x32_bf16 a[92:95], v[60:63], v[236:239], a[92:95]\n"
"buffer_load_dwordx4 v110, s[40:43], s94 offen sc1 lds\n"
"v_mfma_f32_16x16x32_bf16 a[92:95], v[56:59], v[232:235], a[92:95]\n"
"v_exp_f32 v201, v201\n"
"v_mfma_f32_16x16x32_bf16 a[108:111], v[64:67], v[232:235], a[108:111]\n"
"v_cvt_pk_bf16_f32 v228, v200, v201\n"
"v_mfma_f32_16x16x32_bf16 a[108:111], v[68:71], v[236:239], a[108:111]\n"
"s_add_u32 m0, s78, 0x4400\n"
"v_exp_f32 v202, v202\n"
"v_mfma_f32_16x16x32_bf16 a[124:127], v[76:79], v[236:239], a[124:127]\n"
"buffer_load_dwordx4 v111, s[40:43], s94 offen sc1 lds\n"
"v_mfma_f32_16x16x32_bf16 a[124:127], v[72:75], v[232:235], a[124:127]\n"
"v_exp_f32 v203, v203\n"
"v_mfma_f32_16x16x32_bf16 v[92:95], v[96:99], v[232:235], v[92:95]\n"
"v_cvt_pk_bf16_f32 v229, v202, v203\n"
"v_mfma_f32_16x16x32_bf16 v[92:95], v[96:99], v[236:239], v[92:95]\n"
"s_add_u32 m0, s78, 0x4800\n"
"v_exp_f32 v204, v204\n"
"v_mfma_f32_16x16x32_bf16 v[208:211], a[192:195], a[144:147], v[244:247]\n"
"buffer_load_dwordx4 v112, s[40:43], s94 offen sc1 lds\n"
"v_mfma_f32_16x16x32_bf16 v[208:211], a[196:199], a[148:151], v[208:211]\n"
"v_exp_f32 v205, v205\n"
"v_mfma_f32_16x16x32_bf16 v[208:211], a[200:203], a[152:155], v[208:211]\n"
"v_cvt_pk_bf16_f32 v230, v204, v205\n"
"v_mfma_f32_16x16x32_bf16 v[208:211], a[204:207], a[156:159], v[208:211]\n"
"s_add_u32 m0, s78, 0x4c00\n"
"v_exp_f32 v206, v206\n"
"v_mfma_f32_16x16x32_bf16 v[212:215], a[220:223], a[156:159], v[244:247]\n"
"buffer_load_dwordx4 v113, s[40:43], s94 offen sc1 lds\n"
"v_mfma_f32_16x16x32_bf16 v[212:215], a[216:219], a[152:155], v[212:215]\n"
"v_exp_f32 v207, v207\n"
"v_mfma_f32_16x16x32_bf16 v[212:215], a[212:215], a[148:151], v[212:215]\n"
"v_cvt_pk_bf16_f32 v231, v206, v207\n"
"v_mfma_f32_16x16x32_bf16 v[212:215], a[208:211], a[144:147], v[212:215]\n"
"s_add_u32 s94, s94, 0x4000\n"
"ds_read_b64_tr_b16 v[16:17], v108 offset:32768\n"
"v_mfma_f32_16x16x32_bf16 v[216:219], a[224:227], a[144:147], v[244:247]\n"
"s_add_u32 s95, s95, 0x4000\n"
"v_exp_f32 v208, v208\n"
"v_mfma_f32_16x16x32_bf16 v[216:219], a[228:231], a[148:151], v[216:219]\n"
"ds_read_b64_tr_b16 v[18:19], v108 offset:36864\n"
"v_mfma_f32_16x16x32_bf16 v[216:219], a[232:235], a[152:155], v[216:219]\n"
"v_exp_f32 v209, v209\n"
"v_mfma_f32_16x16x32_bf16 v[216:219], a[236:239], a[156:159], v[216:219]\n"
"ds_read_b64_tr_b16 v[20:21], v108 offset:40960\n"
"v_cvt_pk_bf16_f32 v232, v208, v209\n"
"v_mfma_f32_16x16x32_bf16 v[220:223], a[252:255], a[156:159], v[244:247]\n"
"v_exp_f32 v210, v210\n"
"v_mfma_f32_16x16x32_bf16 v[220:223], a[248:251], a[152:155], v[220:223]\n"
"ds_read_b64_tr_b16 v[22:23], v108 offset:45056\n"
"v_mfma_f32_16x16x32_bf16 v[220:223], a[244:247], a[148:151], v[220:223]\n"
"v_exp_f32 v211, v211\n"
"v_mfma_f32_16x16x32_bf16 v[220:223], a[240:243], a[144:147], v[220:223]\n"
"ds_read_b64_tr_b16 v[24:25], v109 offset:32768\n"
"v_cvt_pk_bf16_f32 v233, v210, v211\n"
"v_mfma_f32_16x16x32_bf16 a[0:3], v[128:131], v[224:227], a[0:3]\n"
"v_exp_f32 v212, v212\n"
"v_mfma_f32_16x16x32_bf16 a[0:3], v[132:135], v[228:231], a[0:3]\n"
"ds_read_b64_tr_b16 v[26:27], v109 offset:36864\n"
"v_mfma_f32_16x16x32_bf16 a[16:19], v[140:143], v[228:231], a[16:19]\n"
"v_exp_f32 v213, v213\n"
"v_mfma_f32_16x16x32_bf16 a[16:19], v[136:139], v[224:227], a[16:19]\n"
"ds_read_b64_tr_b16 v[28:29], v109 offset:40960\n"
"v_cvt_pk_bf16_f32 v234, v212, v213\n"
"v_mfma_f32_16x16x32_bf16 a[32:35], v[144:147], v[224:227], a[32:35]\n"
"v_exp_f32 v214, v214\n"
"v_mfma_f32_16x16x32_bf16 a[32:35], v[148:151], v[228:231], a[32:35]\n"
"ds_read_b64_tr_b16 v[30:31], v109 offset:45056\n"
"v_mfma_f32_16x16x32_bf16 a[48:51], v[156:159], v[228:231], a[48:51]\n"
"v_exp_f32 v215, v215\n"
"v_mfma_f32_16x16x32_bf16 a[48:51], v[152:155], v[224:227], a[48:51]\n"
"ds_read_b64_tr_b16 v[32:33], v108 offset:33280\n"
"v_cvt_pk_bf16_f32 v235, v214, v215\n"
"v_mfma_f32_16x16x32_bf16 a[64:67], v[160:163], v[224:227], a[64:67]\n"
"ds_read_b64_tr_b16 v[34:35], v108 offset:37376\n"
"v_mfma_f32_16x16x32_bf16 a[64:67], v[164:167], v[228:231], a[64:67]\n"
"v_exp_f32 v216, v216\n"
"v_mfma_f32_16x16x32_bf16 a[80:83], v[172:175], v[228:231], a[80:83]\n"
"ds_read_b64_tr_b16 v[36:37], v108 offset:41472\n"
"v_mfma_f32_16x16x32_bf16 a[80:83], v[168:171], v[224:227], a[80:83]\n"
"v_exp_f32 v217, v217\n"
"v_mfma_f32_16x16x32_bf16 a[96:99], v[176:179], v[224:227], a[96:99]\n"
"ds_read_b64_tr_b16 v[38:39], v108 offset:45568\n"
"v_cvt_pk_bf16_f32 v236, v216, v217\n"
"v_mfma_f32_16x16x32_bf16 a[96:99], v[180:183], v[228:231], a[96:99]\n"
"v_exp_f32 v218, v218\n"
"v_mfma_f32_16x16x32_bf16 a[112:115], v[188:191], v[228:231], a[112:115]\n"
"ds_read_b64_tr_b16 v[40:41], v109 offset:33280\n"
"v_mfma_f32_16x16x32_bf16 a[112:115], v[184:187], v[224:227], a[112:115]\n"
"v_exp_f32 v219, v219\n"
"v_mfma_f32_16x16x32_bf16 v[80:83], v[96:99], v[224:227], v[80:83]\n"
"ds_read_b64_tr_b16 v[42:43], v109 offset:37376\n"
"v_cvt_pk_bf16_f32 v237, v218, v219\n"
"v_mfma_f32_16x16x32_bf16 v[80:83], v[96:99], v[228:231], v[80:83]\n"
"v_exp_f32 v220, v220\n"
"v_mfma_f32_16x16x32_bf16 v[192:195], a[192:195], a[160:163], v[248:251]\n"
"ds_read_b64_tr_b16 v[44:45], v109 offset:41472\n"
"v_mfma_f32_16x16x32_bf16 v[192:195], a[196:199], a[164:167], v[192:195]\n"
"v_exp_f32 v221, v221\n"
"v_mfma_f32_16x16x32_bf16 v[192:195], a[200:203], a[168:171], v[192:195]\n"
"ds_read_b64_tr_b16 v[46:47], v109 offset:45568\n"
"v_cvt_pk_bf16_f32 v238, v220, v221\n"
"v_mfma_f32_16x16x32_bf16 v[192:195], a[204:207], a[172:175], v[192:195]\n"
"v_exp_f32 v222, v222\n"
"v_mfma_f32_16x16x32_bf16 v[196:199], a[220:223], a[172:175], v[248:251]\n"
"ds_read_b64_tr_b16 v[48:49], v108 offset:33792\n"
"v_mfma_f32_16x16x32_bf16 v[196:199], a[216:219], a[168:171], v[196:199]\n"
"v_exp_f32 v223, v223\n"
"v_mfma_f32_16x16x32_bf16 v[196:199], a[212:215], a[164:167], v[196:199]\n"
"ds_read_b64_tr_b16 v[50:51], v108 offset:37888\n"
"v_cvt_pk_bf16_f32 v239, v222, v223\n"
"v_mfma_f32_16x16x32_bf16 v[196:199], a[208:211], a[160:163], v[196:199]\n"
"ds_read_b64_tr_b16 v[52:53], v108 offset:41984\n"
"v_mfma_f32_16x16x32_bf16 v[200:203], a[224:227], a[160:163], v[248:251]\n"
"v_exp_f32 v192, v192\n"
"v_mfma_f32_16x16x32_bf16 v[200:203], a[228:231], a[164:167], v[200:203]\n"
"ds_read_b64_tr_b16 v[54:55], v108 offset:46080\n"
"v_mfma_f32_16x16x32_bf16 v[200:203], a[232:235], a[168:171], v[200:203]\n"
"v_exp_f32 v193, v193\n"
"v_mfma_f32_16x16x32_bf16 v[200:203], a[236:239], a[172:175], v[200:203]\n"
"ds_read_b64_tr_b16 v[56:57], v109 offset:33792\n"
"v_cvt_pk_bf16_f32 v224, v192, v193\n"
"v_mfma_f32_16x16x32_bf16 v[204:207], a[252:255], a[172:175], v[248:251]\n"
"v_exp_f32 v194, v194\n"
"v_mfma_f32_16x16x32_bf16 v[204:207], a[248:251], a[168:171], v[204:207]\n"
"ds_read_b64_tr_b16 v[58:59], v109 offset:37888\n"
"v_mfma_f32_16x16x32_bf16 v[204:207], a[244:247], a[164:167], v[204:207]\n"
"v_exp_f32 v195, v195\n"
"v_mfma_f32_16x16x32_bf16 v[204:207], a[240:243], a[160:163], v[204:207]\n"
"ds_read_b64_tr_b16 v[60:61], v109 offset:41984\n"
"v_cvt_pk_bf16_f32 v225, v194, v195\n"
"v_mfma_f32_16x16x32_bf16 a[4:7], v[128:131], v[232:235], a[4:7]\n"
"v_exp_f32 v196, v196\n"
"v_mfma_f32_16x16x32_bf16 a[4:7], v[132:135], v[236:239], a[4:7]\n"
"ds_read_b64_tr_b16 v[62:63], v109 offset:46080\n"
"v_mfma_f32_16x16x32_bf16 a[20:23], v[140:143], v[236:239], a[20:23]\n"
"v_exp_f32 v197, v197\n"
"v_mfma_f32_16x16x32_bf16 a[20:23], v[136:139], v[232:235], a[20:23]\n"
"ds_read_b64_tr_b16 v[64:65], v108 offset:34304\n"
"v_cvt_pk_bf16_f32 v226, v196, v197\n"
"v_mfma_f32_16x16x32_bf16 a[36:39], v[144:147], v[232:235], a[36:39]\n"
"v_exp_f32 v198, v198\n"
"v_mfma_f32_16x16x32_bf16 a[36:39], v[148:151], v[236:239], a[36:39]\n"
"ds_read_b64_tr_b16 v[66:67], v108 offset:38400\n"
"v_mfma_f32_16x16x32_bf16 a[52:55], v[156:159], v[236:239], a[52:55]\n"
"v_exp_f32 v199, v199\n"
"v_mfma_f32_16x16x32_bf16 a[52:55], v[152:155], v[232:235], a[52:55]\n"
"ds_read_b64_tr_b16 v[68:69], v108 offset:42496\n"
"v_cvt_pk_bf16_f32 v227, v198, v199\n"
"v_mfma_f32_16x16x32_bf16 a[68:71], v[160:163], v[232:235], a[68:71]\n"
"ds_read_b64_tr_b16 v[70:71], v108 offset:46592\n"
"v_mfma_f32_16x16x32_bf16 a[68:71], v[164:167], v[236:239], a[68:71]\n"
"v_exp_f32 v200, v200\n"
"v_mfma_f32_16x16x32_bf16 a[84:87], v[172:175], v[236:239], a[84:87]\n"
"ds_read_b64_tr_b16 v[72:73], v109 offset:34304\n"
"v_mfma_f32_16x16x32_bf16 a[84:87], v[168:171], v[232:235], a[84:87]\n"
"v_exp_f32 v201, v201\n"
"v_mfma_f32_16x16x32_bf16 a[100:103], v[176:179], v[232:235], a[100:103]\n"
"ds_read_b64_tr_b16 v[74:75], v109 offset:38400\n"
"v_cvt_pk_bf16_f32 v228, v200, v201\n"
"v_mfma_f32_16x16x32_bf16 a[100:103], v[180:183], v[236:239], a[100:103]\n"
"v_exp_f32 v202, v202\n"
"v_mfma_f32_16x16x32_bf16 a[116:119], v[188:191], v[236:239], a[116:119]\n"
"ds_read_b64_tr_b16 v[76:77], v109 offset:42496\n"
"v_mfma_f32_16x16x32_bf16 a[116:119], v[184:187], v[232:235], a[116:119]\n"
"v_exp_f32 v203, v203\n"
"v_mfma_f32_16x16x32_bf16 v[84:87], v[96:99], v[232:235], v[84:87]\n"
"ds_read_b64_tr_b16 v[78:79], v109 offset:46592\n"
"v_cvt_pk_bf16_f32 v229, v202, v203\n"
"v_mfma_f32_16x16x32_bf16 v[84:87], v[96:99], v[236:239], v[84:87]\n"
"v_exp_f32 v204, v204\n"
"v_mfma_f32_16x16x32_bf16 v[208:211], a[192:195], a[176:179], v[252:255]\n"
"v_mfma_f32_16x16x32_bf16 v[208:211], a[196:199], a[180:183], v[208:211]\n"
"v_exp_f32 v205, v205\n"
"v_mfma_f32_16x16x32_bf16 v[208:211], a[200:203], a[184:187], v[208:211]\n"
"ds_read_b128 a[192:195], v104 offset:32768\n"
"v_cvt_pk_bf16_f32 v230, v204, v205\n"
"v_mfma_f32_16x16x32_bf16 v[208:211], a[204:207], a[188:191], v[208:211]\n"
"v_exp_f32 v206, v206\n"
"v_mfma_f32_16x16x32_bf16 v[212:215], a[220:223], a[188:191], v[252:255]\n"
"ds_read_b128 a[196:199], v105 offset:32768\n"
"v_mfma_f32_16x16x32_bf16 v[212:215], a[216:219], a[184:187], v[212:215]\n"
"v_exp_f32 v207, v207\n"
"v_mfma_f32_16x16x32_bf16 v[212:215], a[212:215], a[180:183], v[212:215]\n"
"ds_read_b128 a[200:203], v106 offset:32768\n"
"v_cvt_pk_bf16_f32 v231, v206, v207\n"
"v_mfma_f32_16x16x32_bf16 v[212:215], a[208:211], a[176:179], v[212:215]\n"
"ds_read_b128 a[204:207], v107 offset:32768\n"
"v_mfma_f32_16x16x32_bf16 v[216:219], a[224:227], a[176:179], v[252:255]\n"
"v_exp_f32 v208, v208\n"
"v_mfma_f32_16x16x32_bf16 v[216:219], a[228:231], a[180:183], v[216:219]\n"
"ds_read_b128 a[208:211], v104 offset:36864\n"
"v_mfma_f32_16x16x32_bf16 v[216:219], a[232:235], a[184:187], v[216:219]\n"
"v_exp_f32 v209, v209\n"
"v_mfma_f32_16x16x32_bf16 v[216:219], a[236:239], a[188:191], v[216:219]\n"
"ds_read_b128 a[212:215], v105 offset:36864\n"
"v_cvt_pk_bf16_f32 v232, v208, v209\n"
"v_mfma_f32_16x16x32_bf16 v[220:223], a[252:255], a[188:191], v[252:255]\n"
"v_exp_f32 v210, v210\n"
"v_mfma_f32_16x16x32_bf16 v[220:223], a[248:251], a[184:187], v[220:223]\n"
"ds_read_b128 a[216:219], v106 offset:36864\n"
"v_mfma_f32_16x16x32_bf16 v[220:223], a[244:247], a[180:183], v[220:223]\n"
"v_exp_f32 v211, v211\n"
"v_mfma_f32_16x16x32_bf16 v[220:223], a[240:243], a[176:179], v[220:223]\n"
"ds_read_b128 a[220:223], v107 offset:36864\n"
"v_cvt_pk_bf16_f32 v233, v210, v211\n"
"v_mfma_f32_16x16x32_bf16 a[8:11], v[128:131], v[224:227], a[8:11]\n"
"v_exp_f32 v212, v212\n"
"v_mfma_f32_16x16x32_bf16 a[8:11], v[132:135], v[228:231], a[8:11]\n"
"ds_read_b128 a[224:227], v104 offset:40960\n"
"v_mfma_f32_16x16x32_bf16 a[24:27], v[140:143], v[228:231], a[24:27]\n"
"v_exp_f32 v213, v213\n"
"v_mfma_f32_16x16x32_bf16 a[24:27], v[136:139], v[224:227], a[24:27]\n"
"ds_read_b128 a[228:231], v105 offset:40960\n"
"v_cvt_pk_bf16_f32 v234, v212, v213\n"
"v_mfma_f32_16x16x32_bf16 a[40:43], v[144:147], v[224:227], a[40:43]\n"
"v_exp_f32 v214, v214\n"
"v_mfma_f32_16x16x32_bf16 a[40:43], v[148:151], v[228:231], a[40:43]\n"
"ds_read_b128 a[232:235], v106 offset:40960\n"
"v_mfma_f32_16x16x32_bf16 a[56:59], v[156:159], v[228:231], a[56:59]\n"
"v_exp_f32 v215, v215\n"
"v_mfma_f32_16x16x32_bf16 a[56:59], v[152:155], v[224:227], a[56:59]\n"
"ds_read_b128 a[236:239], v107 offset:40960\n"
"v_cvt_pk_bf16_f32 v235, v214, v215\n"
"v_mfma_f32_16x16x32_bf16 a[72:75], v[160:163], v[224:227], a[72:75]\n"
"ds_read_b128 a[240:243], v104 offset:45056\n"
"v_mfma_f32_16x16x32_bf16 a[72:75], v[164:167], v[228:231], a[72:75]\n"
"v_exp_f32 v216, v216\n"
"v_mfma_f32_16x16x32_bf16 a[88:91], v[172:175], v[228:231], a[88:91]\n"
"ds_read_b128 a[244:247], v105 offset:45056\n"
"v_mfma_f32_16x16x32_bf16 a[88:91], v[168:171], v[224:227], a[88:91]\n"
"v_exp_f32 v217, v217\n"
"v_mfma_f32_16x16x32_bf16 a[104:107], v[176:179], v[224:227], a[104:107]\n"
"ds_read_b128 a[248:251], v106 offset:45056\n"
"v_cvt_pk_bf16_f32 v236, v216, v217\n"
"v_mfma_f32_16x16x32_bf16 a[104:107], v[180:183], v[228:231], a[104:107]\n"
"v_exp_f32 v218, v218\n"
"v_mfma_f32_16x16x32_bf16 a[120:123], v[188:191], v[228:231], a[120:123]\n"
"ds_read_b128 a[252:255], v107 offset:45056\n"
"v_mfma_f32_16x16x32_bf16 a[120:123], v[184:187], v[224:227], a[120:123]\n"
"v_exp_f32 v219, v219\n"
"v_mfma_f32_16x16x32_bf16 v[88:91], v[96:99], v[224:227], v[88:91]\n"
"v_cvt_pk_bf16_f32 v237, v218, v219\n"
"v_mfma_f32_16x16x32_bf16 v[88:91], v[96:99], v[228:231], v[88:91]\n"
"s_waitcnt lgkmcnt(4)\n"
"v_exp_f32 v220, v220\n"
"v_mfma_f32_16x16x32_bf16 v[192:195], a[192:195], a[128:131], v[240:243]\n"
"v_mfma_f32_16x16x32_bf16 v[192:195], a[196:199], a[132:135], v[192:195]\n"
"v_exp_f32 v221, v221\n"
"v_mfma_f32_16x16x32_bf16 v[192:195], a[200:203], a[136:139], v[192:195]\n"
"v_cvt_pk_bf16_f32 v238, v220, v221\n"
"v_mfma_f32_16x16x32_bf16 v[192:195], a[204:207], a[140:143], v[192:195]\n"
"v_exp_f32 v222, v222\n"
"v_mfma_f32_16x16x32_bf16 v[196:199], a[220:223], a[140:143], v[240:243]\n"
"v_mfma_f32_16x16x32_bf16 v[196:199], a[216:219], a[136:139], v[196:199]\n"
"v_exp_f32 v223, v223\n"
"v_mfma_f32_16x16x32_bf16 v[196:199], a[212:215], a[132:135], v[196:199]\n"
"v_cvt_pk_bf16_f32 v239, v222, v223\n"
"v_mfma_f32_16x16x32_bf16 v[196:199], a[208:211], a[128:131], v[196:199]\n"
"v_mfma_f32_16x16x32_bf16 v[200:203], a[224:227], a[128:131], v[240:243]\n"
"s_waitcnt vmcnt(16) lgkmcnt(0)\n"
"s_barrier\n"
"v_exp_f32 v192, v192\n"
"v_mfma_f32_16x16x32_bf16 v[200:203], a[228:231], a[132:135], v[200:203]\n"
"v_mfma_f32_16x16x32_bf16 v[200:203], a[232:235], a[136:139], v[200:203]\n"
"s_add_u32 m0, s78, 0x18000\n"
"v_exp_f32 v193, v193\n"
"v_mfma_f32_16x16x32_bf16 v[200:203], a[236:239], a[140:143], v[200:203]\n"
"buffer_load_dwordx4 v114, s[44:47], s95 offen sc1 lds\n"
"v_cvt_pk_bf16_f32 v224, v192, v193\n"
"v_mfma_f32_16x16x32_bf16 v[204:207], a[252:255], a[140:143], v[240:243]\n"
"v_exp_f32 v194, v194\n"
"v_mfma_f32_16x16x32_bf16 v[204:207], a[248:251], a[136:139], v[204:207]\n"
"v_mfma_f32_16x16x32_bf16 v[204:207], a[244:247], a[132:135], v[204:207]\n"
"s_add_u32 m0, s78, 0x18400\n"
"v_exp_f32 v195, v195\n"
"v_mfma_f32_16x16x32_bf16 v[204:207], a[240:243], a[128:131], v[204:207]\n"
"buffer_load_dwordx4 v115, s[44:47], s95 offen sc1 lds\n"
"v_cvt_pk_bf16_f32 v225, v194, v195\n"
"v_mfma_f32_16x16x32_bf16 a[12:15], v[128:131], v[232:235], a[12:15]\n"
"v_exp_f32 v196, v196\n"
"v_mfma_f32_16x16x32_bf16 a[12:15], v[132:135], v[236:239], a[12:15]\n"
"v_mfma_f32_16x16x32_bf16 a[28:31], v[140:143], v[236:239], a[28:31]\n"
"s_add_u32 m0, s78, 0x18800\n"
"v_exp_f32 v197, v197\n"
"v_mfma_f32_16x16x32_bf16 a[28:31], v[136:139], v[232:235], a[28:31]\n"
"buffer_load_dwordx4 v116, s[44:47], s95 offen sc1 lds\n"
"v_cvt_pk_bf16_f32 v226, v196, v197\n"
"v_mfma_f32_16x16x32_bf16 a[44:47], v[144:147], v[232:235], a[44:47]\n"
"v_exp_f32 v198, v198\n"
"v_mfma_f32_16x16x32_bf16 a[44:47], v[148:151], v[236:239], a[44:47]\n"
"v_mfma_f32_16x16x32_bf16 a[60:63], v[156:159], v[236:239], a[60:63]\n"
"s_add_u32 m0, s78, 0x18c00\n"
"v_exp_f32 v199, v199\n"
"v_mfma_f32_16x16x32_bf16 a[60:63], v[152:155], v[232:235], a[60:63]\n"
"buffer_load_dwordx4 v117, s[44:47], s95 offen sc1 lds\n"
"v_cvt_pk_bf16_f32 v227, v198, v199\n"
"v_mfma_f32_16x16x32_bf16 a[76:79], v[160:163], v[232:235], a[76:79]\n"
"v_mfma_f32_16x16x32_bf16 a[76:79], v[164:167], v[236:239], a[76:79]\n"
"s_add_u32 m0, s78, 0x8000\n"
"v_exp_f32 v200, v200\n"
"v_mfma_f32_16x16x32_bf16 a[92:95], v[172:175], v[236:239], a[92:95]\n"
"buffer_load_dwordx4 v110, s[40:43], s94 offen sc1 lds\n"
"v_mfma_f32_16x16x32_bf16 a[92:95], v[168:171], v[232:235], a[92:95]\n"
"v_exp_f32 v201, v201\n"
"v_mfma_f32_16x16x32_bf16 a[108:111], v[176:179], v[232:235], a[108:111]\n"
"v_cvt_pk_bf16_f32 v228, v200, v201\n"
"v_mfma_f32_16x16x32_bf16 a[108:111], v[180:183], v[236:239], a[108:111]\n"
"s_add_u32 m0, s78, 0x8400\n"
"v_exp_f32 v202, v202\n"
"v_mfma_f32_16x16x32_bf16 a[124:127], v[188:191], v[236:239], a[124:127]\n"
"buffer_load_dwordx4 v111, s[40:43], s94 offen sc1 lds\n"
"v_mfma_f32_16x16x32_bf16 a[124:127], v[184:187], v[232:235], a[124:127]\n"
"v_exp_f32 v203, v203\n"
"v_mfma_f32_16x16x32_bf16 v[92:95], v[96:99], v[232:235], v[92:95]\n"
"v_cvt_pk_bf16_f32 v229, v202, v203\n"
"v_mfma_f32_16x16x32_bf16 v[92:95], v[96:99], v[236:239], v[92:95]\n"
"s_add_u32 m0, s78, 0x8800\n"
"v_exp_f32 v204, v204\n"
"v_mfma_f32_16x16x32_bf16 v[208:211], a[192:195], a[144:147], v[244:247]\n"
"buffer_load_dwordx4 v112, s[40:43], s94 offen sc1 lds\n"
"v_mfma_f32_16x16x32_bf16 v[208:211], a[196:199], a[148:151], v[208:211]\n"
"v_exp_f32 v205, v205\n"
"v_mfma_f32_16x16x32_bf16 v[208:211], a[200:203], a[152:155], v[208:211]\n"
"v_cvt_pk_bf16_f32 v230, v204, v205\n"
"v_mfma_f32_16x16x32_bf16 v[208:211], a[204:207], a[156:159], v[208:211]\n"
"s_add_u32 m0, s78, 0x8c00\n"
"v_exp_f32 v206, v206\n"
"v_mfma_f32_16x16x32_bf16 v[212:215], a[220:223], a[156:159], v[244:247]\n"
"buffer_load_dwordx4 v113, s[40:43], s94 offen sc1 lds\n"
"v_mfma_f32_16x16x32_bf16 v[212:215], a[216:219], a[152:155], v[212:215]\n"
"v_exp_f32 v207, v207\n"
"v_mfma_f32_16x16x32_bf16 v[212:215], a[212:215], a[148:151], v[212:215]\n"
"v_cvt_pk_bf16_f32 v231, v206, v207\n"
"v_mfma_f32_16x16x32_bf16 v[212:215], a[208:211], a[144:147], v[212:215]\n"
"s_add_u32 s94, s94, 0x4000\n"
"ds_read_b64_tr_b16 v[128:129], v108 offset:49152\n"
"v_mfma_f32_16x16x32_bf16 v[216:219], a[224:227], a[144:147], v[244:247]\n"
"s_add_u32 s95, s95, 0x4000\n"
"v_exp_f32 v208, v208\n"
"v_mfma_f32_16x16x32_bf16 v[216:219], a[228:231], a[148:151], v[216:219]\n"
"ds_read_b64_tr_b16 v[130:131], v108 offset:53248\n"
"v_mfma_f32_16x16x32_bf16 v[216:219], a[232:235], a[152:155], v[216:219]\n"
"v_exp_f32 v209, v209\n"
"v_mfma_f32_16x16x32_bf16 v[216:219], a[236:239], a[156:159], v[216:219]\n"
"ds_read_b64_tr_b16 v[132:133], v108 offset:57344\n"
"v_cvt_pk_bf16_f32 v232, v208, v209\n"
"v_mfma_f32_16x16x32_bf16 v[220:223], a[252:255], a[156:159], v[244:247]\n"
"v_exp_f32 v210, v210\n"
"v_mfma_f32_16x16x32_bf16 v[220:223], a[248:251], a[152:155], v[220:223]\n"
"ds_read_b64_tr_b16 v[134:135], v108 offset:61440\n"
"v_mfma_f32_16x16x32_bf16 v[220:223], a[244:247], a[148:151], v[220:223]\n"
"v_exp_f32 v211, v211\n"
"v_mfma_f32_16x16x32_bf16 v[220:223], a[240:243], a[144:147], v[220:223]\n"
"ds_read_b64_tr_b16 v[136:137], v109 offset:49152\n"
"v_cvt_pk_bf16_f32 v233, v210, v211\n"
"v_mfma_f32_16x16x32_bf16 a[0:3], v[16:19], v[224:227], a[0:3]\n"
"v_exp_f32 v212, v212\n"
"v_mfma_f32_16x16x32_bf16 a[0:3], v[20:23], v[228:231], a[0:3]\n"
"ds_read_b64_tr_b16 v[138:139], v109 offset:53248\n"
"v_mfma_f32_16x16x32_bf16 a[16:19], v[28:31], v[228:231], a[16:19]\n"
"v_exp_f32 v213, v213\n"
"v_mfma_f32_16x16x32_bf16 a[16:19], v[24:27], v[224:227], a[16:19]\n"
"ds_read_b64_tr_b16 v[140:141], v109 offset:57344\n"
"v_cvt_pk_bf16_f32 v234, v212, v213\n"
"v_mfma_f32_16x16x32_bf16 a[32:35], v[32:35], v[224:227], a[32:35]\n"
"v_exp_f32 v214, v214\n"
"v_mfma_f32_16x16x32_bf16 a[32:35], v[36:39], v[228:231], a[32:35]\n"
"ds_read_b64_tr_b16 v[142:143], v109 offset:61440\n"
"v_mfma_f32_16x16x32_bf16 a[48:51], v[44:47], v[228:231], a[48:51]\n"
"v_exp_f32 v215, v215\n"
"v_mfma_f32_16x16x32_bf16 a[48:51], v[40:43], v[224:227], a[48:51]\n"
"ds_read_b64_tr_b16 v[144:145], v108 offset:49664\n"
"v_cvt_pk_bf16_f32 v235, v214, v215\n"
"v_mfma_f32_16x16x32_bf16 a[64:67], v[48:51], v[224:227], a[64:67]\n"
"ds_read_b64_tr_b16 v[146:147], v108 offset:53760\n"
"v_mfma_f32_16x16x32_bf16 a[64:67], v[52:55], v[228:231], a[64:67]\n"
"v_exp_f32 v216, v216\n"
"v_mfma_f32_16x16x32_bf16 a[80:83], v[60:63], v[228:231], a[80:83]\n"
"ds_read_b64_tr_b16 v[148:149], v108 offset:57856\n"
"v_mfma_f32_16x16x32_bf16 a[80:83], v[56:59], v[224:227], a[80:83]\n"
"v_exp_f32 v217, v217\n"
"v_mfma_f32_16x16x32_bf16 a[96:99], v[64:67], v[224:227], a[96:99]\n"
"ds_read_b64_tr_b16 v[150:151], v108 offset:61952\n"
"v_cvt_pk_bf16_f32 v236, v216, v217\n"
"v_mfma_f32_16x16x32_bf16 a[96:99], v[68:71], v[228:231], a[96:99]\n"
"v_exp_f32 v218, v218\n"
"v_mfma_f32_16x16x32_bf16 a[112:115], v[76:79], v[228:231], a[112:115]\n"
"ds_read_b64_tr_b16 v[152:153], v109 offset:49664\n"
"v_mfma_f32_16x16x32_bf16 a[112:115], v[72:75], v[224:227], a[112:115]\n"
"v_exp_f32 v219, v219\n"
"v_mfma_f32_16x16x32_bf16 v[80:83], v[96:99], v[224:227], v[80:83]\n"
"ds_read_b64_tr_b16 v[154:155], v109 offset:53760\n"
"v_cvt_pk_bf16_f32 v237, v218, v219\n"
"v_mfma_f32_16x16x32_bf16 v[80:83], v[96:99], v[228:231], v[80:83]\n"
"v_exp_f32 v220, v220\n"
"v_mfma_f32_16x16x32_bf16 v[192:195], a[192:195], a[160:163], v[248:251]\n"
"ds_read_b64_tr_b16 v[156:157], v109 offset:57856\n"
"v_mfma_f32_16x16x32_bf16 v[192:195], a[196:199], a[164:167], v[192:195]\n"
"v_exp_f32 v221, v221\n"
"v_mfma_f32_16x16x32_bf16 v[192:195], a[200:203], a[168:171], v[192:195]\n"
"ds_read_b64_tr_b16 v[158:159], v109 offset:61952\n"
"v_cvt_pk_bf16_f32 v238, v220, v221\n"
"v_mfma_f32_16x16x32_bf16 v[192:195], a[204:207], a[172:175], v[192:195]\n"
"v_exp_f32 v222, v222\n"
"v_mfma_f32_16x16x32_bf16 v[196:199], a[220:223], a[172:175], v[248:251]\n"
"ds_read_b64_tr_b16 v[160:161], v108 offset:50176\n"
"v_mfma_f32_16x16x32_bf16 v[196:199], a[216:219], a[168:171], v[196:199]\n"
"v_exp_f32 v223, v223\n"
"v_mfma_f32_16x16x32_bf16 v[196:199], a[212:215], a[164:167], v[196:199]\n"
"ds_read_b64_tr_b16 v[162:163], v108 offset:54272\n"
"v_cvt_pk_bf16_f32 v239, v222, v223\n"
"v_mfma_f32_16x16x32_bf16 v[196:199], a[208:211], a[160:163], v[196:199]\n"
"ds_read_b64_tr_b16 v[164:165], v108 offset:58368\n"
"v_mfma_f32_16x16x32_bf16 v[200:203], a[224:227], a[160:163], v[248:251]\n"
"v_exp_f32 v192, v192\n"
"v_mfma_f32_16x16x32_bf16 v[200:203], a[228:231], a[164:167], v[200:203]\n"
"ds_read_b64_tr_b16 v[166:167], v108 offset:62464\n"
"v_mfma_f32_16x16x32_bf16 v[200:203], a[232:235], a[168:171], v[200:203]\n"
"v_exp_f32 v193, v193\n"
"v_mfma_f32_16x16x32_bf16 v[200:203], a[236:239], a[172:175], v[200:203]\n"
"ds_read_b64_tr_b16 v[168:169], v109 offset:50176\n"
"v_cvt_pk_bf16_f32 v224, v192, v193\n"
"v_mfma_f32_16x16x32_bf16 v[204:207], a[252:255], a[172:175], v[248:251]\n"
"v_exp_f32 v194, v194\n"
"v_mfma_f32_16x16x32_bf16 v[204:207], a[248:251], a[168:171], v[204:207]\n"
"ds_read_b64_tr_b16 v[170:171], v109 offset:54272\n"
"v_mfma_f32_16x16x32_bf16 v[204:207], a[244:247], a[164:167], v[204:207]\n"
"v_exp_f32 v195, v195\n"
"v_mfma_f32_16x16x32_bf16 v[204:207], a[240:243], a[160:163], v[204:207]\n"
"ds_read_b64_tr_b16 v[172:173], v109 offset:58368\n"
"v_cvt_pk_bf16_f32 v225, v194, v195\n"
"v_mfma_f32_16x16x32_bf16 a[4:7], v[16:19], v[232:235], a[4:7]\n"
"v_exp_f32 v196, v196\n"
"v_mfma_f32_16x16x32_bf16 a[4:7], v[20:23], v[236:239], a[4:7]\n"
"ds_read_b64_tr_b16 v[174:175], v109 offset:62464\n"
"v_mfma_f32_16x16x32_bf16 a[20:23], v[28:31], v[236:239], a[20:23]\n"
"v_exp_f32 v197, v197\n"
"v_mfma_f32_16x16x32_bf16 a[20:23], v[24:27], v[232:235], a[20:23]\n"
"ds_read_b64_tr_b16 v[176:177], v108 offset:50688\n"
"v_cvt_pk_bf16_f32 v226, v196, v197\n"
"v_mfma_f32_16x16x32_bf16 a[36:39], v[32:35], v[232:235], a[36:39]\n"
"v_exp_f32 v198, v198\n"
"v_mfma_f32_16x16x32_bf16 a[36:39], v[36:39], v[236:239], a[36:39]\n"
"ds_read_b64_tr_b16 v[178:179], v108 offset:54784\n"
"v_mfma_f32_16x16x32_bf16 a[52:55], v[44:47], v[236:239], a[52:55]\n"
"v_exp_f32 v199, v199\n"
"v_mfma_f32_16x16x32_bf16 a[52:55], v[40:43], v[232:235], a[52:55]\n"
"ds_read_b64_tr_b16 v[180:181], v108 offset:58880\n"
"v_cvt_pk_bf16_f32 v227, v198, v199\n"
"v_mfma_f32_16x16x32_bf16 a[68:71], v[48:51], v[232:235], a[68:71]\n"
"ds_read_b64_tr_b16 v[182:183], v108 offset:62976\n"
"v_mfma_f32_16x16x32_bf16 a[68:71], v[52:55], v[236:239], a[68:71]\n"
"v_exp_f32 v200, v200\n"
"v_mfma_f32_16x16x32_bf16 a[84:87], v[60:63], v[236:239], a[84:87]\n"
"ds_read_b64_tr_b16 v[184:185], v109 offset:50688\n"
"v_mfma_f32_16x16x32_bf16 a[84:87], v[56:59], v[232:235], a[84:87]\n"
"v_exp_f32 v201, v201\n"
"v_mfma_f32_16x16x32_bf16 a[100:103], v[64:67], v[232:235], a[100:103]\n"
"ds_read_b64_tr_b16 v[186:187], v109 offset:54784\n"
"v_cvt_pk_bf16_f32 v228, v200, v201\n"
"v_mfma_f32_16x16x32_bf16 a[100:103], v[68:71], v[236:239], a[100:103]\n"
"v_exp_f32 v202, v202\n"
"v_mfma_f32_16x16x32_bf16 a[116:119], v[76:79], v[236:239], a[116:119]\n"
"ds_read_b64_tr_b16 v[188:189], v109 offset:58880\n"
"v_mfma_f32_16x16x32_bf16 a[116:119], v[72:75], v[232:235], a[116:119]\n"
"v_exp_f32 v203, v203\n"
"v_mfma_f32_16x16x32_bf16 v[84:87], v[96:99], v[232:235], v[84:87]\n"
"ds_read_b64_tr_b16 v[190:191], v109 offset:62976\n"
"v_cvt_pk_bf16_f32 v229, v202, v203\n"
"v_mfma_f32_16x16x32_bf16 v[84:87], v[96:99], v[236:239], v[84:87]\n"
"v_exp_f32 v204, v204\n"
"v_mfma_f32_16x16x32_bf16 v[208:211], a[192:195], a[176:179], v[252:255]\n"
"v_mfma_f32_16x16x32_bf16 v[208:211], a[196:199], a[180:183], v[208:211]\n"
"v_exp_f32 v205, v205\n"
"v_mfma_f32_16x16x32_bf16 v[208:211], a[200:203], a[184:187], v[208:211]\n"
"ds_read_b128 a[192:195], v104 offset:49152\n"
"v_cvt_pk_bf16_f32 v230, v204, v205\n"
"v_mfma_f32_16x16x32_bf16 v[208:211], a[204:207], a[188:191], v[208:211]\n"
"v_exp_f32 v206, v206\n"
"v_mfma_f32_16x16x32_bf16 v[212:215], a[220:223], a[188:191], v[252:255]\n"
"ds_read_b128 a[196:199], v105 offset:49152\n"
"v_mfma_f32_16x16x32_bf16 v[212:215], a[216:219], a[184:187], v[212:215]\n"
"v_exp_f32 v207, v207\n"
"v_mfma_f32_16x16x32_bf16 v[212:215], a[212:215], a[180:183], v[212:215]\n"
"ds_read_b128 a[200:203], v106 offset:49152\n"
"v_cvt_pk_bf16_f32 v231, v206, v207\n"
"v_mfma_f32_16x16x32_bf16 v[212:215], a[208:211], a[176:179], v[212:215]\n"
"ds_read_b128 a[204:207], v107 offset:49152\n"
"v_mfma_f32_16x16x32_bf16 v[216:219], a[224:227], a[176:179], v[252:255]\n"
"v_exp_f32 v208, v208\n"
"v_mfma_f32_16x16x32_bf16 v[216:219], a[228:231], a[180:183], v[216:219]\n"
"ds_read_b128 a[208:211], v104 offset:53248\n"
"v_mfma_f32_16x16x32_bf16 v[216:219], a[232:235], a[184:187], v[216:219]\n"
"v_exp_f32 v209, v209\n"
"v_mfma_f32_16x16x32_bf16 v[216:219], a[236:239], a[188:191], v[216:219]\n"
"ds_read_b128 a[212:215], v105 offset:53248\n"
"v_cvt_pk_bf16_f32 v232, v208, v209\n"
"v_mfma_f32_16x16x32_bf16 v[220:223], a[252:255], a[188:191], v[252:255]\n"
"v_exp_f32 v210, v210\n"
"v_mfma_f32_16x16x32_bf16 v[220:223], a[248:251], a[184:187], v[220:223]\n"
"ds_read_b128 a[216:219], v106 offset:53248\n"
"v_mfma_f32_16x16x32_bf16 v[220:223], a[244:247], a[180:183], v[220:223]\n"
"v_exp_f32 v211, v211\n"
"v_mfma_f32_16x16x32_bf16 v[220:223], a[240:243], a[176:179], v[220:223]\n"
"ds_read_b128 a[220:223], v107 offset:53248\n"
"v_cvt_pk_bf16_f32 v233, v210, v211\n"
"v_mfma_f32_16x16x32_bf16 a[8:11], v[16:19], v[224:227], a[8:11]\n"
"v_exp_f32 v212, v212\n"
"v_mfma_f32_16x16x32_bf16 a[8:11], v[20:23], v[228:231], a[8:11]\n"
"ds_read_b128 a[224:227], v104 offset:57344\n"
"v_mfma_f32_16x16x32_bf16 a[24:27], v[28:31], v[228:231], a[24:27]\n"
"v_exp_f32 v213, v213\n"
"v_mfma_f32_16x16x32_bf16 a[24:27], v[24:27], v[224:227], a[24:27]\n"
"ds_read_b128 a[228:231], v105 offset:57344\n"
"v_cvt_pk_bf16_f32 v234, v212, v213\n"
"v_mfma_f32_16x16x32_bf16 a[40:43], v[32:35], v[224:227], a[40:43]\n"
"v_exp_f32 v214, v214\n"
"v_mfma_f32_16x16x32_bf16 a[40:43], v[36:39], v[228:231], a[40:43]\n"
"ds_read_b128 a[232:235], v106 offset:57344\n"
"v_mfma_f32_16x16x32_bf16 a[56:59], v[44:47], v[228:231], a[56:59]\n"
"v_exp_f32 v215, v215\n"
"v_mfma_f32_16x16x32_bf16 a[56:59], v[40:43], v[224:227], a[56:59]\n"
"ds_read_b128 a[236:239], v107 offset:57344\n"
"v_cvt_pk_bf16_f32 v235, v214, v215\n"
"v_mfma_f32_16x16x32_bf16 a[72:75], v[48:51], v[224:227], a[72:75]\n"
"ds_read_b128 a[240:243], v104 offset:61440\n"
"v_mfma_f32_16x16x32_bf16 a[72:75], v[52:55], v[228:231], a[72:75]\n"
"v_exp_f32 v216, v216\n"
"v_mfma_f32_16x16x32_bf16 a[88:91], v[60:63], v[228:231], a[88:91]\n"
"ds_read_b128 a[244:247], v105 offset:61440\n"
"v_mfma_f32_16x16x32_bf16 a[88:91], v[56:59], v[224:227], a[88:91]\n"
"v_exp_f32 v217, v217\n"
"v_mfma_f32_16x16x32_bf16 a[104:107], v[64:67], v[224:227], a[104:107]\n"
"ds_read_b128 a[248:251], v106 offset:61440\n"
"v_cvt_pk_bf16_f32 v236, v216, v217\n"
"v_mfma_f32_16x16x32_bf16 a[104:107], v[68:71], v[228:231], a[104:107]\n"
"v_exp_f32 v218, v218\n"
"v_mfma_f32_16x16x32_bf16 a[120:123], v[76:79], v[228:231], a[120:123]\n"
"ds_read_b128 a[252:255], v107 offset:61440\n"
"v_mfma_f32_16x16x32_bf16 a[120:123], v[72:75], v[224:227], a[120:123]\n"
"v_exp_f32 v219, v219\n"
"v_mfma_f32_16x16x32_bf16 v[88:91], v[96:99], v[224:227], v[88:91]\n"
"v_cvt_pk_bf16_f32 v237, v218, v219\n"
"v_mfma_f32_16x16x32_bf16 v[88:91], v[96:99], v[228:231], v[88:91]\n"
"s_waitcnt lgkmcnt(4)\n"
"v_exp_f32 v220, v220\n"
"v_mfma_f32_16x16x32_bf16 v[192:195], a[192:195], a[128:131], v[240:243]\n"
"v_mfma_f32_16x16x32_bf16 v[192:195], a[196:199], a[132:135], v[192:195]\n"
"v_exp_f32 v221, v221\n"
"v_mfma_f32_16x16x32_bf16 v[192:195], a[200:203], a[136:139], v[192:195]\n"
"v_cvt_pk_bf16_f32 v238, v220, v221\n"
"v_mfma_f32_16x16x32_bf16 v[192:195], a[204:207], a[140:143], v[192:195]\n"
"v_exp_f32 v222, v222\n"
"v_mfma_f32_16x16x32_bf16 v[196:199], a[220:223], a[140:143], v[240:243]\n"
"v_mfma_f32_16x16x32_bf16 v[196:199], a[216:219], a[136:139], v[196:199]\n"
"v_exp_f32 v223, v223\n"
"v_mfma_f32_16x16x32_bf16 v[196:199], a[212:215], a[132:135], v[196:199]\n"
"v_cvt_pk_bf16_f32 v239, v222, v223\n"
"v_mfma_f32_16x16x32_bf16 v[196:199], a[208:211], a[128:131], v[196:199]\n"
"v_mfma_f32_16x16x32_bf16 v[200:203], a[224:227], a[128:131], v[240:243]\n"
"s_waitcnt vmcnt(16) lgkmcnt(0)\n"
"s_barrier\n"
"v_exp_f32 v192, v192\n"
"v_mfma_f32_16x16x32_bf16 v[200:203], a[228:231], a[132:135], v[200:203]\n"
"v_mfma_f32_16x16x32_bf16 v[200:203], a[232:235], a[136:139], v[200:203]\n"
"s_add_u32 m0, s78, 0x1c000\n"
"v_exp_f32 v193, v193\n"
"v_mfma_f32_16x16x32_bf16 v[200:203], a[236:239], a[140:143], v[200:203]\n"
"buffer_load_dwordx4 v114, s[44:47], s95 offen sc1 lds\n"
"v_cvt_pk_bf16_f32 v224, v192, v193\n"
"v_mfma_f32_16x16x32_bf16 v[204:207], a[252:255], a[140:143], v[240:243]\n"
"v_exp_f32 v194, v194\n"
"v_mfma_f32_16x16x32_bf16 v[204:207], a[248:251], a[136:139], v[204:207]\n"
"v_mfma_f32_16x16x32_bf16 v[204:207], a[244:247], a[132:135], v[204:207]\n"
"s_add_u32 m0, s78, 0x1c400\n"
"v_exp_f32 v195, v195\n"
"v_mfma_f32_16x16x32_bf16 v[204:207], a[240:243], a[128:131], v[204:207]\n"
"buffer_load_dwordx4 v115, s[44:47], s95 offen sc1 lds\n"
"v_cvt_pk_bf16_f32 v225, v194, v195\n"
"v_mfma_f32_16x16x32_bf16 a[12:15], v[16:19], v[232:235], a[12:15]\n"
"v_exp_f32 v196, v196\n"
"v_mfma_f32_16x16x32_bf16 a[12:15], v[20:23], v[236:239], a[12:15]\n"
"v_mfma_f32_16x16x32_bf16 a[28:31], v[28:31], v[236:239], a[28:31]\n"
"s_add_u32 m0, s78, 0x1c800\n"
"v_exp_f32 v197, v197\n"
"v_mfma_f32_16x16x32_bf16 a[28:31], v[24:27], v[232:235], a[28:31]\n"
"buffer_load_dwordx4 v116, s[44:47], s95 offen sc1 lds\n"
"v_cvt_pk_bf16_f32 v226, v196, v197\n"
"v_mfma_f32_16x16x32_bf16 a[44:47], v[32:35], v[232:235], a[44:47]\n"
"v_exp_f32 v198, v198\n"
"v_mfma_f32_16x16x32_bf16 a[44:47], v[36:39], v[236:239], a[44:47]\n"
"v_mfma_f32_16x16x32_bf16 a[60:63], v[44:47], v[236:239], a[60:63]\n"
"s_add_u32 m0, s78, 0x1cc00\n"
"v_exp_f32 v199, v199\n"
"v_mfma_f32_16x16x32_bf16 a[60:63], v[40:43], v[232:235], a[60:63]\n"
"buffer_load_dwordx4 v117, s[44:47], s95 offen sc1 lds\n"
"v_cvt_pk_bf16_f32 v227, v198, v199\n"
"v_mfma_f32_16x16x32_bf16 a[76:79], v[48:51], v[232:235], a[76:79]\n"
"v_mfma_f32_16x16x32_bf16 a[76:79], v[52:55], v[236:239], a[76:79]\n"
"s_add_u32 m0, s78, 0xc000\n"
"v_exp_f32 v200, v200\n"
"v_mfma_f32_16x16x32_bf16 a[92:95], v[60:63], v[236:239], a[92:95]\n"
"buffer_load_dwordx4 v110, s[40:43], s94 offen sc1 lds\n"
"v_mfma_f32_16x16x32_bf16 a[92:95], v[56:59], v[232:235], a[92:95]\n"
"v_exp_f32 v201, v201\n"
"v_mfma_f32_16x16x32_bf16 a[108:111], v[64:67], v[232:235], a[108:111]\n"
"v_cvt_pk_bf16_f32 v228, v200, v201\n"
"v_mfma_f32_16x16x32_bf16 a[108:111], v[68:71], v[236:239], a[108:111]\n"
"s_add_u32 m0, s78, 0xc400\n"
"v_exp_f32 v202, v202\n"
"v_mfma_f32_16x16x32_bf16 a[124:127], v[76:79], v[236:239], a[124:127]\n"
"buffer_load_dwordx4 v111, s[40:43], s94 offen sc1 lds\n"
"v_mfma_f32_16x16x32_bf16 a[124:127], v[72:75], v[232:235], a[124:127]\n"
"v_exp_f32 v203, v203\n"
"v_mfma_f32_16x16x32_bf16 v[92:95], v[96:99], v[232:235], v[92:95]\n"
"v_cvt_pk_bf16_f32 v229, v202, v203\n"
"v_mfma_f32_16x16x32_bf16 v[92:95], v[96:99], v[236:239], v[92:95]\n"
"s_add_u32 m0, s78, 0xc800\n"
"v_exp_f32 v204, v204\n"
"v_mfma_f32_16x16x32_bf16 v[208:211], a[192:195], a[144:147], v[244:247]\n"
"buffer_load_dwordx4 v112, s[40:43], s94 offen sc1 lds\n"
"v_mfma_f32_16x16x32_bf16 v[208:211], a[196:199], a[148:151], v[208:211]\n"
"v_exp_f32 v205, v205\n"
"v_mfma_f32_16x16x32_bf16 v[208:211], a[200:203], a[152:155], v[208:211]\n"
"v_cvt_pk_bf16_f32 v230, v204, v205\n"
"v_mfma_f32_16x16x32_bf16 v[208:211], a[204:207], a[156:159], v[208:211]\n"
"s_add_u32 m0, s78, 0xcc00\n"
"v_exp_f32 v206, v206\n"
"v_mfma_f32_16x16x32_bf16 v[212:215], a[220:223], a[156:159], v[244:247]\n"
"buffer_load_dwordx4 v113, s[40:43], s94 offen sc1 lds\n"
"v_mfma_f32_16x16x32_bf16 v[212:215], a[216:219], a[152:155], v[212:215]\n"
"v_exp_f32 v207, v207\n"
"v_mfma_f32_16x16x32_bf16 v[212:215], a[212:215], a[148:151], v[212:215]\n"
"v_cvt_pk_bf16_f32 v231, v206, v207\n"
"v_mfma_f32_16x16x32_bf16 v[212:215], a[208:211], a[144:147], v[212:215]\n"
"s_add_u32 s94, s94, 0x4000\n"
"ds_read_b64_tr_b16 v[16:17], v108\n"
"v_mfma_f32_16x16x32_bf16 v[216:219], a[224:227], a[144:147], v[244:247]\n"
"s_add_u32 s95, s95, 0x4000\n"
"v_exp_f32 v208, v208\n"
"v_mfma_f32_16x16x32_bf16 v[216:219], a[228:231], a[148:151], v[216:219]\n"
"ds_read_b64_tr_b16 v[18:19], v108 offset:4096\n"
"v_mfma_f32_16x16x32_bf16 v[216:219], a[232:235], a[152:155], v[216:219]\n"
"v_exp_f32 v209, v209\n"
"v_mfma_f32_16x16x32_bf16 v[216:219], a[236:239], a[156:159], v[216:219]\n"
"ds_read_b64_tr_b16 v[20:21], v108 offset:8192\n"
"v_cvt_pk_bf16_f32 v232, v208, v209\n"
"v_mfma_f32_16x16x32_bf16 v[220:223], a[252:255], a[156:159], v[244:247]\n"
"v_exp_f32 v210, v210\n"
"v_mfma_f32_16x16x32_bf16 v[220:223], a[248:251], a[152:155], v[220:223]\n"
"ds_read_b64_tr_b16 v[22:23], v108 offset:12288\n"
"v_mfma_f32_16x16x32_bf16 v[220:223], a[244:247], a[148:151], v[220:223]\n"
"v_exp_f32 v211, v211\n"
"v_mfma_f32_16x16x32_bf16 v[220:223], a[240:243], a[144:147], v[220:223]\n"
"ds_read_b64_tr_b16 v[24:25], v109\n"
"v_cvt_pk_bf16_f32 v233, v210, v211\n"
"v_mfma_f32_16x16x32_bf16 a[0:3], v[128:131], v[224:227], a[0:3]\n"
"v_exp_f32 v212, v212\n"
"v_mfma_f32_16x16x32_bf16 a[0:3], v[132:135], v[228:231], a[0:3]\n"
"ds_read_b64_tr_b16 v[26:27], v109 offset:4096\n"
"v_mfma_f32_16x16x32_bf16 a[16:19], v[140:143], v[228:231], a[16:19]\n"
"v_exp_f32 v213, v213\n"
"v_mfma_f32_16x16x32_bf16 a[16:19], v[136:139], v[224:227], a[16:19]\n"
"ds_read_b64_tr_b16 v[28:29], v109 offset:8192\n"
"v_cvt_pk_bf16_f32 v234, v212, v213\n"
"v_mfma_f32_16x16x32_bf16 a[32:35], v[144:147], v[224:227], a[32:35]\n"
"v_exp_f32 v214, v214\n"
"v_mfma_f32_16x16x32_bf16 a[32:35], v[148:151], v[228:231], a[32:35]\n"
"ds_read_b64_tr_b16 v[30:31], v109 offset:12288\n"
"v_mfma_f32_16x16x32_bf16 a[48:51], v[156:159], v[228:231], a[48:51]\n"
"v_exp_f32 v215, v215\n"
"v_mfma_f32_16x16x32_bf16 a[48:51], v[152:155], v[224:227], a[48:51]\n"
"ds_read_b64_tr_b16 v[32:33], v108 offset:512\n"
"v_cvt_pk_bf16_f32 v235, v214, v215\n"
"v_mfma_f32_16x16x32_bf16 a[64:67], v[160:163], v[224:227], a[64:67]\n"
"ds_read_b64_tr_b16 v[34:35], v108 offset:4608\n"
"v_mfma_f32_16x16x32_bf16 a[64:67], v[164:167], v[228:231], a[64:67]\n"
"v_exp_f32 v216, v216\n"
"v_mfma_f32_16x16x32_bf16 a[80:83], v[172:175], v[228:231], a[80:83]\n"
"ds_read_b64_tr_b16 v[36:37], v108 offset:8704\n"
"v_mfma_f32_16x16x32_bf16 a[80:83], v[168:171], v[224:227], a[80:83]\n"
"v_exp_f32 v217, v217\n"
"v_mfma_f32_16x16x32_bf16 a[96:99], v[176:179], v[224:227], a[96:99]\n"
"ds_read_b64_tr_b16 v[38:39], v108 offset:12800\n"
"v_cvt_pk_bf16_f32 v236, v216, v217\n"
"v_mfma_f32_16x16x32_bf16 a[96:99], v[180:183], v[228:231], a[96:99]\n"
"v_exp_f32 v218, v218\n"
"v_mfma_f32_16x16x32_bf16 a[112:115], v[188:191], v[228:231], a[112:115]\n"
"ds_read_b64_tr_b16 v[40:41], v109 offset:512\n"
"v_mfma_f32_16x16x32_bf16 a[112:115], v[184:187], v[224:227], a[112:115]\n"
"v_exp_f32 v219, v219\n"
"v_mfma_f32_16x16x32_bf16 v[80:83], v[96:99], v[224:227], v[80:83]\n"
"ds_read_b64_tr_b16 v[42:43], v109 offset:4608\n"
"v_cvt_pk_bf16_f32 v237, v218, v219\n"
"v_mfma_f32_16x16x32_bf16 v[80:83], v[96:99], v[228:231], v[80:83]\n"
"v_exp_f32 v220, v220\n"
"v_mfma_f32_16x16x32_bf16 v[192:195], a[192:195], a[160:163], v[248:251]\n"
"ds_read_b64_tr_b16 v[44:45], v109 offset:8704\n"
"v_mfma_f32_16x16x32_bf16 v[192:195], a[196:199], a[164:167], v[192:195]\n"
"v_exp_f32 v221, v221\n"
"v_mfma_f32_16x16x32_bf16 v[192:195], a[200:203], a[168:171], v[192:195]\n"
"ds_read_b64_tr_b16 v[46:47], v109 offset:12800\n"
"v_cvt_pk_bf16_f32 v238, v220, v221\n"
"v_mfma_f32_16x16x32_bf16 v[192:195], a[204:207], a[172:175], v[192:195]\n"
"v_exp_f32 v222, v222\n"
"v_mfma_f32_16x16x32_bf16 v[196:199], a[220:223], a[172:175], v[248:251]\n"
"ds_read_b64_tr_b16 v[48:49], v108 offset:1024\n"
"v_mfma_f32_16x16x32_bf16 v[196:199], a[216:219], a[168:171], v[196:199]\n"
"v_exp_f32 v223, v223\n"
"v_mfma_f32_16x16x32_bf16 v[196:199], a[212:215], a[164:167], v[196:199]\n"
"ds_read_b64_tr_b16 v[50:51], v108 offset:5120\n"
"v_cvt_pk_bf16_f32 v239, v222, v223\n"
"v_mfma_f32_16x16x32_bf16 v[196:199], a[208:211], a[160:163], v[196:199]\n"
"ds_read_b64_tr_b16 v[52:53], v108 offset:9216\n"
"v_mfma_f32_16x16x32_bf16 v[200:203], a[224:227], a[160:163], v[248:251]\n"
"v_exp_f32 v192, v192\n"
"v_mfma_f32_16x16x32_bf16 v[200:203], a[228:231], a[164:167], v[200:203]\n"
"ds_read_b64_tr_b16 v[54:55], v108 offset:13312\n"
"v_mfma_f32_16x16x32_bf16 v[200:203], a[232:235], a[168:171], v[200:203]\n"
"v_exp_f32 v193, v193\n"
"v_mfma_f32_16x16x32_bf16 v[200:203], a[236:239], a[172:175], v[200:203]\n"
"ds_read_b64_tr_b16 v[56:57], v109 offset:1024\n"
"v_cvt_pk_bf16_f32 v224, v192, v193\n"
"v_mfma_f32_16x16x32_bf16 v[204:207], a[252:255], a[172:175], v[248:251]\n"
"v_exp_f32 v194, v194\n"
"v_mfma_f32_16x16x32_bf16 v[204:207], a[248:251], a[168:171], v[204:207]\n"
"ds_read_b64_tr_b16 v[58:59], v109 offset:5120\n"
"v_mfma_f32_16x16x32_bf16 v[204:207], a[244:247], a[164:167], v[204:207]\n"
"v_exp_f32 v195, v195\n"
"v_mfma_f32_16x16x32_bf16 v[204:207], a[240:243], a[160:163], v[204:207]\n"
"ds_read_b64_tr_b16 v[60:61], v109 offset:9216\n"
"v_cvt_pk_bf16_f32 v225, v194, v195\n"
"v_mfma_f32_16x16x32_bf16 a[4:7], v[128:131], v[232:235], a[4:7]\n"
"v_exp_f32 v196, v196\n"
"v_mfma_f32_16x16x32_bf16 a[4:7], v[132:135], v[236:239], a[4:7]\n"
"ds_read_b64_tr_b16 v[62:63], v109 offset:13312\n"
"v_mfma_f32_16x16x32_bf16 a[20:23], v[140:143], v[236:239], a[20:23]\n"
"v_exp_f32 v197, v197\n"
"v_mfma_f32_16x16x32_bf16 a[20:23], v[136:139], v[232:235], a[20:23]\n"
"ds_read_b64_tr_b16 v[64:65], v108 offset:1536\n"
"v_cvt_pk_bf16_f32 v226, v196, v197\n"
"v_mfma_f32_16x16x32_bf16 a[36:39], v[144:147], v[232:235], a[36:39]\n"
"v_exp_f32 v198, v198\n"
"v_mfma_f32_16x16x32_bf16 a[36:39], v[148:151], v[236:239], a[36:39]\n"
"ds_read_b64_tr_b16 v[66:67], v108 offset:5632\n"
"v_mfma_f32_16x16x32_bf16 a[52:55], v[156:159], v[236:239], a[52:55]\n"
"v_exp_f32 v199, v199\n"
"v_mfma_f32_16x16x32_bf16 a[52:55], v[152:155], v[232:235], a[52:55]\n"
"ds_read_b64_tr_b16 v[68:69], v108 offset:9728\n"
"v_cvt_pk_bf16_f32 v227, v198, v199\n"
"v_mfma_f32_16x16x32_bf16 a[68:71], v[160:163], v[232:235], a[68:71]\n"
"ds_read_b64_tr_b16 v[70:71], v108 offset:13824\n"
"v_mfma_f32_16x16x32_bf16 a[68:71], v[164:167], v[236:239], a[68:71]\n"
"v_exp_f32 v200, v200\n"
"v_mfma_f32_16x16x32_bf16 a[84:87], v[172:175], v[236:239], a[84:87]\n"
"ds_read_b64_tr_b16 v[72:73], v109 offset:1536\n"
"v_mfma_f32_16x16x32_bf16 a[84:87], v[168:171], v[232:235], a[84:87]\n"
"v_exp_f32 v201, v201\n"
"v_mfma_f32_16x16x32_bf16 a[100:103], v[176:179], v[232:235], a[100:103]\n"
"ds_read_b64_tr_b16 v[74:75], v109 offset:5632\n"
"v_cvt_pk_bf16_f32 v228, v200, v201\n"
"v_mfma_f32_16x16x32_bf16 a[100:103], v[180:183], v[236:239], a[100:103]\n"
"v_exp_f32 v202, v202\n"
"v_mfma_f32_16x16x32_bf16 a[116:119], v[188:191], v[236:239], a[116:119]\n"
"ds_read_b64_tr_b16 v[76:77], v109 offset:9728\n"
"v_mfma_f32_16x16x32_bf16 a[116:119], v[184:187], v[232:235], a[116:119]\n"
"v_exp_f32 v203, v203\n"
"v_mfma_f32_16x16x32_bf16 v[84:87], v[96:99], v[232:235], v[84:87]\n"
"ds_read_b64_tr_b16 v[78:79], v109 offset:13824\n"
"v_cvt_pk_bf16_f32 v229, v202, v203\n"
"v_mfma_f32_16x16x32_bf16 v[84:87], v[96:99], v[236:239], v[84:87]\n"
"v_exp_f32 v204, v204\n"
"v_mfma_f32_16x16x32_bf16 v[208:211], a[192:195], a[176:179], v[252:255]\n"
"v_mfma_f32_16x16x32_bf16 v[208:211], a[196:199], a[180:183], v[208:211]\n"
"v_exp_f32 v205, v205\n"
"v_mfma_f32_16x16x32_bf16 v[208:211], a[200:203], a[184:187], v[208:211]\n"
"ds_read_b128 a[192:195], v104\n"
"v_cvt_pk_bf16_f32 v230, v204, v205\n"
"v_mfma_f32_16x16x32_bf16 v[208:211], a[204:207], a[188:191], v[208:211]\n"
"v_exp_f32 v206, v206\n"
"v_mfma_f32_16x16x32_bf16 v[212:215], a[220:223], a[188:191], v[252:255]\n"
"ds_read_b128 a[196:199], v105\n"
"v_mfma_f32_16x16x32_bf16 v[212:215], a[216:219], a[184:187], v[212:215]\n"
"v_exp_f32 v207, v207\n"
"v_mfma_f32_16x16x32_bf16 v[212:215], a[212:215], a[180:183], v[212:215]\n"
"ds_read_b128 a[200:203], v106\n"
"v_cvt_pk_bf16_f32 v231, v206, v207\n"
"v_mfma_f32_16x16x32_bf16 v[212:215], a[208:211], a[176:179], v[212:215]\n"
"ds_read_b128 a[204:207], v107\n"
"v_mfma_f32_16x16x32_bf16 v[216:219], a[224:227], a[176:179], v[252:255]\n"
"v_exp_f32 v208, v208\n"
"v_mfma_f32_16x16x32_bf16 v[216:219], a[228:231], a[180:183], v[216:219]\n"
"ds_read_b128 a[208:211], v104 offset:4096\n"
"v_mfma_f32_16x16x32_bf16 v[216:219], a[232:235], a[184:187], v[216:219]\n"
"v_exp_f32 v209, v209\n"
"v_mfma_f32_16x16x32_bf16 v[216:219], a[236:239], a[188:191], v[216:219]\n"
"ds_read_b128 a[212:215], v105 offset:4096\n"
"v_cvt_pk_bf16_f32 v232, v208, v209\n"
"v_mfma_f32_16x16x32_bf16 v[220:223], a[252:255], a[188:191], v[252:255]\n"
"v_exp_f32 v210, v210\n"
"v_mfma_f32_16x16x32_bf16 v[220:223], a[248:251], a[184:187], v[220:223]\n"
"ds_read_b128 a[216:219], v106 offset:4096\n"
"v_mfma_f32_16x16x32_bf16 v[220:223], a[244:247], a[180:183], v[220:223]\n"
"v_exp_f32 v211, v211\n"
"v_mfma_f32_16x16x32_bf16 v[220:223], a[240:243], a[176:179], v[220:223]\n"
"ds_read_b128 a[220:223], v107 offset:4096\n"
"v_cvt_pk_bf16_f32 v233, v210, v211\n"
"v_mfma_f32_16x16x32_bf16 a[8:11], v[128:131], v[224:227], a[8:11]\n"
"v_exp_f32 v212, v212\n"
"v_mfma_f32_16x16x32_bf16 a[8:11], v[132:135], v[228:231], a[8:11]\n"
"ds_read_b128 a[224:227], v104 offset:8192\n"
"v_mfma_f32_16x16x32_bf16 a[24:27], v[140:143], v[228:231], a[24:27]\n"
"v_exp_f32 v213, v213\n"
"v_mfma_f32_16x16x32_bf16 a[24:27], v[136:139], v[224:227], a[24:27]\n"
"ds_read_b128 a[228:231], v105 offset:8192\n"
"v_cvt_pk_bf16_f32 v234, v212, v213\n"
"v_mfma_f32_16x16x32_bf16 a[40:43], v[144:147], v[224:227], a[40:43]\n"
"v_exp_f32 v214, v214\n"
"v_mfma_f32_16x16x32_bf16 a[40:43], v[148:151], v[228:231], a[40:43]\n"
"ds_read_b128 a[232:235], v106 offset:8192\n"
"v_mfma_f32_16x16x32_bf16 a[56:59], v[156:159], v[228:231], a[56:59]\n"
"v_exp_f32 v215, v215\n"
"v_mfma_f32_16x16x32_bf16 a[56:59], v[152:155], v[224:227], a[56:59]\n"
"ds_read_b128 a[236:239], v107 offset:8192\n"
"v_cvt_pk_bf16_f32 v235, v214, v215\n"
"v_mfma_f32_16x16x32_bf16 a[72:75], v[160:163], v[224:227], a[72:75]\n"
"ds_read_b128 a[240:243], v104 offset:12288\n"
"v_mfma_f32_16x16x32_bf16 a[72:75], v[164:167], v[228:231], a[72:75]\n"
"v_exp_f32 v216, v216\n"
"v_mfma_f32_16x16x32_bf16 a[88:91], v[172:175], v[228:231], a[88:91]\n"
"ds_read_b128 a[244:247], v105 offset:12288\n"
"v_mfma_f32_16x16x32_bf16 a[88:91], v[168:171], v[224:227], a[88:91]\n"
"v_exp_f32 v217, v217\n"
"v_mfma_f32_16x16x32_bf16 a[104:107], v[176:179], v[224:227], a[104:107]\n"
"ds_read_b128 a[248:251], v106 offset:12288\n"
"v_cvt_pk_bf16_f32 v236, v216, v217\n"
"v_mfma_f32_16x16x32_bf16 a[104:107], v[180:183], v[228:231], a[104:107]\n"
"v_exp_f32 v218, v218\n"
"v_mfma_f32_16x16x32_bf16 a[120:123], v[188:191], v[228:231], a[120:123]\n"
"ds_read_b128 a[252:255], v107 offset:12288\n"
"v_mfma_f32_16x16x32_bf16 a[120:123], v[184:187], v[224:227], a[120:123]\n"
"v_exp_f32 v219, v219\n"
"v_mfma_f32_16x16x32_bf16 v[88:91], v[96:99], v[224:227], v[88:91]\n"
"v_cvt_pk_bf16_f32 v237, v218, v219\n"
"v_mfma_f32_16x16x32_bf16 v[88:91], v[96:99], v[228:231], v[88:91]\n"
"s_waitcnt lgkmcnt(4)\n"
"v_exp_f32 v220, v220\n"
"s_sub_u32 s96, s96, 1\n"
"s_cmp_lg_u32 s96, 0\n"
"s_cbranch_scc1 ALOOP_%=\n"
"v_mfma_f32_16x16x32_bf16 v[192:195], a[192:195], a[128:131], v[240:243]\n"
"v_mfma_f32_16x16x32_bf16 v[192:195], a[196:199], a[132:135], v[192:195]\n"
"v_exp_f32 v221, v221\n"
"v_mfma_f32_16x16x32_bf16 v[192:195], a[200:203], a[136:139], v[192:195]\n"
"v_cvt_pk_bf16_f32 v238, v220, v221\n"
"v_mfma_f32_16x16x32_bf16 v[192:195], a[204:207], a[140:143], v[192:195]\n"
"v_exp_f32 v222, v222\n"
"v_mfma_f32_16x16x32_bf16 v[196:199], a[220:223], a[140:143], v[240:243]\n"
"v_mfma_f32_16x16x32_bf16 v[196:199], a[216:219], a[136:139], v[196:199]\n"
"v_exp_f32 v223, v223\n"
"v_mfma_f32_16x16x32_bf16 v[196:199], a[212:215], a[132:135], v[196:199]\n"
"v_cvt_pk_bf16_f32 v239, v222, v223\n"
"v_mfma_f32_16x16x32_bf16 v[196:199], a[208:211], a[128:131], v[196:199]\n"
"v_mfma_f32_16x16x32_bf16 v[200:203], a[224:227], a[128:131], v[240:243]\n"
"s_waitcnt vmcnt(16) lgkmcnt(0)\n"
"s_barrier\n"
"s_lshl_b32 s76, s75, 8\n"
"s_add_u32 s76, s76, 0x1800\n"
"v_and_b32 v122, 31, v5\n"
"v_lshlrev_b32 v122, 3, v122\n"
"v_add_u32 v122, s76, v122\n"
"global_load_dwordx2 v[124:125], v122, s[92:93] sc1\n"
"buffer_load_dword v11, v14, s[52:55], 0 offen\n"
"v_exp_f32 v192, v192\n"
"v_mfma_f32_16x16x32_bf16 v[200:203], a[228:231], a[132:135], v[200:203]\n"
"v_mfma_f32_16x16x32_bf16 v[200:203], a[232:235], a[136:139], v[200:203]\n"
"v_exp_f32 v193, v193\n"
"v_mfma_f32_16x16x32_bf16 v[200:203], a[236:239], a[140:143], v[200:203]\n"
"v_cvt_pk_bf16_f32 v224, v192, v193\n"
"v_mfma_f32_16x16x32_bf16 v[204:207], a[252:255], a[140:143], v[240:243]\n"
"v_exp_f32 v194, v194\n"
"v_mfma_f32_16x16x32_bf16 v[204:207], a[248:251], a[136:139], v[204:207]\n"
"v_mfma_f32_16x16x32_bf16 v[204:207], a[244:247], a[132:135], v[204:207]\n"
"v_exp_f32 v195, v195\n"
"v_mfma_f32_16x16x32_bf16 v[204:207], a[240:243], a[128:131], v[204:207]\n"
"v_cvt_pk_bf16_f32 v225, v194, v195\n"
"v_mfma_f32_16x16x32_bf16 a[12:15], v[128:131], v[232:235], a[12:15]\n"
"v_exp_f32 v196, v196\n"
"v_mfma_f32_16x16x32_bf16 a[12:15], v[132:135], v[236:239], a[12:15]\n"
"v_mfma_f32_16x16x32_bf16 a[28:31], v[140:143], v[236:239], a[28:31]\n"
"v_exp_f32 v197, v197\n"
"v_mfma_f32_16x16x32_bf16 a[28:31], v[136:139], v[232:235], a[28:31]\n"
"v_cvt_pk_bf16_f32 v226, v196, v197\n"
"v_mfma_f32_16x16x32_bf16 a[44:47], v[144:147], v[232:235], a[44:47]\n"
"v_exp_f32 v198, v198\n"
"v_mfma_f32_16x16x32_bf16 a[44:47], v[148:151], v[236:239], a[44:47]\n"
"v_mfma_f32_16x16x32_bf16 a[60:63], v[156:159], v[236:239], a[60:63]\n"
"v_exp_f32 v199, v199\n"
"v_mfma_f32_16x16x32_bf16 a[60:63], v[152:155], v[232:235], a[60:63]\n"
"v_cvt_pk_bf16_f32 v227, v198, v199\n"
"v_mfma_f32_16x16x32_bf16 a[76:79], v[160:163], v[232:235], a[76:79]\n"
"v_mfma_f32_16x16x32_bf16 a[76:79], v[164:167], v[236:239], a[76:79]\n"
"v_exp_f32 v200, v200\n"
"v_mfma_f32_16x16x32_bf16 a[92:95], v[172:175], v[236:239], a[92:95]\n"
"v_mfma_f32_16x16x32_bf16 a[92:95], v[168:171], v[232:235], a[92:95]\n"
"v_exp_f32 v201, v201\n"
"v_mfma_f32_16x16x32_bf16 a[108:111], v[176:179], v[232:235], a[108:111]\n"
"v_cvt_pk_bf16_f32 v228, v200, v201\n"
"v_mfma_f32_16x16x32_bf16 a[108:111], v[180:183], v[236:239], a[108:111]\n"
"v_exp_f32 v202, v202\n"
"v_mfma_f32_16x16x32_bf16 a[124:127], v[188:191], v[236:239], a[124:127]\n"
"v_mfma_f32_16x16x32_bf16 a[124:127], v[184:187], v[232:235], a[124:127]\n"
"v_exp_f32 v203, v203\n"
"v_mfma_f32_16x16x32_bf16 v[92:95], v[96:99], v[232:235], v[92:95]\n"
"ds_read_b64_tr_b16 v[128:129], v108 offset:16384\n"
"v_cvt_pk_bf16_f32 v229, v202, v203\n"
"v_mfma_f32_16x16x32_bf16 v[92:95], v[96:99], v[236:239], v[92:95]\n"
"v_exp_f32 v204, v204\n"
"v_mfma_f32_16x16x32_bf16 v[208:211], a[192:195], a[144:147], v[244:247]\n"
"ds_read_b64_tr_b16 v[130:131], v108 offset:20480\n"
"v_mfma_f32_16x16x32_bf16 v[208:211], a[196:199], a[148:151], v[208:211]\n"
"v_exp_f32 v205, v205\n"
"v_mfma_f32_16x16x32_bf16 v[208:211], a[200:203], a[152:155], v[208:211]\n"
"ds_read_b64_tr_b16 v[132:133], v108 offset:24576\n"
"v_cvt_pk_bf16_f32 v230, v204, v205\n"
"v_mfma_f32_16x16x32_bf16 v[208:211], a[204:207], a[156:159], v[208:211]\n"
"v_exp_f32 v206, v206\n"
"v_mfma_f32_16x16x32_bf16 v[212:215], a[220:223], a[156:159], v[244:247]\n"
"ds_read_b64_tr_b16 v[134:135], v108 offset:28672\n"
"v_mfma_f32_16x16x32_bf16 v[212:215], a[216:219], a[152:155], v[212:215]\n"
"v_exp_f32 v207, v207\n"
"v_mfma_f32_16x16x32_bf16 v[212:215], a[212:215], a[148:151], v[212:215]\n"
"ds_read_b64_tr_b16 v[136:137], v109 offset:16384\n"
"v_cvt_pk_bf16_f32 v231, v206, v207\n"
"v_mfma_f32_16x16x32_bf16 v[212:215], a[208:211], a[144:147], v[212:215]\n"
"ds_read_b64_tr_b16 v[138:139], v109 offset:20480\n"
"v_mfma_f32_16x16x32_bf16 v[216:219], a[224:227], a[144:147], v[244:247]\n"
"v_exp_f32 v208, v208\n"
"v_mfma_f32_16x16x32_bf16 v[216:219], a[228:231], a[148:151], v[216:219]\n"
"ds_read_b64_tr_b16 v[140:141], v109 offset:24576\n"
"v_mfma_f32_16x16x32_bf16 v[216:219], a[232:235], a[152:155], v[216:219]\n"
"v_exp_f32 v209, v209\n"
"v_mfma_f32_16x16x32_bf16 v[216:219], a[236:239], a[156:159], v[216:219]\n"
"ds_read_b64_tr_b16 v[142:143], v109 offset:28672\n"
"v_cvt_pk_bf16_f32 v232, v208, v209\n"
"v_mfma_f32_16x16x32_bf16 v[220:223], a[252:255], a[156:159], v[244:247]\n"
"v_exp_f32 v210, v210\n"
"v_mfma_f32_16x16x32_bf16 v[220:223], a[248:251], a[152:155], v[220:223]\n"
"ds_read_b64_tr_b16 v[144:145], v108 offset:16896\n"
"v_mfma_f32_16x16x32_bf16 v[220:223], a[244:247], a[148:151], v[220:223]\n"
"v_exp_f32 v211, v211\n"
"v_mfma_f32_16x16x32_bf16 v[220:223], a[240:243], a[144:147], v[220:223]\n"
"ds_read_b64_tr_b16 v[146:147], v108 offset:20992\n"
"v_cvt_pk_bf16_f32 v233, v210, v211\n"
"v_mfma_f32_16x16x32_bf16 a[0:3], v[16:19], v[224:227], a[0:3]\n"
"v_exp_f32 v212, v212\n"
"v_mfma_f32_16x16x32_bf16 a[0:3], v[20:23], v[228:231], a[0:3]\n"
"ds_read_b64_tr_b16 v[148:149], v108 offset:25088\n"
"v_mfma_f32_16x16x32_bf16 a[16:19], v[28:31], v[228:231], a[16:19]\n"
"v_exp_f32 v213, v213\n"
"v_mfma_f32_16x16x32_bf16 a[16:19], v[24:27], v[224:227], a[16:19]\n"
"ds_read_b64_tr_b16 v[150:151], v108 offset:29184\n"
"v_cvt_pk_bf16_f32 v234, v212, v213\n"
"v_mfma_f32_16x16x32_bf16 a[32:35], v[32:35], v[224:227], a[32:35]\n"
"v_exp_f32 v214, v214\n"
"v_mfma_f32_16x16x32_bf16 a[32:35], v[36:39], v[228:231], a[32:35]\n"
"ds_read_b64_tr_b16 v[152:153], v109 offset:16896\n"
"v_mfma_f32_16x16x32_bf16 a[48:51], v[44:47], v[228:231], a[48:51]\n"
"v_exp_f32 v215, v215\n"
"v_mfma_f32_16x16x32_bf16 a[48:51], v[40:43], v[224:227], a[48:51]\n"
"ds_read_b64_tr_b16 v[154:155], v109 offset:20992\n"
"v_cvt_pk_bf16_f32 v235, v214, v215\n"
"v_mfma_f32_16x16x32_bf16 a[64:67], v[48:51], v[224:227], a[64:67]\n"
"ds_read_b64_tr_b16 v[156:157], v109 offset:25088\n"
"v_mfma_f32_16x16x32_bf16 a[64:67], v[52:55], v[228:231], a[64:67]\n"
"v_exp_f32 v216, v216\n"
"v_mfma_f32_16x16x32_bf16 a[80:83], v[60:63], v[228:231], a[80:83]\n"
"ds_read_b64_tr_b16 v[158:159], v109 offset:29184\n"
"v_mfma_f32_16x16x32_bf16 a[80:83], v[56:59], v[224:227], a[80:83]\n"
"v_exp_f32 v217, v217\n"
"v_mfma_f32_16x16x32_bf16 a[96:99], v[64:67], v[224:227], a[96:99]\n"
"ds_read_b64_tr_b16 v[160:161], v108 offset:17408\n"
"v_cvt_pk_bf16_f32 v236, v216, v217\n"
"v_mfma_f32_16x16x32_bf16 a[96:99], v[68:71], v[228:231], a[96:99]\n"
"v_exp_f32 v218, v218\n"
"v_mfma_f32_16x16x32_bf16 a[112:115], v[76:79], v[228:231], a[112:115]\n"
"ds_read_b64_tr_b16 v[162:163], v108 offset:21504\n"
"v_mfma_f32_16x16x32_bf16 a[112:115], v[72:75], v[224:227], a[112:115]\n"
"v_exp_f32 v219, v219\n"
"v_mfma_f32_16x16x32_bf16 v[80:83], v[96:99], v[224:227], v[80:83]\n"
"ds_read_b64_tr_b16 v[164:165], v108 offset:25600\n"
"v_cvt_pk_bf16_f32 v237, v218, v219\n"
"v_mfma_f32_16x16x32_bf16 v[80:83], v[96:99], v[228:231], v[80:83]\n"
"v_exp_f32 v220, v220\n"
"v_mfma_f32_16x16x32_bf16 v[192:195], a[192:195], a[160:163], v[248:251]\n"
"ds_read_b64_tr_b16 v[166:167], v108 offset:29696\n"
"v_mfma_f32_16x16x32_bf16 v[192:195], a[196:199], a[164:167], v[192:195]\n"
"v_exp_f32 v221, v221\n"
"v_mfma_f32_16x16x32_bf16 v[192:195], a[200:203], a[168:171], v[192:195]\n"
"ds_read_b64_tr_b16 v[168:169], v109 offset:17408\n"
"v_cvt_pk_bf16_f32 v238, v220, v221\n"
"v_mfma_f32_16x16x32_bf16 v[192:195], a[204:207], a[172:175], v[192:195]\n"
"v_exp_f32 v222, v222\n"
"v_mfma_f32_16x16x32_bf16 v[196:199], a[220:223], a[172:175], v[248:251]\n"
"ds_read_b64_tr_b16 v[170:171], v109 offset:21504\n"
"v_mfma_f32_16x16x32_bf16 v[196:199], a[216:219], a[168:171], v[196:199]\n"
"v_exp_f32 v223, v223\n"
"v_mfma_f32_16x16x32_bf16 v[196:199], a[212:215], a[164:167], v[196:199]\n"
"ds_read_b64_tr_b16 v[172:173], v109 offset:25600\n"
"v_cvt_pk_bf16_f32 v239, v222, v223\n"
"v_mfma_f32_16x16x32_bf16 v[196:199], a[208:211], a[160:163], v[196:199]\n"
"ds_read_b64_tr_b16 v[174:175], v109 offset:29696\n"
"v_mfma_f32_16x16x32_bf16 v[200:203], a[224:227], a[160:163], v[248:251]\n"
"v_exp_f32 v192, v192\n"
"v_mfma_f32_16x16x32_bf16 v[200:203], a[228:231], a[164:167], v[200:203]\n"
"ds_read_b64_tr_b16 v[176:177], v108 offset:17920\n"
"v_mfma_f32_16x16x32_bf16 v[200:203], a[232:235], a[168:171], v[200:203]\n"
"v_exp_f32 v193, v193\n"
"v_mfma_f32_16x16x32_bf16 v[200:203], a[236:239], a[172:175], v[200:203]\n"
"ds_read_b64_tr_b16 v[178:179], v108 offset:22016\n"
"v_cvt_pk_bf16_f32 v224, v192, v193\n"
"v_mfma_f32_16x16x32_bf16 v[204:207], a[252:255], a[172:175], v[248:251]\n"
"v_exp_f32 v194, v194\n"
"v_mfma_f32_16x16x32_bf16 v[204:207], a[248:251], a[168:171], v[204:207]\n"
"ds_read_b64_tr_b16 v[180:181], v108 offset:26112\n"
"v_mfma_f32_16x16x32_bf16 v[204:207], a[244:247], a[164:167], v[204:207]\n"
"v_exp_f32 v195, v195\n"
"v_mfma_f32_16x16x32_bf16 v[204:207], a[240:243], a[160:163], v[204:207]\n"
"ds_read_b64_tr_b16 v[182:183], v108 offset:30208\n"
"v_cvt_pk_bf16_f32 v225, v194, v195\n"
"v_mfma_f32_16x16x32_bf16 a[4:7], v[16:19], v[232:235], a[4:7]\n"
"v_exp_f32 v196, v196\n"
"v_mfma_f32_16x16x32_bf16 a[4:7], v[20:23], v[236:239], a[4:7]\n"
"ds_read_b64_tr_b16 v[184:185], v109 offset:17920\n"
"v_mfma_f32_16x16x32_bf16 a[20:23], v[28:31], v[236:239], a[20:23]\n"
"v_exp_f32 v197, v197\n"
"v_mfma_f32_16x16x32_bf16 a[20:23], v[24:27], v[232:235], a[20:23]\n"
"ds_read_b64_tr_b16 v[186:187], v109 offset:22016\n"
"v_cvt_pk_bf16_f32 v226, v196, v197\n"
"v_mfma_f32_16x16x32_bf16 a[36:39], v[32:35], v[232:235], a[36:39]\n"
"v_exp_f32 v198, v198\n"
"v_mfma_f32_16x16x32_bf16 a[36:39], v[36:39], v[236:239], a[36:39]\n"
"ds_read_b64_tr_b16 v[188:189], v109 offset:26112\n"
"v_mfma_f32_16x16x32_bf16 a[52:55], v[44:47], v[236:239], a[52:55]\n"
"v_exp_f32 v199, v199\n"
"v_mfma_f32_16x16x32_bf16 a[52:55], v[40:43], v[232:235], a[52:55]\n"
"ds_read_b64_tr_b16 v[190:191], v109 offset:30208\n"
"v_cvt_pk_bf16_f32 v227, v198, v199\n"
"v_mfma_f32_16x16x32_bf16 a[68:71], v[48:51], v[232:235], a[68:71]\n"
"v_mfma_f32_16x16x32_bf16 a[68:71], v[52:55], v[236:239], a[68:71]\n"
"v_exp_f32 v200, v200\n"
"v_mfma_f32_16x16x32_bf16 a[84:87], v[60:63], v[236:239], a[84:87]\n"
"v_mfma_f32_16x16x32_bf16 a[84:87], v[56:59], v[232:235], a[84:87]\n"
"v_exp_f32 v201, v201\n"
"v_mfma_f32_16x16x32_bf16 a[100:103], v[64:67], v[232:235], a[100:103]\n"
"v_cvt_pk_bf16_f32 v228, v200, v201\n"
"v_mfma_f32_16x16x32_bf16 a[100:103], v[68:71], v[236:239], a[100:103]\n"
"v_exp_f32 v202, v202\n"
"v_mfma_f32_16x16x32_bf16 a[116:119], v[76:79], v[236:239], a[116:119]\n"
"v_mfma_f32_16x16x32_bf16 a[116:119], v[72:75], v[232:235], a[116:119]\n"
"v_exp_f32 v203, v203\n"
"v_mfma_f32_16x16x32_bf16 v[84:87], v[96:99], v[232:235], v[84:87]\n"
"v_cvt_pk_bf16_f32 v229, v202, v203\n"
"v_mfma_f32_16x16x32_bf16 v[84:87], v[96:99], v[236:239], v[84:87]\n"
"v_exp_f32 v204, v204\n"
"v_mfma_f32_16x16x32_bf16 v[208:211], a[192:195], a[176:179], v[252:255]\n"
"v_mfma_f32_16x16x32_bf16 v[208:211], a[196:199], a[180:183], v[208:211]\n"
"v_exp_f32 v205, v205\n"
"v_mfma_f32_16x16x32_bf16 v[208:211], a[200:203], a[184:187], v[208:211]\n"
"ds_read_b128 a[192:195], v104 offset:16384\n"
"v_cvt_pk_bf16_f32 v230, v204, v205\n"
"v_mfma_f32_16x16x32_bf16 v[208:211], a[204:207], a[188:191], v[208:211]\n"
"v_exp_f32 v206, v206\n"
"v_mfma_f32_16x16x32_bf16 v[212:215], a[220:223], a[188:191], v[252:255]\n"
"ds_read_b128 a[196:199], v105 offset:16384\n"
"v_mfma_f32_16x16x32_bf16 v[212:215], a[216:219], a[184:187], v[212:215]\n"
"v_exp_f32 v207, v207\n"
"v_mfma_f32_16x16x32_bf16 v[212:215], a[212:215], a[180:183], v[212:215]\n"
"ds_read_b128 a[200:203], v106 offset:16384\n"
"v_cvt_pk_bf16_f32 v231, v206, v207\n"
"v_mfma_f32_16x16x32_bf16 v[212:215], a[208:211], a[176:179], v[212:215]\n"
"ds_read_b128 a[204:207], v107 offset:16384\n"
"v_mfma_f32_16x16x32_bf16 v[216:219], a[224:227], a[176:179], v[252:255]\n"
"v_exp_f32 v208, v208\n"
"v_mfma_f32_16x16x32_bf16 v[216:219], a[228:231], a[180:183], v[216:219]\n"
"ds_read_b128 a[208:211], v104 offset:20480\n"
"v_mfma_f32_16x16x32_bf16 v[216:219], a[232:235], a[184:187], v[216:219]\n"
"v_exp_f32 v209, v209\n"
"v_mfma_f32_16x16x32_bf16 v[216:219], a[236:239], a[188:191], v[216:219]\n"
"ds_read_b128 a[212:215], v105 offset:20480\n"
"v_cvt_pk_bf16_f32 v232, v208, v209\n"
"v_mfma_f32_16x16x32_bf16 v[220:223], a[252:255], a[188:191], v[252:255]\n"
"v_exp_f32 v210, v210\n"
"v_mfma_f32_16x16x32_bf16 v[220:223], a[248:251], a[184:187], v[220:223]\n"
"ds_read_b128 a[216:219], v106 offset:20480\n"
"v_mfma_f32_16x16x32_bf16 v[220:223], a[244:247], a[180:183], v[220:223]\n"
"v_exp_f32 v211, v211\n"
"v_mfma_f32_16x16x32_bf16 v[220:223], a[240:243], a[176:179], v[220:223]\n"
"ds_read_b128 a[220:223], v107 offset:20480\n"
"v_cvt_pk_bf16_f32 v233, v210, v211\n"
"v_mfma_f32_16x16x32_bf16 a[8:11], v[16:19], v[224:227], a[8:11]\n"
"v_exp_f32 v212, v212\n"
"v_mfma_f32_16x16x32_bf16 a[8:11], v[20:23], v[228:231], a[8:11]\n"
"ds_read_b128 a[224:227], v104 offset:24576\n"
"v_mfma_f32_16x16x32_bf16 a[24:27], v[28:31], v[228:231], a[24:27]\n"
"v_exp_f32 v213, v213\n"
"v_mfma_f32_16x16x32_bf16 a[24:27], v[24:27], v[224:227], a[24:27]\n"
"ds_read_b128 a[228:231], v105 offset:24576\n"
"v_cvt_pk_bf16_f32 v234, v212, v213\n"
"v_mfma_f32_16x16x32_bf16 a[40:43], v[32:35], v[224:227], a[40:43]\n"
"v_exp_f32 v214, v214\n"
"v_mfma_f32_16x16x32_bf16 a[40:43], v[36:39], v[228:231], a[40:43]\n"
"ds_read_b128 a[232:235], v106 offset:24576\n"
"v_mfma_f32_16x16x32_bf16 a[56:59], v[44:47], v[228:231], a[56:59]\n"
"v_exp_f32 v215, v215\n"
"v_mfma_f32_16x16x32_bf16 a[56:59], v[40:43], v[224:227], a[56:59]\n"
"ds_read_b128 a[236:239], v107 offset:24576\n"
"v_cvt_pk_bf16_f32 v235, v214, v215\n"
"v_mfma_f32_16x16x32_bf16 a[72:75], v[48:51], v[224:227], a[72:75]\n"
"ds_read_b128 a[240:243], v104 offset:28672\n"
"v_mfma_f32_16x16x32_bf16 a[72:75], v[52:55], v[228:231], a[72:75]\n"
"v_exp_f32 v216, v216\n"
"v_mfma_f32_16x16x32_bf16 a[88:91], v[60:63], v[228:231], a[88:91]\n"
"ds_read_b128 a[244:247], v105 offset:28672\n"
"v_mfma_f32_16x16x32_bf16 a[88:91], v[56:59], v[224:227], a[88:91]\n"
"v_exp_f32 v217, v217\n"
"v_mfma_f32_16x16x32_bf16 a[104:107], v[64:67], v[224:227], a[104:107]\n"
"ds_read_b128 a[248:251], v106 offset:28672\n"
"v_cvt_pk_bf16_f32 v236, v216, v217\n"
"v_mfma_f32_16x16x32_bf16 a[104:107], v[68:71], v[228:231], a[104:107]\n"
"v_exp_f32 v218, v218\n"
"v_mfma_f32_16x16x32_bf16 a[120:123], v[76:79], v[228:231], a[120:123]\n"
"ds_read_b128 a[252:255], v107 offset:28672\n"
"v_mfma_f32_16x16x32_bf16 a[120:123], v[72:75], v[224:227], a[120:123]\n"
"v_exp_f32 v219, v219\n"
"v_mfma_f32_16x16x32_bf16 v[88:91], v[96:99], v[224:227], v[88:91]\n"
"v_cvt_pk_bf16_f32 v237, v218, v219\n"
"v_mfma_f32_16x16x32_bf16 v[88:91], v[96:99], v[228:231], v[88:91]\n"
"s_waitcnt lgkmcnt(4)\n"
"v_exp_f32 v220, v220\n"
"v_mfma_f32_16x16x32_bf16 v[192:195], a[192:195], a[128:131], v[240:243]\n"
"v_mfma_f32_16x16x32_bf16 v[192:195], a[196:199], a[132:135], v[192:195]\n"
"v_exp_f32 v221, v221\n"
"v_mfma_f32_16x16x32_bf16 v[192:195], a[200:203], a[136:139], v[192:195]\n"
"v_cvt_pk_bf16_f32 v238, v220, v221\n"
"v_mfma_f32_16x16x32_bf16 v[192:195], a[204:207], a[140:143], v[192:195]\n"
"v_exp_f32 v222, v222\n"
"v_mfma_f32_16x16x32_bf16 v[196:199], a[220:223], a[140:143], v[240:243]\n"
"v_mfma_f32_16x16x32_bf16 v[196:199], a[216:219], a[136:139], v[196:199]\n"
"v_exp_f32 v223, v223\n"
"v_mfma_f32_16x16x32_bf16 v[196:199], a[212:215], a[132:135], v[196:199]\n"
"v_cvt_pk_bf16_f32 v239, v222, v223\n"
"v_mfma_f32_16x16x32_bf16 v[196:199], a[208:211], a[128:131], v[196:199]\n"
"v_mfma_f32_16x16x32_bf16 v[200:203], a[224:227], a[128:131], v[240:243]\n"
"s_waitcnt vmcnt(8) lgkmcnt(0)\n"
"s_barrier\n"
"v_exp_f32 v192, v192\n"
"v_mfma_f32_16x16x32_bf16 v[200:203], a[228:231], a[132:135], v[200:203]\n"
"v_mfma_f32_16x16x32_bf16 v[200:203], a[232:235], a[136:139], v[200:203]\n"
"v_exp_f32 v193, v193\n"
"v_mfma_f32_16x16x32_bf16 v[200:203], a[236:239], a[140:143], v[200:203]\n"
"v_cvt_pk_bf16_f32 v224, v192, v193\n"
"v_mfma_f32_16x16x32_bf16 v[204:207], a[252:255], a[140:143], v[240:243]\n"
"v_exp_f32 v194, v194\n"
"v_mfma_f32_16x16x32_bf16 v[204:207], a[248:251], a[136:139], v[204:207]\n"
"v_mfma_f32_16x16x32_bf16 v[204:207], a[244:247], a[132:135], v[204:207]\n"
"v_exp_f32 v195, v195\n"
"v_mfma_f32_16x16x32_bf16 v[204:207], a[240:243], a[128:131], v[204:207]\n"
"v_cvt_pk_bf16_f32 v225, v194, v195\n"
"v_mfma_f32_16x16x32_bf16 a[12:15], v[16:19], v[232:235], a[12:15]\n"
"v_exp_f32 v196, v196\n"
"v_mfma_f32_16x16x32_bf16 a[12:15], v[20:23], v[236:239], a[12:15]\n"
"v_mfma_f32_16x16x32_bf16 a[28:31], v[28:31], v[236:239], a[28:31]\n"
"v_exp_f32 v197, v197\n"
"v_mfma_f32_16x16x32_bf16 a[28:31], v[24:27], v[232:235], a[28:31]\n"
"v_cvt_pk_bf16_f32 v226, v196, v197\n"
"v_mfma_f32_16x16x32_bf16 a[44:47], v[32:35], v[232:235], a[44:47]\n"
"v_exp_f32 v198, v198\n"
"v_mfma_f32_16x16x32_bf16 a[44:47], v[36:39], v[236:239], a[44:47]\n"
"v_mfma_f32_16x16x32_bf16 a[60:63], v[44:47], v[236:239], a[60:63]\n"
"v_exp_f32 v199, v199\n"
"v_mfma_f32_16x16x32_bf16 a[60:63], v[40:43], v[232:235], a[60:63]\n"
"v_cvt_pk_bf16_f32 v227, v198, v199\n"
"v_mfma_f32_16x16x32_bf16 a[76:79], v[48:51], v[232:235], a[76:79]\n"
"v_mfma_f32_16x16x32_bf16 a[76:79], v[52:55], v[236:239], a[76:79]\n"
"v_exp_f32 v200, v200\n"
"v_mfma_f32_16x16x32_bf16 a[92:95], v[60:63], v[236:239], a[92:95]\n"
"v_mfma_f32_16x16x32_bf16 a[92:95], v[56:59], v[232:235], a[92:95]\n"
"v_exp_f32 v201, v201\n"
"v_mfma_f32_16x16x32_bf16 a[108:111], v[64:67], v[232:235], a[108:111]\n"
"v_cvt_pk_bf16_f32 v228, v200, v201\n"
"v_mfma_f32_16x16x32_bf16 a[108:111], v[68:71], v[236:239], a[108:111]\n"
"v_exp_f32 v202, v202\n"
"v_mfma_f32_16x16x32_bf16 a[124:127], v[76:79], v[236:239], a[124:127]\n"
"v_mfma_f32_16x16x32_bf16 a[124:127], v[72:75], v[232:235], a[124:127]\n"
"v_exp_f32 v203, v203\n"
"v_mfma_f32_16x16x32_bf16 v[92:95], v[96:99], v[232:235], v[92:95]\n"
"ds_read_b64_tr_b16 v[16:17], v108 offset:32768\n"
"v_cvt_pk_bf16_f32 v229, v202, v203\n"
"v_mfma_f32_16x16x32_bf16 v[92:95], v[96:99], v[236:239], v[92:95]\n"
"v_exp_f32 v204, v204\n"
"v_mfma_f32_16x16x32_bf16 v[208:211], a[192:195], a[144:147], v[244:247]\n"
"ds_read_b64_tr_b16 v[18:19], v108 offset:36864\n"
"v_mfma_f32_16x16x32_bf16 v[208:211], a[196:199], a[148:151], v[208:211]\n"
"v_exp_f32 v205, v205\n"
"v_mfma_f32_16x16x32_bf16 v[208:211], a[200:203], a[152:155], v[208:211]\n"
"ds_read_b64_tr_b16 v[20:21], v108 offset:40960\n"
"v_cvt_pk_bf16_f32 v230, v204, v205\n"
"v_mfma_f32_16x16x32_bf16 v[208:211], a[204:207], a[156:159], v[208:211]\n"
"v_exp_f32 v206, v206\n"
"v_mfma_f32_16x16x32_bf16 v[212:215], a[220:223], a[156:159], v[244:247]\n"
"ds_read_b64_tr_b16 v[22:23], v108 offset:45056\n"
"v_mfma_f32_16x16x32_bf16 v[212:215], a[216:219], a[152:155], v[212:215]\n"
"v_exp_f32 v207, v207\n"
"v_mfma_f32_16x16x32_bf16 v[212:215], a[212:215], a[148:151], v[212:215]\n"
"ds_read_b64_tr_b16 v[24:25], v109 offset:32768\n"
"v_cvt_pk_bf16_f32 v231, v206, v207\n"
"v_mfma_f32_16x16x32_bf16 v[212:215], a[208:211], a[144:147], v[212:215]\n"
"ds_read_b64_tr_b16 v[26:27], v109 offset:36864\n"
"v_mfma_f32_16x16x32_bf16 v[216:219], a[224:227], a[144:147], v[244:247]\n"
"v_exp_f32 v208, v208\n"
"v_mfma_f32_16x16x32_bf16 v[216:219], a[228:231], a[148:151], v[216:219]\n"
"ds_read_b64_tr_b16 v[28:29], v109 offset:40960\n"
"v_mfma_f32_16x16x32_bf16 v[216:219], a[232:235], a[152:155], v[216:219]\n"
"v_exp_f32 v209, v209\n"
"v_mfma_f32_16x16x32_bf16 v[216:219], a[236:239], a[156:159], v[216:219]\n"
"ds_read_b64_tr_b16 v[30:31], v109 offset:45056\n"
"v_cvt_pk_bf16_f32 v232, v208, v209\n"
"v_mfma_f32_16x16x32_bf16 v[220:223], a[252:255], a[156:159], v[244:247]\n"
"v_exp_f32 v210, v210\n"
"v_mfma_f32_16x16x32_bf16 v[220:223], a[248:251], a[152:155], v[220:223]\n"
"ds_read_b64_tr_b16 v[32:33], v108 offset:33280\n"
"v_mfma_f32_16x16x32_bf16 v[220:223], a[244:247], a[148:151], v[220:223]\n"
"v_exp_f32 v211, v211\n"
"v_mfma_f32_16x16x32_bf16 v[220:223], a[240:243], a[144:147], v[220:223]\n"
"ds_read_b64_tr_b16 v[34:35], v108 offset:37376\n"
"v_cvt_pk_bf16_f32 v233, v210, v211\n"
"v_mfma_f32_16x16x32_bf16 a[0:3], v[128:131], v[224:227], a[0:3]\n"
"v_exp_f32 v212, v212\n"
"v_mfma_f32_16x16x32_bf16 a[0:3], v[132:135], v[228:231], a[0:3]\n"
"ds_read_b64_tr_b16 v[36:37], v108 offset:41472\n"
"v_mfma_f32_16x16x32_bf16 a[16:19], v[140:143], v[228:231], a[16:19]\n"
"v_exp_f32 v213, v213\n"
"v_mfma_f32_16x16x32_bf16 a[16:19], v[136:139], v[224:227], a[16:19]\n"
"ds_read_b64_tr_b16 v[38:39], v108 offset:45568\n"
"v_cvt_pk_bf16_f32 v234, v212, v213\n"
"v_mfma_f32_16x16x32_bf16 a[32:35], v[144:147], v[224:227], a[32:35]\n"
"v_exp_f32 v214, v214\n"
"v_mfma_f32_16x16x32_bf16 a[32:35], v[148:151], v[228:231], a[32:35]\n"
"ds_read_b64_tr_b16 v[40:41], v109 offset:33280\n"
"v_mfma_f32_16x16x32_bf16 a[48:51], v[156:159], v[228:231], a[48:51]\n"
"v_exp_f32 v215, v215\n"
"v_mfma_f32_16x16x32_bf16 a[48:51], v[152:155], v[224:227], a[48:51]\n"
"ds_read_b64_tr_b16 v[42:43], v109 offset:37376\n"
"v_cvt_pk_bf16_f32 v235, v214, v215\n"
"v_mfma_f32_16x16x32_bf16 a[64:67], v[160:163], v[224:227], a[64:67]\n"
"ds_read_b64_tr_b16 v[44:45], v109 offset:41472\n"
"v_mfma_f32_16x16x32_bf16 a[64:67], v[164:167], v[228:231], a[64:67]\n"
"v_exp_f32 v216, v216\n"
"v_mfma_f32_16x16x32_bf16 a[80:83], v[172:175], v[228:231], a[80:83]\n"
"ds_read_b64_tr_b16 v[46:47], v109 offset:45568\n"
"v_mfma_f32_16x16x32_bf16 a[80:83], v[168:171], v[224:227], a[80:83]\n"
"v_exp_f32 v217, v217\n"
"v_mfma_f32_16x16x32_bf16 a[96:99], v[176:179], v[224:227], a[96:99]\n"
"ds_read_b64_tr_b16 v[48:49], v108 offset:33792\n"
"v_cvt_pk_bf16_f32 v236, v216, v217\n"
"v_mfma_f32_16x16x32_bf16 a[96:99], v[180:183], v[228:231], a[96:99]\n"
"v_exp_f32 v218, v218\n"
"v_mfma_f32_16x16x32_bf16 a[112:115], v[188:191], v[228:231], a[112:115]\n"
"ds_read_b64_tr_b16 v[50:51], v108 offset:37888\n"
"v_mfma_f32_16x16x32_bf16 a[112:115], v[184:187], v[224:227], a[112:115]\n"
"v_exp_f32 v219, v219\n"
"v_mfma_f32_16x16x32_bf16 v[80:83], v[96:99], v[224:227], v[80:83]\n"
"ds_read_b64_tr_b16 v[52:53], v108 offset:41984\n"
"v_cvt_pk_bf16_f32 v237, v218, v219\n"
"v_mfma_f32_16x16x32_bf16 v[80:83], v[96:99], v[228:231], v[80:83]\n"
"v_exp_f32 v220, v220\n"
"v_mfma_f32_16x16x32_bf16 v[192:195], a[192:195], a[160:163], v[248:251]\n"
"ds_read_b64_tr_b16 v[54:55], v108 offset:46080\n"
"v_mfma_f32_16x16x32_bf16 v[192:195], a[196:199], a[164:167], v[192:195]\n"
"v_exp_f32 v221, v221\n"
"v_mfma_f32_16x16x32_bf16 v[192:195], a[200:203], a[168:171], v[192:195]\n"
"ds_read_b64_tr_b16 v[56:57], v109 offset:33792\n"
"v_cvt_pk_bf16_f32 v238, v220, v221\n"
"v_mfma_f32_16x16x32_bf16 v[192:195], a[204:207], a[172:175], v[192:195]\n"
"v_exp_f32 v222, v222\n"
"v_mfma_f32_16x16x32_bf16 v[196:199], a[220:223], a[172:175], v[248:251]\n"
"ds_read_b64_tr_b16 v[58:59], v109 offset:37888\n"
"v_mfma_f32_16x16x32_bf16 v[196:199], a[216:219], a[168:171], v[196:199]\n"
"v_exp_f32 v223, v223\n"
"v_mfma_f32_16x16x32_bf16 v[196:199], a[212:215], a[164:167], v[196:199]\n"
"ds_read_b64_tr_b16 v[60:61], v109 offset:41984\n"
"v_cvt_pk_bf16_f32 v239, v222, v223\n"
"v_mfma_f32_16x16x32_bf16 v[196:199], a[208:211], a[160:163], v[196:199]\n"
"ds_read_b64_tr_b16 v[62:63], v109 offset:46080\n"
"v_mfma_f32_16x16x32_bf16 v[200:203], a[224:227], a[160:163], v[248:251]\n"
"v_exp_f32 v192, v192\n"
"v_mfma_f32_16x16x32_bf16 v[200:203], a[228:231], a[164:167], v[200:203]\n"
"ds_read_b64_tr_b16 v[64:65], v108 offset:34304\n"
"v_mfma_f32_16x16x32_bf16 v[200:203], a[232:235], a[168:171], v[200:203]\n"
"v_exp_f32 v193, v193\n"
"v_mfma_f32_16x16x32_bf16 v[200:203], a[236:239], a[172:175], v[200:203]\n"
"ds_read_b64_tr_b16 v[66:67], v108 offset:38400\n"
"v_cvt_pk_bf16_f32 v224, v192, v193\n"
"v_mfma_f32_16x16x32_bf16 v[204:207], a[252:255], a[172:175], v[248:251]\n"
"v_exp_f32 v194, v194\n"
"v_mfma_f32_16x16x32_bf16 v[204:207], a[248:251], a[168:171], v[204:207]\n"
"ds_read_b64_tr_b16 v[68:69], v108 offset:42496\n"
"v_mfma_f32_16x16x32_bf16 v[204:207], a[244:247], a[164:167], v[204:207]\n"
"v_exp_f32 v195, v195\n"
"v_mfma_f32_16x16x32_bf16 v[204:207], a[240:243], a[160:163], v[204:207]\n"
"ds_read_b64_tr_b16 v[70:71], v108 offset:46592\n"
"v_cvt_pk_bf16_f32 v225, v194, v195\n"
"v_mfma_f32_16x16x32_bf16 a[4:7], v[128:131], v[232:235], a[4:7]\n"
"v_exp_f32 v196, v196\n"
"v_mfma_f32_16x16x32_bf16 a[4:7], v[132:135], v[236:239], a[4:7]\n"
"ds_read_b64_tr_b16 v[72:73], v109 offset:34304\n"
"v_mfma_f32_16x16x32_bf16 a[20:23], v[140:143], v[236:239], a[20:23]\n"
"v_exp_f32 v197, v197\n"
"v_mfma_f32_16x16x32_bf16 a[20:23], v[136:139], v[232:235], a[20:23]\n"
"ds_read_b64_tr_b16 v[74:75], v109 offset:38400\n"
"v_cvt_pk_bf16_f32 v226, v196, v197\n"
"v_mfma_f32_16x16x32_bf16 a[36:39], v[144:147], v[232:235], a[36:39]\n"
"v_exp_f32 v198, v198\n"
"v_mfma_f32_16x16x32_bf16 a[36:39], v[148:151], v[236:239], a[36:39]\n"
"ds_read_b64_tr_b16 v[76:77], v109 offset:42496\n"
"v_mfma_f32_16x16x32_bf16 a[52:55], v[156:159], v[236:239], a[52:55]\n"
"v_exp_f32 v199, v199\n"
"v_mfma_f32_16x16x32_bf16 a[52:55], v[152:155], v[232:235], a[52:55]\n"
"ds_read_b64_tr_b16 v[78:79], v109 offset:46592\n"
"v_cvt_pk_bf16_f32 v227, v198, v199\n"
"v_mfma_f32_16x16x32_bf16 a[68:71], v[160:163], v[232:235], a[68:71]\n"
"v_mfma_f32_16x16x32_bf16 a[68:71], v[164:167], v[236:239], a[68:71]\n"
"v_exp_f32 v200, v200\n"
"v_mfma_f32_16x16x32_bf16 a[84:87], v[172:175], v[236:239], a[84:87]\n"
"v_mfma_f32_16x16x32_bf16 a[84:87], v[168:171], v[232:235], a[84:87]\n"
"v_exp_f32 v201, v201\n"
"v_mfma_f32_16x16x32_bf16 a[100:103], v[176:179], v[232:235], a[100:103]\n"
"v_cvt_pk_bf16_f32 v228, v200, v201\n"
"v_mfma_f32_16x16x32_bf16 a[100:103], v[180:183], v[236:239], a[100:103]\n"
"v_exp_f32 v202, v202\n"
"v_mfma_f32_16x16x32_bf16 a[116:119], v[188:191], v[236:239], a[116:119]\n"
"v_mfma_f32_16x16x32_bf16 a[116:119], v[184:187], v[232:235], a[116:119]\n"
"v_exp_f32 v203, v203\n"
"v_mfma_f32_16x16x32_bf16 v[84:87], v[96:99], v[232:235], v[84:87]\n"
"v_cvt_pk_bf16_f32 v229, v202, v203\n"
"v_mfma_f32_16x16x32_bf16 v[84:87], v[96:99], v[236:239], v[84:87]\n"
"v_exp_f32 v204, v204\n"
"v_mfma_f32_16x16x32_bf16 v[208:211], a[192:195], a[176:179], v[252:255]\n"
"v_mfma_f32_16x16x32_bf16 v[208:211], a[196:199], a[180:183], v[208:211]\n"
"v_exp_f32 v205, v205\n"
"v_mfma_f32_16x16x32_bf16 v[208:211], a[200:203], a[184:187], v[208:211]\n"
"ds_read_b128 a[192:195], v104 offset:32768\n"
"v_cvt_pk_bf16_f32 v230, v204, v205\n"
"v_mfma_f32_16x16x32_bf16 v[208:211], a[204:207], a[188:191], v[208:211]\n"
"v_exp_f32 v206, v206\n"
"v_mfma_f32_16x16x32_bf16 v[212:215], a[220:223], a[188:191], v[252:255]\n"
"ds_read_b128 a[196:199], v105 offset:32768\n"
"v_mfma_f32_16x16x32_bf16 v[212:215], a[216:219], a[184:187], v[212:215]\n"
"v_exp_f32 v207, v207\n"
"v_mfma_f32_16x16x32_bf16 v[212:215], a[212:215], a[180:183], v[212:215]\n"
"ds_read_b128 a[200:203], v106 offset:32768\n"
"v_cvt_pk_bf16_f32 v231, v206, v207\n"
"v_mfma_f32_16x16x32_bf16 v[212:215], a[208:211], a[176:179], v[212:215]\n"
"ds_read_b128 a[204:207], v107 offset:32768\n"
"v_mfma_f32_16x16x32_bf16 v[216:219], a[224:227], a[176:179], v[252:255]\n"
"v_exp_f32 v208, v208\n"
"v_mfma_f32_16x16x32_bf16 v[216:219], a[228:231], a[180:183], v[216:219]\n"
"ds_read_b128 a[208:211], v104 offset:36864\n"
"v_mfma_f32_16x16x32_bf16 v[216:219], a[232:235], a[184:187], v[216:219]\n"
"v_exp_f32 v209, v209\n"
"v_mfma_f32_16x16x32_bf16 v[216:219], a[236:239], a[188:191], v[216:219]\n"
"ds_read_b128 a[212:215], v105 offset:36864\n"
"v_cvt_pk_bf16_f32 v232, v208, v209\n"
"v_mfma_f32_16x16x32_bf16 v[220:223], a[252:255], a[188:191], v[252:255]\n"
"v_exp_f32 v210, v210\n"
"v_mfma_f32_16x16x32_bf16 v[220:223], a[248:251], a[184:187], v[220:223]\n"
"ds_read_b128 a[216:219], v106 offset:36864\n"
"v_mfma_f32_16x16x32_bf16 v[220:223], a[244:247], a[180:183], v[220:223]\n"
"v_exp_f32 v211, v211\n"
"v_mfma_f32_16x16x32_bf16 v[220:223], a[240:243], a[176:179], v[220:223]\n"
"ds_read_b128 a[220:223], v107 offset:36864\n"
"v_cvt_pk_bf16_f32 v233, v210, v211\n"
"v_mfma_f32_16x16x32_bf16 a[8:11], v[128:131], v[224:227], a[8:11]\n"
"v_exp_f32 v212, v212\n"
"v_mfma_f32_16x16x32_bf16 a[8:11], v[132:135], v[228:231], a[8:11]\n"
"ds_read_b128 a[224:227], v104 offset:40960\n"
"v_mfma_f32_16x16x32_bf16 a[24:27], v[140:143], v[228:231], a[24:27]\n"
"v_exp_f32 v213, v213\n"
"v_mfma_f32_16x16x32_bf16 a[24:27], v[136:139], v[224:227], a[24:27]\n"
"ds_read_b128 a[228:231], v105 offset:40960\n"
"v_cvt_pk_bf16_f32 v234, v212, v213\n"
"v_mfma_f32_16x16x32_bf16 a[40:43], v[144:147], v[224:227], a[40:43]\n"
"v_exp_f32 v214, v214\n"
"v_mfma_f32_16x16x32_bf16 a[40:43], v[148:151], v[228:231], a[40:43]\n"
"ds_read_b128 a[232:235], v106 offset:40960\n"
"v_mfma_f32_16x16x32_bf16 a[56:59], v[156:159], v[228:231], a[56:59]\n"
"v_exp_f32 v215, v215\n"
"v_mfma_f32_16x16x32_bf16 a[56:59], v[152:155], v[224:227], a[56:59]\n"
"ds_read_b128 a[236:239], v107 offset:40960\n"
"v_cvt_pk_bf16_f32 v235, v214, v215\n"
"v_mfma_f32_16x16x32_bf16 a[72:75], v[160:163], v[224:227], a[72:75]\n"
"ds_read_b128 a[240:243], v104 offset:45056\n"
"v_mfma_f32_16x16x32_bf16 a[72:75], v[164:167], v[228:231], a[72:75]\n"
"v_exp_f32 v216, v216\n"
"v_mfma_f32_16x16x32_bf16 a[88:91], v[172:175], v[228:231], a[88:91]\n"
"ds_read_b128 a[244:247], v105 offset:45056\n"
"v_mfma_f32_16x16x32_bf16 a[88:91], v[168:171], v[224:227], a[88:91]\n"
"v_exp_f32 v217, v217\n"
"v_mfma_f32_16x16x32_bf16 a[104:107], v[176:179], v[224:227], a[104:107]\n"
"ds_read_b128 a[248:251], v106 offset:45056\n"
"v_cvt_pk_bf16_f32 v236, v216, v217\n"
"v_mfma_f32_16x16x32_bf16 a[104:107], v[180:183], v[228:231], a[104:107]\n"
"v_exp_f32 v218, v218\n"
"v_mfma_f32_16x16x32_bf16 a[120:123], v[188:191], v[228:231], a[120:123]\n"
"ds_read_b128 a[252:255], v107 offset:45056\n"
"v_mfma_f32_16x16x32_bf16 a[120:123], v[184:187], v[224:227], a[120:123]\n"
"v_exp_f32 v219, v219\n"
"v_mfma_f32_16x16x32_bf16 v[88:91], v[96:99], v[224:227], v[88:91]\n"
"v_cvt_pk_bf16_f32 v237, v218, v219\n"
"v_mfma_f32_16x16x32_bf16 v[88:91], v[96:99], v[228:231], v[88:91]\n"
"s_waitcnt lgkmcnt(4)\n"
"v_exp_f32 v220, v220\n"
"v_mfma_f32_16x16x32_bf16 v[192:195], a[192:195], a[128:131], v[240:243]\n"
"v_mfma_f32_16x16x32_bf16 v[192:195], a[196:199], a[132:135], v[192:195]\n"
"v_exp_f32 v221, v221\n"
"v_mfma_f32_16x16x32_bf16 v[192:195], a[200:203], a[136:139], v[192:195]\n"
"v_cvt_pk_bf16_f32 v238, v220, v221\n"
"v_mfma_f32_16x16x32_bf16 v[192:195], a[204:207], a[140:143], v[192:195]\n"
"v_exp_f32 v222, v222\n"
"v_mfma_f32_16x16x32_bf16 v[196:199], a[220:223], a[140:143], v[240:243]\n"
"v_mfma_f32_16x16x32_bf16 v[196:199], a[216:219], a[136:139], v[196:199]\n"
"v_exp_f32 v223, v223\n"
"v_mfma_f32_16x16x32_bf16 v[196:199], a[212:215], a[132:135], v[196:199]\n"
"v_cvt_pk_bf16_f32 v239, v222, v223\n"
"v_mfma_f32_16x16x32_bf16 v[196:199], a[208:211], a[128:131], v[196:199]\n"
"v_mfma_f32_16x16x32_bf16 v[200:203], a[224:227], a[128:131], v[240:243]\n"
"s_waitcnt vmcnt(0) lgkmcnt(0)\n"
"s_barrier\n"
"s_lshl_b32 s98, s81, 3\n"
"s_lshl_b32 s98, 0xff, s98\n"
"v_cmp_eq_u64 vcc, v[124:125], s[88:89]\n"
"s_cmp_lg_u32 s73, 0\n"
"s_cbranch_scc1 POLLDH_%=\n"
"s_and_b32 s76, vcc_lo, s98\n"
"s_mov_b32 s99, 0\n"
"s_cmp_eq_u32 s76, s98\n"
"s_cbranch_scc1 POLLDH_%=\n"
"POLLH_%=:\n"
"s_sleep 1\n"
"global_load_dwordx2 v[124:125], v122, s[92:93] sc1\n"
"s_waitcnt vmcnt(0)\n"
"v_cmp_eq_u64 vcc, v[124:125], s[88:89]\n"
"s_nop 3\n"
"s_and_b32 s76, vcc_lo, s98\n"
"s_cmp_eq_u32 s76, s98\n"
"s_cbranch_scc1 POLLDH_%=\n"
"s_add_u32 s99, s99, 1\n"
"s_cmp_gt_u32 s99, 0x40000\n"
"s_cbranch_scc0 POLLH_%=\n"
"POLLDH_%=:\n"
"v_exp_f32 v192, v192\n"
"v_mfma_f32_16x16x32_bf16 v[200:203], a[228:231], a[132:135], v[200:203]\n"
"v_mfma_f32_16x16x32_bf16 v[200:203], a[232:235], a[136:139], v[200:203]\n"
"v_exp_f32 v193, v193\n"
"v_mfma_f32_16x16x32_bf16 v[200:203], a[236:239], a[140:143], v[200:203]\n"
"v_cvt_pk_bf16_f32 v224, v192, v193\n"
"v_mfma_f32_16x16x32_bf16 v[204:207], a[252:255], a[140:143], v[240:243]\n"
"v_exp_f32 v194, v194\n"
"v_mfma_f32_16x16x32_bf16 v[204:207], a[248:251], a[136:139], v[204:207]\n"
"v_mfma_f32_16x16x32_bf16 v[204:207], a[244:247], a[132:135], v[204:207]\n"
"v_exp_f32 v195, v195\n"
"v_mfma_f32_16x16x32_bf16 v[204:207], a[240:243], a[128:131], v[204:207]\n"
"v_cvt_pk_bf16_f32 v225, v194, v195\n"
"v_mfma_f32_16x16x32_bf16 a[12:15], v[128:131], v[232:235], a[12:15]\n"
"v_exp_f32 v196, v196\n"
"v_mfma_f32_16x16x32_bf16 a[12:15], v[132:135], v[236:239], a[12:15]\n"
"v_mfma_f32_16x16x32_bf16 a[28:31], v[140:143], v[236:239], a[28:31]\n"
"v_exp_f32 v197, v197\n"
"v_mfma_f32_16x16x32_bf16 a[28:31], v[136:139], v[232:235], a[28:31]\n"
"v_cvt_pk_bf16_f32 v226, v196, v197\n"
"v_mfma_f32_16x16x32_bf16 a[44:47], v[144:147], v[232:235], a[44:47]\n"
"v_exp_f32 v198, v198\n"
"v_mfma_f32_16x16x32_bf16 a[44:47], v[148:151], v[236:239], a[44:47]\n"
"v_mfma_f32_16x16x32_bf16 a[60:63], v[156:159], v[236:239], a[60:63]\n"
"v_exp_f32 v199, v199\n"
"v_mfma_f32_16x16x32_bf16 a[60:63], v[152:155], v[232:235], a[60:63]\n"
"v_cvt_pk_bf16_f32 v227, v198, v199\n"
"v_mfma_f32_16x16x32_bf16 a[76:79], v[160:163], v[232:235], a[76:79]\n"
"v_mfma_f32_16x16x32_bf16 a[76:79], v[164:167], v[236:239], a[76:79]\n"
"v_exp_f32 v200, v200\n"
"v_mfma_f32_16x16x32_bf16 a[92:95], v[172:175], v[236:239], a[92:95]\n"
"v_mfma_f32_16x16x32_bf16 a[92:95], v[168:171], v[232:235], a[92:95]\n"
"v_exp_f32 v201, v201\n"
"v_mfma_f32_16x16x32_bf16 a[108:111], v[176:179], v[232:235], a[108:111]\n"
"v_cvt_pk_bf16_f32 v228, v200, v201\n"
"v_mfma_f32_16x16x32_bf16 a[108:111], v[180:183], v[236:239], a[108:111]\n"
"v_exp_f32 v202, v202\n"
"v_mfma_f32_16x16x32_bf16 a[124:127], v[188:191], v[236:239], a[124:127]\n"
"v_mfma_f32_16x16x32_bf16 a[124:127], v[184:187], v[232:235], a[124:127]\n"
"v_exp_f32 v203, v203\n"
"v_mfma_f32_16x16x32_bf16 v[92:95], v[96:99], v[232:235], v[92:95]\n"
"ds_read_b64_tr_b16 v[128:129], v108 offset:49152\n"
"v_cvt_pk_bf16_f32 v229, v202, v203\n"
"v_mfma_f32_16x16x32_bf16 v[92:95], v[96:99], v[236:239], v[92:95]\n"
"v_exp_f32 v204, v204\n"
"v_mfma_f32_16x16x32_bf16 v[208:211], a[192:195], a[144:147], v[244:247]\n"
"ds_read_b64_tr_b16 v[130:131], v108 offset:53248\n"
"v_mfma_f32_16x16x32_bf16 v[208:211], a[196:199], a[148:151], v[208:211]\n"
"v_exp_f32 v205, v205\n"
"v_mfma_f32_16x16x32_bf16 v[208:211], a[200:203], a[152:155], v[208:211]\n"
"ds_read_b64_tr_b16 v[132:133], v108 offset:57344\n"
"v_cvt_pk_bf16_f32 v230, v204, v205\n"
"v_mfma_f32_16x16x32_bf16 v[208:211], a[204:207], a[156:159], v[208:211]\n"
"v_exp_f32 v206, v206\n"
"v_mfma_f32_16x16x32_bf16 v[212:215], a[220:223], a[156:159], v[244:247]\n"
"ds_read_b64_tr_b16 v[134:135], v108 offset:61440\n"
"v_mfma_f32_16x16x32_bf16 v[212:215], a[216:219], a[152:155], v[212:215]\n"
"v_exp_f32 v207, v207\n"
"v_mfma_f32_16x16x32_bf16 v[212:215], a[212:215], a[148:151], v[212:215]\n"
"ds_read_b64_tr_b16 v[136:137], v109 offset:49152\n"
"v_cvt_pk_bf16_f32 v231, v206, v207\n"
"v_mfma_f32_16x16x32_bf16 v[212:215], a[208:211], a[144:147], v[212:215]\n"
"ds_read_b64_tr_b16 v[138:139], v109 offset:53248\n"
"v_mfma_f32_16x16x32_bf16 v[216:219], a[224:227], a[144:147], v[244:247]\n"
"v_exp_f32 v208, v208\n"
"v_mfma_f32_16x16x32_bf16 v[216:219], a[228:231], a[148:151], v[216:219]\n"
"ds_read_b64_tr_b16 v[140:141], v109 offset:57344\n"
"v_mfma_f32_16x16x32_bf16 v[216:219], a[232:235], a[152:155], v[216:219]\n"
"v_exp_f32 v209, v209\n"
"v_mfma_f32_16x16x32_bf16 v[216:219], a[236:239], a[156:159], v[216:219]\n"
"ds_read_b64_tr_b16 v[142:143], v109 offset:61440\n"
"v_cvt_pk_bf16_f32 v232, v208, v209\n"
"v_mfma_f32_16x16x32_bf16 v[220:223], a[252:255], a[156:159], v[244:247]\n"
"v_exp_f32 v210, v210\n"
"v_mfma_f32_16x16x32_bf16 v[220:223], a[248:251], a[152:155], v[220:223]\n"
"ds_read_b64_tr_b16 v[144:145], v108 offset:49664\n"
"v_mfma_f32_16x16x32_bf16 v[220:223], a[244:247], a[148:151], v[220:223]\n"
"v_exp_f32 v211, v211\n"
"v_mfma_f32_16x16x32_bf16 v[220:223], a[240:243], a[144:147], v[220:223]\n"
"ds_read_b64_tr_b16 v[146:147], v108 offset:53760\n"
"v_cvt_pk_bf16_f32 v233, v210, v211\n"
"v_mfma_f32_16x16x32_bf16 a[0:3], v[16:19], v[224:227], a[0:3]\n"
"v_exp_f32 v212, v212\n"
"v_mfma_f32_16x16x32_bf16 a[0:3], v[20:23], v[228:231], a[0:3]\n"
"ds_read_b64_tr_b16 v[148:149], v108 offset:57856\n"
"v_mfma_f32_16x16x32_bf16 a[16:19], v[28:31], v[228:231], a[16:19]\n"
"v_exp_f32 v213, v213\n"
"v_mfma_f32_16x16x32_bf16 a[16:19], v[24:27], v[224:227], a[16:19]\n"
"ds_read_b64_tr_b16 v[150:151], v108 offset:61952\n"
"v_cvt_pk_bf16_f32 v234, v212, v213\n"
"v_mfma_f32_16x16x32_bf16 a[32:35], v[32:35], v[224:227], a[32:35]\n"
"v_exp_f32 v214, v214\n"
"v_mfma_f32_16x16x32_bf16 a[32:35], v[36:39], v[228:231], a[32:35]\n"
"ds_read_b64_tr_b16 v[152:153], v109 offset:49664\n"
"v_mfma_f32_16x16x32_bf16 a[48:51], v[44:47], v[228:231], a[48:51]\n"
"v_exp_f32 v215, v215\n"
"v_mfma_f32_16x16x32_bf16 a[48:51], v[40:43], v[224:227], a[48:51]\n"
"ds_read_b64_tr_b16 v[154:155], v109 offset:53760\n"
"v_cvt_pk_bf16_f32 v235, v214, v215\n"
"v_mfma_f32_16x16x32_bf16 a[64:67], v[48:51], v[224:227], a[64:67]\n"
"ds_read_b64_tr_b16 v[156:157], v109 offset:57856\n"
"v_mfma_f32_16x16x32_bf16 a[64:67], v[52:55], v[228:231], a[64:67]\n"
"v_exp_f32 v216, v216\n"
"v_mfma_f32_16x16x32_bf16 a[80:83], v[60:63], v[228:231], a[80:83]\n"
"ds_read_b64_tr_b16 v[158:159], v109 offset:61952\n"
"v_mfma_f32_16x16x32_bf16 a[80:83], v[56:59], v[224:227], a[80:83]\n"
"v_exp_f32 v217, v217\n"
"v_mfma_f32_16x16x32_bf16 a[96:99], v[64:67], v[224:227], a[96:99]\n"
"ds_read_b64_tr_b16 v[160:161], v108 offset:50176\n"
"v_cvt_pk_bf16_f32 v236, v216, v217\n"
"v_mfma_f32_16x16x32_bf16 a[96:99], v[68:71], v[228:231], a[96:99]\n"
"v_exp_f32 v218, v218\n"
"v_mfma_f32_16x16x32_bf16 a[112:115], v[76:79], v[228:231], a[112:115]\n"
"ds_read_b64_tr_b16 v[162:163], v108 offset:54272\n"
"v_mfma_f32_16x16x32_bf16 a[112:115], v[72:75], v[224:227], a[112:115]\n"
"v_exp_f32 v219, v219\n"
"v_mfma_f32_16x16x32_bf16 v[80:83], v[96:99], v[224:227], v[80:83]\n"
"ds_read_b64_tr_b16 v[164:165], v108 offset:58368\n"
"v_cvt_pk_bf16_f32 v237, v218, v219\n"
"v_mfma_f32_16x16x32_bf16 v[80:83], v[96:99], v[228:231], v[80:83]\n"
"v_exp_f32 v220, v220\n"
"v_mfma_f32_16x16x32_bf16 v[192:195], a[192:195], a[160:163], v[248:251]\n"
"ds_read_b64_tr_b16 v[166:167], v108 offset:62464\n"
"v_mfma_f32_16x16x32_bf16 v[192:195], a[196:199], a[164:167], v[192:195]\n"
"v_exp_f32 v221, v221\n"
"v_mfma_f32_16x16x32_bf16 v[192:195], a[200:203], a[168:171], v[192:195]\n"
"ds_read_b64_tr_b16 v[168:169], v109 offset:50176\n"
"v_cvt_pk_bf16_f32 v238, v220, v221\n"
"v_mfma_f32_16x16x32_bf16 v[192:195], a[204:207], a[172:175], v[192:195]\n"
"v_exp_f32 v222, v222\n"
"v_mfma_f32_16x16x32_bf16 v[196:199], a[220:223], a[172:175], v[248:251]\n"
"ds_read_b64_tr_b16 v[170:171], v109 offset:54272\n"
"v_mfma_f32_16x16x32_bf16 v[196:199], a[216:219], a[168:171], v[196:199]\n"
"v_exp_f32 v223, v223\n"
"v_mfma_f32_16x16x32_bf16 v[196:199], a[212:215], a[164:167], v[196:199]\n"
"ds_read_b64_tr_b16 v[172:173], v109 offset:58368\n"
"v_cvt_pk_bf16_f32 v239, v222, v223\n"
"v_mfma_f32_16x16x32_bf16 v[196:199], a[208:211], a[160:163], v[196:199]\n"
"ds_read_b64_tr_b16 v[174:175], v109 offset:62464\n"
"v_mfma_f32_16x16x32_bf16 v[200:203], a[224:227], a[160:163], v[248:251]\n"
"v_exp_f32 v192, v192\n"
"v_mfma_f32_16x16x32_bf16 v[200:203], a[228:231], a[164:167], v[200:203]\n"
"ds_read_b64_tr_b16 v[176:177], v108 offset:50688\n"
"v_mfma_f32_16x16x32_bf16 v[200:203], a[232:235], a[168:171], v[200:203]\n"
"v_exp_f32 v193, v193\n"
"v_mfma_f32_16x16x32_bf16 v[200:203], a[236:239], a[172:175], v[200:203]\n"
"ds_read_b64_tr_b16 v[178:179], v108 offset:54784\n"
"v_cvt_pk_bf16_f32 v224, v192, v193\n"
"v_mfma_f32_16x16x32_bf16 v[204:207], a[252:255], a[172:175], v[248:251]\n"
"v_exp_f32 v194, v194\n"
"v_mfma_f32_16x16x32_bf16 v[204:207], a[248:251], a[168:171], v[204:207]\n"
"ds_read_b64_tr_b16 v[180:181], v108 offset:58880\n"
"v_mfma_f32_16x16x32_bf16 v[204:207], a[244:247], a[164:167], v[204:207]\n"
"v_exp_f32 v195, v195\n"
"v_mfma_f32_16x16x32_bf16 v[204:207], a[240:243], a[160:163], v[204:207]\n"
"ds_read_b64_tr_b16 v[182:183], v108 offset:62976\n"
"v_cvt_pk_bf16_f32 v225, v194, v195\n"
"v_mfma_f32_16x16x32_bf16 a[4:7], v[16:19], v[232:235], a[4:7]\n"
"v_exp_f32 v196, v196\n"
"v_mfma_f32_16x16x32_bf16 a[4:7], v[20:23], v[236:239], a[4:7]\n"
"ds_read_b64_tr_b16 v[184:185], v109 offset:50688\n"
"v_mfma_f32_16x16x32_bf16 a[20:23], v[28:31], v[236:239], a[20:23]\n"
"v_exp_f32 v197, v197\n"
"v_mfma_f32_16x16x32_bf16 a[20:23], v[24:27], v[232:235], a[20:23]\n"
"ds_read_b64_tr_b16 v[186:187], v109 offset:54784\n"
"v_cvt_pk_bf16_f32 v226, v196, v197\n"
"v_mfma_f32_16x16x32_bf16 a[36:39], v[32:35], v[232:235], a[36:39]\n"
"v_exp_f32 v198, v198\n"
"v_mfma_f32_16x16x32_bf16 a[36:39], v[36:39], v[236:239], a[36:39]\n"
"ds_read_b64_tr_b16 v[188:189], v109 offset:58880\n"
"v_mfma_f32_16x16x32_bf16 a[52:55], v[44:47], v[236:239], a[52:55]\n"
"v_exp_f32 v199, v199\n"
"v_mfma_f32_16x16x32_bf16 a[52:55], v[40:43], v[232:235], a[52:55]\n"
"ds_read_b64_tr_b16 v[190:191], v109 offset:62976\n"
"v_cvt_pk_bf16_f32 v227, v198, v199\n"
"v_mfma_f32_16x16x32_bf16 a[68:71], v[48:51], v[232:235], a[68:71]\n"
"v_mfma_f32_16x16x32_bf16 a[68:71], v[52:55], v[236:239], a[68:71]\n"
"v_exp_f32 v200, v200\n"
"v_mfma_f32_16x16x32_bf16 a[84:87], v[60:63], v[236:239], a[84:87]\n"
"v_mfma_f32_16x16x32_bf16 a[84:87], v[56:59], v[232:235], a[84:87]\n"
"v_exp_f32 v201, v201\n"
"v_mfma_f32_16x16x32_bf16 a[100:103], v[64:67], v[232:235], a[100:103]\n"
"v_cvt_pk_bf16_f32 v228, v200, v201\n"
"v_mfma_f32_16x16x32_bf16 a[100:103], v[68:71], v[236:239], a[100:103]\n"
"v_exp_f32 v202, v202\n"
"v_mfma_f32_16x16x32_bf16 a[116:119], v[76:79], v[236:239], a[116:119]\n"
"v_mfma_f32_16x16x32_bf16 a[116:119], v[72:75], v[232:235], a[116:119]\n"
"v_exp_f32 v203, v203\n"
"v_mfma_f32_16x16x32_bf16 v[84:87], v[96:99], v[232:235], v[84:87]\n"
"v_cvt_pk_bf16_f32 v229, v202, v203\n"
"v_mfma_f32_16x16x32_bf16 v[84:87], v[96:99], v[236:239], v[84:87]\n"
"v_exp_f32 v204, v204\n"
"v_mfma_f32_16x16x32_bf16 v[208:211], a[192:195], a[176:179], v[252:255]\n"
"v_mfma_f32_16x16x32_bf16 v[208:211], a[196:199], a[180:183], v[208:211]\n"
"v_exp_f32 v205, v205\n"
"v_mfma_f32_16x16x32_bf16 v[208:211], a[200:203], a[184:187], v[208:211]\n"
"ds_read_b128 a[192:195], v104 offset:49152\n"
"v_cvt_pk_bf16_f32 v230, v204, v205\n"
"v_mfma_f32_16x16x32_bf16 v[208:211], a[204:207], a[188:191], v[208:211]\n"
"v_exp_f32 v206, v206\n"
"v_mfma_f32_16x16x32_bf16 v[212:215], a[220:223], a[188:191], v[252:255]\n"
"ds_read_b128 a[196:199], v105 offset:49152\n"
"v_mfma_f32_16x16x32_bf16 v[212:215], a[216:219], a[184:187], v[212:215]\n"
"v_exp_f32 v207, v207\n"
"v_mfma_f32_16x16x32_bf16 v[212:215], a[212:215], a[180:183], v[212:215]\n"
"ds_read_b128 a[200:203], v106 offset:49152\n"
"v_cvt_pk_bf16_f32 v231, v206, v207\n"
"v_mfma_f32_16x16x32_bf16 v[212:215], a[208:211], a[176:179], v[212:215]\n"
"ds_read_b128 a[204:207], v107 offset:49152\n"
"v_mfma_f32_16x16x32_bf16 v[216:219], a[224:227], a[176:179], v[252:255]\n"
"v_exp_f32 v208, v208\n"
"v_mfma_f32_16x16x32_bf16 v[216:219], a[228:231], a[180:183], v[216:219]\n"
"ds_read_b128 a[208:211], v104 offset:53248\n"
"v_mfma_f32_16x16x32_bf16 v[216:219], a[232:235], a[184:187], v[216:219]\n"
"v_exp_f32 v209, v209\n"
"v_mfma_f32_16x16x32_bf16 v[216:219], a[236:239], a[188:191], v[216:219]\n"
"ds_read_b128 a[212:215], v105 offset:53248\n"
"v_cvt_pk_bf16_f32 v232, v208, v209\n"
"v_mfma_f32_16x16x32_bf16 v[220:223], a[252:255], a[188:191], v[252:255]\n"
"v_exp_f32 v210, v210\n"
"v_mfma_f32_16x16x32_bf16 v[220:223], a[248:251], a[184:187], v[220:223]\n"
"ds_read_b128 a[216:219], v106 offset:53248\n"
"v_mfma_f32_16x16x32_bf16 v[220:223], a[244:247], a[180:183], v[220:223]\n"
"v_exp_f32 v211, v211\n"
"v_mfma_f32_16x16x32_bf16 v[220:223], a[240:243], a[176:179], v[220:223]\n"
"ds_read_b128 a[220:223], v107 offset:53248\n"
"v_cvt_pk_bf16_f32 v233, v210, v211\n"
"v_mfma_f32_16x16x32_bf16 a[8:11], v[16:19], v[224:227], a[8:11]\n"
"v_exp_f32 v212, v212\n"
"v_mfma_f32_16x16x32_bf16 a[8:11], v[20:23], v[228:231], a[8:11]\n"
"ds_read_b128 a[224:227], v104 offset:57344\n"
"v_mfma_f32_16x16x32_bf16 a[24:27], v[28:31], v[228:231], a[24:27]\n"
"v_exp_f32 v213, v213\n"
"v_mfma_f32_16x16x32_bf16 a[24:27], v[24:27], v[224:227], a[24:27]\n"
"ds_read_b128 a[228:231], v105 offset:57344\n"
"v_cvt_pk_bf16_f32 v234, v212, v213\n"
"v_mfma_f32_16x16x32_bf16 a[40:43], v[32:35], v[224:227], a[40:43]\n"
"v_exp_f32 v214, v214\n"
"v_mfma_f32_16x16x32_bf16 a[40:43], v[36:39], v[228:231], a[40:43]\n"
"ds_read_b128 a[232:235], v106 offset:57344\n"
"v_mfma_f32_16x16x32_bf16 a[56:59], v[44:47], v[228:231], a[56:59]\n"
"v_exp_f32 v215, v215\n"
"v_mfma_f32_16x16x32_bf16 a[56:59], v[40:43], v[224:227], a[56:59]\n"
"ds_read_b128 a[236:239], v107 offset:57344\n"
"v_cvt_pk_bf16_f32 v235, v214, v215\n"
"v_mfma_f32_16x16x32_bf16 a[72:75], v[48:51], v[224:227], a[72:75]\n"
"ds_read_b128 a[240:243], v104 offset:61440\n"
"v_mfma_f32_16x16x32_bf16 a[72:75], v[52:55], v[228:231], a[72:75]\n"
"v_exp_f32 v216, v216\n"
"v_mfma_f32_16x16x32_bf16 a[88:91], v[60:63], v[228:231], a[88:91]\n"
"ds_read_b128 a[244:247], v105 offset:61440\n"
"v_mfma_f32_16x16x32_bf16 a[88:91], v[56:59], v[224:227], a[88:91]\n"
"v_exp_f32 v217, v217\n"
"v_mfma_f32_16x16x32_bf16 a[104:107], v[64:67], v[224:227], a[104:107]\n"
"ds_read_b128 a[248:251], v106 offset:61440\n"
"v_cvt_pk_bf16_f32 v236, v216, v217\n"
"v_mfma_f32_16x16x32_bf16 a[104:107], v[68:71], v[228:231], a[104:107]\n"
"v_exp_f32 v218, v218\n"
"v_mfma_f32_16x16x32_bf16 a[120:123], v[76:79], v[228:231], a[120:123]\n"
"ds_read_b128 a[252:255], v107 offset:61440\n"
"v_mfma_f32_16x16x32_bf16 a[120:123], v[72:75], v[224:227], a[120:123]\n"
"v_exp_f32 v219, v219\n"
"v_mfma_f32_16x16x32_bf16 v[88:91], v[96:99], v[224:227], v[88:91]\n"
"v_cvt_pk_bf16_f32 v237, v218, v219\n"
"v_mfma_f32_16x16x32_bf16 v[88:91], v[96:99], v[228:231], v[88:91]\n"
"s_waitcnt lgkmcnt(4)\n"
"v_exp_f32 v220, v220\n"
"v_mfma_f32_16x16x32_bf16 v[192:195], a[192:195], a[128:131], v[240:243]\n"
"v_mfma_f32_16x16x32_bf16 v[192:195], a[196:199], a[132:135], v[192:195]\n"
"v_exp_f32 v221, v221\n"
"v_mfma_f32_16x16x32_bf16 v[192:195], a[200:203], a[136:139], v[192:195]\n"
"v_cvt_pk_bf16_f32 v238, v220, v221\n"
"v_mfma_f32_16x16x32_bf16 v[192:195], a[204:207], a[140:143], v[192:195]\n"
"v_exp_f32 v222, v222\n"
"v_mfma_f32_16x16x32_bf16 v[196:199], a[220:223], a[140:143], v[240:243]\n"
"v_mfma_f32_16x16x32_bf16 v[196:199], a[216:219], a[136:139], v[196:199]\n"
"v_exp_f32 v223, v223\n"
"v_mfma_f32_16x16x32_bf16 v[196:199], a[212:215], a[132:135], v[196:199]\n"
"v_cvt_pk_bf16_f32 v239, v222, v223\n"
"v_mfma_f32_16x16x32_bf16 v[196:199], a[208:211], a[128:131], v[196:199]\n"
"v_mfma_f32_16x16x32_bf16 v[200:203], a[224:227], a[128:131], v[240:243]\n"
"s_waitcnt vmcnt(0) lgkmcnt(0)\n"
"s_barrier\n"
"s_mov_b64 s[98:99], %7\n"
"s_mov_b64 s[76:77], %4\n"
"s_sub_u32 s76, s76, 0x24000\n"
"s_subb_u32 s77, s77, 0\n"
"s_cmp_lg_u32 s73, 0\n"
"s_cselect_b32 s60, s76, s98\n"
"s_cselect_b32 s61, s77, s99\n"
"s_and_b32 s61, s61, 0xffff\n"
"s_cmp_lg_u32 s73, 0\n"
"s_mov_b32 s62, 0x510000\n"
"s_cmov_b32 s62, 0x4a4000\n"
"s_mov_b32 s63, 0x20000\n"
"s_mov_b64 s[98:99], %8\n"
"s_add_u32 s76, s98, 0x1fff000\n"
"s_addc_u32 s77, s99, 0\n"
"s_add_u32 s98, s98, 0x800000\n"
"s_addc_u32 s99, s99, 0\n"
"s_cmp_lg_u32 s73, 0\n"
"s_cselect_b32 s64, s76, s98\n"
"s_cselect_b32 s65, s77, s99\n"
"s_and_b32 s65, s65, 0xffff\n"
"s_cmp_lg_u32 s73, 0\n"
"s_mov_b32 s66, 0x1200000\n"
"s_cmov_b32 s66, 0x1001000\n"
"s_mov_b32 s67, 0x20000\n"
"s_cmp_lg_u32 s73, 0\n"
"s_mov_b32 s98, 0x480\n"
"s_cmov_b32 s98, 0x400\n"
"s_mov_b32 s99, 0x90\n"
"s_cmov_b32 s99, 0x80\n"
"s_cselect_b32 s87, 2, 0\n"
"s_lshr_b32 s77, s72, 1\n"
"s_lshl_b32 s77, s77, 5\n"
"s_mul_i32 s84, s81, s98\n"
"s_add_u32 s84, s84, s77\n"
"s_mul_i32 s84, s84, 0x480\n"
"s_mul_i32 s76, s82, 0x120\n"
"s_add_u32 s84, s84, s76\n"
"s_lshl_b32 s76, s75, 4\n"
"s_lshl_b32 s77, s72, 2\n"
"s_add_u32 s76, s76, s77\n"
"s_mul_i32 s85, s76, s99\n"
"s_lshr_b32 s77, s99, 2\n"
"s_mul_i32 s77, s77, s81\n"
"s_add_u32 s85, s85, s77\n"
"s_lshl_b32 s85, s85, 10\n"
"s_lshl_b32 s86, s99, 10\n"
"s_lshr_b32 s76, s72, 1\n"
"s_mul_i32 s78, s76, 0x2400\n"
"s_and_b32 s76, s72, 1\n"
"s_mul_i32 s76, s76, 0x1400\n"
"s_add_u32 s78, s78, s76\n"
"s_lshl_b32 s76, s83, 3\n"
"s_add_u32 s76, s76, s80\n"
"s_lshl_b32 s76, s76, 2\n"
"s_add_u32 s76, s76, s72\n"
"s_mul_i32 s90, s76, 0x4800\n"
"s_lshl_b32 s76, s83, 5\n"
"s_add_u32 s76, s76, s72\n"
"s_mul_i32 s91, s76, 0x4800\n"
"s_lshl_b32 s76, s75, 8\n"
"s_lshl_b32 s77, s72, 6\n"
"s_add_u32 s76, s76, s77\n"
"s_mul_i32 s76, s76, 0x480\n"
"s_mul_i32 s77, s82, 0x120\n"
"s_add_u32 s94, s76, s77\n"
"s_mov_b64 s[76:77], %8\n"
"s_add_u32 s76, s76, 0x3800000\n"
"s_addc_u32 s77, s77, 0\n"
"s_mov_b32 s52, s76\n"
"s_and_b32 s53, s77, 0xffff\n"
"s_mov_b32 s54, 0x1200000\n"
"s_mov_b32 s55, 0x20000\n"
"s_mov_b64 s[76:77], %0\n"
"s_mov_b32 s56, s76\n"
"s_and_b32 s57, s77, 0xffff\n"
"s_mov_b32 s58, 0x240000\n"
"s_mov_b32 s59, 0x20000\n"
"s_mul_i32 s95, s87, 0x12000\n"
"s_add_u32 s95, s95, s84\n"
"s_mul_i32 s96, s87, 0x4800\n"
"s_add_u32 s96, s96, s78\n"
"v_exp_f32 v192, v192\n"
"v_mfma_f32_16x16x32_bf16 v[200:203], a[228:231], a[132:135], v[200:203]\n"
"v_mfma_f32_16x16x32_bf16 v[200:203], a[232:235], a[136:139], v[200:203]\n"
"s_add_u32 s77, s95, 0\n"
"s_add_u32 m0, s96, 0\n"
"v_exp_f32 v193, v193\n"
"v_mfma_f32_16x16x32_bf16 v[200:203], a[236:239], a[140:143], v[200:203]\n"
"buffer_load_dwordx4 v6, s[60:63], s77 offen lds\n"
"v_cvt_pk_bf16_f32 v224, v192, v193\n"
"v_mfma_f32_16x16x32_bf16 v[204:207], a[252:255], a[140:143], v[240:243]\n"
"v_exp_f32 v194, v194\n"
"v_mfma_f32_16x16x32_bf16 v[204:207], a[248:251], a[136:139], v[204:207]\n"
"v_mfma_f32_16x16x32_bf16 v[204:207], a[244:247], a[132:135], v[204:207]\n"
"s_add_u32 s77, s95, 0\n"
"s_add_u32 m0, s96, 0x400\n"
"v_exp_f32 v195, v195\n"
"v_mfma_f32_16x16x32_bf16 v[204:207], a[240:243], a[128:131], v[204:207]\n"
"buffer_load_dwordx4 v7, s[60:63], s77 offen lds\n"
"v_cvt_pk_bf16_f32 v225, v194, v195\n"
"v_mfma_f32_16x16x32_bf16 a[12:15], v[16:19], v[232:235], a[12:15]\n"
"v_exp_f32 v196, v196\n"
"v_mfma_f32_16x16x32_bf16 a[12:15], v[20:23], v[236:239], a[12:15]\n"
"v_mfma_f32_16x16x32_bf16 a[28:31], v[28:31], v[236:239], a[28:31]\n"
"s_add_u32 s77, s95, 0\n"
"s_add_u32 m0, s96, 0x800\n"
"v_exp_f32 v197, v197\n"
"v_mfma_f32_16x16x32_bf16 a[28:31], v[24:27], v[232:235], a[28:31]\n"
"buffer_load_dwordx4 v8, s[60:63], s77 offen lds\n"
"v_cvt_pk_bf16_f32 v226, v196, v197\n"
"v_mfma_f32_16x16x32_bf16 a[44:47], v[32:35], v[232:235], a[44:47]\n"
"v_exp_f32 v198, v198\n"
"v_mfma_f32_16x16x32_bf16 a[44:47], v[36:39], v[236:239], a[44:47]\n"
"v_mfma_f32_16x16x32_bf16 a[60:63], v[44:47], v[236:239], a[60:63]\n"
"s_add_u32 s77, s95, 0\n"
"s_add_u32 m0, s96, 0xc00\n"
"v_exp_f32 v199, v199\n"
"v_mfma_f32_16x16x32_bf16 a[60:63], v[40:43], v[232:235], a[60:63]\n"
"buffer_load_dwordx4 v9, s[60:63], s77 offen lds\n"
"v_cvt_pk_bf16_f32 v227, v198, v199\n"
"v_mfma_f32_16x16x32_bf16 a[76:79], v[48:51], v[232:235], a[76:79]\n"
"v_mfma_f32_16x16x32_bf16 a[76:79], v[52:55], v[236:239], a[76:79]\n"
"s_add_u32 s77, s95, 0\n"
"s_add_u32 m0, s96, 0x1000\n"
"v_exp_f32 v200, v200\n"
"v_mfma_f32_16x16x32_bf16 a[92:95], v[60:63], v[236:239], a[92:95]\n"
"s_bitcmp1_b32 s72, 0\n"
"s_cbranch_scc1 NOP5_1_%=\n"
"buffer_load_dwordx4 v10, s[60:63], s77 offen lds\n"
"NOP5_1_%=:\n"
"v_mfma_f32_16x16x32_bf16 a[92:95], v[56:59], v[232:235], a[92:95]\n"
"v_exp_f32 v201, v201\n"
"v_mfma_f32_16x16x32_bf16 a[108:111], v[64:67], v[232:235], a[108:111]\n"
"v_cvt_pk_bf16_f32 v228, v200, v201\n"
"v_mfma_f32_16x16x32_bf16 a[108:111], v[68:71], v[236:239], a[108:111]\n"
"s_add_u32 s77, s95, 0x12000\n"
"s_add_u32 m0, s96, 0x4800\n"
"v_exp_f32 v202, v202\n"
"v_mfma_f32_16x16x32_bf16 a[124:127], v[76:79], v[236:239], a[124:127]\n"
"buffer_load_dwordx4 v6, s[60:63], s77 offen lds\n"
"v_mfma_f32_16x16x32_bf16 a[124:127], v[72:75], v[232:235], a[124:127]\n"
"v_exp_f32 v203, v203\n"
"v_mfma_f32_16x16x32_bf16 v[92:95], v[96:99], v[232:235], v[92:95]\n"
"v_cvt_pk_bf16_f32 v229, v202, v203\n"
"v_mfma_f32_16x16x32_bf16 v[92:95], v[96:99], v[236:239], v[92:95]\n"
"s_add_u32 s77, s95, 0x12000\n"
"s_add_u32 m0, s96, 0x4c00\n"
"v_exp_f32 v204, v204\n"
"v_mfma_f32_16x16x32_bf16 v[208:211], a[192:195], a[144:147], v[244:247]\n"
"buffer_load_dwordx4 v7, s[60:63], s77 offen lds\n"
"v_mfma_f32_16x16x32_bf16 v[208:211], a[196:199], a[148:151], v[208:211]\n"
"v_exp_f32 v205, v205\n"
"v_mfma_f32_16x16x32_bf16 v[208:211], a[200:203], a[152:155], v[208:211]\n"
"v_cvt_pk_bf16_f32 v230, v204, v205\n"
"v_mov_b32 v72, 0\n"
"v_mov_b32 v74, 0\n"
"v_mfma_f32_16x16x32_bf16 v[208:211], a[204:207], a[156:159], v[208:211]\n"
"s_add_u32 s77, s95, 0x12000\n"
"s_add_u32 m0, s96, 0x5000\n"
"v_exp_f32 v206, v206\n"
"v_mov_b32 v22, 0\n"
"v_mfma_f32_16x16x32_bf16 v[212:215], a[220:223], a[156:159], v[244:247]\n"
"buffer_load_dwordx4 v8, s[60:63], s77 offen lds\n"
"v_mov_b32 v73, 0\n"
"v_mfma_f32_16x16x32_bf16 v[212:215], a[216:219], a[152:155], v[212:215]\n"
"v_exp_f32 v207, v207\n"
"v_mov_b32 v75, 0\n"
"v_mfma_f32_16x16x32_bf16 v[212:215], a[212:215], a[148:151], v[212:215]\n"
"s_add_u32 s77, s95, 0x12000\n"
"s_add_u32 m0, s96, 0x5400\n"
"v_cvt_pk_bf16_f32 v231, v206, v207\n"
"v_mov_b32 v23, 0\n"
"v_mfma_f32_16x16x32_bf16 v[212:215], a[208:211], a[144:147], v[212:215]\n"
"buffer_load_dwordx4 v9, s[60:63], s77 offen lds\n"
"v_mfma_f32_16x16x32_bf16 v[216:219], a[224:227], a[144:147], v[244:247]\n"
"v_exp_f32 v208, v208\n"
"v_mfma_f32_16x16x32_bf16 v[216:219], a[228:231], a[148:151], v[216:219]\n"
"v_mfma_f32_16x16x32_bf16 v[216:219], a[232:235], a[152:155], v[216:219]\n"
"s_add_u32 s77, s95, 0x12000\n"
"s_add_u32 m0, s96, 0x5800\n"
"v_exp_f32 v209, v209\n"
"v_mfma_f32_16x16x32_bf16 v[216:219], a[236:239], a[156:159], v[216:219]\n"
"s_bitcmp1_b32 s72, 0\n"
"s_cbranch_scc1 NOP5_2_%=\n"
"buffer_load_dwordx4 v10, s[60:63], s77 offen lds\n"
"NOP5_2_%=:\n"
"v_cvt_pk_bf16_f32 v232, v208, v209\n"
"v_mfma_f32_16x16x32_bf16 v[220:223], a[252:255], a[156:159], v[244:247]\n"
"v_exp_f32 v210, v210\n"
"v_mfma_f32_16x16x32_bf16 v[220:223], a[248:251], a[152:155], v[220:223]\n"
"v_mfma_f32_16x16x32_bf16 v[220:223], a[244:247], a[148:151], v[220:223]\n"
"s_add_u32 s77, s95, 0x24000\n"
"s_add_u32 m0, s96, 0x9000\n"
"v_exp_f32 v211, v211\n"
"v_mfma_f32_16x16x32_bf16 v[220:223], a[240:243], a[144:147], v[220:223]\n"
"buffer_load_dwordx4 v6, s[60:63], s77 offen lds\n"
"v_cvt_pk_bf16_f32 v233, v210, v211\n"
"v_mfma_f32_16x16x32_bf16 a[0:3], v[128:131], v[224:227], a[0:3]\n"
"v_exp_f32 v212, v212\n"
"v_mfma_f32_16x16x32_bf16 a[0:3], v[132:135], v[228:231], a[0:3]\n"
"v_mfma_f32_16x16x32_bf16 a[16:19], v[140:143], v[228:231], a[16:19]\n"
"s_add_u32 s77, s95, 0x24000\n"
"s_add_u32 m0, s96, 0x9400\n"
"v_exp_f32 v213, v213\n"
"v_mfma_f32_16x16x32_bf16 a[16:19], v[136:139], v[224:227], a[16:19]\n"
"buffer_load_dwordx4 v7, s[60:63], s77 offen lds\n"
"v_cvt_pk_bf16_f32 v234, v212, v213\n"
"v_mfma_f32_16x16x32_bf16 a[32:35], v[144:147], v[224:227], a[32:35]\n"
"v_exp_f32 v214, v214\n"
"v_mfma_f32_16x16x32_bf16 a[32:35], v[148:151], v[228:231], a[32:35]\n"
"v_mfma_f32_16x16x32_bf16 a[48:51], v[156:159], v[228:231], a[48:51]\n"
"s_add_u32 s77, s95, 0x24000\n"
"s_add_u32 m0, s96, 0x9800\n"
"v_exp_f32 v215, v215\n"
"v_mfma_f32_16x16x32_bf16 a[48:51], v[152:155], v[224:227], a[48:51]\n"
"buffer_load_dwordx4 v8, s[60:63], s77 offen lds\n"
"v_cvt_pk_bf16_f32 v235, v214, v215\n"
"v_mfma_f32_16x16x32_bf16 a[64:67], v[160:163], v[224:227], a[64:67]\n"
"v_mfma_f32_16x16x32_bf16 a[64:67], v[164:167], v[228:231], a[64:67]\n"
"s_add_u32 s77, s95, 0x24000\n"
"s_add_u32 m0, s96, 0x9c00\n"
"v_exp_f32 v216, v216\n"
"v_mfma_f32_16x16x32_bf16 a[80:83], v[172:175], v[228:231], a[80:83]\n"
"buffer_load_dwordx4 v9, s[60:63], s77 offen lds\n"
"v_mfma_f32_16x16x32_bf16 a[80:83], v[168:171], v[224:227], a[80:83]\n"
"v_exp_f32 v217, v217\n"
"v_mfma_f32_16x16x32_bf16 a[96:99], v[176:179], v[224:227], a[96:99]\n"
"v_cvt_pk_bf16_f32 v236, v216, v217\n"
"v_mfma_f32_16x16x32_bf16 a[96:99], v[180:183], v[228:231], a[96:99]\n"
"s_add_u32 s77, s95, 0x24000\n"
"s_add_u32 m0, s96, 0xa000\n"
"v_exp_f32 v218, v218\n"
"v_mfma_f32_16x16x32_bf16 a[112:115], v[188:191], v[228:231], a[112:115]\n"
"s_bitcmp1_b32 s72, 0\n"
"s_cbranch_scc1 NOP5_3_%=\n"
"buffer_load_dwordx4 v10, s[60:63], s77 offen lds\n"
"NOP5_3_%=:\n"
"v_mfma_f32_16x16x32_bf16 a[112:115], v[184:187], v[224:227], a[112:115]\n"
"v_exp_f32 v219, v219\n"
"v_mfma_f32_16x16x32_bf16 v[80:83], v[96:99], v[224:227], v[80:83]\n"
"v_cvt_pk_bf16_f32 v237, v218, v219\n"
"v_mfma_f32_16x16x32_bf16 v[80:83], v[96:99], v[228:231], v[80:83]\n"
"s_add_u32 s77, s95, 0x36000\n"
"s_add_u32 m0, s96, 0xd800\n"
"v_exp_f32 v220, v220\n"
"v_mfma_f32_16x16x32_bf16 v[192:195], a[192:195], a[160:163], v[248:251]\n"
"buffer_load_dwordx4 v6, s[60:63], s77 offen lds\n"
"v_mfma_f32_16x16x32_bf16 v[192:195], a[196:199], a[164:167], v[192:195]\n"
"v_exp_f32 v221, v221\n"
"v_mfma_f32_16x16x32_bf16 v[192:195], a[200:203], a[168:171], v[192:195]\n"
"v_cvt_pk_bf16_f32 v238, v220, v221\n"
"v_mfma_f32_16x16x32_bf16 v[192:195], a[204:207], a[172:175], v[192:195]\n"
"s_add_u32 s77, s95, 0x36000\n"
"s_add_u32 m0, s96, 0xdc00\n"
"v_exp_f32 v222, v222\n"
"v_mfma_f32_16x16x32_bf16 v[196:199], a[220:223], a[172:175], v[248:251]\n"
"buffer_load_dwordx4 v7, s[60:63], s77 offen lds\n"
"v_rcp_f32 v76, v80\n"
"v_mfma_f32_16x16x32_bf16 v[196:199], a[216:219], a[168:171], v[196:199]\n"
"v_exp_f32 v223, v223\n"
"v_accvgpr_read_b32 v24, a0\n"
"v_mfma_f32_16x16x32_bf16 v[196:199], a[212:215], a[164:167], v[196:199]\n"
"s_add_u32 s77, s95, 0x36000\n"
"s_add_u32 m0, s96, 0xe000\n"
"v_cvt_pk_bf16_f32 v239, v222, v223\n"
"v_accvgpr_read_b32 v28, a16\n"
"v_mfma_f32_16x16x32_bf16 v[196:199], a[208:211], a[160:163], v[196:199]\n"
"buffer_load_dwordx4 v8, s[60:63], s77 offen lds\n"
"v_accvgpr_read_b32 v25, a1\n"
"v_mfma_f32_16x16x32_bf16 v[200:203], a[224:227], a[160:163], v[248:251]\n"
"v_exp_f32 v192, v192\n"
"v_accvgpr_read_b32 v29, a17\n"
"v_mfma_f32_16x16x32_bf16 v[200:203], a[228:231], a[164:167], v[200:203]\n"
"v_accvgpr_read_b32 v26, a2\n"
"v_accvgpr_read_b32 v30, a18\n"
"v_accvgpr_read_b32 v27, a3\n"
"v_mfma_f32_16x16x32_bf16 v[200:203], a[232:235], a[168:171], v[200:203]\n"
"s_add_u32 s77, s95, 0x36000\n"
"s_add_u32 m0, s96, 0xe400\n"
"v_exp_f32 v193, v193\n"
"v_accvgpr_read_b32 v31, a19\n"
"v_mfma_f32_16x16x32_bf16 v[200:203], a[236:239], a[172:175], v[200:203]\n"
"buffer_load_dwordx4 v9, s[60:63], s77 offen lds\n"
"v_cvt_pk_bf16_f32 v224, v192, v193\n"
"v_mul_f32 v24, v24, v76\n"
"v_mfma_f32_16x16x32_bf16 v[204:207], a[252:255], a[172:175], v[248:251]\n"
"v_exp_f32 v194, v194\n"
"v_mul_f32 v25, v25, v76\n"
"v_mfma_f32_16x16x32_bf16 v[204:207], a[248:251], a[168:171], v[204:207]\n"
"v_mul_f32 v26, v26, v76\n"
"v_mul_f32 v27, v27, v76\n"
"v_mul_f32 v28, v28, v76\n"
"v_mfma_f32_16x16x32_bf16 v[204:207], a[244:247], a[164:167], v[204:207]\n"
"s_add_u32 s77, s95, 0x36000\n"
"s_add_u32 m0, s96, 0xe800\n"
"v_exp_f32 v195, v195\n"
"v_mul_f32 v29, v29, v76\n"
"v_mfma_f32_16x16x32_bf16 v[204:207], a[240:243], a[160:163], v[204:207]\n"
"s_bitcmp1_b32 s72, 0\n"
"s_cbranch_scc1 NOP5_4_%=\n"
"buffer_load_dwordx4 v10, s[60:63], s77 offen lds\n"
"NOP5_4_%=:\n"
"v_cvt_pk_bf16_f32 v225, v194, v195\n"
"v_mul_f32 v30, v30, v76\n"
"v_mfma_f32_16x16x32_bf16 a[4:7], v[128:131], v[232:235], a[4:7]\n"
"v_exp_f32 v196, v196\n"
"v_mul_f32 v31, v31, v76\n"
"v_mfma_f32_16x16x32_bf16 a[4:7], v[132:135], v[236:239], a[4:7]\n"
"v_cvt_pk_bf16_f32 v32, v24, v25\n"
"v_cvt_pk_bf16_f32 v33, v26, v27\n"
"v_cvt_pk_bf16_f32 v34, v28, v29\n"
"v_mfma_f32_16x16x32_bf16 a[20:23], v[140:143], v[236:239], a[20:23]\n"
"v_exp_f32 v197, v197\n"
"v_cvt_pk_bf16_f32 v35, v30, v31\n"
"v_mfma_f32_16x16x32_bf16 a[20:23], v[136:139], v[232:235], a[20:23]\n"
"v_cvt_pk_bf16_f32 v226, v196, v197\n"
"v_fma_f32 v72, v24, 0, v72\n"
"v_fma_f32 v73, v25, 0, v73\n"
"v_mfma_f32_16x16x32_bf16 a[36:39], v[144:147], v[232:235], a[36:39]\n"
"v_exp_f32 v198, v198\n"
"v_fma_f32 v72, v26, 0, v72\n"
"v_mfma_f32_16x16x32_bf16 a[36:39], v[148:151], v[236:239], a[36:39]\n"
"v_fma_f32 v73, v27, 0, v73\n"
"v_fma_f32 v72, v28, 0, v72\n"
"v_fma_f32 v73, v29, 0, v73\n"
"v_mfma_f32_16x16x32_bf16 a[52:55], v[156:159], v[236:239], a[52:55]\n"
"v_exp_f32 v199, v199\n"
"v_fma_f32 v72, v30, 0, v72\n"
"v_mfma_f32_16x16x32_bf16 a[52:55], v[152:155], v[232:235], a[52:55]\n"
"v_cvt_pk_bf16_f32 v227, v198, v199\n"
"v_fma_f32 v73, v31, 0, v73\n"
"v_mfma_f32_16x16x32_bf16 a[68:71], v[160:163], v[232:235], a[68:71]\n"
"s_add_u32 s98, s97, 0\n"
"buffer_store_dwordx4 v[32:35], v121, s[48:51], s98 offen sc1\n"
"v_accvgpr_read_b32 v36, a32\n"
"v_mfma_f32_16x16x32_bf16 a[68:71], v[164:167], v[236:239], a[68:71]\n"
"v_exp_f32 v200, v200\n"
"v_accvgpr_read_b32 v40, a48\n"
"v_mfma_f32_16x16x32_bf16 a[84:87], v[172:175], v[236:239], a[84:87]\n"
"v_accvgpr_read_b32 v37, a33\n"
"v_accvgpr_read_b32 v41, a49\n"
"v_accvgpr_read_b32 v38, a34\n"
"v_mfma_f32_16x16x32_bf16 a[84:87], v[168:171], v[232:235], a[84:87]\n"
"v_exp_f32 v201, v201\n"
"v_accvgpr_read_b32 v42, a50\n"
"v_mfma_f32_16x16x32_bf16 a[100:103], v[176:179], v[232:235], a[100:103]\n"
"v_cvt_pk_bf16_f32 v228, v200, v201\n"
"v_accvgpr_read_b32 v39, a35\n"
"v_accvgpr_read_b32 v43, a51\n"
"v_mfma_f32_16x16x32_bf16 a[100:103], v[180:183], v[236:239], a[100:103]\n"
"v_exp_f32 v202, v202\n"
"v_mul_f32 v36, v36, v76\n"
"v_mfma_f32_16x16x32_bf16 a[116:119], v[188:191], v[236:239], a[116:119]\n"
"v_mul_f32 v37, v37, v76\n"
"v_mul_f32 v38, v38, v76\n"
"v_mul_f32 v39, v39, v76\n"
"v_mfma_f32_16x16x32_bf16 a[116:119], v[184:187], v[232:235], a[116:119]\n"
"v_exp_f32 v203, v203\n"
"v_mul_f32 v40, v40, v76\n"
"v_mfma_f32_16x16x32_bf16 v[84:87], v[96:99], v[232:235], v[84:87]\n"
"v_cvt_pk_bf16_f32 v229, v202, v203\n"
"v_mul_f32 v41, v41, v76\n"
"v_mul_f32 v42, v42, v76\n"
"v_mfma_f32_16x16x32_bf16 v[84:87], v[96:99], v[236:239], v[84:87]\n"
"v_exp_f32 v204, v204\n"
"v_mul_f32 v43, v43, v76\n"
"v_mfma_f32_16x16x32_bf16 v[208:211], a[192:195], a[176:179], v[252:255]\n"
"v_cvt_pk_bf16_f32 v44, v36, v37\n"
"v_cvt_pk_bf16_f32 v45, v38, v39\n"
"v_cvt_pk_bf16_f32 v46, v40, v41\n"
"v_mfma_f32_16x16x32_bf16 v[208:211], a[196:199], a[180:183], v[208:211]\n"
"v_exp_f32 v205, v205\n"
"v_cvt_pk_bf16_f32 v47, v42, v43\n"
"v_mfma_f32_16x16x32_bf16 v[208:211], a[200:203], a[184:187], v[208:211]\n"
"v_cvt_pk_bf16_f32 v230, v204, v205\n"
"v_fma_f32 v72, v36, 0, v72\n"
"v_fma_f32 v73, v37, 0, v73\n"
"v_mfma_f32_16x16x32_bf16 v[208:211], a[204:207], a[188:191], v[208:211]\n"
"v_exp_f32 v206, v206\n"
"v_fma_f32 v72, v38, 0, v72\n"
"v_mfma_f32_16x16x32_bf16 v[212:215], a[220:223], a[188:191], v[252:255]\n"
"v_fma_f32 v73, v39, 0, v73\n"
"v_fma_f32 v72, v40, 0, v72\n"
"v_fma_f32 v73, v41, 0, v73\n"
"v_mfma_f32_16x16x32_bf16 v[212:215], a[216:219], a[184:187], v[212:215]\n"
"v_exp_f32 v207, v207\n"
"v_fma_f32 v72, v42, 0, v72\n"
"v_mfma_f32_16x16x32_bf16 v[212:215], a[212:215], a[180:183], v[212:215]\n"
"v_cvt_pk_bf16_f32 v231, v206, v207\n"
"v_fma_f32 v73, v43, 0, v73\n"
"v_mfma_f32_16x16x32_bf16 v[212:215], a[208:211], a[176:179], v[212:215]\n"
"s_add_u32 s98, s97, 0\n"
"buffer_store_dwordx4 v[44:47], v121, s[48:51], s98 offen offset:1024 sc1\n"
"v_accvgpr_read_b32 v48, a64\n"
"v_mfma_f32_16x16x32_bf16 v[216:219], a[224:227], a[176:179], v[252:255]\n"
"v_exp_f32 v208, v208\n"
"v_accvgpr_read_b32 v52, a80\n"
"v_mfma_f32_16x16x32_bf16 v[216:219], a[228:231], a[180:183], v[216:219]\n"
"v_accvgpr_read_b32 v49, a65\n"
"v_accvgpr_read_b32 v53, a81\n"
"v_accvgpr_read_b32 v50, a66\n"
"v_mfma_f32_16x16x32_bf16 v[216:219], a[232:235], a[184:187], v[216:219]\n"
"v_exp_f32 v209, v209\n"
"v_accvgpr_read_b32 v54, a82\n"
"v_mfma_f32_16x16x32_bf16 v[216:219], a[236:239], a[188:191], v[216:219]\n"
"v_cvt_pk_bf16_f32 v232, v208, v209\n"
"v_accvgpr_read_b32 v51, a67\n"
"v_accvgpr_read_b32 v55, a83\n"
"v_mfma_f32_16x16x32_bf16 v[220:223], a[252:255], a[188:191], v[252:255]\n"
"v_exp_f32 v210, v210\n"
"v_mul_f32 v48, v48, v76\n"
"v_mfma_f32_16x16x32_bf16 v[220:223], a[248:251], a[184:187], v[220:223]\n"
"v_mul_f32 v49, v49, v76\n"
"v_mul_f32 v50, v50, v76\n"
"v_mul_f32 v51, v51, v76\n"
"v_mfma_f32_16x16x32_bf16 v[220:223], a[244:247], a[180:183], v[220:223]\n"
"v_exp_f32 v211, v211\n"
"v_mul_f32 v52, v52, v76\n"
"v_mfma_f32_16x16x32_bf16 v[220:223], a[240:243], a[176:179], v[220:223]\n"
"v_cvt_pk_bf16_f32 v233, v210, v211\n"
"v_mul_f32 v53, v53, v76\n"
"v_mul_f32 v54, v54, v76\n"
"v_mfma_f32_16x16x32_bf16 a[8:11], v[128:131], v[224:227], a[8:11]\n"
"v_exp_f32 v212, v212\n"
"v_mul_f32 v55, v55, v76\n"
"v_mfma_f32_16x16x32_bf16 a[8:11], v[132:135], v[228:231], a[8:11]\n"
"v_cvt_pk_bf16_f32 v56, v48, v49\n"
"v_cvt_pk_bf16_f32 v57, v50, v51\n"
"v_cvt_pk_bf16_f32 v58, v52, v53\n"
"v_mfma_f32_16x16x32_bf16 a[24:27], v[140:143], v[228:231], a[24:27]\n"
"v_exp_f32 v213, v213\n"
"v_cvt_pk_bf16_f32 v59, v54, v55\n"
"v_mfma_f32_16x16x32_bf16 a[24:27], v[136:139], v[224:227], a[24:27]\n"
"v_cvt_pk_bf16_f32 v234, v212, v213\n"
"v_fma_f32 v72, v48, 0, v72\n"
"v_fma_f32 v73, v49, 0, v73\n"
"v_mfma_f32_16x16x32_bf16 a[40:43], v[144:147], v[224:227], a[40:43]\n"
"v_exp_f32 v214, v214\n"
"v_fma_f32 v72, v50, 0, v72\n"
"v_mfma_f32_16x16x32_bf16 a[40:43], v[148:151], v[228:231], a[40:43]\n"
"v_fma_f32 v73, v51, 0, v73\n"
"v_fma_f32 v72, v52, 0, v72\n"
"v_fma_f32 v73, v53, 0, v73\n"
"v_mfma_f32_16x16x32_bf16 a[56:59], v[156:159], v[228:231], a[56:59]\n"
"v_exp_f32 v215, v215\n"
"v_fma_f32 v72, v54, 0, v72\n"
"v_mfma_f32_16x16x32_bf16 a[56:59], v[152:155], v[224:227], a[56:59]\n"
"v_cvt_pk_bf16_f32 v235, v214, v215\n"
"v_fma_f32 v73, v55, 0, v73\n"
"v_mfma_f32_16x16x32_bf16 a[72:75], v[160:163], v[224:227], a[72:75]\n"
"s_add_u32 s98, s97, 0\n"
"buffer_store_dwordx4 v[56:59], v121, s[48:51], s98 offen offset:2048 sc1\n"
"v_accvgpr_read_b32 v60, a96\n"
"v_mfma_f32_16x16x32_bf16 a[72:75], v[164:167], v[228:231], a[72:75]\n"
"v_exp_f32 v216, v216\n"
"v_accvgpr_read_b32 v64, a112\n"
"v_mfma_f32_16x16x32_bf16 a[88:91], v[172:175], v[228:231], a[88:91]\n"
"v_accvgpr_read_b32 v61, a97\n"
"v_accvgpr_read_b32 v65, a113\n"
"v_accvgpr_read_b32 v62, a98\n"
"v_mfma_f32_16x16x32_bf16 a[88:91], v[168:171], v[224:227], a[88:91]\n"
"v_exp_f32 v217, v217\n"
"v_accvgpr_read_b32 v66, a114\n"
"v_mfma_f32_16x16x32_bf16 a[104:107], v[176:179], v[224:227], a[104:107]\n"
"v_cvt_pk_bf16_f32 v236, v216, v217\n"
"v_accvgpr_read_b32 v63, a99\n"
"v_accvgpr_read_b32 v67, a115\n"
"v_mfma_f32_16x16x32_bf16 a[104:107], v[180:183], v[228:231], a[104:107]\n"
"v_exp_f32 v218, v218\n"
"v_mul_f32 v60, v60, v76\n"
"v_mfma_f32_16x16x32_bf16 a[120:123], v[188:191], v[228:231], a[120:123]\n"
"v_mul_f32 v61, v61, v76\n"
"v_mul_f32 v62, v62, v76\n"
"v_mul_f32 v63, v63, v76\n"
"v_mfma_f32_16x16x32_bf16 a[120:123], v[184:187], v[224:227], a[120:123]\n"
"v_exp_f32 v219, v219\n"
"v_mul_f32 v64, v64, v76\n"
"v_mfma_f32_16x16x32_bf16 v[88:91], v[96:99], v[224:227], v[88:91]\n"
"v_cvt_pk_bf16_f32 v237, v218, v219\n"
"v_mul_f32 v65, v65, v76\n"
"v_mfma_f32_16x16x32_bf16 v[88:91], v[96:99], v[228:231], v[88:91]\n"
"v_exp_f32 v220, v220\n"
"v_mul_f32 v66, v66, v76\n"
"v_mul_f32 v67, v67, v76\n"
"v_cvt_pk_bf16_f32 v68, v60, v61\n"
"v_cvt_pk_bf16_f32 v69, v62, v63\n"
"v_cvt_pk_bf16_f32 v70, v64, v65\n"
"v_cvt_pk_bf16_f32 v71, v66, v67\n"
"v_fma_f32 v72, v60, 0, v72\n"
"v_fma_f32 v73, v61, 0, v73\n"
"v_fma_f32 v72, v62, 0, v72\n"
"v_fma_f32 v73, v63, 0, v73\n"
"v_fma_f32 v72, v64, 0, v72\n"
"v_fma_f32 v73, v65, 0, v73\n"
"v_fma_f32 v72, v66, 0, v72\n"
"v_fma_f32 v73, v67, 0, v73\n"
"s_add_u32 s98, s97, 0\n"
"buffer_store_dwordx4 v[68:71], v121, s[48:51], s98 offen offset:3072 sc1\n"
"v_exp_f32 v221, v221\n"
"s_nop 0\n"
"v_cvt_pk_bf16_f32 v238, v220, v221\n"
"v_exp_f32 v222, v222\n"
"v_exp_f32 v223, v223\n"
"s_nop 0\n"
"v_cvt_pk_bf16_f32 v239, v222, v223\n"
"v_mfma_f32_16x16x32_bf16 a[12:15], v[128:131], v[232:235], a[12:15]\n"
"s_nop 0\n"
"v_mfma_f32_16x16x32_bf16 a[12:15], v[132:135], v[236:239], a[12:15]\n"
"v_mfma_f32_16x16x32_bf16 a[28:31], v[140:143], v[236:239], a[28:31]\n"
"v_mfma_f32_16x16x32_bf16 a[28:31], v[136:139], v[232:235], a[28:31]\n"
"v_mfma_f32_16x16x32_bf16 a[44:47], v[144:147], v[232:235], a[44:47]\n"
"v_mfma_f32_16x16x32_bf16 a[44:47], v[148:151], v[236:239], a[44:47]\n"
"v_mfma_f32_16x16x32_bf16 a[60:63], v[156:159], v[236:239], a[60:63]\n"
"v_mfma_f32_16x16x32_bf16 a[60:63], v[152:155], v[232:235], a[60:63]\n"
"v_mfma_f32_16x16x32_bf16 a[76:79], v[160:163], v[232:235], a[76:79]\n"
"v_mfma_f32_16x16x32_bf16 a[76:79], v[164:167], v[236:239], a[76:79]\n"
"v_mfma_f32_16x16x32_bf16 a[92:95], v[172:175], v[236:239], a[92:95]\n"
"v_mfma_f32_16x16x32_bf16 a[92:95], v[168:171], v[232:235], a[92:95]\n"
"v_mfma_f32_16x16x32_bf16 a[108:111], v[176:179], v[232:235], a[108:111]\n"
"v_mfma_f32_16x16x32_bf16 a[108:111], v[180:183], v[236:239], a[108:111]\n"
"v_mfma_f32_16x16x32_bf16 a[124:127], v[188:191], v[236:239], a[124:127]\n"
"v_mfma_f32_16x16x32_bf16 a[124:127], v[184:187], v[232:235], a[124:127]\n"
"v_mfma_f32_16x16x32_bf16 v[92:95], v[96:99], v[232:235], v[92:95]\n"
"v_mfma_f32_16x16x32_bf16 v[92:95], v[96:99], v[236:239], v[92:95]\n"
"v_mov_b32 v20, v121\n"
"v_mov_b32 v21, v127\n"
"v_add_u32 v16, 0, v126\n"
"v_add_u32 v17, 0xd800, v126\n"
"v_add_u32 v18, 0x1b000, v126\n"
"v_rcp_f32 v78, v84\n"
"v_rcp_f32 v100, v88\n"
"s_nop 0\n"
"v_rcp_f32 v102, v92\n"
"v_mov_b32 v79, v78\n"
"v_mov_b32 v101, v100\n"
"v_mov_b32 v103, v102\n"
"s_cmp_lg_u32 s73, 0\n"
"s_cbranch_scc1 OES_%=\n"
"s_add_u32 s76, s97, 0x20000\n"
"v_accvgpr_read_b32 v24, a4\n"
"v_accvgpr_read_b32 v36, a36\n"
"v_accvgpr_read_b32 v28, a20\n"
"v_accvgpr_read_b32 v40, a52\n"
"v_accvgpr_read_b32 v25, a5\n"
"v_accvgpr_read_b32 v37, a37\n"
"v_accvgpr_read_b32 v29, a21\n"
"v_accvgpr_read_b32 v41, a53\n"
"v_accvgpr_read_b32 v26, a6\n"
"v_accvgpr_read_b32 v38, a38\n"
"v_accvgpr_read_b32 v30, a22\n"
"v_accvgpr_read_b32 v42, a54\n"
"v_accvgpr_read_b32 v27, a7\n"
"v_accvgpr_read_b32 v39, a39\n"
"v_accvgpr_read_b32 v31, a23\n"
"v_accvgpr_read_b32 v43, a55\n"
"v_pk_mul_f32 v[24:25], v[24:25], v[78:79]\n"
"v_pk_mul_f32 v[36:37], v[36:37], v[78:79]\n"
"v_pk_mul_f32 v[26:27], v[26:27], v[78:79]\n"
"v_pk_mul_f32 v[38:39], v[38:39], v[78:79]\n"
"v_pk_mul_f32 v[28:29], v[28:29], v[78:79]\n"
"v_pk_mul_f32 v[40:41], v[40:41], v[78:79]\n"
"v_pk_mul_f32 v[30:31], v[30:31], v[78:79]\n"
"v_pk_mul_f32 v[42:43], v[42:43], v[78:79]\n"
"v_cvt_pk_bf16_f32 v32, v24, v25\n"
"v_cvt_pk_bf16_f32 v44, v36, v37\n"
"v_cvt_pk_bf16_f32 v33, v26, v27\n"
"v_cvt_pk_bf16_f32 v45, v38, v39\n"
"v_cvt_pk_bf16_f32 v34, v28, v29\n"
"v_cvt_pk_bf16_f32 v46, v40, v41\n"
"v_cvt_pk_bf16_f32 v35, v30, v31\n"
"v_cvt_pk_bf16_f32 v47, v42, v43\n"
"v_pk_fma_f32 v[72:73], v[24:25], v[74:75], v[72:73]\n"
"v_pk_fma_f32 v[22:23], v[36:37], v[74:75], v[22:23]\n"
"v_pk_fma_f32 v[72:73], v[26:27], v[74:75], v[72:73]\n"
"v_pk_fma_f32 v[22:23], v[38:39], v[74:75], v[22:23]\n"
"v_pk_fma_f32 v[72:73], v[28:29], v[74:75], v[72:73]\n"
"v_pk_fma_f32 v[22:23], v[40:41], v[74:75], v[22:23]\n"
"v_pk_fma_f32 v[72:73], v[30:31], v[74:75], v[72:73]\n"
"v_pk_fma_f32 v[22:23], v[42:43], v[74:75], v[22:23]\n"
"buffer_store_dwordx4 v[32:35], v121, s[48:51], s76 offen sc1\n"
"s_add_u32 s98, s85, 0\n"
"buffer_load_dwordx4 a[192:195], v20, s[64:67], s98 offen sc1\n"
"buffer_load_dwordx4 a[196:199], v20, s[64:67], s98 offen offset:1024 sc1\n"
"buffer_store_dwordx4 v[44:47], v121, s[48:51], s76 offen offset:1024 sc1\n"
"s_add_u32 s98, s98, s86\n"
"buffer_load_dwordx4 a[200:203], v20, s[64:67], s98 offen sc1\n"
"buffer_load_dwordx4 a[204:207], v20, s[64:67], s98 offen offset:1024 sc1\n"
"v_accvgpr_read_b32 v48, a68\n"
"v_accvgpr_read_b32 v60, a100\n"
"v_accvgpr_read_b32 v52, a84\n"
"v_accvgpr_read_b32 v64, a116\n"
"v_accvgpr_read_b32 v49, a69\n"
"v_accvgpr_read_b32 v61, a101\n"
"v_accvgpr_read_b32 v53, a85\n"
"v_accvgpr_read_b32 v65, a117\n"
"v_accvgpr_read_b32 v50, a70\n"
"v_accvgpr_read_b32 v62, a102\n"
"v_accvgpr_read_b32 v54, a86\n"
"v_accvgpr_read_b32 v66, a118\n"
"v_accvgpr_read_b32 v51, a71\n"
"v_accvgpr_read_b32 v63, a103\n"
"v_accvgpr_read_b32 v55, a87\n"
"v_accvgpr_read_b32 v67, a119\n"
"v_pk_mul_f32 v[48:49], v[48:49], v[78:79]\n"
"v_pk_mul_f32 v[60:61], v[60:61], v[78:79]\n"
"v_pk_mul_f32 v[50:51], v[50:51], v[78:79]\n"
"v_pk_mul_f32 v[62:63], v[62:63], v[78:79]\n"
"v_pk_mul_f32 v[52:53], v[52:53], v[78:79]\n"
"v_pk_mul_f32 v[64:65], v[64:65], v[78:79]\n"
"v_pk_mul_f32 v[54:55], v[54:55], v[78:79]\n"
"v_pk_mul_f32 v[66:67], v[66:67], v[78:79]\n"
"v_cvt_pk_bf16_f32 v56, v48, v49\n"
"v_cvt_pk_bf16_f32 v68, v60, v61\n"
"v_cvt_pk_bf16_f32 v57, v50, v51\n"
"v_cvt_pk_bf16_f32 v69, v62, v63\n"
"v_cvt_pk_bf16_f32 v58, v52, v53\n"
"v_cvt_pk_bf16_f32 v70, v64, v65\n"
"v_cvt_pk_bf16_f32 v59, v54, v55\n"
"v_cvt_pk_bf16_f32 v71, v66, v67\n"
"v_pk_fma_f32 v[72:73], v[48:49], v[74:75], v[72:73]\n"
"v_pk_fma_f32 v[22:23], v[60:61], v[74:75], v[22:23]\n"
"v_pk_fma_f32 v[72:73], v[50:51], v[74:75], v[72:73]\n"
"v_pk_fma_f32 v[22:23], v[62:63], v[74:75], v[22:23]\n"
"v_pk_fma_f32 v[72:73], v[52:53], v[74:75], v[72:73]\n"
"v_pk_fma_f32 v[22:23], v[64:65], v[74:75], v[22:23]\n"
"v_pk_fma_f32 v[72:73], v[54:55], v[74:75], v[72:73]\n"
"v_pk_fma_f32 v[22:23], v[66:67], v[74:75], v[22:23]\n"
"buffer_store_dwordx4 v[56:59], v121, s[48:51], s76 offen offset:2048 sc1\n"
"s_add_u32 s98, s98, s86\n"
"buffer_load_dwordx4 a[208:211], v20, s[64:67], s98 offen sc1\n"
"buffer_load_dwordx4 a[212:215], v20, s[64:67], s98 offen offset:1024 sc1\n"
"buffer_store_dwordx4 v[68:71], v121, s[48:51], s76 offen offset:3072 sc1\n"
"s_add_u32 s98, s98, s86\n"
"buffer_load_dwordx4 a[216:219], v20, s[64:67], s98 offen sc1\n"
"buffer_load_dwordx4 a[220:223], v20, s[64:67], s98 offen offset:1024 sc1\n"
"s_add_u32 s76, s97, 0x40000\n"
"v_accvgpr_read_b32 v24, a8\n"
"v_accvgpr_read_b32 v36, a40\n"
"v_accvgpr_read_b32 v28, a24\n"
"v_accvgpr_read_b32 v40, a56\n"
"v_accvgpr_read_b32 v25, a9\n"
"v_accvgpr_read_b32 v37, a41\n"
"v_accvgpr_read_b32 v29, a25\n"
"v_accvgpr_read_b32 v41, a57\n"
"v_accvgpr_read_b32 v26, a10\n"
"v_accvgpr_read_b32 v38, a42\n"
"v_accvgpr_read_b32 v30, a26\n"
"v_accvgpr_read_b32 v42, a58\n"
"v_accvgpr_read_b32 v27, a11\n"
"v_accvgpr_read_b32 v39, a43\n"
"v_accvgpr_read_b32 v31, a27\n"
"v_accvgpr_read_b32 v43, a59\n"
"v_pk_mul_f32 v[24:25], v[24:25], v[100:101]\n"
"v_pk_mul_f32 v[36:37], v[36:37], v[100:101]\n"
"v_pk_mul_f32 v[26:27], v[26:27], v[100:101]\n"
"v_pk_mul_f32 v[38:39], v[38:39], v[100:101]\n"
"v_pk_mul_f32 v[28:29], v[28:29], v[100:101]\n"
"v_pk_mul_f32 v[40:41], v[40:41], v[100:101]\n"
"v_pk_mul_f32 v[30:31], v[30:31], v[100:101]\n"
"v_pk_mul_f32 v[42:43], v[42:43], v[100:101]\n"
"v_cvt_pk_bf16_f32 v32, v24, v25\n"
"v_cvt_pk_bf16_f32 v44, v36, v37\n"
"v_cvt_pk_bf16_f32 v33, v26, v27\n"
"v_cvt_pk_bf16_f32 v45, v38, v39\n"
"v_cvt_pk_bf16_f32 v34, v28, v29\n"
"v_cvt_pk_bf16_f32 v46, v40, v41\n"
"v_cvt_pk_bf16_f32 v35, v30, v31\n"
"v_cvt_pk_bf16_f32 v47, v42, v43\n"
"v_pk_fma_f32 v[72:73], v[24:25], v[74:75], v[72:73]\n"
"v_pk_fma_f32 v[22:23], v[36:37], v[74:75], v[22:23]\n"
"v_pk_fma_f32 v[72:73], v[26:27], v[74:75], v[72:73]\n"
"v_pk_fma_f32 v[22:23], v[38:39], v[74:75], v[22:23]\n"
"v_pk_fma_f32 v[72:73], v[28:29], v[74:75], v[72:73]\n"
"v_pk_fma_f32 v[22:23], v[40:41], v[74:75], v[22:23]\n"
"v_pk_fma_f32 v[72:73], v[30:31], v[74:75], v[72:73]\n"
"v_pk_fma_f32 v[22:23], v[42:43], v[74:75], v[22:23]\n"
"buffer_store_dwordx4 v[32:35], v121, s[48:51], s76 offen sc1\n"
"s_add_u32 s98, s85, 0x800\n"
"buffer_load_dwordx4 a[224:227], v20, s[64:67], s98 offen sc1\n"
"buffer_load_dwordx4 a[228:231], v20, s[64:67], s98 offen offset:1024 sc1\n"
"buffer_store_dwordx4 v[44:47], v121, s[48:51], s76 offen offset:1024 sc1\n"
"s_add_u32 s98, s98, s86\n"
"buffer_load_dwordx4 a[232:235], v20, s[64:67], s98 offen sc1\n"
"buffer_load_dwordx4 a[236:239], v20, s[64:67], s98 offen offset:1024 sc1\n"
"v_accvgpr_read_b32 v48, a72\n"
"v_accvgpr_read_b32 v60, a104\n"
"v_accvgpr_read_b32 v52, a88\n"
"v_accvgpr_read_b32 v64, a120\n"
"v_accvgpr_read_b32 v49, a73\n"
"v_accvgpr_read_b32 v61, a105\n"
"v_accvgpr_read_b32 v53, a89\n"
"v_accvgpr_read_b32 v65, a121\n"
"v_accvgpr_read_b32 v50, a74\n"
"v_accvgpr_read_b32 v62, a106\n"
"v_accvgpr_read_b32 v54, a90\n"
"v_accvgpr_read_b32 v66, a122\n"
"v_accvgpr_read_b32 v51, a75\n"
"v_accvgpr_read_b32 v63, a107\n"
"v_accvgpr_read_b32 v55, a91\n"
"v_accvgpr_read_b32 v67, a123\n"
"v_pk_mul_f32 v[48:49], v[48:49], v[100:101]\n"
"v_pk_mul_f32 v[60:61], v[60:61], v[100:101]\n"
"v_pk_mul_f32 v[50:51], v[50:51], v[100:101]\n"
"v_pk_mul_f32 v[62:63], v[62:63], v[100:101]\n"
"v_pk_mul_f32 v[52:53], v[52:53], v[100:101]\n"
"v_pk_mul_f32 v[64:65], v[64:65], v[100:101]\n"
"v_pk_mul_f32 v[54:55], v[54:55], v[100:101]\n"
"v_pk_mul_f32 v[66:67], v[66:67], v[100:101]\n"
"v_cvt_pk_bf16_f32 v56, v48, v49\n"
"v_cvt_pk_bf16_f32 v68, v60, v61\n"
"v_cvt_pk_bf16_f32 v57, v50, v51\n"
"v_cvt_pk_bf16_f32 v69, v62, v63\n"
"v_cvt_pk_bf16_f32 v58, v52, v53\n"
"v_cvt_pk_bf16_f32 v70, v64, v65\n"
"v_cvt_pk_bf16_f32 v59, v54, v55\n"
"v_cvt_pk_bf16_f32 v71, v66, v67\n"
"v_pk_fma_f32 v[72:73], v[48:49], v[74:75], v[72:73]\n"
"v_pk_fma_f32 v[22:23], v[60:61], v[74:75], v[22:23]\n"
"v_pk_fma_f32 v[72:73], v[50:51], v[74:75], v[72:73]\n"
"v_pk_fma_f32 v[22:23], v[62:63], v[74:75], v[22:23]\n"
"v_pk_fma_f32 v[72:73], v[52:53], v[74:75], v[72:73]\n"
"v_pk_fma_f32 v[22:23], v[64:65], v[74:75], v[22:23]\n"
"v_pk_fma_f32 v[72:73], v[54:55], v[74:75], v[72:73]\n"
"v_pk_fma_f32 v[22:23], v[66:67], v[74:75], v[22:23]\n"
"buffer_store_dwordx4 v[56:59], v121, s[48:51], s76 offen offset:2048 sc1\n"
"s_add_u32 s98, s98, s86\n"
"buffer_load_dwordx4 a[240:243], v20, s[64:67], s98 offen sc1\n"
"buffer_load_dwordx4 a[244:247], v20, s[64:67], s98 offen offset:1024 sc1\n"
"buffer_store_dwordx4 v[68:71], v121, s[48:51], s76 offen offset:3072 sc1\n"
"s_add_u32 s98, s98, s86\n"
"buffer_load_dwordx4 a[248:251], v20, s[64:67], s98 offen sc1\n"
"buffer_load_dwordx4 a[252:255], v20, s[64:67], s98 offen offset:1024 sc1\n"
"s_add_u32 s76, s97, 0x60000\n"
"v_accvgpr_read_b32 v24, a12\n"
"v_accvgpr_read_b32 v36, a44\n"
"v_accvgpr_read_b32 v28, a28\n"
"v_accvgpr_read_b32 v40, a60\n"
"v_accvgpr_read_b32 v25, a13\n"
"v_accvgpr_read_b32 v37, a45\n"
"v_accvgpr_read_b32 v29, a29\n"
"v_accvgpr_read_b32 v41, a61\n"
"v_accvgpr_read_b32 v26, a14\n"
"v_accvgpr_read_b32 v38, a46\n"
"v_accvgpr_read_b32 v30, a30\n"
"v_accvgpr_read_b32 v42, a62\n"
"v_accvgpr_read_b32 v27, a15\n"
"v_accvgpr_read_b32 v39, a47\n"
"v_accvgpr_read_b32 v31, a31\n"
"v_accvgpr_read_b32 v43, a63\n"
"v_pk_mul_f32 v[24:25], v[24:25], v[102:103]\n"
"v_pk_mul_f32 v[36:37], v[36:37], v[102:103]\n"
"v_pk_mul_f32 v[26:27], v[26:27], v[102:103]\n"
"v_pk_mul_f32 v[38:39], v[38:39], v[102:103]\n"
"v_pk_mul_f32 v[28:29], v[28:29], v[102:103]\n"
"v_pk_mul_f32 v[40:41], v[40:41], v[102:103]\n"
"v_pk_mul_f32 v[30:31], v[30:31], v[102:103]\n"
"v_pk_mul_f32 v[42:43], v[42:43], v[102:103]\n"
"v_cvt_pk_bf16_f32 v32, v24, v25\n"
"v_cvt_pk_bf16_f32 v44, v36, v37\n"
"v_cvt_pk_bf16_f32 v33, v26, v27\n"
"v_cvt_pk_bf16_f32 v45, v38, v39\n"
"v_cvt_pk_bf16_f32 v34, v28, v29\n"
"v_cvt_pk_bf16_f32 v46, v40, v41\n"
"v_cvt_pk_bf16_f32 v35, v30, v31\n"
"v_cvt_pk_bf16_f32 v47, v42, v43\n"
"v_pk_fma_f32 v[72:73], v[24:25], v[74:75], v[72:73]\n"
"v_pk_fma_f32 v[22:23], v[36:37], v[74:75], v[22:23]\n"
"v_pk_fma_f32 v[72:73], v[26:27], v[74:75], v[72:73]\n"
"v_pk_fma_f32 v[22:23], v[38:39], v[74:75], v[22:23]\n"
"v_pk_fma_f32 v[72:73], v[28:29], v[74:75], v[72:73]\n"
"v_pk_fma_f32 v[22:23], v[40:41], v[74:75], v[22:23]\n"
"v_pk_fma_f32 v[72:73], v[30:31], v[74:75], v[72:73]\n"
"v_pk_fma_f32 v[22:23], v[42:43], v[74:75], v[22:23]\n"
"buffer_store_dwordx4 v[32:35], v121, s[48:51], s76 offen sc1\n"
"buffer_store_dwordx4 v[44:47], v121, s[48:51], s76 offen offset:1024 sc1\n"
"v_accvgpr_read_b32 v48, a76\n"
"v_accvgpr_read_b32 v60, a108\n"
"v_accvgpr_read_b32 v52, a92\n"
"v_accvgpr_read_b32 v64, a124\n"
"v_accvgpr_read_b32 v49, a77\n"
"v_accvgpr_read_b32 v61, a109\n"
"v_accvgpr_read_b32 v53, a93\n"
"v_accvgpr_read_b32 v65, a125\n"
"v_accvgpr_read_b32 v50, a78\n"
"v_accvgpr_read_b32 v62, a110\n"
"v_accvgpr_read_b32 v54, a94\n"
"v_accvgpr_read_b32 v66, a126\n"
"v_accvgpr_read_b32 v51, a79\n"
"v_accvgpr_read_b32 v63, a111\n"
"v_accvgpr_read_b32 v55, a95\n"
"v_accvgpr_read_b32 v67, a127\n"
"v_pk_mul_f32 v[48:49], v[48:49], v[102:103]\n"
"v_pk_mul_f32 v[60:61], v[60:61], v[102:103]\n"
"v_pk_mul_f32 v[50:51], v[50:51], v[102:103]\n"
"v_pk_mul_f32 v[62:63], v[62:63], v[102:103]\n"
"v_pk_mul_f32 v[52:53], v[52:53], v[102:103]\n"
"v_pk_mul_f32 v[64:65], v[64:65], v[102:103]\n"
"v_pk_mul_f32 v[54:55], v[54:55], v[102:103]\n"
"v_pk_mul_f32 v[66:67], v[66:67], v[102:103]\n"
"v_cvt_pk_bf16_f32 v56, v48, v49\n"
"v_cvt_pk_bf16_f32 v68, v60, v61\n"
"v_cvt_pk_bf16_f32 v57, v50, v51\n"
"v_cvt_pk_bf16_f32 v69, v62, v63\n"
"v_cvt_pk_bf16_f32 v58, v52, v53\n"
"v_cvt_pk_bf16_f32 v70, v64, v65\n"
"v_cvt_pk_bf16_f32 v59, v54, v55\n"
"v_cvt_pk_bf16_f32 v71, v66, v67\n"
"v_pk_fma_f32 v[72:73], v[48:49], v[74:75], v[72:73]\n"
"v_pk_fma_f32 v[22:23], v[60:61], v[74:75], v[22:23]\n"
"v_pk_fma_f32 v[72:73], v[50:51], v[74:75], v[72:73]\n"
"v_pk_fma_f32 v[22:23], v[62:63], v[74:75], v[22:23]\n"
"v_pk_fma_f32 v[72:73], v[52:53], v[74:75], v[72:73]\n"
"v_pk_fma_f32 v[22:23], v[64:65], v[74:75], v[22:23]\n"
"v_pk_fma_f32 v[72:73], v[54:55], v[74:75], v[72:73]\n"
"v_pk_fma_f32 v[22:23], v[66:67], v[74:75], v[22:23]\n"
"buffer_store_dwordx4 v[56:59], v121, s[48:51], s76 offen offset:2048 sc1\n"
"buffer_store_dwordx4 v[68:71], v121, s[48:51], s76 offen offset:3072 sc1\n"
"s_branch OEJ_%=\n"
"OES_%=:\n"
"s_add_u32 s76, s97, 0x20000\n"
"v_accvgpr_read_b32 v24, a4\n"
"v_accvgpr_read_b32 v36, a36\n"
"v_accvgpr_read_b32 v28, a20\n"
"v_accvgpr_read_b32 v40, a52\n"
"v_accvgpr_read_b32 v25, a5\n"
"v_accvgpr_read_b32 v37, a37\n"
"v_accvgpr_read_b32 v29, a21\n"
"v_accvgpr_read_b32 v41, a53\n"
"v_accvgpr_read_b32 v26, a6\n"
"v_accvgpr_read_b32 v38, a38\n"
"v_accvgpr_read_b32 v30, a22\n"
"v_accvgpr_read_b32 v42, a54\n"
"v_accvgpr_read_b32 v27, a7\n"
"v_accvgpr_read_b32 v39, a39\n"
"v_accvgpr_read_b32 v31, a23\n"
"v_accvgpr_read_b32 v43, a55\n"
"v_pk_mul_f32 v[24:25], v[24:25], v[78:79]\n"
"v_pk_mul_f32 v[36:37], v[36:37], v[78:79]\n"
"v_pk_mul_f32 v[26:27], v[26:27], v[78:79]\n"
"v_pk_mul_f32 v[38:39], v[38:39], v[78:79]\n"
"v_pk_mul_f32 v[28:29], v[28:29], v[78:79]\n"
"v_pk_mul_f32 v[40:41], v[40:41], v[78:79]\n"
"v_pk_mul_f32 v[30:31], v[30:31], v[78:79]\n"
"v_pk_mul_f32 v[42:43], v[42:43], v[78:79]\n"
"v_cvt_pk_bf16_f32 v32, v24, v25\n"
"v_cvt_pk_bf16_f32 v44, v36, v37\n"
"v_cvt_pk_bf16_f32 v33, v26, v27\n"
"v_cvt_pk_bf16_f32 v45, v38, v39\n"
"v_cvt_pk_bf16_f32 v34, v28, v29\n"
"v_cvt_pk_bf16_f32 v46, v40, v41\n"
"v_cvt_pk_bf16_f32 v35, v30, v31\n"
"v_cvt_pk_bf16_f32 v47, v42, v43\n"
"v_pk_fma_f32 v[72:73], v[24:25], v[74:75], v[72:73]\n"
"v_pk_fma_f32 v[22:23], v[36:37], v[74:75], v[22:23]\n"
"v_pk_fma_f32 v[72:73], v[26:27], v[74:75], v[72:73]\n"
"v_pk_fma_f32 v[22:23], v[38:39], v[74:75], v[22:23]\n"
"v_pk_fma_f32 v[72:73], v[28:29], v[74:75], v[72:73]\n"
"v_pk_fma_f32 v[22:23], v[40:41], v[74:75], v[22:23]\n"
"v_pk_fma_f32 v[72:73], v[30:31], v[74:75], v[72:73]\n"
"v_pk_fma_f32 v[22:23], v[42:43], v[74:75], v[22:23]\n"
"buffer_store_dwordx4 v[32:35], v121, s[48:51], s76 offen sc1\n"
"buffer_store_dwordx4 v[44:47], v121, s[48:51], s76 offen offset:1024 sc1\n"
"v_accvgpr_read_b32 v48, a68\n"
"v_accvgpr_read_b32 v60, a100\n"
"v_accvgpr_read_b32 v52, a84\n"
"v_accvgpr_read_b32 v64, a116\n"
"v_accvgpr_read_b32 v49, a69\n"
"v_accvgpr_read_b32 v61, a101\n"
"v_accvgpr_read_b32 v53, a85\n"
"v_accvgpr_read_b32 v65, a117\n"
"v_accvgpr_read_b32 v50, a70\n"
"v_accvgpr_read_b32 v62, a102\n"
"v_accvgpr_read_b32 v54, a86\n"
"v_accvgpr_read_b32 v66, a118\n"
"v_accvgpr_read_b32 v51, a71\n"
"v_accvgpr_read_b32 v63, a103\n"
"v_accvgpr_read_b32 v55, a87\n"
"v_accvgpr_read_b32 v67, a119\n"
"v_pk_mul_f32 v[48:49], v[48:49], v[78:79]\n"
"v_pk_mul_f32 v[60:61], v[60:61], v[78:79]\n"
"v_pk_mul_f32 v[50:51], v[50:51], v[78:79]\n"
"v_pk_mul_f32 v[62:63], v[62:63], v[78:79]\n"
"v_pk_mul_f32 v[52:53], v[52:53], v[78:79]\n"
"v_pk_mul_f32 v[64:65], v[64:65], v[78:79]\n"
"v_pk_mul_f32 v[54:55], v[54:55], v[78:79]\n"
"v_pk_mul_f32 v[66:67], v[66:67], v[78:79]\n"
"v_cvt_pk_bf16_f32 v56, v48, v49\n"
"v_cvt_pk_bf16_f32 v68, v60, v61\n"
"v_cvt_pk_bf16_f32 v57, v50, v51\n"
"v_cvt_pk_bf16_f32 v69, v62, v63\n"
"v_cvt_pk_bf16_f32 v58, v52, v53\n"
"v_cvt_pk_bf16_f32 v70, v64, v65\n"
"v_cvt_pk_bf16_f32 v59, v54, v55\n"
"v_cvt_pk_bf16_f32 v71, v66, v67\n"
"v_pk_fma_f32 v[72:73], v[48:49], v[74:75], v[72:73]\n"
"v_pk_fma_f32 v[22:23], v[60:61], v[74:75], v[22:23]\n"
"v_pk_fma_f32 v[72:73], v[50:51], v[74:75], v[72:73]\n"
"v_pk_fma_f32 v[22:23], v[62:63], v[74:75], v[22:23]\n"
"v_pk_fma_f32 v[72:73], v[52:53], v[74:75], v[72:73]\n"
"v_pk_fma_f32 v[22:23], v[64:65], v[74:75], v[22:23]\n"
"v_pk_fma_f32 v[72:73], v[54:55], v[74:75], v[72:73]\n"
"v_pk_fma_f32 v[22:23], v[66:67], v[74:75], v[22:23]\n"
"buffer_store_dwordx4 v[56:59], v121, s[48:51], s76 offen offset:2048 sc1\n"
"buffer_store_dwordx4 v[68:71], v121, s[48:51], s76 offen offset:3072 sc1\n"
"s_add_u32 s76, s97, 0x40000\n"
"v_accvgpr_read_b32 v24, a8\n"
"v_accvgpr_read_b32 v36, a40\n"
"v_accvgpr_read_b32 v28, a24\n"
"v_accvgpr_read_b32 v40, a56\n"
"v_accvgpr_read_b32 v25, a9\n"
"v_accvgpr_read_b32 v37, a41\n"
"v_accvgpr_read_b32 v29, a25\n"
"v_accvgpr_read_b32 v41, a57\n"
"v_accvgpr_read_b32 v26, a10\n"
"v_accvgpr_read_b32 v38, a42\n"
"v_accvgpr_read_b32 v30, a26\n"
"v_accvgpr_read_b32 v42, a58\n"
"v_accvgpr_read_b32 v27, a11\n"
"v_accvgpr_read_b32 v39, a43\n"
"v_accvgpr_read_b32 v31, a27\n"
"v_accvgpr_read_b32 v43, a59\n"
"v_pk_mul_f32 v[24:25], v[24:25], v[100:101]\n"
"v_pk_mul_f32 v[36:37], v[36:37], v[100:101]\n"
"v_pk_mul_f32 v[26:27], v[26:27], v[100:101]\n"
"v_pk_mul_f32 v[38:39], v[38:39], v[100:101]\n"
"v_pk_mul_f32 v[28:29], v[28:29], v[100:101]\n"
"v_pk_mul_f32 v[40:41], v[40:41], v[100:101]\n"
"v_pk_mul_f32 v[30:31], v[30:31], v[100:101]\n"
"v_pk_mul_f32 v[42:43], v[42:43], v[100:101]\n"
"v_cvt_pk_bf16_f32 v32, v24, v25\n"
"v_cvt_pk_bf16_f32 v44, v36, v37\n"
"v_cvt_pk_bf16_f32 v33, v26, v27\n"
"v_cvt_pk_bf16_f32 v45, v38, v39\n"
"v_cvt_pk_bf16_f32 v34, v28, v29\n"
"v_cvt_pk_bf16_f32 v46, v40, v41\n"
"v_cvt_pk_bf16_f32 v35, v30, v31\n"
"v_cvt_pk_bf16_f32 v47, v42, v43\n"
"v_pk_fma_f32 v[72:73], v[24:25], v[74:75], v[72:73]\n"
"v_pk_fma_f32 v[22:23], v[36:37], v[74:75], v[22:23]\n"
"v_pk_fma_f32 v[72:73], v[26:27], v[74:75], v[72:73]\n"
"v_pk_fma_f32 v[22:23], v[38:39], v[74:75], v[22:23]\n"
"v_pk_fma_f32 v[72:73], v[28:29], v[74:75], v[72:73]\n"
"v_pk_fma_f32 v[22:23], v[40:41], v[74:75], v[22:23]\n"
"v_pk_fma_f32 v[72:73], v[30:31], v[74:75], v[72:73]\n"
"v_pk_fma_f32 v[22:23], v[42:43], v[74:75], v[22:23]\n"
"buffer_store_dwordx4 v[32:35], v121, s[48:51], s76 offen sc1\n"
"buffer_store_dwordx4 v[44:47], v121, s[48:51], s76 offen offset:1024 sc1\n"
"v_accvgpr_read_b32 v48, a72\n"
"v_accvgpr_read_b32 v60, a104\n"
"v_accvgpr_read_b32 v52, a88\n"
"v_accvgpr_read_b32 v64, a120\n"
"v_accvgpr_read_b32 v49, a73\n"
"v_accvgpr_read_b32 v61, a105\n"
"v_accvgpr_read_b32 v53, a89\n"
"v_accvgpr_read_b32 v65, a121\n"
"v_accvgpr_read_b32 v50, a74\n"
"v_accvgpr_read_b32 v62, a106\n"
"v_accvgpr_read_b32 v54, a90\n"
"v_accvgpr_read_b32 v66, a122\n"
"v_accvgpr_read_b32 v51, a75\n"
"v_accvgpr_read_b32 v63, a107\n"
"v_accvgpr_read_b32 v55, a91\n"
"v_accvgpr_read_b32 v67, a123\n"
"v_pk_mul_f32 v[48:49], v[48:49], v[100:101]\n"
"v_pk_mul_f32 v[60:61], v[60:61], v[100:101]\n"
"v_pk_mul_f32 v[50:51], v[50:51], v[100:101]\n"
"v_pk_mul_f32 v[62:63], v[62:63], v[100:101]\n"
"v_pk_mul_f32 v[52:53], v[52:53], v[100:101]\n"
"v_pk_mul_f32 v[64:65], v[64:65], v[100:101]\n"
"v_pk_mul_f32 v[54:55], v[54:55], v[100:101]\n"
"v_pk_mul_f32 v[66:67], v[66:67], v[100:101]\n"
"v_cvt_pk_bf16_f32 v56, v48, v49\n"
"v_cvt_pk_bf16_f32 v68, v60, v61\n"
"v_cvt_pk_bf16_f32 v57, v50, v51\n"
"v_cvt_pk_bf16_f32 v69, v62, v63\n"
"v_cvt_pk_bf16_f32 v58, v52, v53\n"
"v_cvt_pk_bf16_f32 v70, v64, v65\n"
"v_cvt_pk_bf16_f32 v59, v54, v55\n"
"v_cvt_pk_bf16_f32 v71, v66, v67\n"
"v_pk_fma_f32 v[72:73], v[48:49], v[74:75], v[72:73]\n"
"v_pk_fma_f32 v[22:23], v[60:61], v[74:75], v[22:23]\n"
"v_pk_fma_f32 v[72:73], v[50:51], v[74:75], v[72:73]\n"
"v_pk_fma_f32 v[22:23], v[62:63], v[74:75], v[22:23]\n"
"v_pk_fma_f32 v[72:73], v[52:53], v[74:75], v[72:73]\n"
"v_pk_fma_f32 v[22:23], v[64:65], v[74:75], v[22:23]\n"
"v_pk_fma_f32 v[72:73], v[54:55], v[74:75], v[72:73]\n"
"v_pk_fma_f32 v[22:23], v[66:67], v[74:75], v[22:23]\n"
"buffer_store_dwordx4 v[56:59], v121, s[48:51], s76 offen offset:2048 sc1\n"
"buffer_store_dwordx4 v[68:71], v121, s[48:51], s76 offen offset:3072 sc1\n"
"s_add_u32 s76, s97, 0x60000\n"
"v_accvgpr_read_b32 v24, a12\n"
"v_accvgpr_read_b32 v36, a44\n"
"v_accvgpr_read_b32 v28, a28\n"
"v_accvgpr_read_b32 v40, a60\n"
"v_accvgpr_read_b32 v25, a13\n"
"v_accvgpr_read_b32 v37, a45\n"
"v_accvgpr_read_b32 v29, a29\n"
"v_accvgpr_read_b32 v41, a61\n"
"v_accvgpr_read_b32 v26, a14\n"
"v_accvgpr_read_b32 v38, a46\n"
"v_accvgpr_read_b32 v30, a30\n"
"v_accvgpr_read_b32 v42, a62\n"
"v_accvgpr_read_b32 v27, a15\n"
"v_accvgpr_read_b32 v39, a47\n"
"v_accvgpr_read_b32 v31, a31\n"
"v_accvgpr_read_b32 v43, a63\n"
"v_pk_mul_f32 v[24:25], v[24:25], v[102:103]\n"
"v_pk_mul_f32 v[36:37], v[36:37], v[102:103]\n"
"v_pk_mul_f32 v[26:27], v[26:27], v[102:103]\n"
"v_pk_mul_f32 v[38:39], v[38:39], v[102:103]\n"
"v_pk_mul_f32 v[28:29], v[28:29], v[102:103]\n"
"v_pk_mul_f32 v[40:41], v[40:41], v[102:103]\n"
"v_pk_mul_f32 v[30:31], v[30:31], v[102:103]\n"
"v_pk_mul_f32 v[42:43], v[42:43], v[102:103]\n"
"v_cvt_pk_bf16_f32 v32, v24, v25\n"
"v_cvt_pk_bf16_f32 v44, v36, v37\n"
"v_cvt_pk_bf16_f32 v33, v26, v27\n"
"v_cvt_pk_bf16_f32 v45, v38, v39\n"
"v_cvt_pk_bf16_f32 v34, v28, v29\n"
"v_cvt_pk_bf16_f32 v46, v40, v41\n"
"v_cvt_pk_bf16_f32 v35, v30, v31\n"
"v_cvt_pk_bf16_f32 v47, v42, v43\n"
"v_pk_fma_f32 v[72:73], v[24:25], v[74:75], v[72:73]\n"
"v_pk_fma_f32 v[22:23], v[36:37], v[74:75], v[22:23]\n"
"v_pk_fma_f32 v[72:73], v[26:27], v[74:75], v[72:73]\n"
"v_pk_fma_f32 v[22:23], v[38:39], v[74:75], v[22:23]\n"
"v_pk_fma_f32 v[72:73], v[28:29], v[74:75], v[72:73]\n"
"v_pk_fma_f32 v[22:23], v[40:41], v[74:75], v[22:23]\n"
"v_pk_fma_f32 v[72:73], v[30:31], v[74:75], v[72:73]\n"
"v_pk_fma_f32 v[22:23], v[42:43], v[74:75], v[22:23]\n"
"buffer_store_dwordx4 v[32:35], v121, s[48:51], s76 offen sc1\n"
"buffer_store_dwordx4 v[44:47], v121, s[48:51], s76 offen offset:1024 sc1\n"
"v_accvgpr_read_b32 v48, a76\n"
"v_accvgpr_read_b32 v60, a108\n"
"v_accvgpr_read_b32 v52, a92\n"
"v_accvgpr_read_b32 v64, a124\n"
"v_accvgpr_read_b32 v49, a77\n"
"v_accvgpr_read_b32 v61, a109\n"
"v_accvgpr_read_b32 v53, a93\n"
"v_accvgpr_read_b32 v65, a125\n"
"v_accvgpr_read_b32 v50, a78\n"
"v_accvgpr_read_b32 v62, a110\n"
"v_accvgpr_read_b32 v54, a94\n"
"v_accvgpr_read_b32 v66, a126\n"
"v_accvgpr_read_b32 v51, a79\n"
"v_accvgpr_read_b32 v63, a111\n"
"v_accvgpr_read_b32 v55, a95\n"
"v_accvgpr_read_b32 v67, a127\n"
"v_pk_mul_f32 v[48:49], v[48:49], v[102:103]\n"
"v_pk_mul_f32 v[60:61], v[60:61], v[102:103]\n"
"v_pk_mul_f32 v[50:51], v[50:51], v[102:103]\n"
"v_pk_mul_f32 v[62:63], v[62:63], v[102:103]\n"
"v_pk_mul_f32 v[52:53], v[52:53], v[102:103]\n"
"v_pk_mul_f32 v[64:65], v[64:65], v[102:103]\n"
"v_pk_mul_f32 v[54:55], v[54:55], v[102:103]\n"
"v_pk_mul_f32 v[66:67], v[66:67], v[102:103]\n"
"v_cvt_pk_bf16_f32 v56, v48, v49\n"
"v_cvt_pk_bf16_f32 v68, v60, v61\n"
"v_cvt_pk_bf16_f32 v57, v50, v51\n"
"v_cvt_pk_bf16_f32 v69, v62, v63\n"
"v_cvt_pk_bf16_f32 v58, v52, v53\n"
"v_cvt_pk_bf16_f32 v70, v64, v65\n"
"v_cvt_pk_bf16_f32 v59, v54, v55\n"
"v_cvt_pk_bf16_f32 v71, v66, v67\n"
"v_pk_fma_f32 v[72:73], v[48:49], v[74:75], v[72:73]\n"
"v_pk_fma_f32 v[22:23], v[60:61], v[74:75], v[22:23]\n"
"v_pk_fma_f32 v[72:73], v[50:51], v[74:75], v[72:73]\n"
"v_pk_fma_f32 v[22:23], v[62:63], v[74:75], v[22:23]\n"
"v_pk_fma_f32 v[72:73], v[52:53], v[74:75], v[72:73]\n"
"v_pk_fma_f32 v[22:23], v[64:65], v[74:75], v[22:23]\n"
"v_pk_fma_f32 v[72:73], v[54:55], v[74:75], v[72:73]\n"
"v_pk_fma_f32 v[22:23], v[66:67], v[74:75], v[22:23]\n"
"buffer_store_dwordx4 v[56:59], v121, s[48:51], s76 offen offset:2048 sc1\n"
"buffer_store_dwordx4 v[68:71], v121, s[48:51], s76 offen offset:3072 sc1\n"
"OEJ_%=:\n"
"s_mov_b32 s98, 0x5d800000\n"
"v_pk_add_f32 v[72:73], v[72:73], v[22:23]\n"
"v_cmp_u_f32 vcc, v72, v73\n"
"s_nop 0\n"
"s_mov_b64 s[76:77], vcc\n"
"v_cmp_nge_f32 vcc, s98, v80\n"
"s_nop 0\n"
"s_or_b64 s[76:77], s[76:77], vcc\n"
"v_cmp_nge_f32 vcc, s98, v84\n"
"s_nop 0\n"
"s_or_b64 s[76:77], s[76:77], vcc\n"
"v_cmp_nge_f32 vcc, s98, v88\n"
"s_nop 0\n"
"s_or_b64 s[76:77], s[76:77], vcc\n"
"v_cmp_nge_f32 vcc, s98, v92\n"
"s_nop 0\n"
"s_or_b64 s[76:77], s[76:77], vcc\n"
"s_cbranch_scc0 NOBAD_%=\n"
"v_mov_b32 v122, 0x27ff0\n"
"v_mov_b32 v123, 1\n"
"ds_write_b32 v122, v123\n"
"NOBAD_%=:\n"
"v_mov_b32 v122, 0x27ff0\n"
"s_waitcnt lgkmcnt(0)\n"
"s_waitcnt vmcnt(28)\n"
"s_barrier\n"
"ds_read_b32 v123, v122\n"
"s_waitcnt lgkmcnt(0)\n"
"v_readfirstlane_b32 s76, v123\n"
"s_nop 0\n"
"s_sub_u32 s76, s76, s79\n"
"s_cmp_eq_u32 s76, 1\n"
"s_cbranch_scc1 FALLBACK_%=\n"
"s_cmp_lg_u32 s73, 0\n"
"s_cbranch_scc1 SPATH_%=\n"
"ds_read_b64_tr_b16 v[80:81], v16\n"
"ds_read_b64_tr_b16 v[82:83], v16 offset:4608\n"
"ds_read_b64_tr_b16 v[84:85], v16 offset:9216\n"
"ds_read_b64_tr_b16 v[86:87], v16 offset:13824\n"
"ds_read_b64_tr_b16 v[88:89], v16 offset:32\n"
"ds_read_b64_tr_b16 v[90:91], v16 offset:4640\n"
"ds_read_b64_tr_b16 v[92:93], v16 offset:9248\n"
"ds_read_b64_tr_b16 v[94:95], v16 offset:13856\n"
"s_waitcnt vmcnt(26) lgkmcnt(6)\n"
"v_mfma_f32_16x16x32_bf16 v[112:115], v[80:83], a[192:195], 0\n"
"s_waitcnt vmcnt(25) lgkmcnt(4)\n"
"v_mfma_f32_16x16x32_bf16 v[112:115], v[84:87], a[196:199], v[112:115]\n"
"s_add_u32 s98, s85, 0x1000\n"
"ds_read_b64_tr_b16 v[96:97], v16 offset:64\n"
"s_waitcnt vmcnt(22)\n"
"v_mfma_f32_16x16x32_bf16 v[116:119], v[84:87], a[204:207], 0\n"
"buffer_load_dwordx4 a[0:3], v20, s[64:67], s98 offen sc1\n"
"v_mfma_f32_16x16x32_bf16 v[116:119], v[80:83], a[200:203], v[116:119]\n"
"ds_read_b64_tr_b16 v[98:99], v16 offset:4672\n"
"s_waitcnt vmcnt(21)\n"
"v_mfma_f32_16x16x32_bf16 v[120:123], v[80:83], a[208:211], 0\n"
"buffer_load_dwordx4 a[4:7], v20, s[64:67], s98 offen offset:1024 sc1\n"
"s_waitcnt vmcnt(21)\n"
"v_mfma_f32_16x16x32_bf16 v[120:123], v[84:87], a[212:215], v[120:123]\n"
"s_add_u32 s98, s98, s86\n"
"ds_read_b64_tr_b16 v[100:101], v16 offset:9280\n"
"s_waitcnt vmcnt(18)\n"
"v_mfma_f32_16x16x32_bf16 v[124:127], v[84:87], a[220:223], 0\n"
"buffer_load_dwordx4 a[8:11], v20, s[64:67], s98 offen sc1\n"
"v_mfma_f32_16x16x32_bf16 v[124:127], v[80:83], a[216:219], v[124:127]\n"
"ds_read_b64_tr_b16 v[102:103], v16 offset:13888\n"
"s_waitcnt lgkmcnt(6)\n"
"v_mfma_f32_16x16x32_bf16 v[128:131], v[88:91], a[192:195], 0\n"
"buffer_load_dwordx4 a[12:15], v20, s[64:67], s98 offen offset:1024 sc1\n"
"s_waitcnt lgkmcnt(4)\n"
"v_mfma_f32_16x16x32_bf16 v[128:131], v[92:95], a[196:199], v[128:131]\n"
"s_add_u32 s98, s98, s86\n"
"ds_read_b64_tr_b16 v[80:81], v16 offset:96\n"
"v_mfma_f32_16x16x32_bf16 v[132:135], v[92:95], a[204:207], 0\n"
"buffer_load_dwordx4 a[16:19], v20, s[64:67], s98 offen sc1\n"
"v_mfma_f32_16x16x32_bf16 v[132:135], v[88:91], a[200:203], v[132:135]\n"
"ds_read_b64_tr_b16 v[82:83], v16 offset:4704\n"
"v_mfma_f32_16x16x32_bf16 v[136:139], v[88:91], a[208:211], 0\n"
"buffer_load_dwordx4 a[20:23], v20, s[64:67], s98 offen offset:1024 sc1\n"
"v_mfma_f32_16x16x32_bf16 v[136:139], v[92:95], a[212:215], v[136:139]\n"
"ds_read_b64_tr_b16 v[84:85], v16 offset:9312\n"
"v_mfma_f32_16x16x32_bf16 v[140:143], v[92:95], a[220:223], 0\n"
"v_mfma_f32_16x16x32_bf16 v[140:143], v[88:91], a[216:219], v[140:143]\n"
"s_add_u32 s98, s98, s86\n"
"ds_read_b64_tr_b16 v[86:87], v16 offset:13920\n"
"s_waitcnt lgkmcnt(6)\n"
"v_mfma_f32_16x16x32_bf16 v[144:147], v[96:99], a[192:195], 0\n"
"buffer_load_dwordx4 a[24:27], v20, s[64:67], s98 offen sc1\n"
"s_waitcnt lgkmcnt(4)\n"
"v_mfma_f32_16x16x32_bf16 v[144:147], v[100:103], a[196:199], v[144:147]\n"
"ds_read_b64_tr_b16 v[88:89], v16 offset:128\n"
"v_mfma_f32_16x16x32_bf16 v[148:151], v[100:103], a[204:207], 0\n"
"buffer_load_dwordx4 a[28:31], v20, s[64:67], s98 offen offset:1024 sc1\n"
"v_mfma_f32_16x16x32_bf16 v[148:151], v[96:99], a[200:203], v[148:151]\n"
"s_add_u32 s98, s85, 0x1800\n"
"ds_read_b64_tr_b16 v[90:91], v16 offset:4736\n"
"v_mfma_f32_16x16x32_bf16 v[152:155], v[96:99], a[208:211], 0\n"
"buffer_load_dwordx4 a[32:35], v20, s[64:67], s98 offen sc1\n"
"v_mfma_f32_16x16x32_bf16 v[152:155], v[100:103], a[212:215], v[152:155]\n"
"ds_read_b64_tr_b16 v[92:93], v16 offset:9344\n"
"v_mfma_f32_16x16x32_bf16 v[156:159], v[100:103], a[220:223], 0\n"
"buffer_load_dwordx4 a[36:39], v20, s[64:67], s98 offen offset:1024 sc1\n"
"v_mfma_f32_16x16x32_bf16 v[156:159], v[96:99], a[216:219], v[156:159]\n"
"s_add_u32 s98, s98, s86\n"
"ds_read_b64_tr_b16 v[94:95], v16 offset:13952\n"
"s_waitcnt lgkmcnt(6)\n"
"v_mfma_f32_16x16x32_bf16 v[160:163], v[80:83], a[192:195], 0\n"
"buffer_load_dwordx4 a[40:43], v20, s[64:67], s98 offen sc1\n"
"s_waitcnt lgkmcnt(4)\n"
"v_mfma_f32_16x16x32_bf16 v[160:163], v[84:87], a[196:199], v[160:163]\n"
"ds_read_b64_tr_b16 v[96:97], v16 offset:160\n"
"v_mfma_f32_16x16x32_bf16 v[164:167], v[84:87], a[204:207], 0\n"
"buffer_load_dwordx4 a[44:47], v20, s[64:67], s98 offen offset:1024 sc1\n"
"v_mfma_f32_16x16x32_bf16 v[164:167], v[80:83], a[200:203], v[164:167]\n"
"ds_read_b64_tr_b16 v[98:99], v16 offset:4768\n"
"v_mfma_f32_16x16x32_bf16 v[168:171], v[80:83], a[208:211], 0\n"
"v_mfma_f32_16x16x32_bf16 v[168:171], v[84:87], a[212:215], v[168:171]\n"
"s_add_u32 s98, s98, s86\n"
"ds_read_b64_tr_b16 v[100:101], v16 offset:9376\n"
"v_mfma_f32_16x16x32_bf16 v[172:175], v[84:87], a[220:223], 0\n"
"buffer_load_dwordx4 a[48:51], v20, s[64:67], s98 offen sc1\n"
"v_mfma_f32_16x16x32_bf16 v[172:175], v[80:83], a[216:219], v[172:175]\n"
"ds_read_b64_tr_b16 v[102:103], v16 offset:13984\n"
"s_waitcnt lgkmcnt(6)\n"
"v_mfma_f32_16x16x32_bf16 v[176:179], v[88:91], a[192:195], 0\n"
"buffer_load_dwordx4 a[52:55], v20, s[64:67], s98 offen offset:1024 sc1\n"
"s_waitcnt lgkmcnt(4)\n"
"v_mfma_f32_16x16x32_bf16 v[176:179], v[92:95], a[196:199], v[176:179]\n"
"s_add_u32 s98, s98, s86\n"
"ds_read_b64_tr_b16 v[80:81], v16 offset:192\n"
"v_mfma_f32_16x16x32_bf16 v[180:183], v[92:95], a[204:207], 0\n"
"buffer_load_dwordx4 a[56:59], v20, s[64:67], s98 offen sc1\n"
"v_mfma_f32_16x16x32_bf16 v[180:183], v[88:91], a[200:203], v[180:183]\n"
"ds_read_b64_tr_b16 v[82:83], v16 offset:4800\n"
"v_mfma_f32_16x16x32_bf16 v[184:187], v[88:91], a[208:211], 0\n"
"buffer_load_dwordx4 a[60:63], v20, s[64:67], s98 offen offset:1024 sc1\n"
"v_mfma_f32_16x16x32_bf16 v[184:187], v[92:95], a[212:215], v[184:187]\n"
"s_add_u32 s98, s85, 0x2000\n"
"ds_read_b64_tr_b16 v[84:85], v16 offset:9408\n"
"v_mfma_f32_16x16x32_bf16 v[188:191], v[92:95], a[220:223], 0\n"
"buffer_load_dwordx4 a[64:67], v20, s[64:67], s98 offen sc1\n"
"v_mfma_f32_16x16x32_bf16 v[188:191], v[88:91], a[216:219], v[188:191]\n"
"ds_read_b64_tr_b16 v[86:87], v16 offset:14016\n"
"s_waitcnt lgkmcnt(6)\n"
"v_mfma_f32_16x16x32_bf16 v[192:195], v[96:99], a[192:195], 0\n"
"buffer_load_dwordx4 a[68:71], v20, s[64:67], s98 offen offset:1024 sc1\n"
"s_waitcnt lgkmcnt(4)\n"
"v_mfma_f32_16x16x32_bf16 v[192:195], v[100:103], a[196:199], v[192:195]\n"
"ds_read_b64_tr_b16 v[88:89], v16 offset:224\n"
"v_mfma_f32_16x16x32_bf16 v[196:199], v[100:103], a[204:207], 0\n"
"v_mfma_f32_16x16x32_bf16 v[196:199], v[96:99], a[200:203], v[196:199]\n"
"s_add_u32 s98, s98, s86\n"
"ds_read_b64_tr_b16 v[90:91], v16 offset:4832\n"
"v_mfma_f32_16x16x32_bf16 v[200:203], v[96:99], a[208:211], 0\n"
"buffer_load_dwordx4 a[72:75], v20, s[64:67], s98 offen sc1\n"
"v_mfma_f32_16x16x32_bf16 v[200:203], v[100:103], a[212:215], v[200:203]\n"
"ds_read_b64_tr_b16 v[92:93], v16 offset:9440\n"
"v_mfma_f32_16x16x32_bf16 v[204:207], v[100:103], a[220:223], 0\n"
"buffer_load_dwordx4 a[76:79], v20, s[64:67], s98 offen offset:1024 sc1\n"
"v_mfma_f32_16x16x32_bf16 v[204:207], v[96:99], a[216:219], v[204:207]\n"
"s_add_u32 s98, s98, s86\n"
"ds_read_b64_tr_b16 v[94:95], v16 offset:14048\n"
"s_waitcnt lgkmcnt(6)\n"
"v_mfma_f32_16x16x32_bf16 v[208:211], v[80:83], a[192:195], 0\n"
"buffer_load_dwordx4 a[80:83], v20, s[64:67], s98 offen sc1\n"
"s_waitcnt lgkmcnt(4)\n"
"v_mfma_f32_16x16x32_bf16 v[208:211], v[84:87], a[196:199], v[208:211]\n"
"ds_read_b64_tr_b16 v[96:97], v16 offset:256\n"
"v_mfma_f32_16x16x32_bf16 v[212:215], v[84:87], a[204:207], 0\n"
"buffer_load_dwordx4 a[84:87], v20, s[64:67], s98 offen offset:1024 sc1\n"
"v_mfma_f32_16x16x32_bf16 v[212:215], v[80:83], a[200:203], v[212:215]\n"
"s_add_u32 s98, s98, s86\n"
"ds_read_b64_tr_b16 v[98:99], v16 offset:4864\n"
"v_mfma_f32_16x16x32_bf16 v[216:219], v[80:83], a[208:211], 0\n"
"buffer_load_dwordx4 a[88:91], v20, s[64:67], s98 offen sc1\n"
"v_mfma_f32_16x16x32_bf16 v[216:219], v[84:87], a[212:215], v[216:219]\n"
"ds_read_b64_tr_b16 v[100:101], v16 offset:9472\n"
"v_mfma_f32_16x16x32_bf16 v[220:223], v[84:87], a[220:223], 0\n"
"buffer_load_dwordx4 a[92:95], v20, s[64:67], s98 offen offset:1024 sc1\n"
"v_mfma_f32_16x16x32_bf16 v[220:223], v[80:83], a[216:219], v[220:223]\n"
"s_nop 0\n"
"s_waitcnt vmcnt(24)\n"
"s_barrier\n"
"s_cmp_lg_u32 s72, 0\n"
"s_cbranch_scc1 NOSIGOF_%=\n"
"s_lshl_b32 s76, s75, 5\n"
"s_add_u32 s76, s76, s74\n"
"s_lshl_b32 s76, s76, 3\n"
"s_add_u32 s76, s76, 0x2000\n"
"v_mov_b32 v72, s76\n"
"v_mov_b32 v74, s88\n"
"v_mov_b32 v75, s89\n"
"s_mov_b64 exec, 1\n"
"global_store_dwordx2 v72, v[74:75], s[92:93] sc1\n"
"s_mov_b64 exec, -1\n"
"NOSIGOF_%=:\n"
"ds_read_b64_tr_b16 v[102:103], v16 offset:14080\n"
"s_waitcnt lgkmcnt(6)\n"
"v_mfma_f32_16x16x32_bf16 v[224:227], v[88:91], a[192:195], 0\n"
"s_waitcnt lgkmcnt(4)\n"
"v_mfma_f32_16x16x32_bf16 v[224:227], v[92:95], a[196:199], v[224:227]\n"
"s_add_u32 s77, s84, 0x48000\n"
"s_add_u32 m0, s78, 0x12000\n"
"ds_read_b64_tr_b16 v[80:81], v16 offset:18432\n"
"v_mfma_f32_16x16x32_bf16 v[228:231], v[92:95], a[204:207], 0\n"
"buffer_load_dwordx4 v6, s[60:63], s77 offen lds\n"
"v_mfma_f32_16x16x32_bf16 v[228:231], v[88:91], a[200:203], v[228:231]\n"
"s_add_u32 m0, s78, 0x12400\n"
"ds_read_b64_tr_b16 v[82:83], v16 offset:23040\n"
"v_mfma_f32_16x16x32_bf16 v[232:235], v[88:91], a[208:211], 0\n"
"buffer_load_dwordx4 v7, s[60:63], s77 offen lds\n"
"v_mfma_f32_16x16x32_bf16 v[232:235], v[92:95], a[212:215], v[232:235]\n"
"s_add_u32 m0, s78, 0x12800\n"
"ds_read_b64_tr_b16 v[84:85], v16 offset:27648\n"
"v_mfma_f32_16x16x32_bf16 v[236:239], v[92:95], a[220:223], 0\n"
"buffer_load_dwordx4 v8, s[60:63], s77 offen lds\n"
"v_mfma_f32_16x16x32_bf16 v[236:239], v[88:91], a[216:219], v[236:239]\n"
"s_add_u32 m0, s78, 0x12c00\n"
"ds_read_b64_tr_b16 v[86:87], v16 offset:32256\n"
"s_waitcnt lgkmcnt(6)\n"
"v_mfma_f32_16x16x32_bf16 v[240:243], v[96:99], a[192:195], 0\n"
"buffer_load_dwordx4 v9, s[60:63], s77 offen lds\n"
"s_waitcnt lgkmcnt(4)\n"
"v_mfma_f32_16x16x32_bf16 v[240:243], v[100:103], a[196:199], v[240:243]\n"
"s_add_u32 m0, s78, 0x13000\n"
"ds_read_b64_tr_b16 v[88:89], v16 offset:18464\n"
"v_mfma_f32_16x16x32_bf16 v[244:247], v[100:103], a[204:207], 0\n"
"s_bitcmp1_b32 s72, 0\n"
"s_cbranch_scc1 NOP5_5_%=\n"
"buffer_load_dwordx4 v10, s[60:63], s77 offen lds\n"
"NOP5_5_%=:\n"
"v_mfma_f32_16x16x32_bf16 v[244:247], v[96:99], a[200:203], v[244:247]\n"
"ds_read_b64_tr_b16 v[90:91], v16 offset:23072\n"
"v_mfma_f32_16x16x32_bf16 v[248:251], v[96:99], a[208:211], 0\n"
"v_mfma_f32_16x16x32_bf16 v[248:251], v[100:103], a[212:215], v[248:251]\n"
"ds_read_b64_tr_b16 v[92:93], v16 offset:27680\n"
"v_mfma_f32_16x16x32_bf16 v[252:255], v[100:103], a[220:223], 0\n"
"v_mfma_f32_16x16x32_bf16 v[252:255], v[96:99], a[216:219], v[252:255]\n"
"ds_read_b64_tr_b16 v[94:95], v16 offset:32288\n"
"s_nop 0\n"
"s_waitcnt lgkmcnt(6)\n"
"v_mfma_f32_16x16x32_bf16 v[112:115], v[80:83], a[224:227], v[112:115]\n"
"s_waitcnt lgkmcnt(4)\n"
"v_mfma_f32_16x16x32_bf16 v[112:115], v[84:87], a[228:231], v[112:115]\n"
"s_add_u32 s98, s85, 0x2800\n"
"ds_read_b64_tr_b16 v[96:97], v16 offset:18496\n"
"v_mfma_f32_16x16x32_bf16 v[116:119], v[84:87], a[236:239], v[116:119]\n"
"buffer_load_dwordx4 a[96:99], v20, s[64:67], s98 offen sc1\n"
"v_mfma_f32_16x16x32_bf16 v[116:119], v[80:83], a[232:235], v[116:119]\n"
"ds_read_b64_tr_b16 v[98:99], v16 offset:23104\n"
"v_mfma_f32_16x16x32_bf16 v[120:123], v[80:83], a[240:243], v[120:123]\n"
"v_mfma_f32_16x16x32_bf16 v[120:123], v[84:87], a[244:247], v[120:123]\n"
"ds_read_b64_tr_b16 v[100:101], v16 offset:27712\n"
"v_mfma_f32_16x16x32_bf16 v[124:127], v[84:87], a[252:255], v[124:127]\n"
"buffer_load_dwordx4 a[100:103], v20, s[64:67], s98 offen offset:1024 sc1\n"
"v_mfma_f32_16x16x32_bf16 v[124:127], v[80:83], a[248:251], v[124:127]\n"
"ds_read_b64_tr_b16 v[102:103], v16 offset:32320\n"
"s_waitcnt lgkmcnt(6)\n"
"v_mfma_f32_16x16x32_bf16 v[128:131], v[88:91], a[224:227], v[128:131]\n"
"s_waitcnt lgkmcnt(4)\n"
"v_mfma_f32_16x16x32_bf16 v[128:131], v[92:95], a[228:231], v[128:131]\n"
"ds_read_b64_tr_b16 v[80:81], v16 offset:18528\n"
"v_mfma_f32_16x16x32_bf16 v[132:135], v[92:95], a[236:239], v[132:135]\n"
"v_mfma_f32_16x16x32_bf16 v[132:135], v[88:91], a[232:235], v[132:135]\n"
"s_add_u32 s98, s98, s86\n"
"ds_read_b64_tr_b16 v[82:83], v16 offset:23136\n"
"v_mfma_f32_16x16x32_bf16 v[136:139], v[88:91], a[240:243], v[136:139]\n"
"buffer_load_dwordx4 a[104:107], v20, s[64:67], s98 offen sc1\n"
"v_mfma_f32_16x16x32_bf16 v[136:139], v[92:95], a[244:247], v[136:139]\n"
"ds_read_b64_tr_b16 v[84:85], v16 offset:27744\n"
"v_mfma_f32_16x16x32_bf16 v[140:143], v[92:95], a[252:255], v[140:143]\n"
"v_mfma_f32_16x16x32_bf16 v[140:143], v[88:91], a[248:251], v[140:143]\n"
"ds_read_b64_tr_b16 v[86:87], v16 offset:32352\n"
"s_waitcnt lgkmcnt(6)\n"
"v_mfma_f32_16x16x32_bf16 v[144:147], v[96:99], a[224:227], v[144:147]\n"
"buffer_load_dwordx4 a[108:111], v20, s[64:67], s98 offen offset:1024 sc1\n"
"s_waitcnt lgkmcnt(4)\n"
"v_mfma_f32_16x16x32_bf16 v[144:147], v[100:103], a[228:231], v[144:147]\n"
"ds_read_b64_tr_b16 v[88:89], v16 offset:18560\n"
"v_mfma_f32_16x16x32_bf16 v[148:151], v[100:103], a[236:239], v[148:151]\n"
"v_mfma_f32_16x16x32_bf16 v[148:151], v[96:99], a[232:235], v[148:151]\n"
"ds_read_b64_tr_b16 v[90:91], v16 offset:23168\n"
"v_mfma_f32_16x16x32_bf16 v[152:155], v[96:99], a[240:243], v[152:155]\n"
"v_mfma_f32_16x16x32_bf16 v[152:155], v[100:103], a[244:247], v[152:155]\n"
"s_add_u32 s98, s98, s86\n"
"ds_read_b64_tr_b16 v[92:93], v16 offset:27776\n"
"v_mfma_f32_16x16x32_bf16 v[156:159], v[100:103], a[252:255], v[156:159]\n"
"buffer_load_dwordx4 a[112:115], v20, s[64:67], s98 offen sc1\n"
"v_mfma_f32_16x16x32_bf16 v[156:159], v[96:99], a[248:251], v[156:159]\n"
"ds_read_b64_tr_b16 v[94:95], v16 offset:32384\n"
"s_waitcnt lgkmcnt(6)\n"
"v_mfma_f32_16x16x32_bf16 v[160:163], v[80:83], a[224:227], v[160:163]\n"
"s_waitcnt lgkmcnt(4)\n"
"v_mfma_f32_16x16x32_bf16 v[160:163], v[84:87], a[228:231], v[160:163]\n"
"ds_read_b64_tr_b16 v[96:97], v16 offset:18592\n"
"v_mfma_f32_16x16x32_bf16 v[164:167], v[84:87], a[236:239], v[164:167]\n"
"v_mfma_f32_16x16x32_bf16 v[164:167], v[80:83], a[232:235], v[164:167]\n"
"ds_read_b64_tr_b16 v[98:99], v16 offset:23200\n"
"v_mfma_f32_16x16x32_bf16 v[168:171], v[80:83], a[240:243], v[168:171]\n"
"buffer_load_dwordx4 a[116:119], v20, s[64:67], s98 offen offset:1024 sc1\n"
"v_mfma_f32_16x16x32_bf16 v[168:171], v[84:87], a[244:247], v[168:171]\n"
"ds_read_b64_tr_b16 v[100:101], v16 offset:27808\n"
"v_mfma_f32_16x16x32_bf16 v[172:175], v[84:87], a[252:255], v[172:175]\n"
"v_mfma_f32_16x16x32_bf16 v[172:175], v[80:83], a[248:251], v[172:175]\n"
"s_add_u32 s98, s98, s86\n"
"ds_read_b64_tr_b16 v[102:103], v16 offset:32416\n"
"s_waitcnt lgkmcnt(6)\n"
"v_mfma_f32_16x16x32_bf16 v[176:179], v[88:91], a[224:227], v[176:179]\n"
"buffer_load_dwordx4 a[120:123], v20, s[64:67], s98 offen sc1\n"
"s_waitcnt lgkmcnt(4)\n"
"v_mfma_f32_16x16x32_bf16 v[176:179], v[92:95], a[228:231], v[176:179]\n"
"ds_read_b64_tr_b16 v[80:81], v16 offset:18624\n"
"v_mfma_f32_16x16x32_bf16 v[180:183], v[92:95], a[236:239], v[180:183]\n"
"v_mfma_f32_16x16x32_bf16 v[180:183], v[88:91], a[232:235], v[180:183]\n"
"ds_read_b64_tr_b16 v[82:83], v16 offset:23232\n"
"v_mfma_f32_16x16x32_bf16 v[184:187], v[88:91], a[240:243], v[184:187]\n"
"v_mfma_f32_16x16x32_bf16 v[184:187], v[92:95], a[244:247], v[184:187]\n"
"ds_read_b64_tr_b16 v[84:85], v16 offset:27840\n"
"v_mfma_f32_16x16x32_bf16 v[188:191], v[92:95], a[252:255], v[188:191]\n"
"buffer_load_dwordx4 a[124:127], v20, s[64:67], s98 offen offset:1024 sc1\n"
"v_mfma_f32_16x16x32_bf16 v[188:191], v[88:91], a[248:251], v[188:191]\n"
"ds_read_b64_tr_b16 v[86:87], v16 offset:32448\n"
"s_waitcnt lgkmcnt(6)\n"
"v_mfma_f32_16x16x32_bf16 v[192:195], v[96:99], a[224:227], v[192:195]\n"
"s_waitcnt lgkmcnt(4)\n"
"v_mfma_f32_16x16x32_bf16 v[192:195], v[100:103], a[228:231], v[192:195]\n"
"s_add_u32 s77, s84, 0x5a000\n"
"s_add_u32 m0, s78, 0x16800\n"
"ds_read_b64_tr_b16 v[88:89], v16 offset:18656\n"
"v_mfma_f32_16x16x32_bf16 v[196:199], v[100:103], a[236:239], v[196:199]\n"
"buffer_load_dwordx4 v6, s[60:63], s77 offen lds\n"
"v_mfma_f32_16x16x32_bf16 v[196:199], v[96:99], a[232:235], v[196:199]\n"
"ds_read_b64_tr_b16 v[90:91], v16 offset:23264\n"
"v_mfma_f32_16x16x32_bf16 v[200:203], v[96:99], a[240:243], v[200:203]\n"
"v_mfma_f32_16x16x32_bf16 v[200:203], v[100:103], a[244:247], v[200:203]\n"
"ds_read_b64_tr_b16 v[92:93], v16 offset:27872\n"
"v_mfma_f32_16x16x32_bf16 v[204:207], v[100:103], a[252:255], v[204:207]\n"
"v_mfma_f32_16x16x32_bf16 v[204:207], v[96:99], a[248:251], v[204:207]\n"
"s_add_u32 m0, s78, 0x16c00\n"
"ds_read_b64_tr_b16 v[94:95], v16 offset:32480\n"
"s_waitcnt lgkmcnt(6)\n"
"v_mfma_f32_16x16x32_bf16 v[208:211], v[80:83], a[224:227], v[208:211]\n"
"buffer_load_dwordx4 v7, s[60:63], s77 offen lds\n"
"s_waitcnt lgkmcnt(4)\n"
"v_mfma_f32_16x16x32_bf16 v[208:211], v[84:87], a[228:231], v[208:211]\n"
"ds_read_b64_tr_b16 v[96:97], v16 offset:18688\n"
"v_mfma_f32_16x16x32_bf16 v[212:215], v[84:87], a[236:239], v[212:215]\n"
"v_mfma_f32_16x16x32_bf16 v[212:215], v[80:83], a[232:235], v[212:215]\n"
"ds_read_b64_tr_b16 v[98:99], v16 offset:23296\n"
"v_mfma_f32_16x16x32_bf16 v[216:219], v[80:83], a[240:243], v[216:219]\n"
"v_mfma_f32_16x16x32_bf16 v[216:219], v[84:87], a[244:247], v[216:219]\n"
"s_add_u32 m0, s78, 0x17000\n"
"ds_read_b64_tr_b16 v[100:101], v16 offset:27904\n"
"v_mfma_f32_16x16x32_bf16 v[220:223], v[84:87], a[252:255], v[220:223]\n"
"buffer_load_dwordx4 v8, s[60:63], s77 offen lds\n"
"v_mfma_f32_16x16x32_bf16 v[220:223], v[80:83], a[248:251], v[220:223]\n"
"s_nop 0\n"
"s_barrier\n"
"ds_read_b64_tr_b16 v[102:103], v16 offset:32512\n"
"s_waitcnt lgkmcnt(6)\n"
"v_mfma_f32_16x16x32_bf16 v[224:227], v[88:91], a[224:227], v[224:227]\n"
"s_waitcnt lgkmcnt(4)\n"
"v_mfma_f32_16x16x32_bf16 v[224:227], v[92:95], a[228:231], v[224:227]\n"
"s_add_u32 m0, s78, 0x17400\n"
"ds_read_b64_tr_b16 v[80:81], v16 offset:36864\n"
"v_mfma_f32_16x16x32_bf16 v[228:231], v[92:95], a[236:239], v[228:231]\n"
"buffer_load_dwordx4 v9, s[60:63], s77 offen lds\n"
"v_mfma_f32_16x16x32_bf16 v[228:231], v[88:91], a[232:235], v[228:231]\n"
"ds_read_b64_tr_b16 v[82:83], v16 offset:41472\n"
"v_mfma_f32_16x16x32_bf16 v[232:235], v[88:91], a[240:243], v[232:235]\n"
"v_mfma_f32_16x16x32_bf16 v[232:235], v[92:95], a[244:247], v[232:235]\n"
"ds_read_b64_tr_b16 v[84:85], v16 offset:46080\n"
"v_mfma_f32_16x16x32_bf16 v[236:239], v[92:95], a[252:255], v[236:239]\n"
"v_mfma_f32_16x16x32_bf16 v[236:239], v[88:91], a[248:251], v[236:239]\n"
"s_add_u32 m0, s78, 0x17800\n"
"ds_read_b64_tr_b16 v[86:87], v16 offset:50688\n"
"s_waitcnt lgkmcnt(6)\n"
"v_mfma_f32_16x16x32_bf16 v[240:243], v[96:99], a[224:227], v[240:243]\n"
"s_bitcmp1_b32 s72, 0\n"
"s_cbranch_scc1 NOP5_6_%=\n"
"buffer_load_dwordx4 v10, s[60:63], s77 offen lds\n"
"NOP5_6_%=:\n"
"s_waitcnt lgkmcnt(4)\n"
"v_mfma_f32_16x16x32_bf16 v[240:243], v[100:103], a[228:231], v[240:243]\n"
"ds_read_b64_tr_b16 v[88:89], v16 offset:36896\n"
"v_mfma_f32_16x16x32_bf16 v[244:247], v[100:103], a[236:239], v[244:247]\n"
"v_mfma_f32_16x16x32_bf16 v[244:247], v[96:99], a[232:235], v[244:247]\n"
"ds_read_b64_tr_b16 v[90:91], v16 offset:41504\n"
"v_mfma_f32_16x16x32_bf16 v[248:251], v[96:99], a[240:243], v[248:251]\n"
"v_mfma_f32_16x16x32_bf16 v[248:251], v[100:103], a[244:247], v[248:251]\n"
"ds_read_b64_tr_b16 v[92:93], v16 offset:46112\n"
"v_mfma_f32_16x16x32_bf16 v[252:255], v[100:103], a[252:255], v[252:255]\n"
"v_mfma_f32_16x16x32_bf16 v[252:255], v[96:99], a[248:251], v[252:255]\n"
"ds_read_b64_tr_b16 v[94:95], v16 offset:50720\n"
"s_waitcnt vmcnt(32)\n"
"s_waitcnt lgkmcnt(6)\n"
"v_mfma_f32_16x16x32_bf16 v[112:115], v[80:83], a[0:3], v[112:115]\n"
"s_waitcnt lgkmcnt(4)\n"
"v_mfma_f32_16x16x32_bf16 v[112:115], v[84:87], a[4:7], v[112:115]\n"
"s_add_u32 s98, s85, 0x3000\n"
"ds_read_b64_tr_b16 v[96:97], v16 offset:36928\n"
"v_mfma_f32_16x16x32_bf16 v[116:119], v[84:87], a[12:15], v[116:119]\n"
"buffer_load_dwordx4 a[128:131], v20, s[64:67], s98 offen sc1\n"
"v_mfma_f32_16x16x32_bf16 v[116:119], v[80:83], a[8:11], v[116:119]\n"
"ds_read_b64_tr_b16 v[98:99], v16 offset:41536\n"
"v_mfma_f32_16x16x32_bf16 v[120:123], v[80:83], a[16:19], v[120:123]\n"
"v_mfma_f32_16x16x32_bf16 v[120:123], v[84:87], a[20:23], v[120:123]\n"
"ds_read_b64_tr_b16 v[100:101], v16 offset:46144\n"
"v_mfma_f32_16x16x32_bf16 v[124:127], v[84:87], a[28:31], v[124:127]\n"
"buffer_load_dwordx4 a[132:135], v20, s[64:67], s98 offen offset:1024 sc1\n"
"v_mfma_f32_16x16x32_bf16 v[124:127], v[80:83], a[24:27], v[124:127]\n"
"ds_read_b64_tr_b16 v[102:103], v16 offset:50752\n"
"s_waitcnt lgkmcnt(6)\n"
"v_mfma_f32_16x16x32_bf16 v[128:131], v[88:91], a[0:3], v[128:131]\n"
"s_waitcnt lgkmcnt(4)\n"
"v_mfma_f32_16x16x32_bf16 v[128:131], v[92:95], a[4:7], v[128:131]\n"
"ds_read_b64_tr_b16 v[80:81], v16 offset:36960\n"
"v_mfma_f32_16x16x32_bf16 v[132:135], v[92:95], a[12:15], v[132:135]\n"
"v_mfma_f32_16x16x32_bf16 v[132:135], v[88:91], a[8:11], v[132:135]\n"
"s_add_u32 s98, s98, s86\n"
"ds_read_b64_tr_b16 v[82:83], v16 offset:41568\n"
"v_mfma_f32_16x16x32_bf16 v[136:139], v[88:91], a[16:19], v[136:139]\n"
"buffer_load_dwordx4 a[136:139], v20, s[64:67], s98 offen sc1\n"
"v_mfma_f32_16x16x32_bf16 v[136:139], v[92:95], a[20:23], v[136:139]\n"
"ds_read_b64_tr_b16 v[84:85], v16 offset:46176\n"
"v_mfma_f32_16x16x32_bf16 v[140:143], v[92:95], a[28:31], v[140:143]\n"
"v_mfma_f32_16x16x32_bf16 v[140:143], v[88:91], a[24:27], v[140:143]\n"
"ds_read_b64_tr_b16 v[86:87], v16 offset:50784\n"
"s_waitcnt lgkmcnt(6)\n"
"v_mfma_f32_16x16x32_bf16 v[144:147], v[96:99], a[0:3], v[144:147]\n"
"buffer_load_dwordx4 a[140:143], v20, s[64:67], s98 offen offset:1024 sc1\n"
"s_waitcnt lgkmcnt(4)\n"
"v_mfma_f32_16x16x32_bf16 v[144:147], v[100:103], a[4:7], v[144:147]\n"
"ds_read_b64_tr_b16 v[88:89], v16 offset:36992\n"
"v_mfma_f32_16x16x32_bf16 v[148:151], v[100:103], a[12:15], v[148:151]\n"
"v_mfma_f32_16x16x32_bf16 v[148:151], v[96:99], a[8:11], v[148:151]\n"
"ds_read_b64_tr_b16 v[90:91], v16 offset:41600\n"
"v_mfma_f32_16x16x32_bf16 v[152:155], v[96:99], a[16:19], v[152:155]\n"
"v_mfma_f32_16x16x32_bf16 v[152:155], v[100:103], a[20:23], v[152:155]\n"
"s_add_u32 s98, s98, s86\n"
"ds_read_b64_tr_b16 v[92:93], v16 offset:46208\n"
"v_mfma_f32_16x16x32_bf16 v[156:159], v[100:103], a[28:31], v[156:159]\n"
"buffer_load_dwordx4 a[144:147], v20, s[64:67], s98 offen sc1\n"
"v_mfma_f32_16x16x32_bf16 v[156:159], v[96:99], a[24:27], v[156:159]\n"
"ds_read_b64_tr_b16 v[94:95], v16 offset:50816\n"
"s_waitcnt lgkmcnt(6)\n"
"v_mfma_f32_16x16x32_bf16 v[160:163], v[80:83], a[0:3], v[160:163]\n"
"s_waitcnt lgkmcnt(4)\n"
"v_mfma_f32_16x16x32_bf16 v[160:163], v[84:87], a[4:7], v[160:163]\n"
"ds_read_b64_tr_b16 v[96:97], v16 offset:37024\n"
"v_mfma_f32_16x16x32_bf16 v[164:167], v[84:87], a[12:15], v[164:167]\n"
"v_mfma_f32_16x16x32_bf16 v[164:167], v[80:83], a[8:11], v[164:167]\n"
"ds_read_b64_tr_b16 v[98:99], v16 offset:41632\n"
"v_mfma_f32_16x16x32_bf16 v[168:171], v[80:83], a[16:19], v[168:171]\n"
"buffer_load_dwordx4 a[148:151], v20, s[64:67], s98 offen offset:1024 sc1\n"
"v_mfma_f32_16x16x32_bf16 v[168:171], v[84:87], a[20:23], v[168:171]\n"
"ds_read_b64_tr_b16 v[100:101], v16 offset:46240\n"
"v_mfma_f32_16x16x32_bf16 v[172:175], v[84:87], a[28:31], v[172:175]\n"
"v_mfma_f32_16x16x32_bf16 v[172:175], v[80:83], a[24:27], v[172:175]\n"
"s_add_u32 s98, s98, s86\n"
"ds_read_b64_tr_b16 v[102:103], v16 offset:50848\n"
"s_waitcnt lgkmcnt(6)\n"
"v_mfma_f32_16x16x32_bf16 v[176:179], v[88:91], a[0:3], v[176:179]\n"
"buffer_load_dwordx4 a[152:155], v20, s[64:67], s98 offen sc1\n"
"s_waitcnt lgkmcnt(4)\n"
"v_mfma_f32_16x16x32_bf16 v[176:179], v[92:95], a[4:7], v[176:179]\n"
"ds_read_b64_tr_b16 v[80:81], v16 offset:37056\n"
"v_mfma_f32_16x16x32_bf16 v[180:183], v[92:95], a[12:15], v[180:183]\n"
"v_mfma_f32_16x16x32_bf16 v[180:183], v[88:91], a[8:11], v[180:183]\n"
"ds_read_b64_tr_b16 v[82:83], v16 offset:41664\n"
"v_mfma_f32_16x16x32_bf16 v[184:187], v[88:91], a[16:19], v[184:187]\n"
"v_mfma_f32_16x16x32_bf16 v[184:187], v[92:95], a[20:23], v[184:187]\n"
"ds_read_b64_tr_b16 v[84:85], v16 offset:46272\n"
"v_mfma_f32_16x16x32_bf16 v[188:191], v[92:95], a[28:31], v[188:191]\n"
"buffer_load_dwordx4 a[156:159], v20, s[64:67], s98 offen offset:1024 sc1\n"
"v_mfma_f32_16x16x32_bf16 v[188:191], v[88:91], a[24:27], v[188:191]\n"
"ds_read_b64_tr_b16 v[86:87], v16 offset:50880\n"
"s_waitcnt lgkmcnt(6)\n"
"v_mfma_f32_16x16x32_bf16 v[192:195], v[96:99], a[0:3], v[192:195]\n"
"s_waitcnt lgkmcnt(4)\n"
"v_mfma_f32_16x16x32_bf16 v[192:195], v[100:103], a[4:7], v[192:195]\n"
"s_add_u32 s77, s84, 0x6c000\n"
"s_add_u32 m0, s78, 0x1b000\n"
"ds_read_b64_tr_b16 v[88:89], v16 offset:37088\n"
"v_mfma_f32_16x16x32_bf16 v[196:199], v[100:103], a[12:15], v[196:199]\n"
"buffer_load_dwordx4 v6, s[60:63], s77 offen lds\n"
"v_mfma_f32_16x16x32_bf16 v[196:199], v[96:99], a[8:11], v[196:199]\n"
"ds_read_b64_tr_b16 v[90:91], v16 offset:41696\n"
"v_mfma_f32_16x16x32_bf16 v[200:203], v[96:99], a[16:19], v[200:203]\n"
"v_mfma_f32_16x16x32_bf16 v[200:203], v[100:103], a[20:23], v[200:203]\n"
"ds_read_b64_tr_b16 v[92:93], v16 offset:46304\n"
"v_mfma_f32_16x16x32_bf16 v[204:207], v[100:103], a[28:31], v[204:207]\n"
"v_mfma_f32_16x16x32_bf16 v[204:207], v[96:99], a[24:27], v[204:207]\n"
"s_add_u32 m0, s78, 0x1b400\n"
"ds_read_b64_tr_b16 v[94:95], v16 offset:50912\n"
"s_waitcnt lgkmcnt(6)\n"
"v_mfma_f32_16x16x32_bf16 v[208:211], v[80:83], a[0:3], v[208:211]\n"
"buffer_load_dwordx4 v7, s[60:63], s77 offen lds\n"
"s_waitcnt lgkmcnt(4)\n"
"v_mfma_f32_16x16x32_bf16 v[208:211], v[84:87], a[4:7], v[208:211]\n"
"ds_read_b64_tr_b16 v[96:97], v16 offset:37120\n"
"v_mfma_f32_16x16x32_bf16 v[212:215], v[84:87], a[12:15], v[212:215]\n"
"v_mfma_f32_16x16x32_bf16 v[212:215], v[80:83], a[8:11], v[212:215]\n"
"ds_read_b64_tr_b16 v[98:99], v16 offset:41728\n"
"v_mfma_f32_16x16x32_bf16 v[216:219], v[80:83], a[16:19], v[216:219]\n"
"v_mfma_f32_16x16x32_bf16 v[216:219], v[84:87], a[20:23], v[216:219]\n"
"s_add_u32 m0, s78, 0x1b800\n"
"ds_read_b64_tr_b16 v[100:101], v16 offset:46336\n"
"v_mfma_f32_16x16x32_bf16 v[220:223], v[84:87], a[28:31], v[220:223]\n"
"buffer_load_dwordx4 v8, s[60:63], s77 offen lds\n"
"v_mfma_f32_16x16x32_bf16 v[220:223], v[80:83], a[24:27], v[220:223]\n"
"s_nop 0\n"
"s_barrier\n"
"ds_read_b64_tr_b16 v[102:103], v16 offset:50944\n"
"s_waitcnt lgkmcnt(6)\n"
"v_mfma_f32_16x16x32_bf16 v[224:227], v[88:91], a[0:3], v[224:227]\n"
"s_waitcnt lgkmcnt(4)\n"
"v_mfma_f32_16x16x32_bf16 v[224:227], v[92:95], a[4:7], v[224:227]\n"
"s_add_u32 m0, s78, 0x1bc00\n"
"ds_read_b64_tr_b16 v[80:81], v17\n"
"v_mfma_f32_16x16x32_bf16 v[228:231], v[92:95], a[12:15], v[228:231]\n"
"buffer_load_dwordx4 v9, s[60:63], s77 offen lds\n"
"v_mfma_f32_16x16x32_bf16 v[228:231], v[88:91], a[8:11], v[228:231]\n"
"ds_read_b64_tr_b16 v[82:83], v17 offset:4608\n"
"v_mfma_f32_16x16x32_bf16 v[232:235], v[88:91], a[16:19], v[232:235]\n"
"v_mfma_f32_16x16x32_bf16 v[232:235], v[92:95], a[20:23], v[232:235]\n"
"ds_read_b64_tr_b16 v[84:85], v17 offset:9216\n"
"v_mfma_f32_16x16x32_bf16 v[236:239], v[92:95], a[28:31], v[236:239]\n"
"v_mfma_f32_16x16x32_bf16 v[236:239], v[88:91], a[24:27], v[236:239]\n"
"s_add_u32 m0, s78, 0x1c000\n"
"ds_read_b64_tr_b16 v[86:87], v17 offset:13824\n"
"s_waitcnt lgkmcnt(6)\n"
"v_mfma_f32_16x16x32_bf16 v[240:243], v[96:99], a[0:3], v[240:243]\n"
"s_bitcmp1_b32 s72, 0\n"
"s_cbranch_scc1 NOP5_7_%=\n"
"buffer_load_dwordx4 v10, s[60:63], s77 offen lds\n"
"NOP5_7_%=:\n"
"s_waitcnt lgkmcnt(4)\n"
"v_mfma_f32_16x16x32_bf16 v[240:243], v[100:103], a[4:7], v[240:243]\n"
"ds_read_b64_tr_b16 v[88:89], v17 offset:32\n"
"v_mfma_f32_16x16x32_bf16 v[244:247], v[100:103], a[12:15], v[244:247]\n"
"v_mfma_f32_16x16x32_bf16 v[244:247], v[96:99], a[8:11], v[244:247]\n"
"ds_read_b64_tr_b16 v[90:91], v17 offset:4640\n"
"v_mfma_f32_16x16x32_bf16 v[248:251], v[96:99], a[16:19], v[248:251]\n"
"v_mfma_f32_16x16x32_bf16 v[248:251], v[100:103], a[20:23], v[248:251]\n"
"ds_read_b64_tr_b16 v[92:93], v17 offset:9248\n"
"v_mfma_f32_16x16x32_bf16 v[252:255], v[100:103], a[28:31], v[252:255]\n"
"v_mfma_f32_16x16x32_bf16 v[252:255], v[96:99], a[24:27], v[252:255]\n"
"ds_read_b64_tr_b16 v[94:95], v17 offset:13856\n"
"s_waitcnt vmcnt(36)\n"
"s_branch OJOIN_%=\n"
"SPATH_%=:\n"
"s_waitcnt vmcnt(0)\n"
"s_barrier\n"
"s_cmp_lg_u32 s72, 0\n"
"s_cbranch_scc1 NOSIGOS_%=\n"
"s_lshl_b32 s76, s75, 5\n"
"s_add_u32 s76, s76, s74\n"
"s_lshl_b32 s76, s76, 3\n"
"s_add_u32 s76, s76, 0x2000\n"
"v_mov_b32 v72, s76\n"
"v_mov_b32 v74, s88\n"
"v_mov_b32 v75, s89\n"
"s_mov_b64 exec, 1\n"
"global_store_dwordx2 v72, v[74:75], s[92:93] sc1\n"
"s_mov_b64 exec, -1\n"
"NOSIGOS_%=:\n"
"s_lshl_b32 s76, s75, 8\n"
"s_add_u32 s76, s76, 0x2000\n"
"v_and_b32 v76, 31, v5\n"
"v_lshlrev_b32 v76, 3, v76\n"
"v_add_u32 v76, s76, v76\n"
"global_load_dwordx2 v[78:79], v76, s[92:93] sc1\n"
"s_lshl_b32 s98, s81, 3\n"
"s_lshl_b32 s98, 15, s98\n"
"s_waitcnt vmcnt(0)\n"
"v_cmp_eq_u64 vcc, v[78:79], s[88:89]\n"
"s_nop 0\n"
"s_and_b32 s76, vcc_lo, s98\n"
"s_mov_b32 s99, 0\n"
"s_cmp_eq_u32 s76, s98\n"
"s_cbranch_scc1 POLLDO_%=\n"
"global_load_dwordx2 v[24:25], v76, s[92:93] sc1\n"
"s_sleep 3\n"
"global_load_dwordx2 v[26:27], v76, s[92:93] sc1\n"
"s_sleep 3\n"
"global_load_dwordx2 v[28:29], v76, s[92:93] sc1\n"
"s_sleep 3\n"
"POLLO_%=:\n"
"global_load_dwordx2 v[30:31], v76, s[92:93] sc1\n"
"s_waitcnt vmcnt(3)\n"
"v_cmp_eq_u64 vcc, v[24:25], s[88:89]\n"
"s_nop 3\n"
"s_and_b32 s76, vcc_lo, s98\n"
"s_cmp_eq_u32 s76, s98\n"
"s_cbranch_scc1 POLLDO_%=\n"
"s_sleep 2\n"
"global_load_dwordx2 v[24:25], v76, s[92:93] sc1\n"
"s_waitcnt vmcnt(3)\n"
"v_cmp_eq_u64 vcc, v[26:27], s[88:89]\n"
"s_nop 3\n"
"s_and_b32 s76, vcc_lo, s98\n"
"s_cmp_eq_u32 s76, s98\n"
"s_cbranch_scc1 POLLDO_%=\n"
"s_sleep 2\n"
"global_load_dwordx2 v[26:27], v76, s[92:93] sc1\n"
"s_waitcnt vmcnt(3)\n"
"v_cmp_eq_u64 vcc, v[28:29], s[88:89]\n"
"s_nop 3\n"
"s_and_b32 s76, vcc_lo, s98\n"
"s_cmp_eq_u32 s76, s98\n"
"s_cbranch_scc1 POLLDO_%=\n"
"s_sleep 2\n"
"global_load_dwordx2 v[28:29], v76, s[92:93] sc1\n"
"s_waitcnt vmcnt(3)\n"
"v_cmp_eq_u64 vcc, v[30:31], s[88:89]\n"
"s_nop 3\n"
"s_and_b32 s76, vcc_lo, s98\n"
"s_cmp_eq_u32 s76, s98\n"
"s_cbranch_scc1 POLLDO_%=\n"
"s_sleep 2\n"
"s_add_u32 s99, s99, 1\n"
"s_cmp_gt_u32 s99, 0x40000\n"
"s_cbranch_scc0 POLLO_%=\n"
"POLLDO_%=:\n"
"s_add_u32 s98, s85, 0x1000\n"
"buffer_load_dwordx4 a[0:3], v20, s[64:67], s98 offen sc1\n"
"buffer_load_dwordx4 a[4:7], v20, s[64:67], s98 offen offset:1024 sc1\n"
"s_add_u32 s98, s98, s86\n"
"buffer_load_dwordx4 a[8:11], v20, s[64:67], s98 offen sc1\n"
"buffer_load_dwordx4 a[12:15], v20, s[64:67], s98 offen offset:1024 sc1\n"
"s_add_u32 s98, s98, s86\n"
"buffer_load_dwordx4 a[16:19], v20, s[64:67], s98 offen sc1\n"
"buffer_load_dwordx4 a[20:23], v20, s[64:67], s98 offen offset:1024 sc1\n"
"s_add_u32 s98, s98, s86\n"
"buffer_load_dwordx4 a[24:27], v20, s[64:67], s98 offen sc1\n"
"buffer_load_dwordx4 a[28:31], v20, s[64:67], s98 offen offset:1024 sc1\n"
"ds_read_b64_tr_b16 v[80:81], v16 offset:36864\n"
"ds_read_b64_tr_b16 v[82:83], v16 offset:41472\n"
"ds_read_b64_tr_b16 v[84:85], v16 offset:46080\n"
"ds_read_b64_tr_b16 v[86:87], v16 offset:50688\n"
"ds_read_b64_tr_b16 v[88:89], v16 offset:36896\n"
"ds_read_b64_tr_b16 v[90:91], v16 offset:41504\n"
"ds_read_b64_tr_b16 v[92:93], v16 offset:46112\n"
"ds_read_b64_tr_b16 v[94:95], v16 offset:50720\n"
"s_waitcnt vmcnt(7) lgkmcnt(6)\n"
"v_mfma_f32_16x16x32_bf16 v[112:115], v[80:83], a[0:3], 0\n"
"s_waitcnt vmcnt(6) lgkmcnt(4)\n"
"v_mfma_f32_16x16x32_bf16 v[112:115], v[84:87], a[4:7], v[112:115]\n"
"s_add_u32 s98, s85, 0x1800\n"
"ds_read_b64_tr_b16 v[96:97], v16 offset:36928\n"
"s_waitcnt vmcnt(4)\n"
"v_mfma_f32_16x16x32_bf16 v[116:119], v[84:87], a[12:15], 0\n"
"buffer_load_dwordx4 a[32:35], v20, s[64:67], s98 offen sc1\n"
"buffer_load_dwordx4 a[36:39], v20, s[64:67], s98 offen offset:1024 sc1\n"
"v_mfma_f32_16x16x32_bf16 v[116:119], v[80:83], a[8:11], v[116:119]\n"
"s_add_u32 s98, s98, s86\n"
"ds_read_b64_tr_b16 v[98:99], v16 offset:41536\n"
"s_waitcnt vmcnt(5)\n"
"v_mfma_f32_16x16x32_bf16 v[120:123], v[80:83], a[16:19], 0\n"
"buffer_load_dwordx4 a[40:43], v20, s[64:67], s98 offen sc1\n"
"s_waitcnt vmcnt(5)\n"
"v_mfma_f32_16x16x32_bf16 v[120:123], v[84:87], a[20:23], v[120:123]\n"
"ds_read_b64_tr_b16 v[100:101], v16 offset:46144\n"
"s_waitcnt vmcnt(3)\n"
"v_mfma_f32_16x16x32_bf16 v[124:127], v[84:87], a[28:31], 0\n"
"buffer_load_dwordx4 a[44:47], v20, s[64:67], s98 offen offset:1024 sc1\n"
"v_mfma_f32_16x16x32_bf16 v[124:127], v[80:83], a[24:27], v[124:127]\n"
"s_add_u32 s98, s98, s86\n"
"ds_read_b64_tr_b16 v[102:103], v16 offset:50752\n"
"s_waitcnt lgkmcnt(6)\n"
"v_mfma_f32_16x16x32_bf16 v[128:131], v[88:91], a[0:3], 0\n"
"buffer_load_dwordx4 a[48:51], v20, s[64:67], s98 offen sc1\n"
"s_waitcnt lgkmcnt(4)\n"
"v_mfma_f32_16x16x32_bf16 v[128:131], v[92:95], a[4:7], v[128:131]\n"
"ds_read_b64_tr_b16 v[80:81], v16 offset:36960\n"
"v_mfma_f32_16x16x32_bf16 v[132:135], v[92:95], a[12:15], 0\n"
"buffer_load_dwordx4 a[52:55], v20, s[64:67], s98 offen offset:1024 sc1\n"
"v_mfma_f32_16x16x32_bf16 v[132:135], v[88:91], a[8:11], v[132:135]\n"
"s_add_u32 s98, s98, s86\n"
"ds_read_b64_tr_b16 v[82:83], v16 offset:41568\n"
"v_mfma_f32_16x16x32_bf16 v[136:139], v[88:91], a[16:19], 0\n"
"buffer_load_dwordx4 a[56:59], v20, s[64:67], s98 offen sc1\n"
"v_mfma_f32_16x16x32_bf16 v[136:139], v[92:95], a[20:23], v[136:139]\n"
"ds_read_b64_tr_b16 v[84:85], v16 offset:46176\n"
"v_mfma_f32_16x16x32_bf16 v[140:143], v[92:95], a[28:31], 0\n"
"buffer_load_dwordx4 a[60:63], v20, s[64:67], s98 offen offset:1024 sc1\n"
"v_mfma_f32_16x16x32_bf16 v[140:143], v[88:91], a[24:27], v[140:143]\n"
"s_add_u32 s98, s85, 0x2000\n"
"ds_read_b64_tr_b16 v[86:87], v16 offset:50784\n"
"s_waitcnt lgkmcnt(6)\n"
"v_mfma_f32_16x16x32_bf16 v[144:147], v[96:99], a[0:3], 0\n"
"buffer_load_dwordx4 a[64:67], v20, s[64:67], s98 offen sc1\n"
"s_waitcnt lgkmcnt(4)\n"
"v_mfma_f32_16x16x32_bf16 v[144:147], v[100:103], a[4:7], v[144:147]\n"
"ds_read_b64_tr_b16 v[88:89], v16 offset:36992\n"
"v_mfma_f32_16x16x32_bf16 v[148:151], v[100:103], a[12:15], 0\n"
"buffer_load_dwordx4 a[68:71], v20, s[64:67], s98 offen offset:1024 sc1\n"
"v_mfma_f32_16x16x32_bf16 v[148:151], v[96:99], a[8:11], v[148:151]\n"
"s_add_u32 s98, s98, s86\n"
"ds_read_b64_tr_b16 v[90:91], v16 offset:41600\n"
"v_mfma_f32_16x16x32_bf16 v[152:155], v[96:99], a[16:19], 0\n"
"buffer_load_dwordx4 a[72:75], v20, s[64:67], s98 offen sc1\n"
"v_mfma_f32_16x16x32_bf16 v[152:155], v[100:103], a[20:23], v[152:155]\n"
"ds_read_b64_tr_b16 v[92:93], v16 offset:46208\n"
"v_mfma_f32_16x16x32_bf16 v[156:159], v[100:103], a[28:31], 0\n"
"buffer_load_dwordx4 a[76:79], v20, s[64:67], s98 offen offset:1024 sc1\n"
"v_mfma_f32_16x16x32_bf16 v[156:159], v[96:99], a[24:27], v[156:159]\n"
"s_add_u32 s98, s98, s86\n"
"ds_read_b64_tr_b16 v[94:95], v16 offset:50816\n"
"s_waitcnt lgkmcnt(6)\n"
"v_mfma_f32_16x16x32_bf16 v[160:163], v[80:83], a[0:3], 0\n"
"buffer_load_dwordx4 a[80:83], v20, s[64:67], s98 offen sc1\n"
"buffer_load_dwordx4 a[84:87], v20, s[64:67], s98 offen offset:1024 sc1\n"
"s_waitcnt lgkmcnt(4)\n"
"v_mfma_f32_16x16x32_bf16 v[160:163], v[84:87], a[4:7], v[160:163]\n"
"s_add_u32 s98, s98, s86\n"
"ds_read_b64_tr_b16 v[96:97], v16 offset:37024\n"
"v_mfma_f32_16x16x32_bf16 v[164:167], v[84:87], a[12:15], 0\n"
"buffer_load_dwordx4 a[88:91], v20, s[64:67], s98 offen sc1\n"
"v_mfma_f32_16x16x32_bf16 v[164:167], v[80:83], a[8:11], v[164:167]\n"
"ds_read_b64_tr_b16 v[98:99], v16 offset:41632\n"
"v_mfma_f32_16x16x32_bf16 v[168:171], v[80:83], a[16:19], 0\n"
"buffer_load_dwordx4 a[92:95], v20, s[64:67], s98 offen offset:1024 sc1\n"
"v_mfma_f32_16x16x32_bf16 v[168:171], v[84:87], a[20:23], v[168:171]\n"
"s_add_u32 s98, s85, 0x2800\n"
"ds_read_b64_tr_b16 v[100:101], v16 offset:46240\n"
"v_mfma_f32_16x16x32_bf16 v[172:175], v[84:87], a[28:31], 0\n"
"buffer_load_dwordx4 a[96:99], v20, s[64:67], s98 offen sc1\n"
"v_mfma_f32_16x16x32_bf16 v[172:175], v[80:83], a[24:27], v[172:175]\n"
"ds_read_b64_tr_b16 v[102:103], v16 offset:50848\n"
"s_waitcnt lgkmcnt(6)\n"
"v_mfma_f32_16x16x32_bf16 v[176:179], v[88:91], a[0:3], 0\n"
"buffer_load_dwordx4 a[100:103], v20, s[64:67], s98 offen offset:1024 sc1\n"
"s_waitcnt lgkmcnt(4)\n"
"v_mfma_f32_16x16x32_bf16 v[176:179], v[92:95], a[4:7], v[176:179]\n"
"s_add_u32 s98, s98, s86\n"
"ds_read_b64_tr_b16 v[80:81], v16 offset:37056\n"
"v_mfma_f32_16x16x32_bf16 v[180:183], v[92:95], a[12:15], 0\n"
"buffer_load_dwordx4 a[104:107], v20, s[64:67], s98 offen sc1\n"
"v_mfma_f32_16x16x32_bf16 v[180:183], v[88:91], a[8:11], v[180:183]\n"
"ds_read_b64_tr_b16 v[82:83], v16 offset:41664\n"
"v_mfma_f32_16x16x32_bf16 v[184:187], v[88:91], a[16:19], 0\n"
"buffer_load_dwordx4 a[108:111], v20, s[64:67], s98 offen offset:1024 sc1\n"
"v_mfma_f32_16x16x32_bf16 v[184:187], v[92:95], a[20:23], v[184:187]\n"
"s_add_u32 s98, s98, s86\n"
"ds_read_b64_tr_b16 v[84:85], v16 offset:46272\n"
"v_mfma_f32_16x16x32_bf16 v[188:191], v[92:95], a[28:31], 0\n"
"buffer_load_dwordx4 a[112:115], v20, s[64:67], s98 offen sc1\n"
"v_mfma_f32_16x16x32_bf16 v[188:191], v[88:91], a[24:27], v[188:191]\n"
"ds_read_b64_tr_b16 v[86:87], v16 offset:50880\n"
"s_waitcnt lgkmcnt(6)\n"
"v_mfma_f32_16x16x32_bf16 v[192:195], v[96:99], a[0:3], 0\n"
"buffer_load_dwordx4 a[116:119], v20, s[64:67], s98 offen offset:1024 sc1\n"
"s_waitcnt lgkmcnt(4)\n"
"v_mfma_f32_16x16x32_bf16 v[192:195], v[100:103], a[4:7], v[192:195]\n"
"s_add_u32 s98, s98, s86\n"
"ds_read_b64_tr_b16 v[88:89], v16 offset:37088\n"
"v_mfma_f32_16x16x32_bf16 v[196:199], v[100:103], a[12:15], 0\n"
"buffer_load_dwordx4 a[120:123], v20, s[64:67], s98 offen sc1\n"
"v_mfma_f32_16x16x32_bf16 v[196:199], v[96:99], a[8:11], v[196:199]\n"
"ds_read_b64_tr_b16 v[90:91], v16 offset:41696\n"
"v_mfma_f32_16x16x32_bf16 v[200:203], v[96:99], a[16:19], 0\n"
"buffer_load_dwordx4 a[124:127], v20, s[64:67], s98 offen offset:1024 sc1\n"
"v_mfma_f32_16x16x32_bf16 v[200:203], v[100:103], a[20:23], v[200:203]\n"
"s_add_u32 s98, s85, 0x3000\n"
"ds_read_b64_tr_b16 v[92:93], v16 offset:46304\n"
"v_mfma_f32_16x16x32_bf16 v[204:207], v[100:103], a[28:31], 0\n"
"buffer_load_dwordx4 a[128:131], v20, s[64:67], s98 offen sc1\n"
"buffer_load_dwordx4 a[132:135], v20, s[64:67], s98 offen offset:1024 sc1\n"
"v_mfma_f32_16x16x32_bf16 v[204:207], v[96:99], a[24:27], v[204:207]\n"
"s_add_u32 s98, s98, s86\n"
"ds_read_b64_tr_b16 v[94:95], v16 offset:50912\n"
"s_waitcnt lgkmcnt(6)\n"
"v_mfma_f32_16x16x32_bf16 v[208:211], v[80:83], a[0:3], 0\n"
"buffer_load_dwordx4 a[136:139], v20, s[64:67], s98 offen sc1\n"
"s_waitcnt lgkmcnt(4)\n"
"v_mfma_f32_16x16x32_bf16 v[208:211], v[84:87], a[4:7], v[208:211]\n"
"ds_read_b64_tr_b16 v[96:97], v16 offset:37120\n"
"v_mfma_f32_16x16x32_bf16 v[212:215], v[84:87], a[12:15], 0\n"
"buffer_load_dwordx4 a[140:143], v20, s[64:67], s98 offen offset:1024 sc1\n"
"v_mfma_f32_16x16x32_bf16 v[212:215], v[80:83], a[8:11], v[212:215]\n"
"s_add_u32 s98, s98, s86\n"
"ds_read_b64_tr_b16 v[98:99], v16 offset:41728\n"
"v_mfma_f32_16x16x32_bf16 v[216:219], v[80:83], a[16:19], 0\n"
"buffer_load_dwordx4 a[144:147], v20, s[64:67], s98 offen sc1\n"
"v_mfma_f32_16x16x32_bf16 v[216:219], v[84:87], a[20:23], v[216:219]\n"
"ds_read_b64_tr_b16 v[100:101], v16 offset:46336\n"
"v_mfma_f32_16x16x32_bf16 v[220:223], v[84:87], a[28:31], 0\n"
"buffer_load_dwordx4 a[148:151], v20, s[64:67], s98 offen offset:1024 sc1\n"
"v_mfma_f32_16x16x32_bf16 v[220:223], v[80:83], a[24:27], v[220:223]\n"
"s_barrier\n"
"s_add_u32 s98, s98, s86\n"
"ds_read_b64_tr_b16 v[102:103], v16 offset:50944\n"
"s_waitcnt lgkmcnt(6)\n"
"v_mfma_f32_16x16x32_bf16 v[224:227], v[88:91], a[0:3], 0\n"
"buffer_load_dwordx4 a[152:155], v20, s[64:67], s98 offen sc1\n"
"s_waitcnt lgkmcnt(4)\n"
"v_mfma_f32_16x16x32_bf16 v[224:227], v[92:95], a[4:7], v[224:227]\n"
"ds_read_b64_tr_b16 v[80:81], v17\n"
"v_mfma_f32_16x16x32_bf16 v[228:231], v[92:95], a[12:15], 0\n"
"buffer_load_dwordx4 a[156:159], v20, s[64:67], s98 offen offset:1024 sc1\n"
"v_mfma_f32_16x16x32_bf16 v[228:231], v[88:91], a[8:11], v[228:231]\n"
"s_add_u32 s77, s84, 0x6c000\n"
"s_add_u32 m0, s78, 0x1b000\n"
"ds_read_b64_tr_b16 v[82:83], v17 offset:4608\n"
"v_mfma_f32_16x16x32_bf16 v[232:235], v[88:91], a[16:19], 0\n"
"buffer_load_dwordx4 v6, s[60:63], s77 offen lds\n"
"v_mfma_f32_16x16x32_bf16 v[232:235], v[92:95], a[20:23], v[232:235]\n"
"s_add_u32 m0, s78, 0x1b400\n"
"ds_read_b64_tr_b16 v[84:85], v17 offset:9216\n"
"v_mfma_f32_16x16x32_bf16 v[236:239], v[92:95], a[28:31], 0\n"
"buffer_load_dwordx4 v7, s[60:63], s77 offen lds\n"
"v_mfma_f32_16x16x32_bf16 v[236:239], v[88:91], a[24:27], v[236:239]\n"
"s_add_u32 m0, s78, 0x1b800\n"
"ds_read_b64_tr_b16 v[86:87], v17 offset:13824\n"
"s_waitcnt lgkmcnt(6)\n"
"v_mfma_f32_16x16x32_bf16 v[240:243], v[96:99], a[0:3], 0\n"
"buffer_load_dwordx4 v8, s[60:63], s77 offen lds\n"
"s_waitcnt lgkmcnt(4)\n"
"v_mfma_f32_16x16x32_bf16 v[240:243], v[100:103], a[4:7], v[240:243]\n"
"s_add_u32 m0, s78, 0x1bc00\n"
"ds_read_b64_tr_b16 v[88:89], v17 offset:32\n"
"v_mfma_f32_16x16x32_bf16 v[244:247], v[100:103], a[12:15], 0\n"
"buffer_load_dwordx4 v9, s[60:63], s77 offen lds\n"
"v_mfma_f32_16x16x32_bf16 v[244:247], v[96:99], a[8:11], v[244:247]\n"
"s_add_u32 m0, s78, 0x1c000\n"
"ds_read_b64_tr_b16 v[90:91], v17 offset:4640\n"
"v_mfma_f32_16x16x32_bf16 v[248:251], v[96:99], a[16:19], 0\n"
"s_bitcmp1_b32 s72, 0\n"
"s_cbranch_scc1 NOP5_8_%=\n"
"buffer_load_dwordx4 v10, s[60:63], s77 offen lds\n"
"NOP5_8_%=:\n"
"v_mfma_f32_16x16x32_bf16 v[248:251], v[100:103], a[20:23], v[248:251]\n"
"ds_read_b64_tr_b16 v[92:93], v17 offset:9248\n"
"v_mfma_f32_16x16x32_bf16 v[252:255], v[100:103], a[28:31], 0\n"
"v_mfma_f32_16x16x32_bf16 v[252:255], v[96:99], a[24:27], v[252:255]\n"
"ds_read_b64_tr_b16 v[94:95], v17 offset:13856\n"
"s_waitcnt vmcnt(28)\n"
"OJOIN_%=:\n"
"s_waitcnt lgkmcnt(6)\n"
"v_mfma_f32_16x16x32_bf16 v[112:115], v[80:83], a[32:35], v[112:115]\n"
"s_waitcnt lgkmcnt(4)\n"
"v_mfma_f32_16x16x32_bf16 v[112:115], v[84:87], a[36:39], v[112:115]\n"
"s_add_u32 s98, s85, 0x3800\n"
"ds_read_b64_tr_b16 v[96:97], v17 offset:64\n"
"v_mfma_f32_16x16x32_bf16 v[116:119], v[84:87], a[44:47], v[116:119]\n"
"buffer_load_dwordx4 a[160:163], v20, s[64:67], s98 offen sc1\n"
"v_mfma_f32_16x16x32_bf16 v[116:119], v[80:83], a[40:43], v[116:119]\n"
"ds_read_b64_tr_b16 v[98:99], v17 offset:4672\n"
"v_mfma_f32_16x16x32_bf16 v[120:123], v[80:83], a[48:51], v[120:123]\n"
"v_mfma_f32_16x16x32_bf16 v[120:123], v[84:87], a[52:55], v[120:123]\n"
"ds_read_b64_tr_b16 v[100:101], v17 offset:9280\n"
"v_mfma_f32_16x16x32_bf16 v[124:127], v[84:87], a[60:63], v[124:127]\n"
"buffer_load_dwordx4 a[164:167], v20, s[64:67], s98 offen offset:1024 sc1\n"
"v_mfma_f32_16x16x32_bf16 v[124:127], v[80:83], a[56:59], v[124:127]\n"
"ds_read_b64_tr_b16 v[102:103], v17 offset:13888\n"
"s_waitcnt lgkmcnt(6)\n"
"v_mfma_f32_16x16x32_bf16 v[128:131], v[88:91], a[32:35], v[128:131]\n"
"s_waitcnt lgkmcnt(4)\n"
"v_mfma_f32_16x16x32_bf16 v[128:131], v[92:95], a[36:39], v[128:131]\n"
"ds_read_b64_tr_b16 v[80:81], v17 offset:96\n"
"v_mfma_f32_16x16x32_bf16 v[132:135], v[92:95], a[44:47], v[132:135]\n"
"v_mfma_f32_16x16x32_bf16 v[132:135], v[88:91], a[40:43], v[132:135]\n"
"s_add_u32 s98, s98, s86\n"
"ds_read_b64_tr_b16 v[82:83], v17 offset:4704\n"
"v_mfma_f32_16x16x32_bf16 v[136:139], v[88:91], a[48:51], v[136:139]\n"
"buffer_load_dwordx4 a[168:171], v20, s[64:67], s98 offen sc1\n"
"v_mfma_f32_16x16x32_bf16 v[136:139], v[92:95], a[52:55], v[136:139]\n"
"ds_read_b64_tr_b16 v[84:85], v17 offset:9312\n"
"v_mfma_f32_16x16x32_bf16 v[140:143], v[92:95], a[60:63], v[140:143]\n"
"v_mfma_f32_16x16x32_bf16 v[140:143], v[88:91], a[56:59], v[140:143]\n"
"ds_read_b64_tr_b16 v[86:87], v17 offset:13920\n"
"s_waitcnt lgkmcnt(6)\n"
"v_mfma_f32_16x16x32_bf16 v[144:147], v[96:99], a[32:35], v[144:147]\n"
"buffer_load_dwordx4 a[172:175], v20, s[64:67], s98 offen offset:1024 sc1\n"
"s_waitcnt lgkmcnt(4)\n"
"v_mfma_f32_16x16x32_bf16 v[144:147], v[100:103], a[36:39], v[144:147]\n"
"ds_read_b64_tr_b16 v[88:89], v17 offset:128\n"
"v_mfma_f32_16x16x32_bf16 v[148:151], v[100:103], a[44:47], v[148:151]\n"
"v_mfma_f32_16x16x32_bf16 v[148:151], v[96:99], a[40:43], v[148:151]\n"
"ds_read_b64_tr_b16 v[90:91], v17 offset:4736\n"
"v_mfma_f32_16x16x32_bf16 v[152:155], v[96:99], a[48:51], v[152:155]\n"
"v_mfma_f32_16x16x32_bf16 v[152:155], v[100:103], a[52:55], v[152:155]\n"
"s_add_u32 s98, s98, s86\n"
"ds_read_b64_tr_b16 v[92:93], v17 offset:9344\n"
"v_mfma_f32_16x16x32_bf16 v[156:159], v[100:103], a[60:63], v[156:159]\n"
"buffer_load_dwordx4 a[176:179], v20, s[64:67], s98 offen sc1\n"
"v_mfma_f32_16x16x32_bf16 v[156:159], v[96:99], a[56:59], v[156:159]\n"
"ds_read_b64_tr_b16 v[94:95], v17 offset:13952\n"
"s_waitcnt lgkmcnt(6)\n"
"v_mfma_f32_16x16x32_bf16 v[160:163], v[80:83], a[32:35], v[160:163]\n"
"s_waitcnt lgkmcnt(4)\n"
"v_mfma_f32_16x16x32_bf16 v[160:163], v[84:87], a[36:39], v[160:163]\n"
"ds_read_b64_tr_b16 v[96:97], v17 offset:160\n"
"v_mfma_f32_16x16x32_bf16 v[164:167], v[84:87], a[44:47], v[164:167]\n"
"v_mfma_f32_16x16x32_bf16 v[164:167], v[80:83], a[40:43], v[164:167]\n"
"ds_read_b64_tr_b16 v[98:99], v17 offset:4768\n"
"v_mfma_f32_16x16x32_bf16 v[168:171], v[80:83], a[48:51], v[168:171]\n"
"buffer_load_dwordx4 a[180:183], v20, s[64:67], s98 offen offset:1024 sc1\n"
"v_mfma_f32_16x16x32_bf16 v[168:171], v[84:87], a[52:55], v[168:171]\n"
"ds_read_b64_tr_b16 v[100:101], v17 offset:9376\n"
"v_mfma_f32_16x16x32_bf16 v[172:175], v[84:87], a[60:63], v[172:175]\n"
"v_mfma_f32_16x16x32_bf16 v[172:175], v[80:83], a[56:59], v[172:175]\n"
"s_add_u32 s98, s98, s86\n"
"ds_read_b64_tr_b16 v[102:103], v17 offset:13984\n"
"s_waitcnt lgkmcnt(6)\n"
"v_mfma_f32_16x16x32_bf16 v[176:179], v[88:91], a[32:35], v[176:179]\n"
"buffer_load_dwordx4 a[184:187], v20, s[64:67], s98 offen sc1\n"
"s_waitcnt lgkmcnt(4)\n"
"v_mfma_f32_16x16x32_bf16 v[176:179], v[92:95], a[36:39], v[176:179]\n"
"ds_read_b64_tr_b16 v[80:81], v17 offset:192\n"
"v_mfma_f32_16x16x32_bf16 v[180:183], v[92:95], a[44:47], v[180:183]\n"
"v_mfma_f32_16x16x32_bf16 v[180:183], v[88:91], a[40:43], v[180:183]\n"
"ds_read_b64_tr_b16 v[82:83], v17 offset:4800\n"
"v_mfma_f32_16x16x32_bf16 v[184:187], v[88:91], a[48:51], v[184:187]\n"
"v_mfma_f32_16x16x32_bf16 v[184:187], v[92:95], a[52:55], v[184:187]\n"
"ds_read_b64_tr_b16 v[84:85], v17 offset:9408\n"
"v_mfma_f32_16x16x32_bf16 v[188:191], v[92:95], a[60:63], v[188:191]\n"
"buffer_load_dwordx4 a[188:191], v20, s[64:67], s98 offen offset:1024 sc1\n"
"v_mfma_f32_16x16x32_bf16 v[188:191], v[88:91], a[56:59], v[188:191]\n"
"ds_read_b64_tr_b16 v[86:87], v17 offset:14016\n"
"s_waitcnt lgkmcnt(6)\n"
"v_mfma_f32_16x16x32_bf16 v[192:195], v[96:99], a[32:35], v[192:195]\n"
"s_waitcnt lgkmcnt(4)\n"
"v_mfma_f32_16x16x32_bf16 v[192:195], v[100:103], a[36:39], v[192:195]\n"
"s_add_u32 s77, s84, 0x7e000\n"
"s_add_u32 m0, s78, 0x1f800\n"
"ds_read_b64_tr_b16 v[88:89], v17 offset:224\n"
"v_mfma_f32_16x16x32_bf16 v[196:199], v[100:103], a[44:47], v[196:199]\n"
"buffer_load_dwordx4 v6, s[60:63], s77 offen lds\n"
"v_mfma_f32_16x16x32_bf16 v[196:199], v[96:99], a[40:43], v[196:199]\n"
"ds_read_b64_tr_b16 v[90:91], v17 offset:4832\n"
"v_mfma_f32_16x16x32_bf16 v[200:203], v[96:99], a[48:51], v[200:203]\n"
"v_mfma_f32_16x16x32_bf16 v[200:203], v[100:103], a[52:55], v[200:203]\n"
"ds_read_b64_tr_b16 v[92:93], v17 offset:9440\n"
"v_mfma_f32_16x16x32_bf16 v[204:207], v[100:103], a[60:63], v[204:207]\n"
"v_mfma_f32_16x16x32_bf16 v[204:207], v[96:99], a[56:59], v[204:207]\n"
"s_add_u32 m0, s78, 0x1fc00\n"
"ds_read_b64_tr_b16 v[94:95], v17 offset:14048\n"
"s_waitcnt lgkmcnt(6)\n"
"v_mfma_f32_16x16x32_bf16 v[208:211], v[80:83], a[32:35], v[208:211]\n"
"buffer_load_dwordx4 v7, s[60:63], s77 offen lds\n"
"s_waitcnt lgkmcnt(4)\n"
"v_mfma_f32_16x16x32_bf16 v[208:211], v[84:87], a[36:39], v[208:211]\n"
"ds_read_b64_tr_b16 v[96:97], v17 offset:256\n"
"v_mfma_f32_16x16x32_bf16 v[212:215], v[84:87], a[44:47], v[212:215]\n"
"v_mfma_f32_16x16x32_bf16 v[212:215], v[80:83], a[40:43], v[212:215]\n"
"ds_read_b64_tr_b16 v[98:99], v17 offset:4864\n"
"v_mfma_f32_16x16x32_bf16 v[216:219], v[80:83], a[48:51], v[216:219]\n"
"v_mfma_f32_16x16x32_bf16 v[216:219], v[84:87], a[52:55], v[216:219]\n"
"s_add_u32 m0, s78, 0x20000\n"
"ds_read_b64_tr_b16 v[100:101], v17 offset:9472\n"
"v_mfma_f32_16x16x32_bf16 v[220:223], v[84:87], a[60:63], v[220:223]\n"
"buffer_load_dwordx4 v8, s[60:63], s77 offen lds\n"
"v_mfma_f32_16x16x32_bf16 v[220:223], v[80:83], a[56:59], v[220:223]\n"
"s_waitcnt vmcnt(35)\n"
"s_barrier\n"
"ds_read_b64_tr_b16 v[102:103], v17 offset:14080\n"
"s_waitcnt lgkmcnt(6)\n"
"v_mfma_f32_16x16x32_bf16 v[224:227], v[88:91], a[32:35], v[224:227]\n"
"s_waitcnt lgkmcnt(4)\n"
"v_mfma_f32_16x16x32_bf16 v[224:227], v[92:95], a[36:39], v[224:227]\n"
"s_add_u32 m0, s78, 0x20400\n"
"ds_read_b64_tr_b16 v[80:81], v17 offset:18432\n"
"v_mfma_f32_16x16x32_bf16 v[228:231], v[92:95], a[44:47], v[228:231]\n"
"buffer_load_dwordx4 v9, s[60:63], s77 offen lds\n"
"v_mfma_f32_16x16x32_bf16 v[228:231], v[88:91], a[40:43], v[228:231]\n"
"ds_read_b64_tr_b16 v[82:83], v17 offset:23040\n"
"v_mfma_f32_16x16x32_bf16 v[232:235], v[88:91], a[48:51], v[232:235]\n"
"v_mfma_f32_16x16x32_bf16 v[232:235], v[92:95], a[52:55], v[232:235]\n"
"ds_read_b64_tr_b16 v[84:85], v17 offset:27648\n"
"v_mfma_f32_16x16x32_bf16 v[236:239], v[92:95], a[60:63], v[236:239]\n"
"v_mfma_f32_16x16x32_bf16 v[236:239], v[88:91], a[56:59], v[236:239]\n"
"s_add_u32 m0, s78, 0x20800\n"
"ds_read_b64_tr_b16 v[86:87], v17 offset:32256\n"
"s_waitcnt lgkmcnt(6)\n"
"v_mfma_f32_16x16x32_bf16 v[240:243], v[96:99], a[32:35], v[240:243]\n"
"s_bitcmp1_b32 s72, 0\n"
"s_cbranch_scc1 NOP5_9_%=\n"
"buffer_load_dwordx4 v10, s[60:63], s77 offen lds\n"
"NOP5_9_%=:\n"
"s_waitcnt lgkmcnt(4)\n"
"v_mfma_f32_16x16x32_bf16 v[240:243], v[100:103], a[36:39], v[240:243]\n"
"ds_read_b64_tr_b16 v[88:89], v17 offset:18464\n"
"v_mfma_f32_16x16x32_bf16 v[244:247], v[100:103], a[44:47], v[244:247]\n"
"v_mfma_f32_16x16x32_bf16 v[244:247], v[96:99], a[40:43], v[244:247]\n"
"ds_read_b64_tr_b16 v[90:91], v17 offset:23072\n"
"v_mfma_f32_16x16x32_bf16 v[248:251], v[96:99], a[48:51], v[248:251]\n"
"v_mfma_f32_16x16x32_bf16 v[248:251], v[100:103], a[52:55], v[248:251]\n"
"ds_read_b64_tr_b16 v[92:93], v17 offset:27680\n"
"v_mfma_f32_16x16x32_bf16 v[252:255], v[100:103], a[60:63], v[252:255]\n"
"v_mfma_f32_16x16x32_bf16 v[252:255], v[96:99], a[56:59], v[252:255]\n"
"ds_read_b64_tr_b16 v[94:95], v17 offset:32288\n"
"s_waitcnt vmcnt(32)\n"
"s_waitcnt lgkmcnt(6)\n"
"v_mfma_f32_16x16x32_bf16 v[112:115], v[80:83], a[64:67], v[112:115]\n"
"s_waitcnt lgkmcnt(4)\n"
"v_mfma_f32_16x16x32_bf16 v[112:115], v[84:87], a[68:71], v[112:115]\n"
"s_add_u32 s98, s85, 0x4000\n"
"ds_read_b64_tr_b16 v[96:97], v17 offset:18496\n"
"v_mfma_f32_16x16x32_bf16 v[116:119], v[84:87], a[76:79], v[116:119]\n"
"buffer_load_dwordx4 a[192:195], v20, s[64:67], s98 offen sc1\n"
"v_mfma_f32_16x16x32_bf16 v[116:119], v[80:83], a[72:75], v[116:119]\n"
"ds_read_b64_tr_b16 v[98:99], v17 offset:23104\n"
"v_mfma_f32_16x16x32_bf16 v[120:123], v[80:83], a[80:83], v[120:123]\n"
"v_mfma_f32_16x16x32_bf16 v[120:123], v[84:87], a[84:87], v[120:123]\n"
"ds_read_b64_tr_b16 v[100:101], v17 offset:27712\n"
"v_mfma_f32_16x16x32_bf16 v[124:127], v[84:87], a[92:95], v[124:127]\n"
"buffer_load_dwordx4 a[196:199], v20, s[64:67], s98 offen offset:1024 sc1\n"
"v_mfma_f32_16x16x32_bf16 v[124:127], v[80:83], a[88:91], v[124:127]\n"
"ds_read_b64_tr_b16 v[102:103], v17 offset:32320\n"
"s_waitcnt lgkmcnt(6)\n"
"v_mfma_f32_16x16x32_bf16 v[128:131], v[88:91], a[64:67], v[128:131]\n"
"s_waitcnt lgkmcnt(4)\n"
"v_mfma_f32_16x16x32_bf16 v[128:131], v[92:95], a[68:71], v[128:131]\n"
"ds_read_b64_tr_b16 v[80:81], v17 offset:18528\n"
"v_mfma_f32_16x16x32_bf16 v[132:135], v[92:95], a[76:79], v[132:135]\n"
"v_mfma_f32_16x16x32_bf16 v[132:135], v[88:91], a[72:75], v[132:135]\n"
"s_add_u32 s98, s98, s86\n"
"ds_read_b64_tr_b16 v[82:83], v17 offset:23136\n"
"v_mfma_f32_16x16x32_bf16 v[136:139], v[88:91], a[80:83], v[136:139]\n"
"buffer_load_dwordx4 a[200:203], v20, s[64:67], s98 offen sc1\n"
"v_mfma_f32_16x16x32_bf16 v[136:139], v[92:95], a[84:87], v[136:139]\n"
"ds_read_b64_tr_b16 v[84:85], v17 offset:27744\n"
"v_mfma_f32_16x16x32_bf16 v[140:143], v[92:95], a[92:95], v[140:143]\n"
"v_mfma_f32_16x16x32_bf16 v[140:143], v[88:91], a[88:91], v[140:143]\n"
"ds_read_b64_tr_b16 v[86:87], v17 offset:32352\n"
"s_waitcnt lgkmcnt(6)\n"
"v_mfma_f32_16x16x32_bf16 v[144:147], v[96:99], a[64:67], v[144:147]\n"
"buffer_load_dwordx4 a[204:207], v20, s[64:67], s98 offen offset:1024 sc1\n"
"s_waitcnt lgkmcnt(4)\n"
"v_mfma_f32_16x16x32_bf16 v[144:147], v[100:103], a[68:71], v[144:147]\n"
"ds_read_b64_tr_b16 v[88:89], v17 offset:18560\n"
"v_mfma_f32_16x16x32_bf16 v[148:151], v[100:103], a[76:79], v[148:151]\n"
"v_mfma_f32_16x16x32_bf16 v[148:151], v[96:99], a[72:75], v[148:151]\n"
"ds_read_b64_tr_b16 v[90:91], v17 offset:23168\n"
"v_mfma_f32_16x16x32_bf16 v[152:155], v[96:99], a[80:83], v[152:155]\n"
"v_mfma_f32_16x16x32_bf16 v[152:155], v[100:103], a[84:87], v[152:155]\n"
"s_add_u32 s98, s98, s86\n"
"ds_read_b64_tr_b16 v[92:93], v17 offset:27776\n"
"v_mfma_f32_16x16x32_bf16 v[156:159], v[100:103], a[92:95], v[156:159]\n"
"buffer_load_dwordx4 a[208:211], v20, s[64:67], s98 offen sc1\n"
"v_mfma_f32_16x16x32_bf16 v[156:159], v[96:99], a[88:91], v[156:159]\n"
"ds_read_b64_tr_b16 v[94:95], v17 offset:32384\n"
"s_waitcnt lgkmcnt(6)\n"
"v_mfma_f32_16x16x32_bf16 v[160:163], v[80:83], a[64:67], v[160:163]\n"
"s_waitcnt lgkmcnt(4)\n"
"v_mfma_f32_16x16x32_bf16 v[160:163], v[84:87], a[68:71], v[160:163]\n"
"ds_read_b64_tr_b16 v[96:97], v17 offset:18592\n"
"v_mfma_f32_16x16x32_bf16 v[164:167], v[84:87], a[76:79], v[164:167]\n"
"v_mfma_f32_16x16x32_bf16 v[164:167], v[80:83], a[72:75], v[164:167]\n"
"ds_read_b64_tr_b16 v[98:99], v17 offset:23200\n"
"v_mfma_f32_16x16x32_bf16 v[168:171], v[80:83], a[80:83], v[168:171]\n"
"buffer_load_dwordx4 a[212:215], v20, s[64:67], s98 offen offset:1024 sc1\n"
"v_mfma_f32_16x16x32_bf16 v[168:171], v[84:87], a[84:87], v[168:171]\n"
"ds_read_b64_tr_b16 v[100:101], v17 offset:27808\n"
"v_mfma_f32_16x16x32_bf16 v[172:175], v[84:87], a[92:95], v[172:175]\n"
"v_mfma_f32_16x16x32_bf16 v[172:175], v[80:83], a[88:91], v[172:175]\n"
"s_add_u32 s98, s98, s86\n"
"ds_read_b64_tr_b16 v[102:103], v17 offset:32416\n"
"s_waitcnt lgkmcnt(6)\n"
"v_mfma_f32_16x16x32_bf16 v[176:179], v[88:91], a[64:67], v[176:179]\n"
"buffer_load_dwordx4 a[216:219], v20, s[64:67], s98 offen sc1\n"
"s_waitcnt lgkmcnt(4)\n"
"v_mfma_f32_16x16x32_bf16 v[176:179], v[92:95], a[68:71], v[176:179]\n"
"ds_read_b64_tr_b16 v[80:81], v17 offset:18624\n"
"v_mfma_f32_16x16x32_bf16 v[180:183], v[92:95], a[76:79], v[180:183]\n"
"v_mfma_f32_16x16x32_bf16 v[180:183], v[88:91], a[72:75], v[180:183]\n"
"ds_read_b64_tr_b16 v[82:83], v17 offset:23232\n"
"v_mfma_f32_16x16x32_bf16 v[184:187], v[88:91], a[80:83], v[184:187]\n"
"v_mfma_f32_16x16x32_bf16 v[184:187], v[92:95], a[84:87], v[184:187]\n"
"ds_read_b64_tr_b16 v[84:85], v17 offset:27840\n"
"v_mfma_f32_16x16x32_bf16 v[188:191], v[92:95], a[92:95], v[188:191]\n"
"buffer_load_dwordx4 a[220:223], v20, s[64:67], s98 offen offset:1024 sc1\n"
"v_mfma_f32_16x16x32_bf16 v[188:191], v[88:91], a[88:91], v[188:191]\n"
"ds_read_b64_tr_b16 v[86:87], v17 offset:32448\n"
"s_waitcnt lgkmcnt(6)\n"
"v_mfma_f32_16x16x32_bf16 v[192:195], v[96:99], a[64:67], v[192:195]\n"
"s_waitcnt lgkmcnt(4)\n"
"v_mfma_f32_16x16x32_bf16 v[192:195], v[100:103], a[68:71], v[192:195]\n"
"s_add_u32 s77, s84, 0x90000\n"
"s_add_u32 m0, s78, 0\n"
"ds_read_b64_tr_b16 v[88:89], v17 offset:18656\n"
"v_mfma_f32_16x16x32_bf16 v[196:199], v[100:103], a[76:79], v[196:199]\n"
"buffer_load_dwordx4 v6, s[60:63], s77 offen lds\n"
"v_mfma_f32_16x16x32_bf16 v[196:199], v[96:99], a[72:75], v[196:199]\n"
"ds_read_b64_tr_b16 v[90:91], v17 offset:23264\n"
"v_mfma_f32_16x16x32_bf16 v[200:203], v[96:99], a[80:83], v[200:203]\n"
"v_mfma_f32_16x16x32_bf16 v[200:203], v[100:103], a[84:87], v[200:203]\n"
"ds_read_b64_tr_b16 v[92:93], v17 offset:27872\n"
"v_mfma_f32_16x16x32_bf16 v[204:207], v[100:103], a[92:95], v[204:207]\n"
"v_mfma_f32_16x16x32_bf16 v[204:207], v[96:99], a[88:91], v[204:207]\n"
"s_add_u32 m0, s78, 0x400\n"
"ds_read_b64_tr_b16 v[94:95], v17 offset:32480\n"
"s_waitcnt lgkmcnt(6)\n"
"v_mfma_f32_16x16x32_bf16 v[208:211], v[80:83], a[64:67], v[208:211]\n"
"buffer_load_dwordx4 v7, s[60:63], s77 offen lds\n"
"s_waitcnt lgkmcnt(4)\n"
"v_mfma_f32_16x16x32_bf16 v[208:211], v[84:87], a[68:71], v[208:211]\n"
"ds_read_b64_tr_b16 v[96:97], v17 offset:18688\n"
"v_mfma_f32_16x16x32_bf16 v[212:215], v[84:87], a[76:79], v[212:215]\n"
"v_mfma_f32_16x16x32_bf16 v[212:215], v[80:83], a[72:75], v[212:215]\n"
"ds_read_b64_tr_b16 v[98:99], v17 offset:23296\n"
"v_mfma_f32_16x16x32_bf16 v[216:219], v[80:83], a[80:83], v[216:219]\n"
"v_mfma_f32_16x16x32_bf16 v[216:219], v[84:87], a[84:87], v[216:219]\n"
"s_add_u32 m0, s78, 0x800\n"
"ds_read_b64_tr_b16 v[100:101], v17 offset:27904\n"
"v_mfma_f32_16x16x32_bf16 v[220:223], v[84:87], a[92:95], v[220:223]\n"
"buffer_load_dwordx4 v8, s[60:63], s77 offen lds\n"
"v_mfma_f32_16x16x32_bf16 v[220:223], v[80:83], a[88:91], v[220:223]\n"
"s_waitcnt vmcnt(35)\n"
"s_barrier\n"
"ds_read_b64_tr_b16 v[102:103], v17 offset:32512\n"
"s_waitcnt lgkmcnt(6)\n"
"v_mfma_f32_16x16x32_bf16 v[224:227], v[88:91], a[64:67], v[224:227]\n"
"s_waitcnt lgkmcnt(4)\n"
"v_mfma_f32_16x16x32_bf16 v[224:227], v[92:95], a[68:71], v[224:227]\n"
"s_add_u32 m0, s78, 0xc00\n"
"ds_read_b64_tr_b16 v[80:81], v17 offset:36864\n"
"v_mfma_f32_16x16x32_bf16 v[228:231], v[92:95], a[76:79], v[228:231]\n"
"buffer_load_dwordx4 v9, s[60:63], s77 offen lds\n"
"v_mfma_f32_16x16x32_bf16 v[228:231], v[88:91], a[72:75], v[228:231]\n"
"ds_read_b64_tr_b16 v[82:83], v17 offset:41472\n"
"v_mfma_f32_16x16x32_bf16 v[232:235], v[88:91], a[80:83], v[232:235]\n"
"v_mfma_f32_16x16x32_bf16 v[232:235], v[92:95], a[84:87], v[232:235]\n"
"ds_read_b64_tr_b16 v[84:85], v17 offset:46080\n"
"v_mfma_f32_16x16x32_bf16 v[236:239], v[92:95], a[92:95], v[236:239]\n"
"v_mfma_f32_16x16x32_bf16 v[236:239], v[88:91], a[88:91], v[236:239]\n"
"s_add_u32 m0, s78, 0x1000\n"
"ds_read_b64_tr_b16 v[86:87], v17 offset:50688\n"
"s_waitcnt lgkmcnt(6)\n"
"v_mfma_f32_16x16x32_bf16 v[240:243], v[96:99], a[64:67], v[240:243]\n"
"s_bitcmp1_b32 s72, 0\n"
"s_cbranch_scc1 NOP5_10_%=\n"
"buffer_load_dwordx4 v10, s[60:63], s77 offen lds\n"
"NOP5_10_%=:\n"
"s_waitcnt lgkmcnt(4)\n"
"v_mfma_f32_16x16x32_bf16 v[240:243], v[100:103], a[68:71], v[240:243]\n"
"ds_read_b64_tr_b16 v[88:89], v17 offset:36896\n"
"v_mfma_f32_16x16x32_bf16 v[244:247], v[100:103], a[76:79], v[244:247]\n"
"v_mfma_f32_16x16x32_bf16 v[244:247], v[96:99], a[72:75], v[244:247]\n"
"ds_read_b64_tr_b16 v[90:91], v17 offset:41504\n"
"v_mfma_f32_16x16x32_bf16 v[248:251], v[96:99], a[80:83], v[248:251]\n"
"v_mfma_f32_16x16x32_bf16 v[248:251], v[100:103], a[84:87], v[248:251]\n"
"ds_read_b64_tr_b16 v[92:93], v17 offset:46112\n"
"v_mfma_f32_16x16x32_bf16 v[252:255], v[100:103], a[92:95], v[252:255]\n"
"v_mfma_f32_16x16x32_bf16 v[252:255], v[96:99], a[88:91], v[252:255]\n"
"ds_read_b64_tr_b16 v[94:95], v17 offset:50720\n"
"s_nop 0\n"
"s_lshl_b32 s76, s75, 8\n"
"s_add_u32 s76, s76, 0x2000\n"
"v_and_b32 v76, 31, v5\n"
"v_lshlrev_b32 v76, 3, v76\n"
"v_add_u32 v76, s76, v76\n"
"global_load_dwordx2 v[78:79], v76, s[92:93] sc1\n"
"s_waitcnt lgkmcnt(6)\n"
"v_mfma_f32_16x16x32_bf16 v[112:115], v[80:83], a[96:99], v[112:115]\n"
"s_waitcnt lgkmcnt(4)\n"
"v_mfma_f32_16x16x32_bf16 v[112:115], v[84:87], a[100:103], v[112:115]\n"
"s_add_u32 s98, s85, 0x4800\n"
"ds_read_b64_tr_b16 v[96:97], v17 offset:36928\n"
"v_mfma_f32_16x16x32_bf16 v[116:119], v[84:87], a[108:111], v[116:119]\n"
"buffer_load_dwordx4 a[224:227], v20, s[64:67], s98 offen sc1\n"
"v_mfma_f32_16x16x32_bf16 v[116:119], v[80:83], a[104:107], v[116:119]\n"
"ds_read_b64_tr_b16 v[98:99], v17 offset:41536\n"
"v_mfma_f32_16x16x32_bf16 v[120:123], v[80:83], a[112:115], v[120:123]\n"
"v_mfma_f32_16x16x32_bf16 v[120:123], v[84:87], a[116:119], v[120:123]\n"
"ds_read_b64_tr_b16 v[100:101], v17 offset:46144\n"
"v_mfma_f32_16x16x32_bf16 v[124:127], v[84:87], a[124:127], v[124:127]\n"
"buffer_load_dwordx4 a[228:231], v20, s[64:67], s98 offen offset:1024 sc1\n"
"v_mfma_f32_16x16x32_bf16 v[124:127], v[80:83], a[120:123], v[124:127]\n"
"ds_read_b64_tr_b16 v[102:103], v17 offset:50752\n"
"s_waitcnt lgkmcnt(6)\n"
"v_mfma_f32_16x16x32_bf16 v[128:131], v[88:91], a[96:99], v[128:131]\n"
"s_waitcnt lgkmcnt(4)\n"
"v_mfma_f32_16x16x32_bf16 v[128:131], v[92:95], a[100:103], v[128:131]\n"
"ds_read_b64_tr_b16 v[80:81], v17 offset:36960\n"
"v_mfma_f32_16x16x32_bf16 v[132:135], v[92:95], a[108:111], v[132:135]\n"
"v_mfma_f32_16x16x32_bf16 v[132:135], v[88:91], a[104:107], v[132:135]\n"
"s_add_u32 s98, s98, s86\n"
"ds_read_b64_tr_b16 v[82:83], v17 offset:41568\n"
"v_mfma_f32_16x16x32_bf16 v[136:139], v[88:91], a[112:115], v[136:139]\n"
"buffer_load_dwordx4 a[232:235], v20, s[64:67], s98 offen sc1\n"
"v_mfma_f32_16x16x32_bf16 v[136:139], v[92:95], a[116:119], v[136:139]\n"
"ds_read_b64_tr_b16 v[84:85], v17 offset:46176\n"
"v_mfma_f32_16x16x32_bf16 v[140:143], v[92:95], a[124:127], v[140:143]\n"
"v_mfma_f32_16x16x32_bf16 v[140:143], v[88:91], a[120:123], v[140:143]\n"
"ds_read_b64_tr_b16 v[86:87], v17 offset:50784\n"
"s_waitcnt lgkmcnt(6)\n"
"v_mfma_f32_16x16x32_bf16 v[144:147], v[96:99], a[96:99], v[144:147]\n"
"buffer_load_dwordx4 a[236:239], v20, s[64:67], s98 offen offset:1024 sc1\n"
"s_waitcnt lgkmcnt(4)\n"
"v_mfma_f32_16x16x32_bf16 v[144:147], v[100:103], a[100:103], v[144:147]\n"
"ds_read_b64_tr_b16 v[88:89], v17 offset:36992\n"
"v_mfma_f32_16x16x32_bf16 v[148:151], v[100:103], a[108:111], v[148:151]\n"
"v_mfma_f32_16x16x32_bf16 v[148:151], v[96:99], a[104:107], v[148:151]\n"
"ds_read_b64_tr_b16 v[90:91], v17 offset:41600\n"
"v_mfma_f32_16x16x32_bf16 v[152:155], v[96:99], a[112:115], v[152:155]\n"
"v_mfma_f32_16x16x32_bf16 v[152:155], v[100:103], a[116:119], v[152:155]\n"
"s_add_u32 s98, s98, s86\n"
"ds_read_b64_tr_b16 v[92:93], v17 offset:46208\n"
"v_mfma_f32_16x16x32_bf16 v[156:159], v[100:103], a[124:127], v[156:159]\n"
"buffer_load_dwordx4 a[240:243], v20, s[64:67], s98 offen sc1\n"
"v_mfma_f32_16x16x32_bf16 v[156:159], v[96:99], a[120:123], v[156:159]\n"
"ds_read_b64_tr_b16 v[94:95], v17 offset:50816\n"
"s_waitcnt lgkmcnt(6)\n"
"v_mfma_f32_16x16x32_bf16 v[160:163], v[80:83], a[96:99], v[160:163]\n"
"s_waitcnt lgkmcnt(4)\n"
"v_mfma_f32_16x16x32_bf16 v[160:163], v[84:87], a[100:103], v[160:163]\n"
"ds_read_b64_tr_b16 v[96:97], v17 offset:37024\n"
"v_mfma_f32_16x16x32_bf16 v[164:167], v[84:87], a[108:111], v[164:167]\n"
"v_mfma_f32_16x16x32_bf16 v[164:167], v[80:83], a[104:107], v[164:167]\n"
"ds_read_b64_tr_b16 v[98:99], v17 offset:41632\n"
"v_mfma_f32_16x16x32_bf16 v[168:171], v[80:83], a[112:115], v[168:171]\n"
"buffer_load_dwordx4 a[244:247], v20, s[64:67], s98 offen offset:1024 sc1\n"
"v_mfma_f32_16x16x32_bf16 v[168:171], v[84:87], a[116:119], v[168:171]\n"
"ds_read_b64_tr_b16 v[100:101], v17 offset:46240\n"
"v_mfma_f32_16x16x32_bf16 v[172:175], v[84:87], a[124:127], v[172:175]\n"
"v_mfma_f32_16x16x32_bf16 v[172:175], v[80:83], a[120:123], v[172:175]\n"
"s_add_u32 s98, s98, s86\n"
"ds_read_b64_tr_b16 v[102:103], v17 offset:50848\n"
"s_waitcnt lgkmcnt(6)\n"
"v_mfma_f32_16x16x32_bf16 v[176:179], v[88:91], a[96:99], v[176:179]\n"
"buffer_load_dwordx4 a[248:251], v20, s[64:67], s98 offen sc1\n"
"s_waitcnt lgkmcnt(4)\n"
"v_mfma_f32_16x16x32_bf16 v[176:179], v[92:95], a[100:103], v[176:179]\n"
"ds_read_b64_tr_b16 v[80:81], v17 offset:37056\n"
"v_mfma_f32_16x16x32_bf16 v[180:183], v[92:95], a[108:111], v[180:183]\n"
"v_mfma_f32_16x16x32_bf16 v[180:183], v[88:91], a[104:107], v[180:183]\n"
"ds_read_b64_tr_b16 v[82:83], v17 offset:41664\n"
"v_mfma_f32_16x16x32_bf16 v[184:187], v[88:91], a[112:115], v[184:187]\n"
"v_mfma_f32_16x16x32_bf16 v[184:187], v[92:95], a[116:119], v[184:187]\n"
"ds_read_b64_tr_b16 v[84:85], v17 offset:46272\n"
"v_mfma_f32_16x16x32_bf16 v[188:191], v[92:95], a[124:127], v[188:191]\n"
"buffer_load_dwordx4 a[252:255], v20, s[64:67], s98 offen offset:1024 sc1\n"
"v_mfma_f32_16x16x32_bf16 v[188:191], v[88:91], a[120:123], v[188:191]\n"
"ds_read_b64_tr_b16 v[86:87], v17 offset:50880\n"
"s_waitcnt lgkmcnt(6)\n"
"v_mfma_f32_16x16x32_bf16 v[192:195], v[96:99], a[96:99], v[192:195]\n"
"s_waitcnt lgkmcnt(4)\n"
"v_mfma_f32_16x16x32_bf16 v[192:195], v[100:103], a[100:103], v[192:195]\n"
"s_add_u32 s77, s84, 0xa2000\n"
"s_add_u32 m0, s78, 0x4800\n"
"ds_read_b64_tr_b16 v[88:89], v17 offset:37088\n"
"v_mfma_f32_16x16x32_bf16 v[196:199], v[100:103], a[108:111], v[196:199]\n"
"buffer_load_dwordx4 v6, s[60:63], s77 offen lds\n"
"v_mfma_f32_16x16x32_bf16 v[196:199], v[96:99], a[104:107], v[196:199]\n"
"ds_read_b64_tr_b16 v[90:91], v17 offset:41696\n"
"v_mfma_f32_16x16x32_bf16 v[200:203], v[96:99], a[112:115], v[200:203]\n"
"v_mfma_f32_16x16x32_bf16 v[200:203], v[100:103], a[116:119], v[200:203]\n"
"ds_read_b64_tr_b16 v[92:93], v17 offset:46304\n"
"v_mfma_f32_16x16x32_bf16 v[204:207], v[100:103], a[124:127], v[204:207]\n"
"v_mfma_f32_16x16x32_bf16 v[204:207], v[96:99], a[120:123], v[204:207]\n"
"s_add_u32 m0, s78, 0x4c00\n"
"ds_read_b64_tr_b16 v[94:95], v17 offset:50912\n"
"s_waitcnt lgkmcnt(6)\n"
"v_mfma_f32_16x16x32_bf16 v[208:211], v[80:83], a[96:99], v[208:211]\n"
"buffer_load_dwordx4 v7, s[60:63], s77 offen lds\n"
"s_waitcnt lgkmcnt(4)\n"
"v_mfma_f32_16x16x32_bf16 v[208:211], v[84:87], a[100:103], v[208:211]\n"
"ds_read_b64_tr_b16 v[96:97], v17 offset:37120\n"
"v_mfma_f32_16x16x32_bf16 v[212:215], v[84:87], a[108:111], v[212:215]\n"
"v_mfma_f32_16x16x32_bf16 v[212:215], v[80:83], a[104:107], v[212:215]\n"
"ds_read_b64_tr_b16 v[98:99], v17 offset:41728\n"
"v_mfma_f32_16x16x32_bf16 v[216:219], v[80:83], a[112:115], v[216:219]\n"
"v_mfma_f32_16x16x32_bf16 v[216:219], v[84:87], a[116:119], v[216:219]\n"
"s_add_u32 m0, s78, 0x5000\n"
"ds_read_b64_tr_b16 v[100:101], v17 offset:46336\n"
"v_mfma_f32_16x16x32_bf16 v[220:223], v[84:87], a[124:127], v[220:223]\n"
"buffer_load_dwordx4 v8, s[60:63], s77 offen lds\n"
"v_mfma_f32_16x16x32_bf16 v[220:223], v[80:83], a[120:123], v[220:223]\n"
"s_waitcnt vmcnt(36)\n"
"s_barrier\n"
"ds_read_b64_tr_b16 v[102:103], v17 offset:50944\n"
"s_waitcnt lgkmcnt(6)\n"
"v_mfma_f32_16x16x32_bf16 v[224:227], v[88:91], a[96:99], v[224:227]\n"
"s_waitcnt lgkmcnt(4)\n"
"v_mfma_f32_16x16x32_bf16 v[224:227], v[92:95], a[100:103], v[224:227]\n"
"s_add_u32 m0, s78, 0x5400\n"
"ds_read_b64_tr_b16 v[80:81], v18\n"
"v_mfma_f32_16x16x32_bf16 v[228:231], v[92:95], a[108:111], v[228:231]\n"
"buffer_load_dwordx4 v9, s[60:63], s77 offen lds\n"
"v_mfma_f32_16x16x32_bf16 v[228:231], v[88:91], a[104:107], v[228:231]\n"
"ds_read_b64_tr_b16 v[82:83], v18 offset:4608\n"
"v_mfma_f32_16x16x32_bf16 v[232:235], v[88:91], a[112:115], v[232:235]\n"
"v_mfma_f32_16x16x32_bf16 v[232:235], v[92:95], a[116:119], v[232:235]\n"
"ds_read_b64_tr_b16 v[84:85], v18 offset:9216\n"
"v_mfma_f32_16x16x32_bf16 v[236:239], v[92:95], a[124:127], v[236:239]\n"
"v_mfma_f32_16x16x32_bf16 v[236:239], v[88:91], a[120:123], v[236:239]\n"
"s_add_u32 m0, s78, 0x5800\n"
"ds_read_b64_tr_b16 v[86:87], v18 offset:13824\n"
"s_waitcnt lgkmcnt(6)\n"
"v_mfma_f32_16x16x32_bf16 v[240:243], v[96:99], a[96:99], v[240:243]\n"
"s_bitcmp1_b32 s72, 0\n"
"s_cbranch_scc1 NOP5_11_%=\n"
"buffer_load_dwordx4 v10, s[60:63], s77 offen lds\n"
"NOP5_11_%=:\n"
"s_waitcnt lgkmcnt(4)\n"
"v_mfma_f32_16x16x32_bf16 v[240:243], v[100:103], a[100:103], v[240:243]\n"
"ds_read_b64_tr_b16 v[88:89], v18 offset:32\n"
"v_mfma_f32_16x16x32_bf16 v[244:247], v[100:103], a[108:111], v[244:247]\n"
"v_mfma_f32_16x16x32_bf16 v[244:247], v[96:99], a[104:107], v[244:247]\n"
"ds_read_b64_tr_b16 v[90:91], v18 offset:4640\n"
"v_mfma_f32_16x16x32_bf16 v[248:251], v[96:99], a[112:115], v[248:251]\n"
"v_mfma_f32_16x16x32_bf16 v[248:251], v[100:103], a[116:119], v[248:251]\n"
"ds_read_b64_tr_b16 v[92:93], v18 offset:9248\n"
"v_mfma_f32_16x16x32_bf16 v[252:255], v[100:103], a[124:127], v[252:255]\n"
"v_mfma_f32_16x16x32_bf16 v[252:255], v[96:99], a[120:123], v[252:255]\n"
"ds_read_b64_tr_b16 v[94:95], v18 offset:13856\n"
"s_nop 0\n"
"s_lshl_b32 s98, s81, 3\n"
"s_lshl_b32 s98, 0xf0, s98\n"
"s_waitcnt vmcnt(12)\n"
"v_cmp_eq_u64 vcc, v[78:79], s[88:89]\n"
"s_cmp_eq_u32 s73, 0\n"
"s_cbranch_scc1 POLLDO2_%=\n"
"s_and_b32 s76, vcc_lo, s98\n"
"s_mov_b32 s99, 0\n"
"s_cmp_eq_u32 s76, s98\n"
"s_cbranch_scc1 POLLDO2_%=\n"
"global_load_dwordx2 v[32:33], v76, s[92:93] sc1\n"
"s_sleep 3\n"
"global_load_dwordx2 v[34:35], v76, s[92:93] sc1\n"
"s_sleep 3\n"
"global_load_dwordx2 v[36:37], v76, s[92:93] sc1\n"
"s_sleep 3\n"
"POLLO2_%=:\n"
"global_load_dwordx2 v[38:39], v76, s[92:93] sc1\n"
"s_waitcnt vmcnt(3)\n"
"v_cmp_eq_u64 vcc, v[32:33], s[88:89]\n"
"s_nop 3\n"
"s_and_b32 s76, vcc_lo, s98\n"
"s_cmp_eq_u32 s76, s98\n"
"s_cbranch_scc1 POLLDO2_%=\n"
"s_sleep 2\n"
"global_load_dwordx2 v[32:33], v76, s[92:93] sc1\n"
"s_waitcnt vmcnt(3)\n"
"v_cmp_eq_u64 vcc, v[34:35], s[88:89]\n"
"s_nop 3\n"
"s_and_b32 s76, vcc_lo, s98\n"
"s_cmp_eq_u32 s76, s98\n"
"s_cbranch_scc1 POLLDO2_%=\n"
"s_sleep 2\n"
"global_load_dwordx2 v[34:35], v76, s[92:93] sc1\n"
"s_waitcnt vmcnt(3)\n"
"v_cmp_eq_u64 vcc, v[36:37], s[88:89]\n"
"s_nop 3\n"
"s_and_b32 s76, vcc_lo, s98\n"
"s_cmp_eq_u32 s76, s98\n"
"s_cbranch_scc1 POLLDO2_%=\n"
"s_sleep 2\n"
"global_load_dwordx2 v[36:37], v76, s[92:93] sc1\n"
"s_waitcnt vmcnt(3)\n"
"v_cmp_eq_u64 vcc, v[38:39], s[88:89]\n"
"s_nop 3\n"
"s_and_b32 s76, vcc_lo, s98\n"
"s_cmp_eq_u32 s76, s98\n"
"s_cbranch_scc1 POLLDO2_%=\n"
"s_sleep 2\n"
"s_add_u32 s99, s99, 1\n"
"s_cmp_gt_u32 s99, 0x40000\n"
"s_cbranch_scc0 POLLO2_%=\n"
"POLLDO2_%=:\n"
"s_waitcnt lgkmcnt(6)\n"
"v_mfma_f32_16x16x32_bf16 v[112:115], v[80:83], a[128:131], v[112:115]\n"
"s_waitcnt lgkmcnt(4)\n"
"v_mfma_f32_16x16x32_bf16 v[112:115], v[84:87], a[132:135], v[112:115]\n"
"s_add_u32 s98, s85, 0x5000\n"
"ds_read_b64_tr_b16 v[96:97], v18 offset:64\n"
"v_mfma_f32_16x16x32_bf16 v[116:119], v[84:87], a[140:143], v[116:119]\n"
"buffer_load_dwordx4 a[0:3], v20, s[64:67], s98 offen sc1\n"
"v_mfma_f32_16x16x32_bf16 v[116:119], v[80:83], a[136:139], v[116:119]\n"
"ds_read_b64_tr_b16 v[98:99], v18 offset:4672\n"
"v_mfma_f32_16x16x32_bf16 v[120:123], v[80:83], a[144:147], v[120:123]\n"
"v_mfma_f32_16x16x32_bf16 v[120:123], v[84:87], a[148:151], v[120:123]\n"
"ds_read_b64_tr_b16 v[100:101], v18 offset:9280\n"
"v_mfma_f32_16x16x32_bf16 v[124:127], v[84:87], a[156:159], v[124:127]\n"
"buffer_load_dwordx4 a[4:7], v20, s[64:67], s98 offen offset:1024 sc1\n"
"v_mfma_f32_16x16x32_bf16 v[124:127], v[80:83], a[152:155], v[124:127]\n"
"ds_read_b64_tr_b16 v[102:103], v18 offset:13888\n"
"s_waitcnt lgkmcnt(6)\n"
"v_mfma_f32_16x16x32_bf16 v[128:131], v[88:91], a[128:131], v[128:131]\n"
"s_waitcnt lgkmcnt(4)\n"
"v_mfma_f32_16x16x32_bf16 v[128:131], v[92:95], a[132:135], v[128:131]\n"
"ds_read_b64_tr_b16 v[80:81], v18 offset:96\n"
"v_mfma_f32_16x16x32_bf16 v[132:135], v[92:95], a[140:143], v[132:135]\n"
"v_mfma_f32_16x16x32_bf16 v[132:135], v[88:91], a[136:139], v[132:135]\n"
"s_add_u32 s98, s98, s86\n"
"ds_read_b64_tr_b16 v[82:83], v18 offset:4704\n"
"v_mfma_f32_16x16x32_bf16 v[136:139], v[88:91], a[144:147], v[136:139]\n"
"buffer_load_dwordx4 a[8:11], v20, s[64:67], s98 offen sc1\n"
"v_mfma_f32_16x16x32_bf16 v[136:139], v[92:95], a[148:151], v[136:139]\n"
"ds_read_b64_tr_b16 v[84:85], v18 offset:9312\n"
"v_mfma_f32_16x16x32_bf16 v[140:143], v[92:95], a[156:159], v[140:143]\n"
"v_mfma_f32_16x16x32_bf16 v[140:143], v[88:91], a[152:155], v[140:143]\n"
"ds_read_b64_tr_b16 v[86:87], v18 offset:13920\n"
"s_waitcnt lgkmcnt(6)\n"
"v_mfma_f32_16x16x32_bf16 v[144:147], v[96:99], a[128:131], v[144:147]\n"
"buffer_load_dwordx4 a[12:15], v20, s[64:67], s98 offen offset:1024 sc1\n"
"s_waitcnt lgkmcnt(4)\n"
"v_mfma_f32_16x16x32_bf16 v[144:147], v[100:103], a[132:135], v[144:147]\n"
"ds_read_b64_tr_b16 v[88:89], v18 offset:128\n"
"v_mfma_f32_16x16x32_bf16 v[148:151], v[100:103], a[140:143], v[148:151]\n"
"v_mfma_f32_16x16x32_bf16 v[148:151], v[96:99], a[136:139], v[148:151]\n"
"ds_read_b64_tr_b16 v[90:91], v18 offset:4736\n"
"v_mfma_f32_16x16x32_bf16 v[152:155], v[96:99], a[144:147], v[152:155]\n"
"v_mfma_f32_16x16x32_bf16 v[152:155], v[100:103], a[148:151], v[152:155]\n"
"s_add_u32 s98, s98, s86\n"
"ds_read_b64_tr_b16 v[92:93], v18 offset:9344\n"
"v_mfma_f32_16x16x32_bf16 v[156:159], v[100:103], a[156:159], v[156:159]\n"
"buffer_load_dwordx4 a[16:19], v20, s[64:67], s98 offen sc1\n"
"v_mfma_f32_16x16x32_bf16 v[156:159], v[96:99], a[152:155], v[156:159]\n"
"ds_read_b64_tr_b16 v[94:95], v18 offset:13952\n"
"s_waitcnt lgkmcnt(6)\n"
"v_mfma_f32_16x16x32_bf16 v[160:163], v[80:83], a[128:131], v[160:163]\n"
"s_waitcnt lgkmcnt(4)\n"
"v_mfma_f32_16x16x32_bf16 v[160:163], v[84:87], a[132:135], v[160:163]\n"
"ds_read_b64_tr_b16 v[96:97], v18 offset:160\n"
"v_mfma_f32_16x16x32_bf16 v[164:167], v[84:87], a[140:143], v[164:167]\n"
"v_mfma_f32_16x16x32_bf16 v[164:167], v[80:83], a[136:139], v[164:167]\n"
"ds_read_b64_tr_b16 v[98:99], v18 offset:4768\n"
"v_mfma_f32_16x16x32_bf16 v[168:171], v[80:83], a[144:147], v[168:171]\n"
"buffer_load_dwordx4 a[20:23], v20, s[64:67], s98 offen offset:1024 sc1\n"
"v_mfma_f32_16x16x32_bf16 v[168:171], v[84:87], a[148:151], v[168:171]\n"
"ds_read_b64_tr_b16 v[100:101], v18 offset:9376\n"
"v_mfma_f32_16x16x32_bf16 v[172:175], v[84:87], a[156:159], v[172:175]\n"
"v_mfma_f32_16x16x32_bf16 v[172:175], v[80:83], a[152:155], v[172:175]\n"
"s_add_u32 s98, s98, s86\n"
"ds_read_b64_tr_b16 v[102:103], v18 offset:13984\n"
"s_waitcnt lgkmcnt(6)\n"
"v_mfma_f32_16x16x32_bf16 v[176:179], v[88:91], a[128:131], v[176:179]\n"
"buffer_load_dwordx4 a[24:27], v20, s[64:67], s98 offen sc1\n"
"s_waitcnt lgkmcnt(4)\n"
"v_mfma_f32_16x16x32_bf16 v[176:179], v[92:95], a[132:135], v[176:179]\n"
"ds_read_b64_tr_b16 v[80:81], v18 offset:192\n"
"v_mfma_f32_16x16x32_bf16 v[180:183], v[92:95], a[140:143], v[180:183]\n"
"v_mfma_f32_16x16x32_bf16 v[180:183], v[88:91], a[136:139], v[180:183]\n"
"ds_read_b64_tr_b16 v[82:83], v18 offset:4800\n"
"v_mfma_f32_16x16x32_bf16 v[184:187], v[88:91], a[144:147], v[184:187]\n"
"v_mfma_f32_16x16x32_bf16 v[184:187], v[92:95], a[148:151], v[184:187]\n"
"ds_read_b64_tr_b16 v[84:85], v18 offset:9408\n"
"v_mfma_f32_16x16x32_bf16 v[188:191], v[92:95], a[156:159], v[188:191]\n"
"buffer_load_dwordx4 a[28:31], v20, s[64:67], s98 offen offset:1024 sc1\n"
"v_mfma_f32_16x16x32_bf16 v[188:191], v[88:91], a[152:155], v[188:191]\n"
"ds_read_b64_tr_b16 v[86:87], v18 offset:14016\n"
"s_waitcnt lgkmcnt(6)\n"
"v_mfma_f32_16x16x32_bf16 v[192:195], v[96:99], a[128:131], v[192:195]\n"
"s_waitcnt lgkmcnt(4)\n"
"v_mfma_f32_16x16x32_bf16 v[192:195], v[100:103], a[132:135], v[192:195]\n"
"s_add_u32 s77, s84, 0xb4000\n"
"s_add_u32 m0, s78, 0x9000\n"
"ds_read_b64_tr_b16 v[88:89], v18 offset:224\n"
"v_mfma_f32_16x16x32_bf16 v[196:199], v[100:103], a[140:143], v[196:199]\n"
"buffer_load_dwordx4 v6, s[60:63], s77 offen lds\n"
"v_mfma_f32_16x16x32_bf16 v[196:199], v[96:99], a[136:139], v[196:199]\n"
"ds_read_b64_tr_b16 v[90:91], v18 offset:4832\n"
"v_mfma_f32_16x16x32_bf16 v[200:203], v[96:99], a[144:147], v[200:203]\n"
"v_mfma_f32_16x16x32_bf16 v[200:203], v[100:103], a[148:151], v[200:203]\n"
"ds_read_b64_tr_b16 v[92:93], v18 offset:9440\n"
"v_mfma_f32_16x16x32_bf16 v[204:207], v[100:103], a[156:159], v[204:207]\n"
"v_mfma_f32_16x16x32_bf16 v[204:207], v[96:99], a[152:155], v[204:207]\n"
"s_add_u32 m0, s78, 0x9400\n"
"ds_read_b64_tr_b16 v[94:95], v18 offset:14048\n"
"s_waitcnt lgkmcnt(6)\n"
"v_mfma_f32_16x16x32_bf16 v[208:211], v[80:83], a[128:131], v[208:211]\n"
"buffer_load_dwordx4 v7, s[60:63], s77 offen lds\n"
"s_waitcnt lgkmcnt(4)\n"
"v_mfma_f32_16x16x32_bf16 v[208:211], v[84:87], a[132:135], v[208:211]\n"
"ds_read_b64_tr_b16 v[96:97], v18 offset:256\n"
"v_mfma_f32_16x16x32_bf16 v[212:215], v[84:87], a[140:143], v[212:215]\n"
"v_mfma_f32_16x16x32_bf16 v[212:215], v[80:83], a[136:139], v[212:215]\n"
"ds_read_b64_tr_b16 v[98:99], v18 offset:4864\n"
"v_mfma_f32_16x16x32_bf16 v[216:219], v[80:83], a[144:147], v[216:219]\n"
"v_mfma_f32_16x16x32_bf16 v[216:219], v[84:87], a[148:151], v[216:219]\n"
"s_add_u32 m0, s78, 0x9800\n"
"ds_read_b64_tr_b16 v[100:101], v18 offset:9472\n"
"v_mfma_f32_16x16x32_bf16 v[220:223], v[84:87], a[156:159], v[220:223]\n"
"buffer_load_dwordx4 v8, s[60:63], s77 offen lds\n"
"v_mfma_f32_16x16x32_bf16 v[220:223], v[80:83], a[152:155], v[220:223]\n"
"s_nop 0\n"
"s_barrier\n"
"ds_read_b64_tr_b16 v[102:103], v18 offset:14080\n"
"s_waitcnt lgkmcnt(6)\n"
"v_mfma_f32_16x16x32_bf16 v[224:227], v[88:91], a[128:131], v[224:227]\n"
"s_waitcnt lgkmcnt(4)\n"
"v_mfma_f32_16x16x32_bf16 v[224:227], v[92:95], a[132:135], v[224:227]\n"
"s_add_u32 m0, s78, 0x9c00\n"
"ds_read_b64_tr_b16 v[80:81], v18 offset:18432\n"
"v_mfma_f32_16x16x32_bf16 v[228:231], v[92:95], a[140:143], v[228:231]\n"
"buffer_load_dwordx4 v9, s[60:63], s77 offen lds\n"
"v_mfma_f32_16x16x32_bf16 v[228:231], v[88:91], a[136:139], v[228:231]\n"
"ds_read_b64_tr_b16 v[82:83], v18 offset:23040\n"
"v_mfma_f32_16x16x32_bf16 v[232:235], v[88:91], a[144:147], v[232:235]\n"
"v_mfma_f32_16x16x32_bf16 v[232:235], v[92:95], a[148:151], v[232:235]\n"
"ds_read_b64_tr_b16 v[84:85], v18 offset:27648\n"
"v_mfma_f32_16x16x32_bf16 v[236:239], v[92:95], a[156:159], v[236:239]\n"
"v_mfma_f32_16x16x32_bf16 v[236:239], v[88:91], a[152:155], v[236:239]\n"
"s_add_u32 m0, s78, 0xa000\n"
"ds_read_b64_tr_b16 v[86:87], v18 offset:32256\n"
"s_waitcnt lgkmcnt(6)\n"
"v_mfma_f32_16x16x32_bf16 v[240:243], v[96:99], a[128:131], v[240:243]\n"
"s_bitcmp1_b32 s72, 0\n"
"s_cbranch_scc1 NOP5_12_%=\n"
"buffer_load_dwordx4 v10, s[60:63], s77 offen lds\n"
"NOP5_12_%=:\n"
"s_waitcnt lgkmcnt(4)\n"
"v_mfma_f32_16x16x32_bf16 v[240:243], v[100:103], a[132:135], v[240:243]\n"
"ds_read_b64_tr_b16 v[88:89], v18 offset:18464\n"
"v_mfma_f32_16x16x32_bf16 v[244:247], v[100:103], a[140:143], v[244:247]\n"
"v_mfma_f32_16x16x32_bf16 v[244:247], v[96:99], a[136:139], v[244:247]\n"
"ds_read_b64_tr_b16 v[90:91], v18 offset:23072\n"
"v_mfma_f32_16x16x32_bf16 v[248:251], v[96:99], a[144:147], v[248:251]\n"
"v_mfma_f32_16x16x32_bf16 v[248:251], v[100:103], a[148:151], v[248:251]\n"
"ds_read_b64_tr_b16 v[92:93], v18 offset:27680\n"
"v_mfma_f32_16x16x32_bf16 v[252:255], v[100:103], a[156:159], v[252:255]\n"
"v_mfma_f32_16x16x32_bf16 v[252:255], v[96:99], a[152:155], v[252:255]\n"
"ds_read_b64_tr_b16 v[94:95], v18 offset:32288\n"
"s_nop 0\n"
"s_waitcnt lgkmcnt(6)\n"
"v_mfma_f32_16x16x32_bf16 v[112:115], v[80:83], a[160:163], v[112:115]\n"
"s_waitcnt lgkmcnt(4)\n"
"v_mfma_f32_16x16x32_bf16 v[112:115], v[84:87], a[164:167], v[112:115]\n"
"s_add_u32 s98, s85, 0x5800\n"
"ds_read_b64_tr_b16 v[96:97], v18 offset:18496\n"
"v_mfma_f32_16x16x32_bf16 v[116:119], v[84:87], a[172:175], v[116:119]\n"
"buffer_load_dwordx4 a[32:35], v20, s[64:67], s98 offen sc1\n"
"v_mfma_f32_16x16x32_bf16 v[116:119], v[80:83], a[168:171], v[116:119]\n"
"ds_read_b64_tr_b16 v[98:99], v18 offset:23104\n"
"v_mfma_f32_16x16x32_bf16 v[120:123], v[80:83], a[176:179], v[120:123]\n"
"v_mfma_f32_16x16x32_bf16 v[120:123], v[84:87], a[180:183], v[120:123]\n"
"ds_read_b64_tr_b16 v[100:101], v18 offset:27712\n"
"v_mfma_f32_16x16x32_bf16 v[124:127], v[84:87], a[188:191], v[124:127]\n"
"buffer_load_dwordx4 a[36:39], v20, s[64:67], s98 offen offset:1024 sc1\n"
"v_mfma_f32_16x16x32_bf16 v[124:127], v[80:83], a[184:187], v[124:127]\n"
"ds_read_b64_tr_b16 v[102:103], v18 offset:32320\n"
"s_waitcnt lgkmcnt(6)\n"
"v_mfma_f32_16x16x32_bf16 v[128:131], v[88:91], a[160:163], v[128:131]\n"
"s_waitcnt lgkmcnt(4)\n"
"v_mfma_f32_16x16x32_bf16 v[128:131], v[92:95], a[164:167], v[128:131]\n"
"ds_read_b64_tr_b16 v[80:81], v18 offset:18528\n"
"v_mfma_f32_16x16x32_bf16 v[132:135], v[92:95], a[172:175], v[132:135]\n"
"v_mfma_f32_16x16x32_bf16 v[132:135], v[88:91], a[168:171], v[132:135]\n"
"s_add_u32 s98, s98, s86\n"
"ds_read_b64_tr_b16 v[82:83], v18 offset:23136\n"
"v_mfma_f32_16x16x32_bf16 v[136:139], v[88:91], a[176:179], v[136:139]\n"
"buffer_load_dwordx4 a[40:43], v20, s[64:67], s98 offen sc1\n"
"v_mfma_f32_16x16x32_bf16 v[136:139], v[92:95], a[180:183], v[136:139]\n"
"ds_read_b64_tr_b16 v[84:85], v18 offset:27744\n"
"v_mfma_f32_16x16x32_bf16 v[140:143], v[92:95], a[188:191], v[140:143]\n"
"v_mfma_f32_16x16x32_bf16 v[140:143], v[88:91], a[184:187], v[140:143]\n"
"ds_read_b64_tr_b16 v[86:87], v18 offset:32352\n"
"s_waitcnt lgkmcnt(6)\n"
"v_mfma_f32_16x16x32_bf16 v[144:147], v[96:99], a[160:163], v[144:147]\n"
"buffer_load_dwordx4 a[44:47], v20, s[64:67], s98 offen offset:1024 sc1\n"
"s_waitcnt lgkmcnt(4)\n"
"v_mfma_f32_16x16x32_bf16 v[144:147], v[100:103], a[164:167], v[144:147]\n"
"ds_read_b64_tr_b16 v[88:89], v18 offset:18560\n"
"v_mfma_f32_16x16x32_bf16 v[148:151], v[100:103], a[172:175], v[148:151]\n"
"v_mfma_f32_16x16x32_bf16 v[148:151], v[96:99], a[168:171], v[148:151]\n"
"ds_read_b64_tr_b16 v[90:91], v18 offset:23168\n"
"v_mfma_f32_16x16x32_bf16 v[152:155], v[96:99], a[176:179], v[152:155]\n"
"v_mfma_f32_16x16x32_bf16 v[152:155], v[100:103], a[180:183], v[152:155]\n"
"s_add_u32 s98, s98, s86\n"
"ds_read_b64_tr_b16 v[92:93], v18 offset:27776\n"
"v_mfma_f32_16x16x32_bf16 v[156:159], v[100:103], a[188:191], v[156:159]\n"
"buffer_load_dwordx4 a[48:51], v20, s[64:67], s98 offen sc1\n"
"v_mfma_f32_16x16x32_bf16 v[156:159], v[96:99], a[184:187], v[156:159]\n"
"ds_read_b64_tr_b16 v[94:95], v18 offset:32384\n"
"s_waitcnt lgkmcnt(6)\n"
"v_mfma_f32_16x16x32_bf16 v[160:163], v[80:83], a[160:163], v[160:163]\n"
"s_waitcnt lgkmcnt(4)\n"
"v_mfma_f32_16x16x32_bf16 v[160:163], v[84:87], a[164:167], v[160:163]\n"
"ds_read_b64_tr_b16 v[96:97], v18 offset:18592\n"
"v_mfma_f32_16x16x32_bf16 v[164:167], v[84:87], a[172:175], v[164:167]\n"
"v_mfma_f32_16x16x32_bf16 v[164:167], v[80:83], a[168:171], v[164:167]\n"
"ds_read_b64_tr_b16 v[98:99], v18 offset:23200\n"
"v_mfma_f32_16x16x32_bf16 v[168:171], v[80:83], a[176:179], v[168:171]\n"
"buffer_load_dwordx4 a[52:55], v20, s[64:67], s98 offen offset:1024 sc1\n"
"v_mfma_f32_16x16x32_bf16 v[168:171], v[84:87], a[180:183], v[168:171]\n"
"ds_read_b64_tr_b16 v[100:101], v18 offset:27808\n"
"v_mfma_f32_16x16x32_bf16 v[172:175], v[84:87], a[188:191], v[172:175]\n"
"v_mfma_f32_16x16x32_bf16 v[172:175], v[80:83], a[184:187], v[172:175]\n"
"s_add_u32 s98, s98, s86\n"
"ds_read_b64_tr_b16 v[102:103], v18 offset:32416\n"
"s_waitcnt lgkmcnt(6)\n"
"v_mfma_f32_16x16x32_bf16 v[176:179], v[88:91], a[160:163], v[176:179]\n"
"buffer_load_dwordx4 a[56:59], v20, s[64:67], s98 offen sc1\n"
"s_waitcnt lgkmcnt(4)\n"
"v_mfma_f32_16x16x32_bf16 v[176:179], v[92:95], a[164:167], v[176:179]\n"
"ds_read_b64_tr_b16 v[80:81], v18 offset:18624\n"
"v_mfma_f32_16x16x32_bf16 v[180:183], v[92:95], a[172:175], v[180:183]\n"
"v_mfma_f32_16x16x32_bf16 v[180:183], v[88:91], a[168:171], v[180:183]\n"
"ds_read_b64_tr_b16 v[82:83], v18 offset:23232\n"
"v_mfma_f32_16x16x32_bf16 v[184:187], v[88:91], a[176:179], v[184:187]\n"
"v_mfma_f32_16x16x32_bf16 v[184:187], v[92:95], a[180:183], v[184:187]\n"
"ds_read_b64_tr_b16 v[84:85], v18 offset:27840\n"
"v_mfma_f32_16x16x32_bf16 v[188:191], v[92:95], a[188:191], v[188:191]\n"
"buffer_load_dwordx4 a[60:63], v20, s[64:67], s98 offen offset:1024 sc1\n"
"v_mfma_f32_16x16x32_bf16 v[188:191], v[88:91], a[184:187], v[188:191]\n"
"ds_read_b64_tr_b16 v[86:87], v18 offset:32448\n"
"s_waitcnt lgkmcnt(6)\n"
"v_mfma_f32_16x16x32_bf16 v[192:195], v[96:99], a[160:163], v[192:195]\n"
"s_waitcnt lgkmcnt(4)\n"
"v_mfma_f32_16x16x32_bf16 v[192:195], v[100:103], a[164:167], v[192:195]\n"
"s_add_u32 s77, s84, 0xc6000\n"
"s_add_u32 m0, s78, 0xd800\n"
"ds_read_b64_tr_b16 v[88:89], v18 offset:18656\n"
"v_mfma_f32_16x16x32_bf16 v[196:199], v[100:103], a[172:175], v[196:199]\n"
"buffer_load_dwordx4 v6, s[60:63], s77 offen lds\n"
"v_mfma_f32_16x16x32_bf16 v[196:199], v[96:99], a[168:171], v[196:199]\n"
"ds_read_b64_tr_b16 v[90:91], v18 offset:23264\n"
"v_mfma_f32_16x16x32_bf16 v[200:203], v[96:99], a[176:179], v[200:203]\n"
"v_mfma_f32_16x16x32_bf16 v[200:203], v[100:103], a[180:183], v[200:203]\n"
"ds_read_b64_tr_b16 v[92:93], v18 offset:27872\n"
"v_mfma_f32_16x16x32_bf16 v[204:207], v[100:103], a[188:191], v[204:207]\n"
"v_mfma_f32_16x16x32_bf16 v[204:207], v[96:99], a[184:187], v[204:207]\n"
"s_add_u32 m0, s78, 0xdc00\n"
"ds_read_b64_tr_b16 v[94:95], v18 offset:32480\n"
"s_waitcnt lgkmcnt(6)\n"
"v_mfma_f32_16x16x32_bf16 v[208:211], v[80:83], a[160:163], v[208:211]\n"
"buffer_load_dwordx4 v7, s[60:63], s77 offen lds\n"
"s_waitcnt lgkmcnt(4)\n"
"v_mfma_f32_16x16x32_bf16 v[208:211], v[84:87], a[164:167], v[208:211]\n"
"ds_read_b64_tr_b16 v[96:97], v18 offset:18688\n"
"v_mfma_f32_16x16x32_bf16 v[212:215], v[84:87], a[172:175], v[212:215]\n"
"v_mfma_f32_16x16x32_bf16 v[212:215], v[80:83], a[168:171], v[212:215]\n"
"ds_read_b64_tr_b16 v[98:99], v18 offset:23296\n"
"v_mfma_f32_16x16x32_bf16 v[216:219], v[80:83], a[176:179], v[216:219]\n"
"v_mfma_f32_16x16x32_bf16 v[216:219], v[84:87], a[180:183], v[216:219]\n"
"s_add_u32 m0, s78, 0xe000\n"
"ds_read_b64_tr_b16 v[100:101], v18 offset:27904\n"
"v_mfma_f32_16x16x32_bf16 v[220:223], v[84:87], a[188:191], v[220:223]\n"
"buffer_load_dwordx4 v8, s[60:63], s77 offen lds\n"
"v_mfma_f32_16x16x32_bf16 v[220:223], v[80:83], a[184:187], v[220:223]\n"
"s_nop 0\n"
"s_barrier\n"
"ds_read_b64_tr_b16 v[102:103], v18 offset:32512\n"
"s_waitcnt lgkmcnt(6)\n"
"v_mfma_f32_16x16x32_bf16 v[224:227], v[88:91], a[160:163], v[224:227]\n"
"s_waitcnt lgkmcnt(4)\n"
"v_mfma_f32_16x16x32_bf16 v[224:227], v[92:95], a[164:167], v[224:227]\n"
"s_add_u32 m0, s78, 0xe400\n"
"ds_read_b64_tr_b16 v[80:81], v16\n"
"v_mfma_f32_16x16x32_bf16 v[228:231], v[92:95], a[172:175], v[228:231]\n"
"buffer_load_dwordx4 v9, s[60:63], s77 offen lds\n"
"v_mfma_f32_16x16x32_bf16 v[228:231], v[88:91], a[168:171], v[228:231]\n"
"ds_read_b64_tr_b16 v[82:83], v16 offset:4608\n"
"v_mfma_f32_16x16x32_bf16 v[232:235], v[88:91], a[176:179], v[232:235]\n"
"v_mfma_f32_16x16x32_bf16 v[232:235], v[92:95], a[180:183], v[232:235]\n"
"ds_read_b64_tr_b16 v[84:85], v16 offset:9216\n"
"v_mfma_f32_16x16x32_bf16 v[236:239], v[92:95], a[188:191], v[236:239]\n"
"v_mfma_f32_16x16x32_bf16 v[236:239], v[88:91], a[184:187], v[236:239]\n"
"s_add_u32 m0, s78, 0xe800\n"
"ds_read_b64_tr_b16 v[86:87], v16 offset:13824\n"
"s_waitcnt lgkmcnt(6)\n"
"v_mfma_f32_16x16x32_bf16 v[240:243], v[96:99], a[160:163], v[240:243]\n"
"s_bitcmp1_b32 s72, 0\n"
"s_cbranch_scc1 NOP5_13_%=\n"
"buffer_load_dwordx4 v10, s[60:63], s77 offen lds\n"
"NOP5_13_%=:\n"
"s_waitcnt lgkmcnt(4)\n"
"v_mfma_f32_16x16x32_bf16 v[240:243], v[100:103], a[164:167], v[240:243]\n"
"ds_read_b64_tr_b16 v[88:89], v16 offset:32\n"
"v_mfma_f32_16x16x32_bf16 v[244:247], v[100:103], a[172:175], v[244:247]\n"
"v_mfma_f32_16x16x32_bf16 v[244:247], v[96:99], a[168:171], v[244:247]\n"
"ds_read_b64_tr_b16 v[90:91], v16 offset:4640\n"
"v_mfma_f32_16x16x32_bf16 v[248:251], v[96:99], a[176:179], v[248:251]\n"
"v_mfma_f32_16x16x32_bf16 v[248:251], v[100:103], a[180:183], v[248:251]\n"
"ds_read_b64_tr_b16 v[92:93], v16 offset:9248\n"
"v_mfma_f32_16x16x32_bf16 v[252:255], v[100:103], a[188:191], v[252:255]\n"
"v_mfma_f32_16x16x32_bf16 v[252:255], v[96:99], a[184:187], v[252:255]\n"
"ds_read_b64_tr_b16 v[94:95], v16 offset:13856\n"
"s_nop 0\n"
"s_waitcnt lgkmcnt(6)\n"
"v_mfma_f32_16x16x32_bf16 v[112:115], v[80:83], a[192:195], v[112:115]\n"
"s_waitcnt vmcnt(48)\n"
"s_waitcnt lgkmcnt(4)\n"
"v_mfma_f32_16x16x32_bf16 v[112:115], v[84:87], a[196:199], v[112:115]\n"
"s_add_u32 s98, s85, 0x6000\n"
"ds_read_b64_tr_b16 v[96:97], v16 offset:64\n"
"v_mfma_f32_16x16x32_bf16 v[116:119], v[84:87], a[204:207], v[116:119]\n"
"buffer_load_dwordx4 a[64:67], v20, s[64:67], s98 offen sc1\n"
"v_mfma_f32_16x16x32_bf16 v[116:119], v[80:83], a[200:203], v[116:119]\n"
"ds_read_b64_tr_b16 v[98:99], v16 offset:4672\n"
"v_mfma_f32_16x16x32_bf16 v[120:123], v[80:83], a[208:211], v[120:123]\n"
"v_mfma_f32_16x16x32_bf16 v[120:123], v[84:87], a[212:215], v[120:123]\n"
"ds_read_b64_tr_b16 v[100:101], v16 offset:9280\n"
"v_mfma_f32_16x16x32_bf16 v[124:127], v[84:87], a[220:223], v[124:127]\n"
"buffer_load_dwordx4 a[68:71], v20, s[64:67], s98 offen offset:1024 sc1\n"
"v_mfma_f32_16x16x32_bf16 v[124:127], v[80:83], a[216:219], v[124:127]\n"
"ds_read_b64_tr_b16 v[102:103], v16 offset:13888\n"
"s_waitcnt lgkmcnt(6)\n"
"v_mfma_f32_16x16x32_bf16 v[128:131], v[88:91], a[192:195], v[128:131]\n"
"s_waitcnt lgkmcnt(4)\n"
"v_mfma_f32_16x16x32_bf16 v[128:131], v[92:95], a[196:199], v[128:131]\n"
"ds_read_b64_tr_b16 v[80:81], v16 offset:96\n"
"v_mfma_f32_16x16x32_bf16 v[132:135], v[92:95], a[204:207], v[132:135]\n"
"v_mfma_f32_16x16x32_bf16 v[132:135], v[88:91], a[200:203], v[132:135]\n"
"s_add_u32 s98, s98, s86\n"
"ds_read_b64_tr_b16 v[82:83], v16 offset:4704\n"
"v_mfma_f32_16x16x32_bf16 v[136:139], v[88:91], a[208:211], v[136:139]\n"
"buffer_load_dwordx4 a[72:75], v20, s[64:67], s98 offen sc1\n"
"v_mfma_f32_16x16x32_bf16 v[136:139], v[92:95], a[212:215], v[136:139]\n"
"ds_read_b64_tr_b16 v[84:85], v16 offset:9312\n"
"v_mfma_f32_16x16x32_bf16 v[140:143], v[92:95], a[220:223], v[140:143]\n"
"v_mfma_f32_16x16x32_bf16 v[140:143], v[88:91], a[216:219], v[140:143]\n"
"ds_read_b64_tr_b16 v[86:87], v16 offset:13920\n"
"s_waitcnt lgkmcnt(6)\n"
"v_mfma_f32_16x16x32_bf16 v[144:147], v[96:99], a[192:195], v[144:147]\n"
"buffer_load_dwordx4 a[76:79], v20, s[64:67], s98 offen offset:1024 sc1\n"
"s_waitcnt lgkmcnt(4)\n"
"v_mfma_f32_16x16x32_bf16 v[144:147], v[100:103], a[196:199], v[144:147]\n"
"ds_read_b64_tr_b16 v[88:89], v16 offset:128\n"
"v_mfma_f32_16x16x32_bf16 v[148:151], v[100:103], a[204:207], v[148:151]\n"
"v_mfma_f32_16x16x32_bf16 v[148:151], v[96:99], a[200:203], v[148:151]\n"
"ds_read_b64_tr_b16 v[90:91], v16 offset:4736\n"
"v_mfma_f32_16x16x32_bf16 v[152:155], v[96:99], a[208:211], v[152:155]\n"
"v_mfma_f32_16x16x32_bf16 v[152:155], v[100:103], a[212:215], v[152:155]\n"
"s_add_u32 s98, s98, s86\n"
"ds_read_b64_tr_b16 v[92:93], v16 offset:9344\n"
"v_mfma_f32_16x16x32_bf16 v[156:159], v[100:103], a[220:223], v[156:159]\n"
"buffer_load_dwordx4 a[80:83], v20, s[64:67], s98 offen sc1\n"
"v_mfma_f32_16x16x32_bf16 v[156:159], v[96:99], a[216:219], v[156:159]\n"
"ds_read_b64_tr_b16 v[94:95], v16 offset:13952\n"
"s_waitcnt lgkmcnt(6)\n"
"v_mfma_f32_16x16x32_bf16 v[160:163], v[80:83], a[192:195], v[160:163]\n"
"s_waitcnt lgkmcnt(4)\n"
"v_mfma_f32_16x16x32_bf16 v[160:163], v[84:87], a[196:199], v[160:163]\n"
"ds_read_b64_tr_b16 v[96:97], v16 offset:160\n"
"v_mfma_f32_16x16x32_bf16 v[164:167], v[84:87], a[204:207], v[164:167]\n"
"v_mfma_f32_16x16x32_bf16 v[164:167], v[80:83], a[200:203], v[164:167]\n"
"ds_read_b64_tr_b16 v[98:99], v16 offset:4768\n"
"v_mfma_f32_16x16x32_bf16 v[168:171], v[80:83], a[208:211], v[168:171]\n"
"buffer_load_dwordx4 a[84:87], v20, s[64:67], s98 offen offset:1024 sc1\n"
"v_mfma_f32_16x16x32_bf16 v[168:171], v[84:87], a[212:215], v[168:171]\n"
"ds_read_b64_tr_b16 v[100:101], v16 offset:9376\n"
"v_mfma_f32_16x16x32_bf16 v[172:175], v[84:87], a[220:223], v[172:175]\n"
"v_mfma_f32_16x16x32_bf16 v[172:175], v[80:83], a[216:219], v[172:175]\n"
"s_add_u32 s98, s98, s86\n"
"ds_read_b64_tr_b16 v[102:103], v16 offset:13984\n"
"s_waitcnt lgkmcnt(6)\n"
"v_mfma_f32_16x16x32_bf16 v[176:179], v[88:91], a[192:195], v[176:179]\n"
"buffer_load_dwordx4 a[88:91], v20, s[64:67], s98 offen sc1\n"
"s_waitcnt lgkmcnt(4)\n"
"v_mfma_f32_16x16x32_bf16 v[176:179], v[92:95], a[196:199], v[176:179]\n"
"ds_read_b64_tr_b16 v[80:81], v16 offset:192\n"
"v_mfma_f32_16x16x32_bf16 v[180:183], v[92:95], a[204:207], v[180:183]\n"
"v_mfma_f32_16x16x32_bf16 v[180:183], v[88:91], a[200:203], v[180:183]\n"
"ds_read_b64_tr_b16 v[82:83], v16 offset:4800\n"
"v_mfma_f32_16x16x32_bf16 v[184:187], v[88:91], a[208:211], v[184:187]\n"
"v_mfma_f32_16x16x32_bf16 v[184:187], v[92:95], a[212:215], v[184:187]\n"
"ds_read_b64_tr_b16 v[84:85], v16 offset:9408\n"
"v_mfma_f32_16x16x32_bf16 v[188:191], v[92:95], a[220:223], v[188:191]\n"
"buffer_load_dwordx4 a[92:95], v20, s[64:67], s98 offen offset:1024 sc1\n"
"v_mfma_f32_16x16x32_bf16 v[188:191], v[88:91], a[216:219], v[188:191]\n"
"ds_read_b64_tr_b16 v[86:87], v16 offset:14016\n"
"s_waitcnt lgkmcnt(6)\n"
"v_mfma_f32_16x16x32_bf16 v[192:195], v[96:99], a[192:195], v[192:195]\n"
"s_waitcnt lgkmcnt(4)\n"
"v_mfma_f32_16x16x32_bf16 v[192:195], v[100:103], a[196:199], v[192:195]\n"
"s_add_u32 s77, s84, 0xd8000\n"
"s_add_u32 m0, s78, 0x12000\n"
"ds_read_b64_tr_b16 v[88:89], v16 offset:224\n"
"v_mfma_f32_16x16x32_bf16 v[196:199], v[100:103], a[204:207], v[196:199]\n"
"buffer_load_dwordx4 v6, s[60:63], s77 offen lds\n"
"v_mfma_f32_16x16x32_bf16 v[196:199], v[96:99], a[200:203], v[196:199]\n"
"ds_read_b64_tr_b16 v[90:91], v16 offset:4832\n"
"v_mfma_f32_16x16x32_bf16 v[200:203], v[96:99], a[208:211], v[200:203]\n"
"v_mfma_f32_16x16x32_bf16 v[200:203], v[100:103], a[212:215], v[200:203]\n"
"ds_read_b64_tr_b16 v[92:93], v16 offset:9440\n"
"v_mfma_f32_16x16x32_bf16 v[204:207], v[100:103], a[220:223], v[204:207]\n"
"v_mfma_f32_16x16x32_bf16 v[204:207], v[96:99], a[216:219], v[204:207]\n"
"s_add_u32 m0, s78, 0x12400\n"
"ds_read_b64_tr_b16 v[94:95], v16 offset:14048\n"
"s_waitcnt lgkmcnt(6)\n"
"v_mfma_f32_16x16x32_bf16 v[208:211], v[80:83], a[192:195], v[208:211]\n"
"buffer_load_dwordx4 v7, s[60:63], s77 offen lds\n"
"s_waitcnt lgkmcnt(4)\n"
"v_mfma_f32_16x16x32_bf16 v[208:211], v[84:87], a[196:199], v[208:211]\n"
"ds_read_b64_tr_b16 v[96:97], v16 offset:256\n"
"v_mfma_f32_16x16x32_bf16 v[212:215], v[84:87], a[204:207], v[212:215]\n"
"v_mfma_f32_16x16x32_bf16 v[212:215], v[80:83], a[200:203], v[212:215]\n"
"ds_read_b64_tr_b16 v[98:99], v16 offset:4864\n"
"v_mfma_f32_16x16x32_bf16 v[216:219], v[80:83], a[208:211], v[216:219]\n"
"v_mfma_f32_16x16x32_bf16 v[216:219], v[84:87], a[212:215], v[216:219]\n"
"s_add_u32 m0, s78, 0x12800\n"
"ds_read_b64_tr_b16 v[100:101], v16 offset:9472\n"
"v_mfma_f32_16x16x32_bf16 v[220:223], v[84:87], a[220:223], v[220:223]\n"
"buffer_load_dwordx4 v8, s[60:63], s77 offen lds\n"
"v_mfma_f32_16x16x32_bf16 v[220:223], v[80:83], a[216:219], v[220:223]\n"
"s_waitcnt vmcnt(42)\n"
"s_barrier\n"
"ds_read_b64_tr_b16 v[102:103], v16 offset:14080\n"
"s_waitcnt lgkmcnt(6)\n"
"v_mfma_f32_16x16x32_bf16 v[224:227], v[88:91], a[192:195], v[224:227]\n"
"s_waitcnt lgkmcnt(4)\n"
"v_mfma_f32_16x16x32_bf16 v[224:227], v[92:95], a[196:199], v[224:227]\n"
"s_add_u32 m0, s78, 0x12c00\n"
"ds_read_b64_tr_b16 v[80:81], v16 offset:18432\n"
"v_mfma_f32_16x16x32_bf16 v[228:231], v[92:95], a[204:207], v[228:231]\n"
"buffer_load_dwordx4 v9, s[60:63], s77 offen lds\n"
"v_mfma_f32_16x16x32_bf16 v[228:231], v[88:91], a[200:203], v[228:231]\n"
"ds_read_b64_tr_b16 v[82:83], v16 offset:23040\n"
"v_mfma_f32_16x16x32_bf16 v[232:235], v[88:91], a[208:211], v[232:235]\n"
"v_mfma_f32_16x16x32_bf16 v[232:235], v[92:95], a[212:215], v[232:235]\n"
"ds_read_b64_tr_b16 v[84:85], v16 offset:27648\n"
"v_mfma_f32_16x16x32_bf16 v[236:239], v[92:95], a[220:223], v[236:239]\n"
"v_mfma_f32_16x16x32_bf16 v[236:239], v[88:91], a[216:219], v[236:239]\n"
"s_add_u32 m0, s78, 0x13000\n"
"ds_read_b64_tr_b16 v[86:87], v16 offset:32256\n"
"s_waitcnt lgkmcnt(6)\n"
"v_mfma_f32_16x16x32_bf16 v[240:243], v[96:99], a[192:195], v[240:243]\n"
"s_bitcmp1_b32 s72, 0\n"
"s_cbranch_scc1 NOP5_14_%=\n"
"buffer_load_dwordx4 v10, s[60:63], s77 offen lds\n"
"NOP5_14_%=:\n"
"s_waitcnt lgkmcnt(4)\n"
"v_mfma_f32_16x16x32_bf16 v[240:243], v[100:103], a[196:199], v[240:243]\n"
"ds_read_b64_tr_b16 v[88:89], v16 offset:18464\n"
"v_mfma_f32_16x16x32_bf16 v[244:247], v[100:103], a[204:207], v[244:247]\n"
"v_mfma_f32_16x16x32_bf16 v[244:247], v[96:99], a[200:203], v[244:247]\n"
"ds_read_b64_tr_b16 v[90:91], v16 offset:23072\n"
"v_mfma_f32_16x16x32_bf16 v[248:251], v[96:99], a[208:211], v[248:251]\n"
"v_mfma_f32_16x16x32_bf16 v[248:251], v[100:103], a[212:215], v[248:251]\n"
"ds_read_b64_tr_b16 v[92:93], v16 offset:27680\n"
"v_mfma_f32_16x16x32_bf16 v[252:255], v[100:103], a[220:223], v[252:255]\n"
"v_mfma_f32_16x16x32_bf16 v[252:255], v[96:99], a[216:219], v[252:255]\n"
"ds_read_b64_tr_b16 v[94:95], v16 offset:32288\n"
"s_nop 0\n"
"s_waitcnt lgkmcnt(6)\n"
"v_mfma_f32_16x16x32_bf16 v[112:115], v[80:83], a[224:227], v[112:115]\n"
"s_waitcnt vmcnt(48)\n"
"s_waitcnt lgkmcnt(4)\n"
"v_mfma_f32_16x16x32_bf16 v[112:115], v[84:87], a[228:231], v[112:115]\n"
"s_add_u32 s98, s85, 0x6800\n"
"ds_read_b64_tr_b16 v[96:97], v16 offset:18496\n"
"v_mfma_f32_16x16x32_bf16 v[116:119], v[84:87], a[236:239], v[116:119]\n"
"buffer_load_dwordx4 a[96:99], v20, s[64:67], s98 offen sc1\n"
"v_mfma_f32_16x16x32_bf16 v[116:119], v[80:83], a[232:235], v[116:119]\n"
"ds_read_b64_tr_b16 v[98:99], v16 offset:23104\n"
"v_mfma_f32_16x16x32_bf16 v[120:123], v[80:83], a[240:243], v[120:123]\n"
"v_mfma_f32_16x16x32_bf16 v[120:123], v[84:87], a[244:247], v[120:123]\n"
"ds_read_b64_tr_b16 v[100:101], v16 offset:27712\n"
"v_mfma_f32_16x16x32_bf16 v[124:127], v[84:87], a[252:255], v[124:127]\n"
"buffer_load_dwordx4 a[100:103], v20, s[64:67], s98 offen offset:1024 sc1\n"
"v_mfma_f32_16x16x32_bf16 v[124:127], v[80:83], a[248:251], v[124:127]\n"
"ds_read_b64_tr_b16 v[102:103], v16 offset:32320\n"
"s_waitcnt lgkmcnt(6)\n"
"v_mfma_f32_16x16x32_bf16 v[128:131], v[88:91], a[224:227], v[128:131]\n"
"s_waitcnt lgkmcnt(4)\n"
"v_mfma_f32_16x16x32_bf16 v[128:131], v[92:95], a[228:231], v[128:131]\n"
"ds_read_b64_tr_b16 v[80:81], v16 offset:18528\n"
"v_mfma_f32_16x16x32_bf16 v[132:135], v[92:95], a[236:239], v[132:135]\n"
"v_mfma_f32_16x16x32_bf16 v[132:135], v[88:91], a[232:235], v[132:135]\n"
"s_add_u32 s98, s98, s86\n"
"ds_read_b64_tr_b16 v[82:83], v16 offset:23136\n"
"v_mfma_f32_16x16x32_bf16 v[136:139], v[88:91], a[240:243], v[136:139]\n"
"buffer_load_dwordx4 a[104:107], v20, s[64:67], s98 offen sc1\n"
"v_mfma_f32_16x16x32_bf16 v[136:139], v[92:95], a[244:247], v[136:139]\n"
"ds_read_b64_tr_b16 v[84:85], v16 offset:27744\n"
"v_mfma_f32_16x16x32_bf16 v[140:143], v[92:95], a[252:255], v[140:143]\n"
"v_mfma_f32_16x16x32_bf16 v[140:143], v[88:91], a[248:251], v[140:143]\n"
"ds_read_b64_tr_b16 v[86:87], v16 offset:32352\n"
"s_waitcnt lgkmcnt(6)\n"
"v_mfma_f32_16x16x32_bf16 v[144:147], v[96:99], a[224:227], v[144:147]\n"
"buffer_load_dwordx4 a[108:111], v20, s[64:67], s98 offen offset:1024 sc1\n"
"s_waitcnt lgkmcnt(4)\n"
"v_mfma_f32_16x16x32_bf16 v[144:147], v[100:103], a[228:231], v[144:147]\n"
"ds_read_b64_tr_b16 v[88:89], v16 offset:18560\n"
"v_mfma_f32_16x16x32_bf16 v[148:151], v[100:103], a[236:239], v[148:151]\n"
"v_mfma_f32_16x16x32_bf16 v[148:151], v[96:99], a[232:235], v[148:151]\n"
"ds_read_b64_tr_b16 v[90:91], v16 offset:23168\n"
"v_mfma_f32_16x16x32_bf16 v[152:155], v[96:99], a[240:243], v[152:155]\n"
"v_mfma_f32_16x16x32_bf16 v[152:155], v[100:103], a[244:247], v[152:155]\n"
"s_add_u32 s98, s98, s86\n"
"ds_read_b64_tr_b16 v[92:93], v16 offset:27776\n"
"v_mfma_f32_16x16x32_bf16 v[156:159], v[100:103], a[252:255], v[156:159]\n"
"buffer_load_dwordx4 a[112:115], v20, s[64:67], s98 offen sc1\n"
"v_mfma_f32_16x16x32_bf16 v[156:159], v[96:99], a[248:251], v[156:159]\n"
"ds_read_b64_tr_b16 v[94:95], v16 offset:32384\n"
"s_waitcnt lgkmcnt(6)\n"
"v_mfma_f32_16x16x32_bf16 v[160:163], v[80:83], a[224:227], v[160:163]\n"
"s_waitcnt lgkmcnt(4)\n"
"v_mfma_f32_16x16x32_bf16 v[160:163], v[84:87], a[228:231], v[160:163]\n"
"ds_read_b64_tr_b16 v[96:97], v16 offset:18592\n"
"v_mfma_f32_16x16x32_bf16 v[164:167], v[84:87], a[236:239], v[164:167]\n"
"v_mfma_f32_16x16x32_bf16 v[164:167], v[80:83], a[232:235], v[164:167]\n"
"ds_read_b64_tr_b16 v[98:99], v16 offset:23200\n"
"v_mfma_f32_16x16x32_bf16 v[168:171], v[80:83], a[240:243], v[168:171]\n"
"buffer_load_dwordx4 a[116:119], v20, s[64:67], s98 offen offset:1024 sc1\n"
"v_mfma_f32_16x16x32_bf16 v[168:171], v[84:87], a[244:247], v[168:171]\n"
"ds_read_b64_tr_b16 v[100:101], v16 offset:27808\n"
"v_mfma_f32_16x16x32_bf16 v[172:175], v[84:87], a[252:255], v[172:175]\n"
"v_mfma_f32_16x16x32_bf16 v[172:175], v[80:83], a[248:251], v[172:175]\n"
"s_add_u32 s98, s98, s86\n"
"ds_read_b64_tr_b16 v[102:103], v16 offset:32416\n"
"s_waitcnt lgkmcnt(6)\n"
"v_mfma_f32_16x16x32_bf16 v[176:179], v[88:91], a[224:227], v[176:179]\n"
"buffer_load_dwordx4 a[120:123], v20, s[64:67], s98 offen sc1\n"
"s_waitcnt lgkmcnt(4)\n"
"v_mfma_f32_16x16x32_bf16 v[176:179], v[92:95], a[228:231], v[176:179]\n"
"ds_read_b64_tr_b16 v[80:81], v16 offset:18624\n"
"v_mfma_f32_16x16x32_bf16 v[180:183], v[92:95], a[236:239], v[180:183]\n"
"v_mfma_f32_16x16x32_bf16 v[180:183], v[88:91], a[232:235], v[180:183]\n"
"ds_read_b64_tr_b16 v[82:83], v16 offset:23232\n"
"v_mfma_f32_16x16x32_bf16 v[184:187], v[88:91], a[240:243], v[184:187]\n"
"v_mfma_f32_16x16x32_bf16 v[184:187], v[92:95], a[244:247], v[184:187]\n"
"ds_read_b64_tr_b16 v[84:85], v16 offset:27840\n"
"v_mfma_f32_16x16x32_bf16 v[188:191], v[92:95], a[252:255], v[188:191]\n"
"buffer_load_dwordx4 a[124:127], v20, s[64:67], s98 offen offset:1024 sc1\n"
"v_mfma_f32_16x16x32_bf16 v[188:191], v[88:91], a[248:251], v[188:191]\n"
"ds_read_b64_tr_b16 v[86:87], v16 offset:32448\n"
"s_waitcnt lgkmcnt(6)\n"
"v_mfma_f32_16x16x32_bf16 v[192:195], v[96:99], a[224:227], v[192:195]\n"
"s_waitcnt lgkmcnt(4)\n"
"v_mfma_f32_16x16x32_bf16 v[192:195], v[100:103], a[228:231], v[192:195]\n"
"s_add_u32 s77, s84, 0xea000\n"
"s_add_u32 m0, s78, 0x16800\n"
"ds_read_b64_tr_b16 v[88:89], v16 offset:18656\n"
"v_mfma_f32_16x16x32_bf16 v[196:199], v[100:103], a[236:239], v[196:199]\n"
"buffer_load_dwordx4 v6, s[60:63], s77 offen lds\n"
"v_mfma_f32_16x16x32_bf16 v[196:199], v[96:99], a[232:235], v[196:199]\n"
"ds_read_b64_tr_b16 v[90:91], v16 offset:23264\n"
"v_mfma_f32_16x16x32_bf16 v[200:203], v[96:99], a[240:243], v[200:203]\n"
"v_mfma_f32_16x16x32_bf16 v[200:203], v[100:103], a[244:247], v[200:203]\n"
"ds_read_b64_tr_b16 v[92:93], v16 offset:27872\n"
"v_mfma_f32_16x16x32_bf16 v[204:207], v[100:103], a[252:255], v[204:207]\n"
"v_mfma_f32_16x16x32_bf16 v[204:207], v[96:99], a[248:251], v[204:207]\n"
"s_add_u32 m0, s78, 0x16c00\n"
"ds_read_b64_tr_b16 v[94:95], v16 offset:32480\n"
"s_waitcnt lgkmcnt(6)\n"
"v_mfma_f32_16x16x32_bf16 v[208:211], v[80:83], a[224:227], v[208:211]\n"
"buffer_load_dwordx4 v7, s[60:63], s77 offen lds\n"
"s_waitcnt lgkmcnt(4)\n"
"v_mfma_f32_16x16x32_bf16 v[208:211], v[84:87], a[228:231], v[208:211]\n"
"ds_read_b64_tr_b16 v[96:97], v16 offset:18688\n"
"v_mfma_f32_16x16x32_bf16 v[212:215], v[84:87], a[236:239], v[212:215]\n"
"v_mfma_f32_16x16x32_bf16 v[212:215], v[80:83], a[232:235], v[212:215]\n"
"ds_read_b64_tr_b16 v[98:99], v16 offset:23296\n"
"v_mfma_f32_16x16x32_bf16 v[216:219], v[80:83], a[240:243], v[216:219]\n"
"v_mfma_f32_16x16x32_bf16 v[216:219], v[84:87], a[244:247], v[216:219]\n"
"s_add_u32 m0, s78, 0x17000\n"
"ds_read_b64_tr_b16 v[100:101], v16 offset:27904\n"
"v_mfma_f32_16x16x32_bf16 v[220:223], v[84:87], a[252:255], v[220:223]\n"
"buffer_load_dwordx4 v8, s[60:63], s77 offen lds\n"
"v_mfma_f32_16x16x32_bf16 v[220:223], v[80:83], a[248:251], v[220:223]\n"
"s_waitcnt vmcnt(35)\n"
"s_barrier\n"
"ds_read_b64_tr_b16 v[102:103], v16 offset:32512\n"
"s_waitcnt lgkmcnt(6)\n"
"v_mfma_f32_16x16x32_bf16 v[224:227], v[88:91], a[224:227], v[224:227]\n"
"s_waitcnt lgkmcnt(4)\n"
"v_mfma_f32_16x16x32_bf16 v[224:227], v[92:95], a[228:231], v[224:227]\n"
"s_add_u32 m0, s78, 0x17400\n"
"ds_read_b64_tr_b16 v[80:81], v16 offset:36864\n"
"v_mfma_f32_16x16x32_bf16 v[228:231], v[92:95], a[236:239], v[228:231]\n"
"buffer_load_dwordx4 v9, s[60:63], s77 offen lds\n"
"v_mfma_f32_16x16x32_bf16 v[228:231], v[88:91], a[232:235], v[228:231]\n"
"ds_read_b64_tr_b16 v[82:83], v16 offset:41472\n"
"v_mfma_f32_16x16x32_bf16 v[232:235], v[88:91], a[240:243], v[232:235]\n"
"v_mfma_f32_16x16x32_bf16 v[232:235], v[92:95], a[244:247], v[232:235]\n"
"ds_read_b64_tr_b16 v[84:85], v16 offset:46080\n"
"v_mfma_f32_16x16x32_bf16 v[236:239], v[92:95], a[252:255], v[236:239]\n"
"v_mfma_f32_16x16x32_bf16 v[236:239], v[88:91], a[248:251], v[236:239]\n"
"s_add_u32 m0, s78, 0x17800\n"
"ds_read_b64_tr_b16 v[86:87], v16 offset:50688\n"
"s_waitcnt lgkmcnt(6)\n"
"v_mfma_f32_16x16x32_bf16 v[240:243], v[96:99], a[224:227], v[240:243]\n"
"s_bitcmp1_b32 s72, 0\n"
"s_cbranch_scc1 NOP5_15_%=\n"
"buffer_load_dwordx4 v10, s[60:63], s77 offen lds\n"
"NOP5_15_%=:\n"
"s_waitcnt lgkmcnt(4)\n"
"v_mfma_f32_16x16x32_bf16 v[240:243], v[100:103], a[228:231], v[240:243]\n"
"ds_read_b64_tr_b16 v[88:89], v16 offset:36896\n"
"v_mfma_f32_16x16x32_bf16 v[244:247], v[100:103], a[236:239], v[244:247]\n"
"v_mfma_f32_16x16x32_bf16 v[244:247], v[96:99], a[232:235], v[244:247]\n"
"ds_read_b64_tr_b16 v[90:91], v16 offset:41504\n"
"v_mfma_f32_16x16x32_bf16 v[248:251], v[96:99], a[240:243], v[248:251]\n"
"v_mfma_f32_16x16x32_bf16 v[248:251], v[100:103], a[244:247], v[248:251]\n"
"ds_read_b64_tr_b16 v[92:93], v16 offset:46112\n"
"v_mfma_f32_16x16x32_bf16 v[252:255], v[100:103], a[252:255], v[252:255]\n"
"v_mfma_f32_16x16x32_bf16 v[252:255], v[96:99], a[248:251], v[252:255]\n"
"ds_read_b64_tr_b16 v[94:95], v16 offset:50720\n"
"s_nop 0\n"
"s_waitcnt lgkmcnt(6)\n"
"v_mfma_f32_16x16x32_bf16 v[112:115], v[80:83], a[0:3], v[112:115]\n"
"s_waitcnt vmcnt(48)\n"
"s_waitcnt lgkmcnt(4)\n"
"v_mfma_f32_16x16x32_bf16 v[112:115], v[84:87], a[4:7], v[112:115]\n"
"s_add_u32 s98, s85, 0x7000\n"
"ds_read_b64_tr_b16 v[96:97], v16 offset:36928\n"
"v_mfma_f32_16x16x32_bf16 v[116:119], v[84:87], a[12:15], v[116:119]\n"
"buffer_load_dwordx4 a[128:131], v20, s[64:67], s98 offen sc1\n"
"v_mfma_f32_16x16x32_bf16 v[116:119], v[80:83], a[8:11], v[116:119]\n"
"ds_read_b64_tr_b16 v[98:99], v16 offset:41536\n"
"v_mfma_f32_16x16x32_bf16 v[120:123], v[80:83], a[16:19], v[120:123]\n"
"v_mfma_f32_16x16x32_bf16 v[120:123], v[84:87], a[20:23], v[120:123]\n"
"ds_read_b64_tr_b16 v[100:101], v16 offset:46144\n"
"v_mfma_f32_16x16x32_bf16 v[124:127], v[84:87], a[28:31], v[124:127]\n"
"buffer_load_dwordx4 a[132:135], v20, s[64:67], s98 offen offset:1024 sc1\n"
"v_mfma_f32_16x16x32_bf16 v[124:127], v[80:83], a[24:27], v[124:127]\n"
"ds_read_b64_tr_b16 v[102:103], v16 offset:50752\n"
"s_waitcnt lgkmcnt(6)\n"
"v_mfma_f32_16x16x32_bf16 v[128:131], v[88:91], a[0:3], v[128:131]\n"
"s_waitcnt lgkmcnt(4)\n"
"v_mfma_f32_16x16x32_bf16 v[128:131], v[92:95], a[4:7], v[128:131]\n"
"ds_read_b64_tr_b16 v[80:81], v16 offset:36960\n"
"v_mfma_f32_16x16x32_bf16 v[132:135], v[92:95], a[12:15], v[132:135]\n"
"v_mfma_f32_16x16x32_bf16 v[132:135], v[88:91], a[8:11], v[132:135]\n"
"s_add_u32 s98, s98, s86\n"
"ds_read_b64_tr_b16 v[82:83], v16 offset:41568\n"
"v_mfma_f32_16x16x32_bf16 v[136:139], v[88:91], a[16:19], v[136:139]\n"
"buffer_load_dwordx4 a[136:139], v20, s[64:67], s98 offen sc1\n"
"v_mfma_f32_16x16x32_bf16 v[136:139], v[92:95], a[20:23], v[136:139]\n"
"ds_read_b64_tr_b16 v[84:85], v16 offset:46176\n"
"v_mfma_f32_16x16x32_bf16 v[140:143], v[92:95], a[28:31], v[140:143]\n"
"v_mfma_f32_16x16x32_bf16 v[140:143], v[88:91], a[24:27], v[140:143]\n"
"ds_read_b64_tr_b16 v[86:87], v16 offset:50784\n"
"s_waitcnt lgkmcnt(6)\n"
"v_mfma_f32_16x16x32_bf16 v[144:147], v[96:99], a[0:3], v[144:147]\n"
"buffer_load_dwordx4 a[140:143], v20, s[64:67], s98 offen offset:1024 sc1\n"
"s_waitcnt lgkmcnt(4)\n"
"v_mfma_f32_16x16x32_bf16 v[144:147], v[100:103], a[4:7], v[144:147]\n"
"ds_read_b64_tr_b16 v[88:89], v16 offset:36992\n"
"v_mfma_f32_16x16x32_bf16 v[148:151], v[100:103], a[12:15], v[148:151]\n"
"v_mfma_f32_16x16x32_bf16 v[148:151], v[96:99], a[8:11], v[148:151]\n"
"ds_read_b64_tr_b16 v[90:91], v16 offset:41600\n"
"v_mfma_f32_16x16x32_bf16 v[152:155], v[96:99], a[16:19], v[152:155]\n"
"v_mfma_f32_16x16x32_bf16 v[152:155], v[100:103], a[20:23], v[152:155]\n"
"s_add_u32 s98, s98, s86\n"
"ds_read_b64_tr_b16 v[92:93], v16 offset:46208\n"
"v_mfma_f32_16x16x32_bf16 v[156:159], v[100:103], a[28:31], v[156:159]\n"
"buffer_load_dwordx4 a[144:147], v20, s[64:67], s98 offen sc1\n"
"v_mfma_f32_16x16x32_bf16 v[156:159], v[96:99], a[24:27], v[156:159]\n"
"ds_read_b64_tr_b16 v[94:95], v16 offset:50816\n"
"s_waitcnt lgkmcnt(6)\n"
"v_mfma_f32_16x16x32_bf16 v[160:163], v[80:83], a[0:3], v[160:163]\n"
"s_waitcnt lgkmcnt(4)\n"
"v_mfma_f32_16x16x32_bf16 v[160:163], v[84:87], a[4:7], v[160:163]\n"
"ds_read_b64_tr_b16 v[96:97], v16 offset:37024\n"
"v_mfma_f32_16x16x32_bf16 v[164:167], v[84:87], a[12:15], v[164:167]\n"
"v_mfma_f32_16x16x32_bf16 v[164:167], v[80:83], a[8:11], v[164:167]\n"
"ds_read_b64_tr_b16 v[98:99], v16 offset:41632\n"
"v_mfma_f32_16x16x32_bf16 v[168:171], v[80:83], a[16:19], v[168:171]\n"
"buffer_load_dwordx4 a[148:151], v20, s[64:67], s98 offen offset:1024 sc1\n"
"v_mfma_f32_16x16x32_bf16 v[168:171], v[84:87], a[20:23], v[168:171]\n"
"ds_read_b64_tr_b16 v[100:101], v16 offset:46240\n"
"v_mfma_f32_16x16x32_bf16 v[172:175], v[84:87], a[28:31], v[172:175]\n"
"v_mfma_f32_16x16x32_bf16 v[172:175], v[80:83], a[24:27], v[172:175]\n"
"s_add_u32 s98, s98, s86\n"
"ds_read_b64_tr_b16 v[102:103], v16 offset:50848\n"
"s_waitcnt lgkmcnt(6)\n"
"v_mfma_f32_16x16x32_bf16 v[176:179], v[88:91], a[0:3], v[176:179]\n"
"buffer_load_dwordx4 a[152:155], v20, s[64:67], s98 offen sc1\n"
"s_waitcnt lgkmcnt(4)\n"
"v_mfma_f32_16x16x32_bf16 v[176:179], v[92:95], a[4:7], v[176:179]\n"
"ds_read_b64_tr_b16 v[80:81], v16 offset:37056\n"
"v_mfma_f32_16x16x32_bf16 v[180:183], v[92:95], a[12:15], v[180:183]\n"
"v_mfma_f32_16x16x32_bf16 v[180:183], v[88:91], a[8:11], v[180:183]\n"
"ds_read_b64_tr_b16 v[82:83], v16 offset:41664\n"
"v_mfma_f32_16x16x32_bf16 v[184:187], v[88:91], a[16:19], v[184:187]\n"
"v_mfma_f32_16x16x32_bf16 v[184:187], v[92:95], a[20:23], v[184:187]\n"
"ds_read_b64_tr_b16 v[84:85], v16 offset:46272\n"
"v_mfma_f32_16x16x32_bf16 v[188:191], v[92:95], a[28:31], v[188:191]\n"
"buffer_load_dwordx4 a[156:159], v20, s[64:67], s98 offen offset:1024 sc1\n"
"v_mfma_f32_16x16x32_bf16 v[188:191], v[88:91], a[24:27], v[188:191]\n"
"ds_read_b64_tr_b16 v[86:87], v16 offset:50880\n"
"s_waitcnt lgkmcnt(6)\n"
"v_mfma_f32_16x16x32_bf16 v[192:195], v[96:99], a[0:3], v[192:195]\n"
"s_waitcnt lgkmcnt(4)\n"
"v_mfma_f32_16x16x32_bf16 v[192:195], v[100:103], a[4:7], v[192:195]\n"
"s_add_u32 s77, s84, 0xfc000\n"
"s_add_u32 m0, s78, 0x1b000\n"
"ds_read_b64_tr_b16 v[88:89], v16 offset:37088\n"
"v_mfma_f32_16x16x32_bf16 v[196:199], v[100:103], a[12:15], v[196:199]\n"
"buffer_load_dwordx4 v6, s[60:63], s77 offen lds\n"
"v_mfma_f32_16x16x32_bf16 v[196:199], v[96:99], a[8:11], v[196:199]\n"
"ds_read_b64_tr_b16 v[90:91], v16 offset:41696\n"
"v_mfma_f32_16x16x32_bf16 v[200:203], v[96:99], a[16:19], v[200:203]\n"
"v_mfma_f32_16x16x32_bf16 v[200:203], v[100:103], a[20:23], v[200:203]\n"
"ds_read_b64_tr_b16 v[92:93], v16 offset:46304\n"
"v_mfma_f32_16x16x32_bf16 v[204:207], v[100:103], a[28:31], v[204:207]\n"
"v_mfma_f32_16x16x32_bf16 v[204:207], v[96:99], a[24:27], v[204:207]\n"
"s_add_u32 m0, s78, 0x1b400\n"
"ds_read_b64_tr_b16 v[94:95], v16 offset:50912\n"
"s_waitcnt lgkmcnt(6)\n"
"v_mfma_f32_16x16x32_bf16 v[208:211], v[80:83], a[0:3], v[208:211]\n"
"buffer_load_dwordx4 v7, s[60:63], s77 offen lds\n"
"s_waitcnt lgkmcnt(4)\n"
"v_mfma_f32_16x16x32_bf16 v[208:211], v[84:87], a[4:7], v[208:211]\n"
"ds_read_b64_tr_b16 v[96:97], v16 offset:37120\n"
"v_mfma_f32_16x16x32_bf16 v[212:215], v[84:87], a[12:15], v[212:215]\n"
"v_mfma_f32_16x16x32_bf16 v[212:215], v[80:83], a[8:11], v[212:215]\n"
"ds_read_b64_tr_b16 v[98:99], v16 offset:41728\n"
"v_mfma_f32_16x16x32_bf16 v[216:219], v[80:83], a[16:19], v[216:219]\n"
"v_mfma_f32_16x16x32_bf16 v[216:219], v[84:87], a[20:23], v[216:219]\n"
"s_add_u32 m0, s78, 0x1b800\n"
"ds_read_b64_tr_b16 v[100:101], v16 offset:46336\n"
"v_mfma_f32_16x16x32_bf16 v[220:223], v[84:87], a[28:31], v[220:223]\n"
"buffer_load_dwordx4 v8, s[60:63], s77 offen lds\n"
"v_mfma_f32_16x16x32_bf16 v[220:223], v[80:83], a[24:27], v[220:223]\n"
"s_waitcnt vmcnt(35)\n"
"s_barrier\n"
"ds_read_b64_tr_b16 v[102:103], v16 offset:50944\n"
"s_waitcnt lgkmcnt(6)\n"
"v_mfma_f32_16x16x32_bf16 v[224:227], v[88:91], a[0:3], v[224:227]\n"
"s_waitcnt lgkmcnt(4)\n"
"v_mfma_f32_16x16x32_bf16 v[224:227], v[92:95], a[4:7], v[224:227]\n"
"s_add_u32 m0, s78, 0x1bc00\n"
"ds_read_b64_tr_b16 v[80:81], v17\n"
"v_mfma_f32_16x16x32_bf16 v[228:231], v[92:95], a[12:15], v[228:231]\n"
"buffer_load_dwordx4 v9, s[60:63], s77 offen lds\n"
"v_mfma_f32_16x16x32_bf16 v[228:231], v[88:91], a[8:11], v[228:231]\n"
"ds_read_b64_tr_b16 v[82:83], v17 offset:4608\n"
"v_mfma_f32_16x16x32_bf16 v[232:235], v[88:91], a[16:19], v[232:235]\n"
"v_mfma_f32_16x16x32_bf16 v[232:235], v[92:95], a[20:23], v[232:235]\n"
"ds_read_b64_tr_b16 v[84:85], v17 offset:9216\n"
"v_mfma_f32_16x16x32_bf16 v[236:239], v[92:95], a[28:31], v[236:239]\n"
"v_mfma_f32_16x16x32_bf16 v[236:239], v[88:91], a[24:27], v[236:239]\n"
"s_add_u32 m0, s78, 0x1c000\n"
"ds_read_b64_tr_b16 v[86:87], v17 offset:13824\n"
"s_waitcnt lgkmcnt(6)\n"
"v_mfma_f32_16x16x32_bf16 v[240:243], v[96:99], a[0:3], v[240:243]\n"
"s_bitcmp1_b32 s72, 0\n"
"s_cbranch_scc1 NOP5_16_%=\n"
"buffer_load_dwordx4 v10, s[60:63], s77 offen lds\n"
"NOP5_16_%=:\n"
"s_waitcnt lgkmcnt(4)\n"
"v_mfma_f32_16x16x32_bf16 v[240:243], v[100:103], a[4:7], v[240:243]\n"
"ds_read_b64_tr_b16 v[88:89], v17 offset:32\n"
"v_mfma_f32_16x16x32_bf16 v[244:247], v[100:103], a[12:15], v[244:247]\n"
"v_mfma_f32_16x16x32_bf16 v[244:247], v[96:99], a[8:11], v[244:247]\n"
"ds_read_b64_tr_b16 v[90:91], v17 offset:4640\n"
"v_mfma_f32_16x16x32_bf16 v[248:251], v[96:99], a[16:19], v[248:251]\n"
"v_mfma_f32_16x16x32_bf16 v[248:251], v[100:103], a[20:23], v[248:251]\n"
"ds_read_b64_tr_b16 v[92:93], v17 offset:9248\n"
"v_mfma_f32_16x16x32_bf16 v[252:255], v[100:103], a[28:31], v[252:255]\n"
"v_mfma_f32_16x16x32_bf16 v[252:255], v[96:99], a[24:27], v[252:255]\n"
"ds_read_b64_tr_b16 v[94:95], v17 offset:13856\n"
"s_nop 0\n"
"s_waitcnt lgkmcnt(6)\n"
"v_mfma_f32_16x16x32_bf16 v[112:115], v[80:83], a[32:35], v[112:115]\n"
"s_waitcnt vmcnt(48)\n"
"s_waitcnt lgkmcnt(4)\n"
"v_mfma_f32_16x16x32_bf16 v[112:115], v[84:87], a[36:39], v[112:115]\n"
"s_add_u32 s98, s85, 0x7800\n"
"ds_read_b64_tr_b16 v[96:97], v17 offset:64\n"
"v_mfma_f32_16x16x32_bf16 v[116:119], v[84:87], a[44:47], v[116:119]\n"
"buffer_load_dwordx4 a[160:163], v20, s[64:67], s98 offen sc1\n"
"v_mfma_f32_16x16x32_bf16 v[116:119], v[80:83], a[40:43], v[116:119]\n"
"ds_read_b64_tr_b16 v[98:99], v17 offset:4672\n"
"v_mfma_f32_16x16x32_bf16 v[120:123], v[80:83], a[48:51], v[120:123]\n"
"v_mfma_f32_16x16x32_bf16 v[120:123], v[84:87], a[52:55], v[120:123]\n"
"ds_read_b64_tr_b16 v[100:101], v17 offset:9280\n"
"v_mfma_f32_16x16x32_bf16 v[124:127], v[84:87], a[60:63], v[124:127]\n"
"buffer_load_dwordx4 a[164:167], v20, s[64:67], s98 offen offset:1024 sc1\n"
"v_mfma_f32_16x16x32_bf16 v[124:127], v[80:83], a[56:59], v[124:127]\n"
"ds_read_b64_tr_b16 v[102:103], v17 offset:13888\n"
"s_waitcnt lgkmcnt(6)\n"
"v_mfma_f32_16x16x32_bf16 v[128:131], v[88:91], a[32:35], v[128:131]\n"
"s_waitcnt lgkmcnt(4)\n"
"v_mfma_f32_16x16x32_bf16 v[128:131], v[92:95], a[36:39], v[128:131]\n"
"ds_read_b64_tr_b16 v[80:81], v17 offset:96\n"
"v_mfma_f32_16x16x32_bf16 v[132:135], v[92:95], a[44:47], v[132:135]\n"
"v_mfma_f32_16x16x32_bf16 v[132:135], v[88:91], a[40:43], v[132:135]\n"
"s_add_u32 s98, s98, s86\n"
"ds_read_b64_tr_b16 v[82:83], v17 offset:4704\n"
"v_mfma_f32_16x16x32_bf16 v[136:139], v[88:91], a[48:51], v[136:139]\n"
"buffer_load_dwordx4 a[168:171], v20, s[64:67], s98 offen sc1\n"
"v_mfma_f32_16x16x32_bf16 v[136:139], v[92:95], a[52:55], v[136:139]\n"
"ds_read_b64_tr_b16 v[84:85], v17 offset:9312\n"
"v_mfma_f32_16x16x32_bf16 v[140:143], v[92:95], a[60:63], v[140:143]\n"
"v_mfma_f32_16x16x32_bf16 v[140:143], v[88:91], a[56:59], v[140:143]\n"
"ds_read_b64_tr_b16 v[86:87], v17 offset:13920\n"
"s_waitcnt lgkmcnt(6)\n"
"v_mfma_f32_16x16x32_bf16 v[144:147], v[96:99], a[32:35], v[144:147]\n"
"buffer_load_dwordx4 a[172:175], v20, s[64:67], s98 offen offset:1024 sc1\n"
"s_waitcnt lgkmcnt(4)\n"
"v_mfma_f32_16x16x32_bf16 v[144:147], v[100:103], a[36:39], v[144:147]\n"
"ds_read_b64_tr_b16 v[88:89], v17 offset:128\n"
"v_mfma_f32_16x16x32_bf16 v[148:151], v[100:103], a[44:47], v[148:151]\n"
"v_mfma_f32_16x16x32_bf16 v[148:151], v[96:99], a[40:43], v[148:151]\n"
"ds_read_b64_tr_b16 v[90:91], v17 offset:4736\n"
"v_mfma_f32_16x16x32_bf16 v[152:155], v[96:99], a[48:51], v[152:155]\n"
"v_mfma_f32_16x16x32_bf16 v[152:155], v[100:103], a[52:55], v[152:155]\n"
"s_add_u32 s98, s98, s86\n"
"ds_read_b64_tr_b16 v[92:93], v17 offset:9344\n"
"v_mfma_f32_16x16x32_bf16 v[156:159], v[100:103], a[60:63], v[156:159]\n"
"buffer_load_dwordx4 a[176:179], v20, s[64:67], s98 offen sc1\n"
"v_mfma_f32_16x16x32_bf16 v[156:159], v[96:99], a[56:59], v[156:159]\n"
"ds_read_b64_tr_b16 v[94:95], v17 offset:13952\n"
"s_waitcnt lgkmcnt(6)\n"
"v_mfma_f32_16x16x32_bf16 v[160:163], v[80:83], a[32:35], v[160:163]\n"
"s_waitcnt lgkmcnt(4)\n"
"v_mfma_f32_16x16x32_bf16 v[160:163], v[84:87], a[36:39], v[160:163]\n"
"ds_read_b64_tr_b16 v[96:97], v17 offset:160\n"
"v_mfma_f32_16x16x32_bf16 v[164:167], v[84:87], a[44:47], v[164:167]\n"
"v_mfma_f32_16x16x32_bf16 v[164:167], v[80:83], a[40:43], v[164:167]\n"
"ds_read_b64_tr_b16 v[98:99], v17 offset:4768\n"
"v_mfma_f32_16x16x32_bf16 v[168:171], v[80:83], a[48:51], v[168:171]\n"
"buffer_load_dwordx4 a[180:183], v20, s[64:67], s98 offen offset:1024 sc1\n"
"v_mfma_f32_16x16x32_bf16 v[168:171], v[84:87], a[52:55], v[168:171]\n"
"ds_read_b64_tr_b16 v[100:101], v17 offset:9376\n"
"v_mfma_f32_16x16x32_bf16 v[172:175], v[84:87], a[60:63], v[172:175]\n"
"v_mfma_f32_16x16x32_bf16 v[172:175], v[80:83], a[56:59], v[172:175]\n"
"s_add_u32 s98, s98, s86\n"
"ds_read_b64_tr_b16 v[102:103], v17 offset:13984\n"
"s_waitcnt lgkmcnt(6)\n"
"v_mfma_f32_16x16x32_bf16 v[176:179], v[88:91], a[32:35], v[176:179]\n"
"buffer_load_dwordx4 a[184:187], v20, s[64:67], s98 offen sc1\n"
"s_waitcnt lgkmcnt(4)\n"
"v_mfma_f32_16x16x32_bf16 v[176:179], v[92:95], a[36:39], v[176:179]\n"
"ds_read_b64_tr_b16 v[80:81], v17 offset:192\n"
"v_mfma_f32_16x16x32_bf16 v[180:183], v[92:95], a[44:47], v[180:183]\n"
"v_mfma_f32_16x16x32_bf16 v[180:183], v[88:91], a[40:43], v[180:183]\n"
"ds_read_b64_tr_b16 v[82:83], v17 offset:4800\n"
"v_mfma_f32_16x16x32_bf16 v[184:187], v[88:91], a[48:51], v[184:187]\n"
"v_mfma_f32_16x16x32_bf16 v[184:187], v[92:95], a[52:55], v[184:187]\n"
"ds_read_b64_tr_b16 v[84:85], v17 offset:9408\n"
"v_mfma_f32_16x16x32_bf16 v[188:191], v[92:95], a[60:63], v[188:191]\n"
"buffer_load_dwordx4 a[188:191], v20, s[64:67], s98 offen offset:1024 sc1\n"
"v_mfma_f32_16x16x32_bf16 v[188:191], v[88:91], a[56:59], v[188:191]\n"
"ds_read_b64_tr_b16 v[86:87], v17 offset:14016\n"
"s_waitcnt lgkmcnt(6)\n"
"v_mfma_f32_16x16x32_bf16 v[192:195], v[96:99], a[32:35], v[192:195]\n"
"s_waitcnt lgkmcnt(4)\n"
"v_mfma_f32_16x16x32_bf16 v[192:195], v[100:103], a[36:39], v[192:195]\n"
"s_add_u32 s77, s84, 0x10e000\n"
"s_add_u32 m0, s78, 0x1f800\n"
"ds_read_b64_tr_b16 v[88:89], v17 offset:224\n"
"v_mfma_f32_16x16x32_bf16 v[196:199], v[100:103], a[44:47], v[196:199]\n"
"buffer_load_dwordx4 v6, s[60:63], s77 offen lds\n"
"v_mfma_f32_16x16x32_bf16 v[196:199], v[96:99], a[40:43], v[196:199]\n"
"ds_read_b64_tr_b16 v[90:91], v17 offset:4832\n"
"v_mfma_f32_16x16x32_bf16 v[200:203], v[96:99], a[48:51], v[200:203]\n"
"v_mfma_f32_16x16x32_bf16 v[200:203], v[100:103], a[52:55], v[200:203]\n"
"ds_read_b64_tr_b16 v[92:93], v17 offset:9440\n"
"v_mfma_f32_16x16x32_bf16 v[204:207], v[100:103], a[60:63], v[204:207]\n"
"v_mfma_f32_16x16x32_bf16 v[204:207], v[96:99], a[56:59], v[204:207]\n"
"s_add_u32 m0, s78, 0x1fc00\n"
"ds_read_b64_tr_b16 v[94:95], v17 offset:14048\n"
"s_waitcnt lgkmcnt(6)\n"
"v_mfma_f32_16x16x32_bf16 v[208:211], v[80:83], a[32:35], v[208:211]\n"
"buffer_load_dwordx4 v7, s[60:63], s77 offen lds\n"
"s_waitcnt lgkmcnt(4)\n"
"v_mfma_f32_16x16x32_bf16 v[208:211], v[84:87], a[36:39], v[208:211]\n"
"ds_read_b64_tr_b16 v[96:97], v17 offset:256\n"
"v_mfma_f32_16x16x32_bf16 v[212:215], v[84:87], a[44:47], v[212:215]\n"
"v_mfma_f32_16x16x32_bf16 v[212:215], v[80:83], a[40:43], v[212:215]\n"
"ds_read_b64_tr_b16 v[98:99], v17 offset:4864\n"
"v_mfma_f32_16x16x32_bf16 v[216:219], v[80:83], a[48:51], v[216:219]\n"
"v_mfma_f32_16x16x32_bf16 v[216:219], v[84:87], a[52:55], v[216:219]\n"
"s_add_u32 m0, s78, 0x20000\n"
"ds_read_b64_tr_b16 v[100:101], v17 offset:9472\n"
"v_mfma_f32_16x16x32_bf16 v[220:223], v[84:87], a[60:63], v[220:223]\n"
"buffer_load_dwordx4 v8, s[60:63], s77 offen lds\n"
"v_mfma_f32_16x16x32_bf16 v[220:223], v[80:83], a[56:59], v[220:223]\n"
"s_waitcnt vmcnt(35)\n"
"s_barrier\n"
"ds_read_b64_tr_b16 v[102:103], v17 offset:14080\n"
"s_waitcnt lgkmcnt(6)\n"
"v_mfma_f32_16x16x32_bf16 v[224:227], v[88:91], a[32:35], v[224:227]\n"
"s_waitcnt lgkmcnt(4)\n"
"v_mfma_f32_16x16x32_bf16 v[224:227], v[92:95], a[36:39], v[224:227]\n"
"s_add_u32 m0, s78, 0x20400\n"
"ds_read_b64_tr_b16 v[80:81], v17 offset:18432\n"
"v_mfma_f32_16x16x32_bf16 v[228:231], v[92:95], a[44:47], v[228:231]\n"
"buffer_load_dwordx4 v9, s[60:63], s77 offen lds\n"
"v_mfma_f32_16x16x32_bf16 v[228:231], v[88:91], a[40:43], v[228:231]\n"
"ds_read_b64_tr_b16 v[82:83], v17 offset:23040\n"
"v_mfma_f32_16x16x32_bf16 v[232:235], v[88:91], a[48:51], v[232:235]\n"
"v_mfma_f32_16x16x32_bf16 v[232:235], v[92:95], a[52:55], v[232:235]\n"
"ds_read_b64_tr_b16 v[84:85], v17 offset:27648\n"
"v_mfma_f32_16x16x32_bf16 v[236:239], v[92:95], a[60:63], v[236:239]\n"
"v_mfma_f32_16x16x32_bf16 v[236:239], v[88:91], a[56:59], v[236:239]\n"
"s_add_u32 m0, s78, 0x20800\n"
"ds_read_b64_tr_b16 v[86:87], v17 offset:32256\n"
"s_waitcnt lgkmcnt(6)\n"
"v_mfma_f32_16x16x32_bf16 v[240:243], v[96:99], a[32:35], v[240:243]\n"
"s_bitcmp1_b32 s72, 0\n"
"s_cbranch_scc1 NOP5_17_%=\n"
"buffer_load_dwordx4 v10, s[60:63], s77 offen lds\n"
"NOP5_17_%=:\n"
"s_waitcnt lgkmcnt(4)\n"
"v_mfma_f32_16x16x32_bf16 v[240:243], v[100:103], a[36:39], v[240:243]\n"
"ds_read_b64_tr_b16 v[88:89], v17 offset:18464\n"
"v_mfma_f32_16x16x32_bf16 v[244:247], v[100:103], a[44:47], v[244:247]\n"
"v_mfma_f32_16x16x32_bf16 v[244:247], v[96:99], a[40:43], v[244:247]\n"
"ds_read_b64_tr_b16 v[90:91], v17 offset:23072\n"
"v_mfma_f32_16x16x32_bf16 v[248:251], v[96:99], a[48:51], v[248:251]\n"
"v_mfma_f32_16x16x32_bf16 v[248:251], v[100:103], a[52:55], v[248:251]\n"
"ds_read_b64_tr_b16 v[92:93], v17 offset:27680\n"
"v_mfma_f32_16x16x32_bf16 v[252:255], v[100:103], a[60:63], v[252:255]\n"
"v_mfma_f32_16x16x32_bf16 v[252:255], v[96:99], a[56:59], v[252:255]\n"
"ds_read_b64_tr_b16 v[94:95], v17 offset:32288\n"
"s_nop 0\n"
"s_waitcnt lgkmcnt(6)\n"
"v_mfma_f32_16x16x32_bf16 v[112:115], v[80:83], a[64:67], v[112:115]\n"
"s_waitcnt vmcnt(48)\n"
"s_waitcnt lgkmcnt(4)\n"
"v_mfma_f32_16x16x32_bf16 v[112:115], v[84:87], a[68:71], v[112:115]\n"
"s_add_u32 s98, s85, 0x8000\n"
"ds_read_b64_tr_b16 v[96:97], v17 offset:18496\n"
"v_mfma_f32_16x16x32_bf16 v[116:119], v[84:87], a[76:79], v[116:119]\n"
"buffer_load_dwordx4 a[192:195], v20, s[64:67], s98 offen sc1\n"
"v_mfma_f32_16x16x32_bf16 v[116:119], v[80:83], a[72:75], v[116:119]\n"
"ds_read_b64_tr_b16 v[98:99], v17 offset:23104\n"
"v_mfma_f32_16x16x32_bf16 v[120:123], v[80:83], a[80:83], v[120:123]\n"
"v_mfma_f32_16x16x32_bf16 v[120:123], v[84:87], a[84:87], v[120:123]\n"
"ds_read_b64_tr_b16 v[100:101], v17 offset:27712\n"
"v_mfma_f32_16x16x32_bf16 v[124:127], v[84:87], a[92:95], v[124:127]\n"
"buffer_load_dwordx4 a[196:199], v20, s[64:67], s98 offen offset:1024 sc1\n"
"v_mfma_f32_16x16x32_bf16 v[124:127], v[80:83], a[88:91], v[124:127]\n"
"ds_read_b64_tr_b16 v[102:103], v17 offset:32320\n"
"s_waitcnt lgkmcnt(6)\n"
"v_mfma_f32_16x16x32_bf16 v[128:131], v[88:91], a[64:67], v[128:131]\n"
"s_waitcnt lgkmcnt(4)\n"
"v_mfma_f32_16x16x32_bf16 v[128:131], v[92:95], a[68:71], v[128:131]\n"
"ds_read_b64_tr_b16 v[80:81], v17 offset:18528\n"
"v_mfma_f32_16x16x32_bf16 v[132:135], v[92:95], a[76:79], v[132:135]\n"
"v_mfma_f32_16x16x32_bf16 v[132:135], v[88:91], a[72:75], v[132:135]\n"
"s_add_u32 s98, s98, s86\n"
"ds_read_b64_tr_b16 v[82:83], v17 offset:23136\n"
"v_mfma_f32_16x16x32_bf16 v[136:139], v[88:91], a[80:83], v[136:139]\n"
"buffer_load_dwordx4 a[200:203], v20, s[64:67], s98 offen sc1\n"
"v_mfma_f32_16x16x32_bf16 v[136:139], v[92:95], a[84:87], v[136:139]\n"
"ds_read_b64_tr_b16 v[84:85], v17 offset:27744\n"
"v_mfma_f32_16x16x32_bf16 v[140:143], v[92:95], a[92:95], v[140:143]\n"
"v_mfma_f32_16x16x32_bf16 v[140:143], v[88:91], a[88:91], v[140:143]\n"
"ds_read_b64_tr_b16 v[86:87], v17 offset:32352\n"
"s_waitcnt lgkmcnt(6)\n"
"v_mfma_f32_16x16x32_bf16 v[144:147], v[96:99], a[64:67], v[144:147]\n"
"buffer_load_dwordx4 a[204:207], v20, s[64:67], s98 offen offset:1024 sc1\n"
"s_waitcnt lgkmcnt(4)\n"
"v_mfma_f32_16x16x32_bf16 v[144:147], v[100:103], a[68:71], v[144:147]\n"
"ds_read_b64_tr_b16 v[88:89], v17 offset:18560\n"
"v_mfma_f32_16x16x32_bf16 v[148:151], v[100:103], a[76:79], v[148:151]\n"
"v_mfma_f32_16x16x32_bf16 v[148:151], v[96:99], a[72:75], v[148:151]\n"
"ds_read_b64_tr_b16 v[90:91], v17 offset:23168\n"
"v_mfma_f32_16x16x32_bf16 v[152:155], v[96:99], a[80:83], v[152:155]\n"
"v_mfma_f32_16x16x32_bf16 v[152:155], v[100:103], a[84:87], v[152:155]\n"
"s_add_u32 s98, s98, s86\n"
"ds_read_b64_tr_b16 v[92:93], v17 offset:27776\n"
"v_mfma_f32_16x16x32_bf16 v[156:159], v[100:103], a[92:95], v[156:159]\n"
"buffer_load_dwordx4 a[208:211], v20, s[64:67], s98 offen sc1\n"
"v_mfma_f32_16x16x32_bf16 v[156:159], v[96:99], a[88:91], v[156:159]\n"
"ds_read_b64_tr_b16 v[94:95], v17 offset:32384\n"
"s_waitcnt lgkmcnt(6)\n"
"v_mfma_f32_16x16x32_bf16 v[160:163], v[80:83], a[64:67], v[160:163]\n"
"s_waitcnt lgkmcnt(4)\n"
"v_mfma_f32_16x16x32_bf16 v[160:163], v[84:87], a[68:71], v[160:163]\n"
"ds_read_b64_tr_b16 v[96:97], v17 offset:18592\n"
"v_mfma_f32_16x16x32_bf16 v[164:167], v[84:87], a[76:79], v[164:167]\n"
"v_mfma_f32_16x16x32_bf16 v[164:167], v[80:83], a[72:75], v[164:167]\n"
"ds_read_b64_tr_b16 v[98:99], v17 offset:23200\n"
"v_mfma_f32_16x16x32_bf16 v[168:171], v[80:83], a[80:83], v[168:171]\n"
"buffer_load_dwordx4 a[212:215], v20, s[64:67], s98 offen offset:1024 sc1\n"
"v_mfma_f32_16x16x32_bf16 v[168:171], v[84:87], a[84:87], v[168:171]\n"
"ds_read_b64_tr_b16 v[100:101], v17 offset:27808\n"
"v_mfma_f32_16x16x32_bf16 v[172:175], v[84:87], a[92:95], v[172:175]\n"
"v_mfma_f32_16x16x32_bf16 v[172:175], v[80:83], a[88:91], v[172:175]\n"
"s_add_u32 s98, s98, s86\n"
"ds_read_b64_tr_b16 v[102:103], v17 offset:32416\n"
"s_waitcnt lgkmcnt(6)\n"
"v_mfma_f32_16x16x32_bf16 v[176:179], v[88:91], a[64:67], v[176:179]\n"
"buffer_load_dwordx4 a[216:219], v20, s[64:67], s98 offen sc1\n"
"s_waitcnt lgkmcnt(4)\n"
"v_mfma_f32_16x16x32_bf16 v[176:179], v[92:95], a[68:71], v[176:179]\n"
"ds_read_b64_tr_b16 v[80:81], v17 offset:18624\n"
"v_mfma_f32_16x16x32_bf16 v[180:183], v[92:95], a[76:79], v[180:183]\n"
"v_mfma_f32_16x16x32_bf16 v[180:183], v[88:91], a[72:75], v[180:183]\n"
"ds_read_b64_tr_b16 v[82:83], v17 offset:23232\n"
"v_mfma_f32_16x16x32_bf16 v[184:187], v[88:91], a[80:83], v[184:187]\n"
"v_mfma_f32_16x16x32_bf16 v[184:187], v[92:95], a[84:87], v[184:187]\n"
"ds_read_b64_tr_b16 v[84:85], v17 offset:27840\n"
"v_mfma_f32_16x16x32_bf16 v[188:191], v[92:95], a[92:95], v[188:191]\n"
"buffer_load_dwordx4 a[220:223], v20, s[64:67], s98 offen offset:1024 sc1\n"
"v_mfma_f32_16x16x32_bf16 v[188:191], v[88:91], a[88:91], v[188:191]\n"
"ds_read_b64_tr_b16 v[86:87], v17 offset:32448\n"
"s_waitcnt lgkmcnt(6)\n"
"v_mfma_f32_16x16x32_bf16 v[192:195], v[96:99], a[64:67], v[192:195]\n"
"s_waitcnt lgkmcnt(4)\n"
"v_mfma_f32_16x16x32_bf16 v[192:195], v[100:103], a[68:71], v[192:195]\n"
"s_add_u32 s77, s84, 0x120000\n"
"s_add_u32 m0, s78, 0\n"
"ds_read_b64_tr_b16 v[88:89], v17 offset:18656\n"
"v_mfma_f32_16x16x32_bf16 v[196:199], v[100:103], a[76:79], v[196:199]\n"
"buffer_load_dwordx4 v6, s[60:63], s77 offen lds\n"
"v_mfma_f32_16x16x32_bf16 v[196:199], v[96:99], a[72:75], v[196:199]\n"
"ds_read_b64_tr_b16 v[90:91], v17 offset:23264\n"
"v_mfma_f32_16x16x32_bf16 v[200:203], v[96:99], a[80:83], v[200:203]\n"
"v_mfma_f32_16x16x32_bf16 v[200:203], v[100:103], a[84:87], v[200:203]\n"
"ds_read_b64_tr_b16 v[92:93], v17 offset:27872\n"
"v_mfma_f32_16x16x32_bf16 v[204:207], v[100:103], a[92:95], v[204:207]\n"
"v_mfma_f32_16x16x32_bf16 v[204:207], v[96:99], a[88:91], v[204:207]\n"
"s_add_u32 m0, s78, 0x400\n"
"ds_read_b64_tr_b16 v[94:95], v17 offset:32480\n"
"s_waitcnt lgkmcnt(6)\n"
"v_mfma_f32_16x16x32_bf16 v[208:211], v[80:83], a[64:67], v[208:211]\n"
"buffer_load_dwordx4 v7, s[60:63], s77 offen lds\n"
"s_waitcnt lgkmcnt(4)\n"
"v_mfma_f32_16x16x32_bf16 v[208:211], v[84:87], a[68:71], v[208:211]\n"
"ds_read_b64_tr_b16 v[96:97], v17 offset:18688\n"
"v_mfma_f32_16x16x32_bf16 v[212:215], v[84:87], a[76:79], v[212:215]\n"
"v_mfma_f32_16x16x32_bf16 v[212:215], v[80:83], a[72:75], v[212:215]\n"
"ds_read_b64_tr_b16 v[98:99], v17 offset:23296\n"
"v_mfma_f32_16x16x32_bf16 v[216:219], v[80:83], a[80:83], v[216:219]\n"
"v_mfma_f32_16x16x32_bf16 v[216:219], v[84:87], a[84:87], v[216:219]\n"
"s_add_u32 m0, s78, 0x800\n"
"ds_read_b64_tr_b16 v[100:101], v17 offset:27904\n"
"v_mfma_f32_16x16x32_bf16 v[220:223], v[84:87], a[92:95], v[220:223]\n"
"buffer_load_dwordx4 v8, s[60:63], s77 offen lds\n"
"v_mfma_f32_16x16x32_bf16 v[220:223], v[80:83], a[88:91], v[220:223]\n"
"s_waitcnt vmcnt(35)\n"
"s_barrier\n"
"ds_read_b64_tr_b16 v[102:103], v17 offset:32512\n"
"s_waitcnt lgkmcnt(6)\n"
"v_mfma_f32_16x16x32_bf16 v[224:227], v[88:91], a[64:67], v[224:227]\n"
"s_waitcnt lgkmcnt(4)\n"
"v_mfma_f32_16x16x32_bf16 v[224:227], v[92:95], a[68:71], v[224:227]\n"
"s_add_u32 m0, s78, 0xc00\n"
"ds_read_b64_tr_b16 v[80:81], v17 offset:36864\n"
"v_mfma_f32_16x16x32_bf16 v[228:231], v[92:95], a[76:79], v[228:231]\n"
"buffer_load_dwordx4 v9, s[60:63], s77 offen lds\n"
"v_mfma_f32_16x16x32_bf16 v[228:231], v[88:91], a[72:75], v[228:231]\n"
"ds_read_b64_tr_b16 v[82:83], v17 offset:41472\n"
"v_mfma_f32_16x16x32_bf16 v[232:235], v[88:91], a[80:83], v[232:235]\n"
"v_mfma_f32_16x16x32_bf16 v[232:235], v[92:95], a[84:87], v[232:235]\n"
"ds_read_b64_tr_b16 v[84:85], v17 offset:46080\n"
"v_mfma_f32_16x16x32_bf16 v[236:239], v[92:95], a[92:95], v[236:239]\n"
"v_mfma_f32_16x16x32_bf16 v[236:239], v[88:91], a[88:91], v[236:239]\n"
"s_add_u32 m0, s78, 0x1000\n"
"ds_read_b64_tr_b16 v[86:87], v17 offset:50688\n"
"s_waitcnt lgkmcnt(6)\n"
"v_mfma_f32_16x16x32_bf16 v[240:243], v[96:99], a[64:67], v[240:243]\n"
"s_bitcmp1_b32 s72, 0\n"
"s_cbranch_scc1 NOP5_18_%=\n"
"buffer_load_dwordx4 v10, s[60:63], s77 offen lds\n"
"NOP5_18_%=:\n"
"s_waitcnt lgkmcnt(4)\n"
"v_mfma_f32_16x16x32_bf16 v[240:243], v[100:103], a[68:71], v[240:243]\n"
"ds_read_b64_tr_b16 v[88:89], v17 offset:36896\n"
"v_mfma_f32_16x16x32_bf16 v[244:247], v[100:103], a[76:79], v[244:247]\n"
"v_mfma_f32_16x16x32_bf16 v[244:247], v[96:99], a[72:75], v[244:247]\n"
"ds_read_b64_tr_b16 v[90:91], v17 offset:41504\n"
"v_mfma_f32_16x16x32_bf16 v[248:251], v[96:99], a[80:83], v[248:251]\n"
"v_mfma_f32_16x16x32_bf16 v[248:251], v[100:103], a[84:87], v[248:251]\n"
"ds_read_b64_tr_b16 v[92:93], v17 offset:46112\n"
"v_mfma_f32_16x16x32_bf16 v[252:255], v[100:103], a[92:95], v[252:255]\n"
"v_mfma_f32_16x16x32_bf16 v[252:255], v[96:99], a[88:91], v[252:255]\n"
"ds_read_b64_tr_b16 v[94:95], v17 offset:50720\n"
"s_nop 0\n"
"s_waitcnt lgkmcnt(6)\n"
"v_mfma_f32_16x16x32_bf16 v[112:115], v[80:83], a[96:99], v[112:115]\n"
"s_waitcnt vmcnt(48)\n"
"s_waitcnt lgkmcnt(4)\n"
"v_mfma_f32_16x16x32_bf16 v[112:115], v[84:87], a[100:103], v[112:115]\n"
"s_add_u32 s98, s85, 0x8800\n"
"ds_read_b64_tr_b16 v[96:97], v17 offset:36928\n"
"v_mfma_f32_16x16x32_bf16 v[116:119], v[84:87], a[108:111], v[116:119]\n"
"buffer_load_dwordx4 a[224:227], v20, s[64:67], s98 offen sc1\n"
"v_mfma_f32_16x16x32_bf16 v[116:119], v[80:83], a[104:107], v[116:119]\n"
"ds_read_b64_tr_b16 v[98:99], v17 offset:41536\n"
"v_mfma_f32_16x16x32_bf16 v[120:123], v[80:83], a[112:115], v[120:123]\n"
"v_mfma_f32_16x16x32_bf16 v[120:123], v[84:87], a[116:119], v[120:123]\n"
"ds_read_b64_tr_b16 v[100:101], v17 offset:46144\n"
"v_mfma_f32_16x16x32_bf16 v[124:127], v[84:87], a[124:127], v[124:127]\n"
"buffer_load_dwordx4 a[228:231], v20, s[64:67], s98 offen offset:1024 sc1\n"
"v_mfma_f32_16x16x32_bf16 v[124:127], v[80:83], a[120:123], v[124:127]\n"
"ds_read_b64_tr_b16 v[102:103], v17 offset:50752\n"
"s_waitcnt lgkmcnt(6)\n"
"v_mfma_f32_16x16x32_bf16 v[128:131], v[88:91], a[96:99], v[128:131]\n"
"s_waitcnt lgkmcnt(4)\n"
"v_mfma_f32_16x16x32_bf16 v[128:131], v[92:95], a[100:103], v[128:131]\n"
"ds_read_b64_tr_b16 v[80:81], v17 offset:36960\n"
"v_mfma_f32_16x16x32_bf16 v[132:135], v[92:95], a[108:111], v[132:135]\n"
"v_mfma_f32_16x16x32_bf16 v[132:135], v[88:91], a[104:107], v[132:135]\n"
"s_add_u32 s98, s98, s86\n"
"ds_read_b64_tr_b16 v[82:83], v17 offset:41568\n"
"v_mfma_f32_16x16x32_bf16 v[136:139], v[88:91], a[112:115], v[136:139]\n"
"buffer_load_dwordx4 a[232:235], v20, s[64:67], s98 offen sc1\n"
"v_mfma_f32_16x16x32_bf16 v[136:139], v[92:95], a[116:119], v[136:139]\n"
"ds_read_b64_tr_b16 v[84:85], v17 offset:46176\n"
"v_mfma_f32_16x16x32_bf16 v[140:143], v[92:95], a[124:127], v[140:143]\n"
"v_mfma_f32_16x16x32_bf16 v[140:143], v[88:91], a[120:123], v[140:143]\n"
"ds_read_b64_tr_b16 v[86:87], v17 offset:50784\n"
"s_waitcnt lgkmcnt(6)\n"
"v_mfma_f32_16x16x32_bf16 v[144:147], v[96:99], a[96:99], v[144:147]\n"
"buffer_load_dwordx4 a[236:239], v20, s[64:67], s98 offen offset:1024 sc1\n"
"s_waitcnt lgkmcnt(4)\n"
"v_mfma_f32_16x16x32_bf16 v[144:147], v[100:103], a[100:103], v[144:147]\n"
"ds_read_b64_tr_b16 v[88:89], v17 offset:36992\n"
"v_mfma_f32_16x16x32_bf16 v[148:151], v[100:103], a[108:111], v[148:151]\n"
"v_mfma_f32_16x16x32_bf16 v[148:151], v[96:99], a[104:107], v[148:151]\n"
"ds_read_b64_tr_b16 v[90:91], v17 offset:41600\n"
"v_mfma_f32_16x16x32_bf16 v[152:155], v[96:99], a[112:115], v[152:155]\n"
"v_mfma_f32_16x16x32_bf16 v[152:155], v[100:103], a[116:119], v[152:155]\n"
"s_add_u32 s98, s98, s86\n"
"ds_read_b64_tr_b16 v[92:93], v17 offset:46208\n"
"v_mfma_f32_16x16x32_bf16 v[156:159], v[100:103], a[124:127], v[156:159]\n"
"buffer_load_dwordx4 a[240:243], v20, s[64:67], s98 offen sc1\n"
"v_mfma_f32_16x16x32_bf16 v[156:159], v[96:99], a[120:123], v[156:159]\n"
"ds_read_b64_tr_b16 v[94:95], v17 offset:50816\n"
"s_waitcnt lgkmcnt(6)\n"
"v_mfma_f32_16x16x32_bf16 v[160:163], v[80:83], a[96:99], v[160:163]\n"
"s_waitcnt lgkmcnt(4)\n"
"v_mfma_f32_16x16x32_bf16 v[160:163], v[84:87], a[100:103], v[160:163]\n"
"ds_read_b64_tr_b16 v[96:97], v17 offset:37024\n"
"v_mfma_f32_16x16x32_bf16 v[164:167], v[84:87], a[108:111], v[164:167]\n"
"v_mfma_f32_16x16x32_bf16 v[164:167], v[80:83], a[104:107], v[164:167]\n"
"ds_read_b64_tr_b16 v[98:99], v17 offset:41632\n"
"v_mfma_f32_16x16x32_bf16 v[168:171], v[80:83], a[112:115], v[168:171]\n"
"buffer_load_dwordx4 a[244:247], v20, s[64:67], s98 offen offset:1024 sc1\n"
"v_mfma_f32_16x16x32_bf16 v[168:171], v[84:87], a[116:119], v[168:171]\n"
"ds_read_b64_tr_b16 v[100:101], v17 offset:46240\n"
"v_mfma_f32_16x16x32_bf16 v[172:175], v[84:87], a[124:127], v[172:175]\n"
"v_mfma_f32_16x16x32_bf16 v[172:175], v[80:83], a[120:123], v[172:175]\n"
"s_add_u32 s98, s98, s86\n"
"ds_read_b64_tr_b16 v[102:103], v17 offset:50848\n"
"s_waitcnt lgkmcnt(6)\n"
"v_mfma_f32_16x16x32_bf16 v[176:179], v[88:91], a[96:99], v[176:179]\n"
"buffer_load_dwordx4 a[248:251], v20, s[64:67], s98 offen sc1\n"
"s_waitcnt lgkmcnt(4)\n"
"v_mfma_f32_16x16x32_bf16 v[176:179], v[92:95], a[100:103], v[176:179]\n"
"ds_read_b64_tr_b16 v[80:81], v17 offset:37056\n"
"v_mfma_f32_16x16x32_bf16 v[180:183], v[92:95], a[108:111], v[180:183]\n"
"v_mfma_f32_16x16x32_bf16 v[180:183], v[88:91], a[104:107], v[180:183]\n"
"ds_read_b64_tr_b16 v[82:83], v17 offset:41664\n"
"v_mfma_f32_16x16x32_bf16 v[184:187], v[88:91], a[112:115], v[184:187]\n"
"v_mfma_f32_16x16x32_bf16 v[184:187], v[92:95], a[116:119], v[184:187]\n"
"ds_read_b64_tr_b16 v[84:85], v17 offset:46272\n"
"v_mfma_f32_16x16x32_bf16 v[188:191], v[92:95], a[124:127], v[188:191]\n"
"buffer_load_dwordx4 a[252:255], v20, s[64:67], s98 offen offset:1024 sc1\n"
"v_mfma_f32_16x16x32_bf16 v[188:191], v[88:91], a[120:123], v[188:191]\n"
"ds_read_b64_tr_b16 v[86:87], v17 offset:50880\n"
"s_waitcnt lgkmcnt(6)\n"
"v_mfma_f32_16x16x32_bf16 v[192:195], v[96:99], a[96:99], v[192:195]\n"
"s_waitcnt lgkmcnt(4)\n"
"v_mfma_f32_16x16x32_bf16 v[192:195], v[100:103], a[100:103], v[192:195]\n"
"s_add_u32 s77, s84, 0x132000\n"
"s_add_u32 m0, s78, 0x4800\n"
"ds_read_b64_tr_b16 v[88:89], v17 offset:37088\n"
"v_mfma_f32_16x16x32_bf16 v[196:199], v[100:103], a[108:111], v[196:199]\n"
"buffer_load_dwordx4 v6, s[60:63], s77 offen lds\n"
"v_mfma_f32_16x16x32_bf16 v[196:199], v[96:99], a[104:107], v[196:199]\n"
"ds_read_b64_tr_b16 v[90:91], v17 offset:41696\n"
"v_mfma_f32_16x16x32_bf16 v[200:203], v[96:99], a[112:115], v[200:203]\n"
"v_mfma_f32_16x16x32_bf16 v[200:203], v[100:103], a[116:119], v[200:203]\n"
"ds_read_b64_tr_b16 v[92:93], v17 offset:46304\n"
"v_mfma_f32_16x16x32_bf16 v[204:207], v[100:103], a[124:127], v[204:207]\n"
"v_mfma_f32_16x16x32_bf16 v[204:207], v[96:99], a[120:123], v[204:207]\n"
"s_add_u32 m0, s78, 0x4c00\n"
"ds_read_b64_tr_b16 v[94:95], v17 offset:50912\n"
"s_waitcnt lgkmcnt(6)\n"
"v_mfma_f32_16x16x32_bf16 v[208:211], v[80:83], a[96:99], v[208:211]\n"
"buffer_load_dwordx4 v7, s[60:63], s77 offen lds\n"
"s_waitcnt lgkmcnt(4)\n"
"v_mfma_f32_16x16x32_bf16 v[208:211], v[84:87], a[100:103], v[208:211]\n"
"ds_read_b64_tr_b16 v[96:97], v17 offset:37120\n"
"v_mfma_f32_16x16x32_bf16 v[212:215], v[84:87], a[108:111], v[212:215]\n"
"v_mfma_f32_16x16x32_bf16 v[212:215], v[80:83], a[104:107], v[212:215]\n"
"ds_read_b64_tr_b16 v[98:99], v17 offset:41728\n"
"v_mfma_f32_16x16x32_bf16 v[216:219], v[80:83], a[112:115], v[216:219]\n"
"v_mfma_f32_16x16x32_bf16 v[216:219], v[84:87], a[116:119], v[216:219]\n"
"s_add_u32 m0, s78, 0x5000\n"
"ds_read_b64_tr_b16 v[100:101], v17 offset:46336\n"
"v_mfma_f32_16x16x32_bf16 v[220:223], v[84:87], a[124:127], v[220:223]\n"
"buffer_load_dwordx4 v8, s[60:63], s77 offen lds\n"
"v_mfma_f32_16x16x32_bf16 v[220:223], v[80:83], a[120:123], v[220:223]\n"
"s_waitcnt vmcnt(35)\n"
"s_barrier\n"
"ds_read_b64_tr_b16 v[102:103], v17 offset:50944\n"
"s_waitcnt lgkmcnt(6)\n"
"v_mfma_f32_16x16x32_bf16 v[224:227], v[88:91], a[96:99], v[224:227]\n"
"s_waitcnt lgkmcnt(4)\n"
"v_mfma_f32_16x16x32_bf16 v[224:227], v[92:95], a[100:103], v[224:227]\n"
"s_add_u32 m0, s78, 0x5400\n"
"ds_read_b64_tr_b16 v[80:81], v18\n"
"v_mfma_f32_16x16x32_bf16 v[228:231], v[92:95], a[108:111], v[228:231]\n"
"buffer_load_dwordx4 v9, s[60:63], s77 offen lds\n"
"v_mfma_f32_16x16x32_bf16 v[228:231], v[88:91], a[104:107], v[228:231]\n"
"ds_read_b64_tr_b16 v[82:83], v18 offset:4608\n"
"v_mfma_f32_16x16x32_bf16 v[232:235], v[88:91], a[112:115], v[232:235]\n"
"v_mfma_f32_16x16x32_bf16 v[232:235], v[92:95], a[116:119], v[232:235]\n"
"ds_read_b64_tr_b16 v[84:85], v18 offset:9216\n"
"v_mfma_f32_16x16x32_bf16 v[236:239], v[92:95], a[124:127], v[236:239]\n"
"v_mfma_f32_16x16x32_bf16 v[236:239], v[88:91], a[120:123], v[236:239]\n"
"s_add_u32 m0, s78, 0x5800\n"
"ds_read_b64_tr_b16 v[86:87], v18 offset:13824\n"
"s_waitcnt lgkmcnt(6)\n"
"v_mfma_f32_16x16x32_bf16 v[240:243], v[96:99], a[96:99], v[240:243]\n"
"s_bitcmp1_b32 s72, 0\n"
"s_cbranch_scc1 NOP5_19_%=\n"
"buffer_load_dwordx4 v10, s[60:63], s77 offen lds\n"
"NOP5_19_%=:\n"
"s_waitcnt lgkmcnt(4)\n"
"v_mfma_f32_16x16x32_bf16 v[240:243], v[100:103], a[100:103], v[240:243]\n"
"ds_read_b64_tr_b16 v[88:89], v18 offset:32\n"
"v_mfma_f32_16x16x32_bf16 v[244:247], v[100:103], a[108:111], v[244:247]\n"
"v_mfma_f32_16x16x32_bf16 v[244:247], v[96:99], a[104:107], v[244:247]\n"
"ds_read_b64_tr_b16 v[90:91], v18 offset:4640\n"
"v_mfma_f32_16x16x32_bf16 v[248:251], v[96:99], a[112:115], v[248:251]\n"
"v_mfma_f32_16x16x32_bf16 v[248:251], v[100:103], a[116:119], v[248:251]\n"
"ds_read_b64_tr_b16 v[92:93], v18 offset:9248\n"
"v_mfma_f32_16x16x32_bf16 v[252:255], v[100:103], a[124:127], v[252:255]\n"
"v_mfma_f32_16x16x32_bf16 v[252:255], v[96:99], a[120:123], v[252:255]\n"
"ds_read_b64_tr_b16 v[94:95], v18 offset:13856\n"
"s_nop 0\n"
"s_waitcnt lgkmcnt(6)\n"
"v_mfma_f32_16x16x32_bf16 v[112:115], v[80:83], a[128:131], v[112:115]\n"
"s_waitcnt lgkmcnt(4)\n"
"v_mfma_f32_16x16x32_bf16 v[112:115], v[84:87], a[132:135], v[112:115]\n"
"ds_read_b64_tr_b16 v[96:97], v18 offset:64\n"
"v_mfma_f32_16x16x32_bf16 v[116:119], v[84:87], a[140:143], v[116:119]\n"
"v_mfma_f32_16x16x32_bf16 v[116:119], v[80:83], a[136:139], v[116:119]\n"
"ds_read_b64_tr_b16 v[98:99], v18 offset:4672\n"
"v_mfma_f32_16x16x32_bf16 v[120:123], v[80:83], a[144:147], v[120:123]\n"
"v_mfma_f32_16x16x32_bf16 v[120:123], v[84:87], a[148:151], v[120:123]\n"
"ds_read_b64_tr_b16 v[100:101], v18 offset:9280\n"
"v_mfma_f32_16x16x32_bf16 v[124:127], v[84:87], a[156:159], v[124:127]\n"
"v_mfma_f32_16x16x32_bf16 v[124:127], v[80:83], a[152:155], v[124:127]\n"
"ds_read_b64_tr_b16 v[102:103], v18 offset:13888\n"
"s_waitcnt lgkmcnt(6)\n"
"v_mfma_f32_16x16x32_bf16 v[128:131], v[88:91], a[128:131], v[128:131]\n"
"s_waitcnt lgkmcnt(4)\n"
"v_mfma_f32_16x16x32_bf16 v[128:131], v[92:95], a[132:135], v[128:131]\n"
"ds_read_b64_tr_b16 v[80:81], v18 offset:96\n"
"v_mfma_f32_16x16x32_bf16 v[132:135], v[92:95], a[140:143], v[132:135]\n"
"v_mfma_f32_16x16x32_bf16 v[132:135], v[88:91], a[136:139], v[132:135]\n"
"ds_read_b64_tr_b16 v[82:83], v18 offset:4704\n"
"v_mfma_f32_16x16x32_bf16 v[136:139], v[88:91], a[144:147], v[136:139]\n"
"v_mfma_f32_16x16x32_bf16 v[136:139], v[92:95], a[148:151], v[136:139]\n"
"ds_read_b64_tr_b16 v[84:85], v18 offset:9312\n"
"v_mfma_f32_16x16x32_bf16 v[140:143], v[92:95], a[156:159], v[140:143]\n"
"v_mfma_f32_16x16x32_bf16 v[140:143], v[88:91], a[152:155], v[140:143]\n"
"ds_read_b64_tr_b16 v[86:87], v18 offset:13920\n"
"s_waitcnt lgkmcnt(6)\n"
"v_mfma_f32_16x16x32_bf16 v[144:147], v[96:99], a[128:131], v[144:147]\n"
"s_waitcnt lgkmcnt(4)\n"
"v_mfma_f32_16x16x32_bf16 v[144:147], v[100:103], a[132:135], v[144:147]\n"
"ds_read_b64_tr_b16 v[88:89], v18 offset:128\n"
"v_mfma_f32_16x16x32_bf16 v[148:151], v[100:103], a[140:143], v[148:151]\n"
"v_mfma_f32_16x16x32_bf16 v[148:151], v[96:99], a[136:139], v[148:151]\n"
"ds_read_b64_tr_b16 v[90:91], v18 offset:4736\n"
"v_mfma_f32_16x16x32_bf16 v[152:155], v[96:99], a[144:147], v[152:155]\n"
"v_mfma_f32_16x16x32_bf16 v[152:155], v[100:103], a[148:151], v[152:155]\n"
"ds_read_b64_tr_b16 v[92:93], v18 offset:9344\n"
"v_mfma_f32_16x16x32_bf16 v[156:159], v[100:103], a[156:159], v[156:159]\n"
"v_mfma_f32_16x16x32_bf16 v[156:159], v[96:99], a[152:155], v[156:159]\n"
"ds_read_b64_tr_b16 v[94:95], v18 offset:13952\n"
"s_waitcnt lgkmcnt(6)\n"
"v_mfma_f32_16x16x32_bf16 v[160:163], v[80:83], a[128:131], v[160:163]\n"
"s_waitcnt lgkmcnt(4)\n"
"v_mfma_f32_16x16x32_bf16 v[160:163], v[84:87], a[132:135], v[160:163]\n"
"ds_read_b64_tr_b16 v[96:97], v18 offset:160\n"
"v_mfma_f32_16x16x32_bf16 v[164:167], v[84:87], a[140:143], v[164:167]\n"
"v_mfma_f32_16x16x32_bf16 v[164:167], v[80:83], a[136:139], v[164:167]\n"
"ds_read_b64_tr_b16 v[98:99], v18 offset:4768\n"
"v_mfma_f32_16x16x32_bf16 v[168:171], v[80:83], a[144:147], v[168:171]\n"
"v_mfma_f32_16x16x32_bf16 v[168:171], v[84:87], a[148:151], v[168:171]\n"
"ds_read_b64_tr_b16 v[100:101], v18 offset:9376\n"
"v_mfma_f32_16x16x32_bf16 v[172:175], v[84:87], a[156:159], v[172:175]\n"
"v_mfma_f32_16x16x32_bf16 v[172:175], v[80:83], a[152:155], v[172:175]\n"
"ds_read_b64_tr_b16 v[102:103], v18 offset:13984\n"
"s_waitcnt lgkmcnt(6)\n"
"v_mfma_f32_16x16x32_bf16 v[176:179], v[88:91], a[128:131], v[176:179]\n"
"s_waitcnt lgkmcnt(4)\n"
"v_mfma_f32_16x16x32_bf16 v[176:179], v[92:95], a[132:135], v[176:179]\n"
"ds_read_b64_tr_b16 v[80:81], v18 offset:192\n"
"v_mfma_f32_16x16x32_bf16 v[180:183], v[92:95], a[140:143], v[180:183]\n"
"v_mfma_f32_16x16x32_bf16 v[180:183], v[88:91], a[136:139], v[180:183]\n"
"ds_read_b64_tr_b16 v[82:83], v18 offset:4800\n"
"v_mfma_f32_16x16x32_bf16 v[184:187], v[88:91], a[144:147], v[184:187]\n"
"v_mfma_f32_16x16x32_bf16 v[184:187], v[92:95], a[148:151], v[184:187]\n"
"ds_read_b64_tr_b16 v[84:85], v18 offset:9408\n"
"v_mfma_f32_16x16x32_bf16 v[188:191], v[92:95], a[156:159], v[188:191]\n"
"v_mfma_f32_16x16x32_bf16 v[188:191], v[88:91], a[152:155], v[188:191]\n"
"ds_read_b64_tr_b16 v[86:87], v18 offset:14016\n"
"s_waitcnt lgkmcnt(6)\n"
"v_mfma_f32_16x16x32_bf16 v[192:195], v[96:99], a[128:131], v[192:195]\n"
"s_waitcnt lgkmcnt(4)\n"
"v_mfma_f32_16x16x32_bf16 v[192:195], v[100:103], a[132:135], v[192:195]\n"
"ds_read_b64_tr_b16 v[88:89], v18 offset:224\n"
"v_mfma_f32_16x16x32_bf16 v[196:199], v[100:103], a[140:143], v[196:199]\n"
"v_mfma_f32_16x16x32_bf16 v[196:199], v[96:99], a[136:139], v[196:199]\n"
"ds_read_b64_tr_b16 v[90:91], v18 offset:4832\n"
"v_mfma_f32_16x16x32_bf16 v[200:203], v[96:99], a[144:147], v[200:203]\n"
"v_mfma_f32_16x16x32_bf16 v[200:203], v[100:103], a[148:151], v[200:203]\n"
"ds_read_b64_tr_b16 v[92:93], v18 offset:9440\n"
"v_mfma_f32_16x16x32_bf16 v[204:207], v[100:103], a[156:159], v[204:207]\n"
"v_mfma_f32_16x16x32_bf16 v[204:207], v[96:99], a[152:155], v[204:207]\n"
"ds_read_b64_tr_b16 v[94:95], v18 offset:14048\n"
"s_waitcnt lgkmcnt(6)\n"
"v_mfma_f32_16x16x32_bf16 v[208:211], v[80:83], a[128:131], v[208:211]\n"
"s_waitcnt lgkmcnt(4)\n"
"v_mfma_f32_16x16x32_bf16 v[208:211], v[84:87], a[132:135], v[208:211]\n"
"ds_read_b64_tr_b16 v[96:97], v18 offset:256\n"
"v_mfma_f32_16x16x32_bf16 v[212:215], v[84:87], a[140:143], v[212:215]\n"
"v_mfma_f32_16x16x32_bf16 v[212:215], v[80:83], a[136:139], v[212:215]\n"
"ds_read_b64_tr_b16 v[98:99], v18 offset:4864\n"
"v_mfma_f32_16x16x32_bf16 v[216:219], v[80:83], a[144:147], v[216:219]\n"
"v_mfma_f32_16x16x32_bf16 v[216:219], v[84:87], a[148:151], v[216:219]\n"
"ds_read_b64_tr_b16 v[100:101], v18 offset:9472\n"
"v_mfma_f32_16x16x32_bf16 v[220:223], v[84:87], a[156:159], v[220:223]\n"
"v_mfma_f32_16x16x32_bf16 v[220:223], v[80:83], a[152:155], v[220:223]\n"
"s_waitcnt vmcnt(0)\n"
"s_nop 0\n"
"s_barrier\n"
"ds_read_b64_tr_b16 v[102:103], v18 offset:14080\n"
"s_waitcnt lgkmcnt(6)\n"
"v_mfma_f32_16x16x32_bf16 v[224:227], v[88:91], a[128:131], v[224:227]\n"
"s_waitcnt lgkmcnt(4)\n"
"v_mfma_f32_16x16x32_bf16 v[224:227], v[92:95], a[132:135], v[224:227]\n"
"ds_read_b64_tr_b16 v[80:81], v18 offset:18432\n"
"v_mfma_f32_16x16x32_bf16 v[228:231], v[92:95], a[140:143], v[228:231]\n"
"v_mfma_f32_16x16x32_bf16 v[228:231], v[88:91], a[136:139], v[228:231]\n"
"ds_read_b64_tr_b16 v[82:83], v18 offset:23040\n"
"v_mfma_f32_16x16x32_bf16 v[232:235], v[88:91], a[144:147], v[232:235]\n"
"v_mfma_f32_16x16x32_bf16 v[232:235], v[92:95], a[148:151], v[232:235]\n"
"ds_read_b64_tr_b16 v[84:85], v18 offset:27648\n"
"v_mfma_f32_16x16x32_bf16 v[236:239], v[92:95], a[156:159], v[236:239]\n"
"v_mfma_f32_16x16x32_bf16 v[236:239], v[88:91], a[152:155], v[236:239]\n"
"ds_read_b64_tr_b16 v[86:87], v18 offset:32256\n"
"s_waitcnt lgkmcnt(6)\n"
"v_mfma_f32_16x16x32_bf16 v[240:243], v[96:99], a[128:131], v[240:243]\n"
"s_waitcnt lgkmcnt(4)\n"
"v_mfma_f32_16x16x32_bf16 v[240:243], v[100:103], a[132:135], v[240:243]\n"
"ds_read_b64_tr_b16 v[88:89], v16\n"
"v_mfma_f32_16x16x32_bf16 v[244:247], v[100:103], a[140:143], v[244:247]\n"
"v_mfma_f32_16x16x32_bf16 v[244:247], v[96:99], a[136:139], v[244:247]\n"
"ds_read_b64_tr_b16 v[90:91], v16 offset:4608\n"
"v_mfma_f32_16x16x32_bf16 v[248:251], v[96:99], a[144:147], v[248:251]\n"
"v_mfma_f32_16x16x32_bf16 v[248:251], v[100:103], a[148:151], v[248:251]\n"
"ds_read_b64_tr_b16 v[92:93], v16 offset:9216\n"
"v_mfma_f32_16x16x32_bf16 v[252:255], v[100:103], a[156:159], v[252:255]\n"
"v_mfma_f32_16x16x32_bf16 v[252:255], v[96:99], a[152:155], v[252:255]\n"
"ds_read_b64_tr_b16 v[94:95], v16 offset:13824\n"
"s_waitcnt lgkmcnt(6)\n"
"v_mfma_f32_16x16x32_bf16 v[112:115], v[80:83], a[160:163], v[112:115]\n"
"s_waitcnt lgkmcnt(4)\n"
"v_mfma_f32_16x16x32_bf16 v[112:115], v[84:87], a[164:167], v[112:115]\n"
"ds_read_b64_tr_b16 v[96:97], v16 offset:18432\n"
"v_mfma_f32_16x16x32_bf16 v[116:119], v[84:87], a[172:175], v[116:119]\n"
"v_mfma_f32_16x16x32_bf16 v[116:119], v[80:83], a[168:171], v[116:119]\n"
"ds_read_b64_tr_b16 v[98:99], v16 offset:23040\n"
"v_mfma_f32_16x16x32_bf16 v[120:123], v[80:83], a[176:179], v[120:123]\n"
"v_mfma_f32_16x16x32_bf16 v[120:123], v[84:87], a[180:183], v[120:123]\n"
"ds_read_b64_tr_b16 v[100:101], v16 offset:27648\n"
"v_mfma_f32_16x16x32_bf16 v[124:127], v[84:87], a[188:191], v[124:127]\n"
"v_mfma_f32_16x16x32_bf16 v[124:127], v[80:83], a[184:187], v[124:127]\n"
"ds_read_b64_tr_b16 v[102:103], v16 offset:32256\n"
"s_waitcnt lgkmcnt(6)\n"
"v_mfma_f32_16x16x32_bf16 v[112:115], v[88:91], a[192:195], v[112:115]\n"
"s_waitcnt lgkmcnt(4)\n"
"v_mfma_f32_16x16x32_bf16 v[112:115], v[92:95], a[196:199], v[112:115]\n"
"ds_read_b64_tr_b16 v[80:81], v18 offset:18464\n"
"v_mfma_f32_16x16x32_bf16 v[116:119], v[92:95], a[204:207], v[116:119]\n"
"v_mfma_f32_16x16x32_bf16 v[116:119], v[88:91], a[200:203], v[116:119]\n"
"ds_read_b64_tr_b16 v[82:83], v18 offset:23072\n"
"v_mfma_f32_16x16x32_bf16 v[120:123], v[88:91], a[208:211], v[120:123]\n"
"v_mfma_f32_16x16x32_bf16 v[120:123], v[92:95], a[212:215], v[120:123]\n"
"ds_read_b64_tr_b16 v[84:85], v18 offset:27680\n"
"v_mfma_f32_16x16x32_bf16 v[124:127], v[92:95], a[220:223], v[124:127]\n"
"v_mfma_f32_16x16x32_bf16 v[124:127], v[88:91], a[216:219], v[124:127]\n"
"ds_read_b64_tr_b16 v[86:87], v18 offset:32288\n"
"s_waitcnt lgkmcnt(6)\n"
"v_mfma_f32_16x16x32_bf16 v[112:115], v[96:99], a[224:227], v[112:115]\n"
"s_waitcnt lgkmcnt(4)\n"
"v_mfma_f32_16x16x32_bf16 v[112:115], v[100:103], a[228:231], v[112:115]\n"
"ds_read_b64_tr_b16 v[88:89], v16 offset:32\n"
"v_mfma_f32_16x16x32_bf16 v[116:119], v[100:103], a[236:239], v[116:119]\n"
"v_mfma_f32_16x16x32_bf16 v[116:119], v[96:99], a[232:235], v[116:119]\n"
"ds_read_b64_tr_b16 v[90:91], v16 offset:4640\n"
"v_mfma_f32_16x16x32_bf16 v[120:123], v[96:99], a[240:243], v[120:123]\n"
"v_mfma_f32_16x16x32_bf16 v[120:123], v[100:103], a[244:247], v[120:123]\n"
"ds_read_b64_tr_b16 v[92:93], v16 offset:9248\n"
"v_mfma_f32_16x16x32_bf16 v[124:127], v[100:103], a[252:255], v[124:127]\n"
"v_mfma_f32_16x16x32_bf16 v[124:127], v[96:99], a[248:251], v[124:127]\n"
"ds_read_b64_tr_b16 v[94:95], v16 offset:13856\n"
"s_waitcnt lgkmcnt(6)\n"
"v_mfma_f32_16x16x32_bf16 v[128:131], v[80:83], a[160:163], v[128:131]\n"
"s_waitcnt lgkmcnt(4)\n"
"v_mfma_f32_16x16x32_bf16 v[128:131], v[84:87], a[164:167], v[128:131]\n"
"ds_read_b64_tr_b16 v[96:97], v16 offset:18464\n"
"v_mfma_f32_16x16x32_bf16 v[132:135], v[84:87], a[172:175], v[132:135]\n"
"v_mfma_f32_16x16x32_bf16 v[132:135], v[80:83], a[168:171], v[132:135]\n"
"ds_read_b64_tr_b16 v[98:99], v16 offset:23072\n"
"v_cvt_pk_bf16_f32 v48, v112, v113\n"
"v_mfma_f32_16x16x32_bf16 v[136:139], v[80:83], a[176:179], v[136:139]\n"
"v_cvt_pk_bf16_f32 v49, v114, v115\n"
"v_cvt_pk_bf16_f32 v50, v116, v117\n"
"v_mfma_f32_16x16x32_bf16 v[136:139], v[84:87], a[180:183], v[136:139]\n"
"ds_read_b64_tr_b16 v[100:101], v16 offset:27680\n"
"v_cvt_pk_bf16_f32 v51, v118, v119\n"
"v_mfma_f32_16x16x32_bf16 v[140:143], v[84:87], a[188:191], v[140:143]\n"
"s_add_u32 s98, s90, 0\n"
"buffer_store_dwordx4 v[48:51], v20, s[52:55], s98 offen sc1\n"
"v_mfma_f32_16x16x32_bf16 v[140:143], v[80:83], a[184:187], v[140:143]\n"
"ds_read_b64_tr_b16 v[102:103], v16 offset:32288\n"
"v_cvt_pk_bf16_f32 v52, v120, v121\n"
"s_waitcnt lgkmcnt(6)\n"
"v_mfma_f32_16x16x32_bf16 v[128:131], v[88:91], a[192:195], v[128:131]\n"
"v_cvt_pk_bf16_f32 v53, v122, v123\n"
"v_cvt_pk_bf16_f32 v54, v124, v125\n"
"s_waitcnt lgkmcnt(4)\n"
"v_mfma_f32_16x16x32_bf16 v[128:131], v[92:95], a[196:199], v[128:131]\n"
"ds_read_b64_tr_b16 v[80:81], v18 offset:18496\n"
"v_cvt_pk_bf16_f32 v55, v126, v127\n"
"v_mfma_f32_16x16x32_bf16 v[132:135], v[92:95], a[204:207], v[132:135]\n"
"s_add_u32 s98, s90, 0\n"
"buffer_store_dwordx4 v[52:55], v20, s[52:55], s98 offen offset:1024 sc1\n"
"v_mfma_f32_16x16x32_bf16 v[132:135], v[88:91], a[200:203], v[132:135]\n"
"ds_read_b64_tr_b16 v[82:83], v18 offset:23104\n"
"v_mfma_f32_16x16x32_bf16 v[136:139], v[88:91], a[208:211], v[136:139]\n"
"v_mfma_f32_16x16x32_bf16 v[136:139], v[92:95], a[212:215], v[136:139]\n"
"ds_read_b64_tr_b16 v[84:85], v18 offset:27712\n"
"v_mfma_f32_16x16x32_bf16 v[140:143], v[92:95], a[220:223], v[140:143]\n"
"v_mfma_f32_16x16x32_bf16 v[140:143], v[88:91], a[216:219], v[140:143]\n"
"ds_read_b64_tr_b16 v[86:87], v18 offset:32320\n"
"s_waitcnt lgkmcnt(6)\n"
"v_mfma_f32_16x16x32_bf16 v[128:131], v[96:99], a[224:227], v[128:131]\n"
"s_waitcnt lgkmcnt(4)\n"
"v_mfma_f32_16x16x32_bf16 v[128:131], v[100:103], a[228:231], v[128:131]\n"
"ds_read_b64_tr_b16 v[88:89], v16 offset:64\n"
"v_mfma_f32_16x16x32_bf16 v[132:135], v[100:103], a[236:239], v[132:135]\n"
"v_mfma_f32_16x16x32_bf16 v[132:135], v[96:99], a[232:235], v[132:135]\n"
"ds_read_b64_tr_b16 v[90:91], v16 offset:4672\n"
"v_mfma_f32_16x16x32_bf16 v[136:139], v[96:99], a[240:243], v[136:139]\n"
"v_mfma_f32_16x16x32_bf16 v[136:139], v[100:103], a[244:247], v[136:139]\n"
"ds_read_b64_tr_b16 v[92:93], v16 offset:9280\n"
"v_mfma_f32_16x16x32_bf16 v[140:143], v[100:103], a[252:255], v[140:143]\n"
"v_mfma_f32_16x16x32_bf16 v[140:143], v[96:99], a[248:251], v[140:143]\n"
"ds_read_b64_tr_b16 v[94:95], v16 offset:13888\n"
"s_waitcnt lgkmcnt(6)\n"
"v_mfma_f32_16x16x32_bf16 v[144:147], v[80:83], a[160:163], v[144:147]\n"
"s_waitcnt lgkmcnt(4)\n"
"v_mfma_f32_16x16x32_bf16 v[144:147], v[84:87], a[164:167], v[144:147]\n"
"ds_read_b64_tr_b16 v[96:97], v16 offset:18496\n"
"v_mfma_f32_16x16x32_bf16 v[148:151], v[84:87], a[172:175], v[148:151]\n"
"v_mfma_f32_16x16x32_bf16 v[148:151], v[80:83], a[168:171], v[148:151]\n"
"ds_read_b64_tr_b16 v[98:99], v16 offset:23104\n"
"v_cvt_pk_bf16_f32 v56, v128, v129\n"
"v_mfma_f32_16x16x32_bf16 v[152:155], v[80:83], a[176:179], v[152:155]\n"
"v_cvt_pk_bf16_f32 v57, v130, v131\n"
"v_cvt_pk_bf16_f32 v58, v132, v133\n"
"v_mfma_f32_16x16x32_bf16 v[152:155], v[84:87], a[180:183], v[152:155]\n"
"ds_read_b64_tr_b16 v[100:101], v16 offset:27712\n"
"v_cvt_pk_bf16_f32 v59, v134, v135\n"
"v_mfma_f32_16x16x32_bf16 v[156:159], v[84:87], a[188:191], v[156:159]\n"
"s_add_u32 s98, s90, 0\n"
"buffer_store_dwordx4 v[56:59], v20, s[52:55], s98 offen offset:2048 sc1\n"
"v_mfma_f32_16x16x32_bf16 v[156:159], v[80:83], a[184:187], v[156:159]\n"
"ds_read_b64_tr_b16 v[102:103], v16 offset:32320\n"
"v_cvt_pk_bf16_f32 v60, v136, v137\n"
"s_waitcnt lgkmcnt(6)\n"
"v_mfma_f32_16x16x32_bf16 v[144:147], v[88:91], a[192:195], v[144:147]\n"
"v_cvt_pk_bf16_f32 v61, v138, v139\n"
"v_cvt_pk_bf16_f32 v62, v140, v141\n"
"s_waitcnt lgkmcnt(4)\n"
"v_mfma_f32_16x16x32_bf16 v[144:147], v[92:95], a[196:199], v[144:147]\n"
"ds_read_b64_tr_b16 v[80:81], v18 offset:18528\n"
"v_cvt_pk_bf16_f32 v63, v142, v143\n"
"v_mfma_f32_16x16x32_bf16 v[148:151], v[92:95], a[204:207], v[148:151]\n"
"s_add_u32 s98, s90, 0\n"
"buffer_store_dwordx4 v[60:63], v20, s[52:55], s98 offen offset:3072 sc1\n"
"v_mfma_f32_16x16x32_bf16 v[148:151], v[88:91], a[200:203], v[148:151]\n"
"ds_read_b64_tr_b16 v[82:83], v18 offset:23136\n"
"v_mfma_f32_16x16x32_bf16 v[152:155], v[88:91], a[208:211], v[152:155]\n"
"v_mfma_f32_16x16x32_bf16 v[152:155], v[92:95], a[212:215], v[152:155]\n"
"ds_read_b64_tr_b16 v[84:85], v18 offset:27744\n"
"v_mfma_f32_16x16x32_bf16 v[156:159], v[92:95], a[220:223], v[156:159]\n"
"v_mfma_f32_16x16x32_bf16 v[156:159], v[88:91], a[216:219], v[156:159]\n"
"ds_read_b64_tr_b16 v[86:87], v18 offset:32352\n"
"s_waitcnt lgkmcnt(6)\n"
"v_mfma_f32_16x16x32_bf16 v[144:147], v[96:99], a[224:227], v[144:147]\n"
"s_waitcnt lgkmcnt(4)\n"
"v_mfma_f32_16x16x32_bf16 v[144:147], v[100:103], a[228:231], v[144:147]\n"
"ds_read_b64_tr_b16 v[88:89], v16 offset:96\n"
"v_mfma_f32_16x16x32_bf16 v[148:151], v[100:103], a[236:239], v[148:151]\n"
"v_mfma_f32_16x16x32_bf16 v[148:151], v[96:99], a[232:235], v[148:151]\n"
"ds_read_b64_tr_b16 v[90:91], v16 offset:4704\n"
"v_mfma_f32_16x16x32_bf16 v[152:155], v[96:99], a[240:243], v[152:155]\n"
"v_mfma_f32_16x16x32_bf16 v[152:155], v[100:103], a[244:247], v[152:155]\n"
"ds_read_b64_tr_b16 v[92:93], v16 offset:9312\n"
"v_mfma_f32_16x16x32_bf16 v[156:159], v[100:103], a[252:255], v[156:159]\n"
"v_mfma_f32_16x16x32_bf16 v[156:159], v[96:99], a[248:251], v[156:159]\n"
"ds_read_b64_tr_b16 v[94:95], v16 offset:13920\n"
"s_waitcnt lgkmcnt(6)\n"
"v_mfma_f32_16x16x32_bf16 v[160:163], v[80:83], a[160:163], v[160:163]\n"
"s_waitcnt lgkmcnt(4)\n"
"v_mfma_f32_16x16x32_bf16 v[160:163], v[84:87], a[164:167], v[160:163]\n"
"ds_read_b64_tr_b16 v[96:97], v16 offset:18528\n"
"v_mfma_f32_16x16x32_bf16 v[164:167], v[84:87], a[172:175], v[164:167]\n"
"v_mfma_f32_16x16x32_bf16 v[164:167], v[80:83], a[168:171], v[164:167]\n"
"ds_read_b64_tr_b16 v[98:99], v16 offset:23136\n"
"v_cvt_pk_bf16_f32 v48, v144, v145\n"
"v_mfma_f32_16x16x32_bf16 v[168:171], v[80:83], a[176:179], v[168:171]\n"
"v_cvt_pk_bf16_f32 v49, v146, v147\n"
"v_cvt_pk_bf16_f32 v50, v148, v149\n"
"v_mfma_f32_16x16x32_bf16 v[168:171], v[84:87], a[180:183], v[168:171]\n"
"ds_read_b64_tr_b16 v[100:101], v16 offset:27744\n"
"v_cvt_pk_bf16_f32 v51, v150, v151\n"
"v_mfma_f32_16x16x32_bf16 v[172:175], v[84:87], a[188:191], v[172:175]\n"
"s_add_u32 s98, s90, 0x1000\n"
"buffer_store_dwordx4 v[48:51], v20, s[52:55], s98 offen sc1\n"
"v_mfma_f32_16x16x32_bf16 v[172:175], v[80:83], a[184:187], v[172:175]\n"
"ds_read_b64_tr_b16 v[102:103], v16 offset:32352\n"
"v_cvt_pk_bf16_f32 v52, v152, v153\n"
"s_waitcnt lgkmcnt(6)\n"
"v_mfma_f32_16x16x32_bf16 v[160:163], v[88:91], a[192:195], v[160:163]\n"
"v_cvt_pk_bf16_f32 v53, v154, v155\n"
"v_cvt_pk_bf16_f32 v54, v156, v157\n"
"s_waitcnt lgkmcnt(4)\n"
"v_mfma_f32_16x16x32_bf16 v[160:163], v[92:95], a[196:199], v[160:163]\n"
"ds_read_b64_tr_b16 v[80:81], v18 offset:18560\n"
"v_cvt_pk_bf16_f32 v55, v158, v159\n"
"v_mfma_f32_16x16x32_bf16 v[164:167], v[92:95], a[204:207], v[164:167]\n"
"s_add_u32 s98, s90, 0x1000\n"
"buffer_store_dwordx4 v[52:55], v20, s[52:55], s98 offen offset:1024 sc1\n"
"v_mfma_f32_16x16x32_bf16 v[164:167], v[88:91], a[200:203], v[164:167]\n"
"ds_read_b64_tr_b16 v[82:83], v18 offset:23168\n"
"v_mfma_f32_16x16x32_bf16 v[168:171], v[88:91], a[208:211], v[168:171]\n"
"v_mfma_f32_16x16x32_bf16 v[168:171], v[92:95], a[212:215], v[168:171]\n"
"ds_read_b64_tr_b16 v[84:85], v18 offset:27776\n"
"v_mfma_f32_16x16x32_bf16 v[172:175], v[92:95], a[220:223], v[172:175]\n"
"v_mfma_f32_16x16x32_bf16 v[172:175], v[88:91], a[216:219], v[172:175]\n"
"ds_read_b64_tr_b16 v[86:87], v18 offset:32384\n"
"s_waitcnt lgkmcnt(6)\n"
"v_mfma_f32_16x16x32_bf16 v[160:163], v[96:99], a[224:227], v[160:163]\n"
"s_waitcnt lgkmcnt(4)\n"
"v_mfma_f32_16x16x32_bf16 v[160:163], v[100:103], a[228:231], v[160:163]\n"
"ds_read_b64_tr_b16 v[88:89], v16 offset:128\n"
"v_mfma_f32_16x16x32_bf16 v[164:167], v[100:103], a[236:239], v[164:167]\n"
"v_mfma_f32_16x16x32_bf16 v[164:167], v[96:99], a[232:235], v[164:167]\n"
"ds_read_b64_tr_b16 v[90:91], v16 offset:4736\n"
"v_mfma_f32_16x16x32_bf16 v[168:171], v[96:99], a[240:243], v[168:171]\n"
"v_mfma_f32_16x16x32_bf16 v[168:171], v[100:103], a[244:247], v[168:171]\n"
"ds_read_b64_tr_b16 v[92:93], v16 offset:9344\n"
"v_mfma_f32_16x16x32_bf16 v[172:175], v[100:103], a[252:255], v[172:175]\n"
"v_mfma_f32_16x16x32_bf16 v[172:175], v[96:99], a[248:251], v[172:175]\n"
"ds_read_b64_tr_b16 v[94:95], v16 offset:13952\n"
"s_waitcnt lgkmcnt(6)\n"
"v_mfma_f32_16x16x32_bf16 v[176:179], v[80:83], a[160:163], v[176:179]\n"
"s_waitcnt lgkmcnt(4)\n"
"v_mfma_f32_16x16x32_bf16 v[176:179], v[84:87], a[164:167], v[176:179]\n"
"ds_read_b64_tr_b16 v[96:97], v16 offset:18560\n"
"v_mfma_f32_16x16x32_bf16 v[180:183], v[84:87], a[172:175], v[180:183]\n"
"v_mfma_f32_16x16x32_bf16 v[180:183], v[80:83], a[168:171], v[180:183]\n"
"ds_read_b64_tr_b16 v[98:99], v16 offset:23168\n"
"v_cvt_pk_bf16_f32 v56, v160, v161\n"
"v_mfma_f32_16x16x32_bf16 v[184:187], v[80:83], a[176:179], v[184:187]\n"
"v_cvt_pk_bf16_f32 v57, v162, v163\n"
"v_cvt_pk_bf16_f32 v58, v164, v165\n"
"v_mfma_f32_16x16x32_bf16 v[184:187], v[84:87], a[180:183], v[184:187]\n"
"ds_read_b64_tr_b16 v[100:101], v16 offset:27776\n"
"v_cvt_pk_bf16_f32 v59, v166, v167\n"
"v_mfma_f32_16x16x32_bf16 v[188:191], v[84:87], a[188:191], v[188:191]\n"
"s_add_u32 s98, s90, 0x1000\n"
"buffer_store_dwordx4 v[56:59], v20, s[52:55], s98 offen offset:2048 sc1\n"
"v_mfma_f32_16x16x32_bf16 v[188:191], v[80:83], a[184:187], v[188:191]\n"
"ds_read_b64_tr_b16 v[102:103], v16 offset:32384\n"
"v_cvt_pk_bf16_f32 v60, v168, v169\n"
"s_waitcnt lgkmcnt(6)\n"
"v_mfma_f32_16x16x32_bf16 v[176:179], v[88:91], a[192:195], v[176:179]\n"
"v_cvt_pk_bf16_f32 v61, v170, v171\n"
"v_cvt_pk_bf16_f32 v62, v172, v173\n"
"s_waitcnt lgkmcnt(4)\n"
"v_mfma_f32_16x16x32_bf16 v[176:179], v[92:95], a[196:199], v[176:179]\n"
"ds_read_b64_tr_b16 v[80:81], v18 offset:18592\n"
"v_cvt_pk_bf16_f32 v63, v174, v175\n"
"v_mfma_f32_16x16x32_bf16 v[180:183], v[92:95], a[204:207], v[180:183]\n"
"s_add_u32 s98, s90, 0x1000\n"
"buffer_store_dwordx4 v[60:63], v20, s[52:55], s98 offen offset:3072 sc1\n"
"v_mfma_f32_16x16x32_bf16 v[180:183], v[88:91], a[200:203], v[180:183]\n"
"ds_read_b64_tr_b16 v[82:83], v18 offset:23200\n"
"v_mfma_f32_16x16x32_bf16 v[184:187], v[88:91], a[208:211], v[184:187]\n"
"v_mfma_f32_16x16x32_bf16 v[184:187], v[92:95], a[212:215], v[184:187]\n"
"ds_read_b64_tr_b16 v[84:85], v18 offset:27808\n"
"v_mfma_f32_16x16x32_bf16 v[188:191], v[92:95], a[220:223], v[188:191]\n"
"v_mfma_f32_16x16x32_bf16 v[188:191], v[88:91], a[216:219], v[188:191]\n"
"ds_read_b64_tr_b16 v[86:87], v18 offset:32416\n"
"s_waitcnt lgkmcnt(6)\n"
"v_mfma_f32_16x16x32_bf16 v[176:179], v[96:99], a[224:227], v[176:179]\n"
"s_waitcnt lgkmcnt(4)\n"
"v_mfma_f32_16x16x32_bf16 v[176:179], v[100:103], a[228:231], v[176:179]\n"
"ds_read_b64_tr_b16 v[88:89], v16 offset:160\n"
"v_mfma_f32_16x16x32_bf16 v[180:183], v[100:103], a[236:239], v[180:183]\n"
"v_mfma_f32_16x16x32_bf16 v[180:183], v[96:99], a[232:235], v[180:183]\n"
"ds_read_b64_tr_b16 v[90:91], v16 offset:4768\n"
"v_mfma_f32_16x16x32_bf16 v[184:187], v[96:99], a[240:243], v[184:187]\n"
"v_mfma_f32_16x16x32_bf16 v[184:187], v[100:103], a[244:247], v[184:187]\n"
"ds_read_b64_tr_b16 v[92:93], v16 offset:9376\n"
"v_mfma_f32_16x16x32_bf16 v[188:191], v[100:103], a[252:255], v[188:191]\n"
"v_mfma_f32_16x16x32_bf16 v[188:191], v[96:99], a[248:251], v[188:191]\n"
"ds_read_b64_tr_b16 v[94:95], v16 offset:13984\n"
"s_waitcnt lgkmcnt(6)\n"
"v_mfma_f32_16x16x32_bf16 v[192:195], v[80:83], a[160:163], v[192:195]\n"
"s_waitcnt lgkmcnt(4)\n"
"v_mfma_f32_16x16x32_bf16 v[192:195], v[84:87], a[164:167], v[192:195]\n"
"ds_read_b64_tr_b16 v[96:97], v16 offset:18592\n"
"v_mfma_f32_16x16x32_bf16 v[196:199], v[84:87], a[172:175], v[196:199]\n"
"v_mfma_f32_16x16x32_bf16 v[196:199], v[80:83], a[168:171], v[196:199]\n"
"ds_read_b64_tr_b16 v[98:99], v16 offset:23200\n"
"v_cvt_pk_bf16_f32 v48, v176, v177\n"
"v_mfma_f32_16x16x32_bf16 v[200:203], v[80:83], a[176:179], v[200:203]\n"
"v_cvt_pk_bf16_f32 v49, v178, v179\n"
"v_cvt_pk_bf16_f32 v50, v180, v181\n"
"v_mfma_f32_16x16x32_bf16 v[200:203], v[84:87], a[180:183], v[200:203]\n"
"ds_read_b64_tr_b16 v[100:101], v16 offset:27808\n"
"v_cvt_pk_bf16_f32 v51, v182, v183\n"
"v_mfma_f32_16x16x32_bf16 v[204:207], v[84:87], a[188:191], v[204:207]\n"
"s_add_u32 s98, s90, 0x2000\n"
"buffer_store_dwordx4 v[48:51], v20, s[52:55], s98 offen sc1\n"
"v_mfma_f32_16x16x32_bf16 v[204:207], v[80:83], a[184:187], v[204:207]\n"
"ds_read_b64_tr_b16 v[102:103], v16 offset:32416\n"
"v_cvt_pk_bf16_f32 v52, v184, v185\n"
"s_waitcnt lgkmcnt(6)\n"
"v_mfma_f32_16x16x32_bf16 v[192:195], v[88:91], a[192:195], v[192:195]\n"
"v_cvt_pk_bf16_f32 v53, v186, v187\n"
"v_cvt_pk_bf16_f32 v54, v188, v189\n"
"s_waitcnt lgkmcnt(4)\n"
"v_mfma_f32_16x16x32_bf16 v[192:195], v[92:95], a[196:199], v[192:195]\n"
"ds_read_b64_tr_b16 v[80:81], v18 offset:18624\n"
"v_cvt_pk_bf16_f32 v55, v190, v191\n"
"v_mfma_f32_16x16x32_bf16 v[196:199], v[92:95], a[204:207], v[196:199]\n"
"s_add_u32 s98, s90, 0x2000\n"
"buffer_store_dwordx4 v[52:55], v20, s[52:55], s98 offen offset:1024 sc1\n"
"v_mfma_f32_16x16x32_bf16 v[196:199], v[88:91], a[200:203], v[196:199]\n"
"ds_read_b64_tr_b16 v[82:83], v18 offset:23232\n"
"v_mfma_f32_16x16x32_bf16 v[200:203], v[88:91], a[208:211], v[200:203]\n"
"v_mfma_f32_16x16x32_bf16 v[200:203], v[92:95], a[212:215], v[200:203]\n"
"ds_read_b64_tr_b16 v[84:85], v18 offset:27840\n"
"v_mfma_f32_16x16x32_bf16 v[204:207], v[92:95], a[220:223], v[204:207]\n"
"v_mfma_f32_16x16x32_bf16 v[204:207], v[88:91], a[216:219], v[204:207]\n"
"ds_read_b64_tr_b16 v[86:87], v18 offset:32448\n"
"s_waitcnt lgkmcnt(6)\n"
"v_mfma_f32_16x16x32_bf16 v[192:195], v[96:99], a[224:227], v[192:195]\n"
"s_waitcnt lgkmcnt(4)\n"
"v_mfma_f32_16x16x32_bf16 v[192:195], v[100:103], a[228:231], v[192:195]\n"
"ds_read_b64_tr_b16 v[88:89], v16 offset:192\n"
"v_mfma_f32_16x16x32_bf16 v[196:199], v[100:103], a[236:239], v[196:199]\n"
"v_mfma_f32_16x16x32_bf16 v[196:199], v[96:99], a[232:235], v[196:199]\n"
"ds_read_b64_tr_b16 v[90:91], v16 offset:4800\n"
"v_mfma_f32_16x16x32_bf16 v[200:203], v[96:99], a[240:243], v[200:203]\n"
"v_mfma_f32_16x16x32_bf16 v[200:203], v[100:103], a[244:247], v[200:203]\n"
"ds_read_b64_tr_b16 v[92:93], v16 offset:9408\n"
"v_mfma_f32_16x16x32_bf16 v[204:207], v[100:103], a[252:255], v[204:207]\n"
"v_mfma_f32_16x16x32_bf16 v[204:207], v[96:99], a[248:251], v[204:207]\n"
"ds_read_b64_tr_b16 v[94:95], v16 offset:14016\n"
"s_waitcnt lgkmcnt(6)\n"
"v_mfma_f32_16x16x32_bf16 v[208:211], v[80:83], a[160:163], v[208:211]\n"
"s_waitcnt lgkmcnt(4)\n"
"v_mfma_f32_16x16x32_bf16 v[208:211], v[84:87], a[164:167], v[208:211]\n"
"ds_read_b64_tr_b16 v[96:97], v16 offset:18624\n"
"v_mfma_f32_16x16x32_bf16 v[212:215], v[84:87], a[172:175], v[212:215]\n"
"v_mfma_f32_16x16x32_bf16 v[212:215], v[80:83], a[168:171], v[212:215]\n"
"ds_read_b64_tr_b16 v[98:99], v16 offset:23232\n"
"v_cvt_pk_bf16_f32 v56, v192, v193\n"
"v_mfma_f32_16x16x32_bf16 v[216:219], v[80:83], a[176:179], v[216:219]\n"
"v_cvt_pk_bf16_f32 v57, v194, v195\n"
"v_cvt_pk_bf16_f32 v58, v196, v197\n"
"v_mfma_f32_16x16x32_bf16 v[216:219], v[84:87], a[180:183], v[216:219]\n"
"ds_read_b64_tr_b16 v[100:101], v16 offset:27840\n"
"v_cvt_pk_bf16_f32 v59, v198, v199\n"
"v_mfma_f32_16x16x32_bf16 v[220:223], v[84:87], a[188:191], v[220:223]\n"
"s_add_u32 s98, s90, 0x2000\n"
"buffer_store_dwordx4 v[56:59], v20, s[52:55], s98 offen offset:2048 sc1\n"
"v_mfma_f32_16x16x32_bf16 v[220:223], v[80:83], a[184:187], v[220:223]\n"
"ds_read_b64_tr_b16 v[102:103], v16 offset:32448\n"
"v_cvt_pk_bf16_f32 v60, v200, v201\n"
"s_waitcnt lgkmcnt(6)\n"
"v_mfma_f32_16x16x32_bf16 v[208:211], v[88:91], a[192:195], v[208:211]\n"
"v_cvt_pk_bf16_f32 v61, v202, v203\n"
"v_cvt_pk_bf16_f32 v62, v204, v205\n"
"s_waitcnt lgkmcnt(4)\n"
"v_mfma_f32_16x16x32_bf16 v[208:211], v[92:95], a[196:199], v[208:211]\n"
"ds_read_b64_tr_b16 v[80:81], v18 offset:18656\n"
"v_cvt_pk_bf16_f32 v63, v206, v207\n"
"v_mfma_f32_16x16x32_bf16 v[212:215], v[92:95], a[204:207], v[212:215]\n"
"s_add_u32 s98, s90, 0x2000\n"
"buffer_store_dwordx4 v[60:63], v20, s[52:55], s98 offen offset:3072 sc1\n"
"v_mfma_f32_16x16x32_bf16 v[212:215], v[88:91], a[200:203], v[212:215]\n"
"ds_read_b64_tr_b16 v[82:83], v18 offset:23264\n"
"v_mfma_f32_16x16x32_bf16 v[216:219], v[88:91], a[208:211], v[216:219]\n"
"v_mfma_f32_16x16x32_bf16 v[216:219], v[92:95], a[212:215], v[216:219]\n"
"ds_read_b64_tr_b16 v[84:85], v18 offset:27872\n"
"v_mfma_f32_16x16x32_bf16 v[220:223], v[92:95], a[220:223], v[220:223]\n"
"v_mfma_f32_16x16x32_bf16 v[220:223], v[88:91], a[216:219], v[220:223]\n"
"ds_read_b64_tr_b16 v[86:87], v18 offset:32480\n"
"s_waitcnt lgkmcnt(6)\n"
"v_mfma_f32_16x16x32_bf16 v[208:211], v[96:99], a[224:227], v[208:211]\n"
"s_waitcnt lgkmcnt(4)\n"
"v_mfma_f32_16x16x32_bf16 v[208:211], v[100:103], a[228:231], v[208:211]\n"
"ds_read_b64_tr_b16 v[88:89], v16 offset:224\n"
"v_mfma_f32_16x16x32_bf16 v[212:215], v[100:103], a[236:239], v[212:215]\n"
"v_mfma_f32_16x16x32_bf16 v[212:215], v[96:99], a[232:235], v[212:215]\n"
"ds_read_b64_tr_b16 v[90:91], v16 offset:4832\n"
"v_mfma_f32_16x16x32_bf16 v[216:219], v[96:99], a[240:243], v[216:219]\n"
"v_mfma_f32_16x16x32_bf16 v[216:219], v[100:103], a[244:247], v[216:219]\n"
"ds_read_b64_tr_b16 v[92:93], v16 offset:9440\n"
"v_mfma_f32_16x16x32_bf16 v[220:223], v[100:103], a[252:255], v[220:223]\n"
"v_mfma_f32_16x16x32_bf16 v[220:223], v[96:99], a[248:251], v[220:223]\n"
"ds_read_b64_tr_b16 v[94:95], v16 offset:14048\n"
"s_waitcnt lgkmcnt(6)\n"
"v_mfma_f32_16x16x32_bf16 v[224:227], v[80:83], a[160:163], v[224:227]\n"
"s_waitcnt lgkmcnt(4)\n"
"v_mfma_f32_16x16x32_bf16 v[224:227], v[84:87], a[164:167], v[224:227]\n"
"ds_read_b64_tr_b16 v[96:97], v16 offset:18656\n"
"v_mfma_f32_16x16x32_bf16 v[228:231], v[84:87], a[172:175], v[228:231]\n"
"v_mfma_f32_16x16x32_bf16 v[228:231], v[80:83], a[168:171], v[228:231]\n"
"ds_read_b64_tr_b16 v[98:99], v16 offset:23264\n"
"v_cvt_pk_bf16_f32 v48, v208, v209\n"
"v_mfma_f32_16x16x32_bf16 v[232:235], v[80:83], a[176:179], v[232:235]\n"
"v_cvt_pk_bf16_f32 v49, v210, v211\n"
"v_cvt_pk_bf16_f32 v50, v212, v213\n"
"v_mfma_f32_16x16x32_bf16 v[232:235], v[84:87], a[180:183], v[232:235]\n"
"ds_read_b64_tr_b16 v[100:101], v16 offset:27872\n"
"v_cvt_pk_bf16_f32 v51, v214, v215\n"
"v_mfma_f32_16x16x32_bf16 v[236:239], v[84:87], a[188:191], v[236:239]\n"
"s_add_u32 s98, s90, 0x3000\n"
"buffer_store_dwordx4 v[48:51], v20, s[52:55], s98 offen sc1\n"
"v_mfma_f32_16x16x32_bf16 v[236:239], v[80:83], a[184:187], v[236:239]\n"
"ds_read_b64_tr_b16 v[102:103], v16 offset:32480\n"
"v_cvt_pk_bf16_f32 v52, v216, v217\n"
"s_waitcnt lgkmcnt(6)\n"
"v_mfma_f32_16x16x32_bf16 v[224:227], v[88:91], a[192:195], v[224:227]\n"
"v_cvt_pk_bf16_f32 v53, v218, v219\n"
"v_cvt_pk_bf16_f32 v54, v220, v221\n"
"s_waitcnt lgkmcnt(4)\n"
"v_mfma_f32_16x16x32_bf16 v[224:227], v[92:95], a[196:199], v[224:227]\n"
"ds_read_b64_tr_b16 v[80:81], v18 offset:18688\n"
"v_cvt_pk_bf16_f32 v55, v222, v223\n"
"v_mfma_f32_16x16x32_bf16 v[228:231], v[92:95], a[204:207], v[228:231]\n"
"s_add_u32 s98, s90, 0x3000\n"
"buffer_store_dwordx4 v[52:55], v20, s[52:55], s98 offen offset:1024 sc1\n"
"v_mfma_f32_16x16x32_bf16 v[228:231], v[88:91], a[200:203], v[228:231]\n"
"ds_read_b64_tr_b16 v[82:83], v18 offset:23296\n"
"v_mfma_f32_16x16x32_bf16 v[232:235], v[88:91], a[208:211], v[232:235]\n"
"v_mfma_f32_16x16x32_bf16 v[232:235], v[92:95], a[212:215], v[232:235]\n"
"ds_read_b64_tr_b16 v[84:85], v18 offset:27904\n"
"v_mfma_f32_16x16x32_bf16 v[236:239], v[92:95], a[220:223], v[236:239]\n"
"v_mfma_f32_16x16x32_bf16 v[236:239], v[88:91], a[216:219], v[236:239]\n"
"ds_read_b64_tr_b16 v[86:87], v18 offset:32512\n"
"s_waitcnt lgkmcnt(6)\n"
"v_mfma_f32_16x16x32_bf16 v[224:227], v[96:99], a[224:227], v[224:227]\n"
"s_waitcnt lgkmcnt(4)\n"
"v_mfma_f32_16x16x32_bf16 v[224:227], v[100:103], a[228:231], v[224:227]\n"
"ds_read_b64_tr_b16 v[88:89], v16 offset:256\n"
"v_mfma_f32_16x16x32_bf16 v[228:231], v[100:103], a[236:239], v[228:231]\n"
"v_mfma_f32_16x16x32_bf16 v[228:231], v[96:99], a[232:235], v[228:231]\n"
"ds_read_b64_tr_b16 v[90:91], v16 offset:4864\n"
"v_mfma_f32_16x16x32_bf16 v[232:235], v[96:99], a[240:243], v[232:235]\n"
"v_mfma_f32_16x16x32_bf16 v[232:235], v[100:103], a[244:247], v[232:235]\n"
"ds_read_b64_tr_b16 v[92:93], v16 offset:9472\n"
"v_mfma_f32_16x16x32_bf16 v[236:239], v[100:103], a[252:255], v[236:239]\n"
"v_mfma_f32_16x16x32_bf16 v[236:239], v[96:99], a[248:251], v[236:239]\n"
"ds_read_b64_tr_b16 v[94:95], v16 offset:14080\n"
"s_waitcnt lgkmcnt(6)\n"
"v_mfma_f32_16x16x32_bf16 v[240:243], v[80:83], a[160:163], v[240:243]\n"
"s_waitcnt lgkmcnt(4)\n"
"v_mfma_f32_16x16x32_bf16 v[240:243], v[84:87], a[164:167], v[240:243]\n"
"ds_read_b64_tr_b16 v[96:97], v16 offset:18688\n"
"v_mfma_f32_16x16x32_bf16 v[244:247], v[84:87], a[172:175], v[244:247]\n"
"v_mfma_f32_16x16x32_bf16 v[244:247], v[80:83], a[168:171], v[244:247]\n"
"ds_read_b64_tr_b16 v[98:99], v16 offset:23296\n"
"v_cvt_pk_bf16_f32 v56, v224, v225\n"
"v_mfma_f32_16x16x32_bf16 v[248:251], v[80:83], a[176:179], v[248:251]\n"
"v_cvt_pk_bf16_f32 v57, v226, v227\n"
"v_cvt_pk_bf16_f32 v58, v228, v229\n"
"v_mfma_f32_16x16x32_bf16 v[248:251], v[84:87], a[180:183], v[248:251]\n"
"ds_read_b64_tr_b16 v[100:101], v16 offset:27904\n"
"v_cvt_pk_bf16_f32 v59, v230, v231\n"
"v_mfma_f32_16x16x32_bf16 v[252:255], v[84:87], a[188:191], v[252:255]\n"
"s_add_u32 s98, s90, 0x3000\n"
"buffer_store_dwordx4 v[56:59], v20, s[52:55], s98 offen offset:2048 sc1\n"
"v_mfma_f32_16x16x32_bf16 v[252:255], v[80:83], a[184:187], v[252:255]\n"
"ds_read_b64_tr_b16 v[102:103], v16 offset:32512\n"
"v_cvt_pk_bf16_f32 v60, v232, v233\n"
"s_waitcnt lgkmcnt(6)\n"
"v_mfma_f32_16x16x32_bf16 v[240:243], v[88:91], a[192:195], v[240:243]\n"
"v_cvt_pk_bf16_f32 v61, v234, v235\n"
"v_cvt_pk_bf16_f32 v62, v236, v237\n"
"s_waitcnt lgkmcnt(4)\n"
"v_mfma_f32_16x16x32_bf16 v[240:243], v[92:95], a[196:199], v[240:243]\n"
"v_cvt_pk_bf16_f32 v63, v238, v239\n"
"v_mfma_f32_16x16x32_bf16 v[244:247], v[92:95], a[204:207], v[244:247]\n"
"s_add_u32 s98, s90, 0x3000\n"
"buffer_store_dwordx4 v[60:63], v20, s[52:55], s98 offen offset:3072 sc1\n"
"v_mfma_f32_16x16x32_bf16 v[244:247], v[88:91], a[200:203], v[244:247]\n"
"v_mfma_f32_16x16x32_bf16 v[248:251], v[88:91], a[208:211], v[248:251]\n"
"v_mfma_f32_16x16x32_bf16 v[248:251], v[92:95], a[212:215], v[248:251]\n"
"v_mfma_f32_16x16x32_bf16 v[252:255], v[92:95], a[220:223], v[252:255]\n"
"v_mfma_f32_16x16x32_bf16 v[252:255], v[88:91], a[216:219], v[252:255]\n"
"s_waitcnt lgkmcnt(2)\n"
"v_mfma_f32_16x16x32_bf16 v[240:243], v[96:99], a[224:227], v[240:243]\n"
"s_waitcnt lgkmcnt(0)\n"
"v_mfma_f32_16x16x32_bf16 v[240:243], v[100:103], a[228:231], v[240:243]\n"
"v_mfma_f32_16x16x32_bf16 v[244:247], v[100:103], a[236:239], v[244:247]\n"
"v_mfma_f32_16x16x32_bf16 v[244:247], v[96:99], a[232:235], v[244:247]\n"
"v_mfma_f32_16x16x32_bf16 v[248:251], v[96:99], a[240:243], v[248:251]\n"
"v_mfma_f32_16x16x32_bf16 v[248:251], v[100:103], a[244:247], v[248:251]\n"
"v_mfma_f32_16x16x32_bf16 v[252:255], v[100:103], a[252:255], v[252:255]\n"
"v_mfma_f32_16x16x32_bf16 v[252:255], v[96:99], a[248:251], v[252:255]\n"
"s_nop 1\n"
"v_cvt_pk_bf16_f32 v48, v240, v241\n"
"v_cvt_pk_bf16_f32 v49, v242, v243\n"
"v_cvt_pk_bf16_f32 v50, v244, v245\n"
"v_cvt_pk_bf16_f32 v51, v246, v247\n"
"s_add_u32 s98, s90, 0x4000\n"
"buffer_store_dwordx4 v[48:51], v20, s[52:55], s98 offen sc1\n"
"v_cvt_pk_bf16_f32 v52, v248, v249\n"
"v_cvt_pk_bf16_f32 v53, v250, v251\n"
"v_cvt_pk_bf16_f32 v54, v252, v253\n"
"v_cvt_pk_bf16_f32 v55, v254, v255\n"
"s_add_u32 s98, s90, 0x4000\n"
"buffer_store_dwordx4 v[52:55], v20, s[52:55], s98 offen offset:1024 sc1\n"
"s_mov_b64 s[76:77], %9\n"
"s_mov_b32 s40, s76\n"
"s_and_b32 s41, s77, 0xffff\n"
"s_mov_b32 s42, 0x240000\n"
"s_mov_b32 s43, 0x20000\n"
"s_add_u32 s76, s80, 0\n"
"s_min_u32 s76, s76, 17\n"
"s_and_b32 s77, s76, 1\n"
"s_mul_i32 s77, s77, 0x9000\n"
"s_lshr_b32 s76, s76, 1\n"
"s_lshl_b32 s76, s76, 5\n"
"s_add_u32 s76, s76, s77\n"
"s_add_u32 s84, s76, s94\n"
"s_add_u32 s76, s84, 0\n"
"buffer_load_dwordx2 v[80:81], v21, s[56:59], s76 offen\n"
"s_add_u32 s76, s84, 0x4800\n"
"buffer_load_dwordx2 v[82:83], v21, s[56:59], s76 offen\n"
"s_add_u32 s76, s80, 8\n"
"s_min_u32 s76, s76, 17\n"
"s_and_b32 s77, s76, 1\n"
"s_mul_i32 s77, s77, 0x9000\n"
"s_lshr_b32 s76, s76, 1\n"
"s_lshl_b32 s76, s76, 5\n"
"s_add_u32 s76, s76, s77\n"
"s_add_u32 s85, s76, s94\n"
"s_add_u32 s76, s85, 0\n"
"buffer_load_dwordx2 v[84:85], v21, s[56:59], s76 offen\n"
"s_add_u32 s76, s85, 0x4800\n"
"buffer_load_dwordx2 v[86:87], v21, s[56:59], s76 offen\n"
"s_add_u32 s76, s80, 16\n"
"s_min_u32 s76, s76, 17\n"
"s_and_b32 s77, s76, 1\n"
"s_mul_i32 s77, s77, 0x9000\n"
"s_lshr_b32 s76, s76, 1\n"
"s_lshl_b32 s76, s76, 5\n"
"s_add_u32 s76, s76, s77\n"
"s_add_u32 s86, s76, s94\n"
"s_add_u32 s76, s86, 0\n"
"buffer_load_dwordx2 v[88:89], v21, s[56:59], s76 offen\n"
"s_add_u32 s76, s86, 0x4800\n"
"buffer_load_dwordx2 v[90:91], v21, s[56:59], s76 offen\n"
"s_waitcnt vmcnt(6)\n"
"s_barrier\n"
"s_cmp_lg_u32 s72, 0\n"
"s_cbranch_scc1 NOSIGX_%=\n"
"s_lshl_b32 s76, s75, 5\n"
"s_add_u32 s76, s76, s74\n"
"s_lshl_b32 s76, s76, 3\n"
"s_add_u32 s76, s76, 0x2800\n"
"v_mov_b32 v72, s76\n"
"v_mov_b32 v74, s88\n"
"v_mov_b32 v75, s89\n"
"s_mov_b64 exec, 1\n"
"global_store_dwordx2 v72, v[74:75], s[92:93] sc1\n"
"s_mov_b64 exec, -1\n"
"NOSIGX_%=:\n"
"s_lshl_b32 s76, s75, 8\n"
"s_add_u32 s76, s76, 0x2800\n"
"v_and_b32 v76, 31, v5\n"
"v_lshlrev_b32 v76, 3, v76\n"
"v_add_u32 v76, s76, v76\n"
"global_load_dwordx2 v[78:79], v76, s[92:93] sc1\n"
"s_lshl_b32 s98, 0x11111111, s82\n"
"s_waitcnt vmcnt(0)\n"
"v_cmp_eq_u64 vcc, v[78:79], s[88:89]\n"
"s_nop 0\n"
"s_and_b32 s76, vcc_lo, s98\n"
"s_mov_b32 s99, 0\n"
"s_cmp_eq_u32 s76, s98\n"
"s_cbranch_scc1 POLLDX_%=\n"
"global_load_dwordx2 v[32:33], v76, s[92:93] sc1\n"
"s_sleep 3\n"
"global_load_dwordx2 v[34:35], v76, s[92:93] sc1\n"
"s_sleep 3\n"
"global_load_dwordx2 v[36:37], v76, s[92:93] sc1\n"
"s_sleep 3\n"
"POLLX_%=:\n"
"global_load_dwordx2 v[38:39], v76, s[92:93] sc1\n"
"s_waitcnt vmcnt(3)\n"
"v_cmp_eq_u64 vcc, v[32:33], s[88:89]\n"
"s_nop 3\n"
"s_and_b32 s76, vcc_lo, s98\n"
"s_cmp_eq_u32 s76, s98\n"
"s_cbranch_scc1 POLLDX_%=\n"
"s_sleep 2\n"
"global_load_dwordx2 v[32:33], v76, s[92:93] sc1\n"
"s_waitcnt vmcnt(3)\n"
"v_cmp_eq_u64 vcc, v[34:35], s[88:89]\n"
"s_nop 3\n"
"s_and_b32 s76, vcc_lo, s98\n"
"s_cmp_eq_u32 s76, s98\n"
"s_cbranch_scc1 POLLDX_%=\n"
"s_sleep 2\n"
"global_load_dwordx2 v[34:35], v76, s[92:93] sc1\n"
"s_waitcnt vmcnt(3)\n"
"v_cmp_eq_u64 vcc, v[36:37], s[88:89]\n"
"s_nop 3\n"
"s_and_b32 s76, vcc_lo, s98\n"
"s_cmp_eq_u32 s76, s98\n"
"s_cbranch_scc1 POLLDX_%=\n"
"s_sleep 2\n"
"global_load_dwordx2 v[36:37], v76, s[92:93] sc1\n"
"s_waitcnt vmcnt(3)\n"
"v_cmp_eq_u64 vcc, v[38:39], s[88:89]\n"
"s_nop 3\n"
"s_and_b32 s76, vcc_lo, s98\n"
"s_cmp_eq_u32 s76, s98\n"
"s_cbranch_scc1 POLLDX_%=\n"
"s_sleep 2\n"
"s_add_u32 s99, s99, 1\n"
"s_cmp_gt_u32 s99, 0x40000\n"
"s_cbranch_scc0 POLLX_%=\n"
"POLLDX_%=:\n"
"s_add_u32 s98, s80, 0\n"
"s_lshl_b32 s98, s98, 10\n"
"s_add_u32 s99, s98, s91\n"
"s_add_u32 s98, s99, 0x12000\n"
"buffer_load_dwordx4 a[4:7], v20, s[52:55], s98 offen sc1\n"
"s_add_u32 s98, s99, 0x36000\n"
"buffer_load_dwordx4 a[12:15], v20, s[52:55], s98 offen sc1\n"
"s_add_u32 s98, s99, 0x5a000\n"
"buffer_load_dwordx4 a[20:23], v20, s[52:55], s98 offen sc1\n"
"s_add_u32 s98, s99, 0x7e000\n"
"buffer_load_dwordx4 a[28:31], v20, s[52:55], s98 offen sc1\n"
"s_add_u32 s98, s80, 8\n"
"s_lshl_b32 s98, s98, 10\n"
"s_add_u32 s99, s98, s91\n"
"s_add_u32 s98, s99, 0x12000\n"
"buffer_load_dwordx4 a[36:39], v20, s[52:55], s98 offen sc1\n"
"s_add_u32 s98, s99, 0x36000\n"
"buffer_load_dwordx4 a[44:47], v20, s[52:55], s98 offen sc1\n"
"s_add_u32 s98, s99, 0x5a000\n"
"buffer_load_dwordx4 a[52:55], v20, s[52:55], s98 offen sc1\n"
"s_add_u32 s98, s99, 0x7e000\n"
"buffer_load_dwordx4 a[60:63], v20, s[52:55], s98 offen sc1\n"
"s_cmp_ge_u32 s80, 2\n"
"s_cbranch_scc1 NOG21_%=\n"
"s_add_u32 s98, s80, 16\n"
"s_lshl_b32 s98, s98, 10\n"
"s_add_u32 s99, s98, s91\n"
"s_add_u32 s98, s99, 0x12000\n"
"buffer_load_dwordx4 a[68:71], v20, s[52:55], s98 offen sc1\n"
"s_add_u32 s98, s99, 0x36000\n"
"buffer_load_dwordx4 a[76:79], v20, s[52:55], s98 offen sc1\n"
"s_add_u32 s98, s99, 0x5a000\n"
"buffer_load_dwordx4 a[84:87], v20, s[52:55], s98 offen sc1\n"
"s_add_u32 s98, s99, 0x7e000\n"
"buffer_load_dwordx4 a[92:95], v20, s[52:55], s98 offen sc1\n"
"NOG21_%=:\n"
"s_add_u32 s98, s80, 0\n"
"s_lshl_b32 s98, s98, 10\n"
"s_add_u32 s99, s98, s91\n"
"s_add_u32 s98, s99, 0\n"
"buffer_load_dwordx4 a[0:3], v20, s[52:55], s98 offen sc1\n"
"s_add_u32 s98, s99, 0x24000\n"
"buffer_load_dwordx4 a[8:11], v20, s[52:55], s98 offen sc1\n"
"s_add_u32 s98, s99, 0x48000\n"
"buffer_load_dwordx4 a[16:19], v20, s[52:55], s98 offen sc1\n"
"s_add_u32 s98, s99, 0x6c000\n"
"buffer_load_dwordx4 a[24:27], v20, s[52:55], s98 offen sc1\n"
"s_add_u32 s98, s80, 8\n"
"s_lshl_b32 s98, s98, 10\n"
"s_add_u32 s99, s98, s91\n"
"s_add_u32 s98, s99, 0\n"
"buffer_load_dwordx4 a[32:35], v20, s[52:55], s98 offen sc1\n"
"s_add_u32 s98, s99, 0x24000\n"
"buffer_load_dwordx4 a[40:43], v20, s[52:55], s98 offen sc1\n"
"s_add_u32 s98, s99, 0x48000\n"
"buffer_load_dwordx4 a[48:51], v20, s[52:55], s98 offen sc1\n"
"s_add_u32 s98, s99, 0x6c000\n"
"buffer_load_dwordx4 a[56:59], v20, s[52:55], s98 offen sc1\n"
"s_cmp_ge_u32 s80, 2\n"
"s_cbranch_scc1 NOG20_%=\n"
"s_add_u32 s98, s80, 16\n"
"s_lshl_b32 s98, s98, 10\n"
"s_add_u32 s99, s98, s91\n"
"s_add_u32 s98, s99, 0\n"
"buffer_load_dwordx4 a[64:67], v20, s[52:55], s98 offen sc1\n"
"s_add_u32 s98, s99, 0x24000\n"
"buffer_load_dwordx4 a[72:75], v20, s[52:55], s98 offen sc1\n"
"s_add_u32 s98, s99, 0x48000\n"
"buffer_load_dwordx4 a[80:83], v20, s[52:55], s98 offen sc1\n"
"s_add_u32 s98, s99, 0x6c000\n"
"buffer_load_dwordx4 a[88:91], v20, s[52:55], s98 offen sc1\n"
"NOG20_%=:\n"
"s_waitcnt vmcnt(15)\n"
"v_mfma_f32_4x4x4_16b_bf16 v[112:115], v[12:13], a[4:5], 0\n"
"v_mfma_f32_4x4x4_16b_bf16 v[116:119], v[12:13], a[6:7], 0\n"
"s_waitcnt vmcnt(11)\n"
"v_mfma_f32_4x4x4_16b_bf16 v[120:123], v[12:13], a[36:37], 0\n"
"v_mfma_f32_4x4x4_16b_bf16 v[124:127], v[12:13], a[38:39], 0\n"
"s_waitcnt vmcnt(15)\n"
"v_mfma_f32_4x4x4_16b_bf16 v[128:131], v[12:13], a[68:69], 0\n"
"v_mfma_f32_4x4x4_16b_bf16 v[132:135], v[12:13], a[70:71], 0\n"
"v_mfma_f32_4x4x4_16b_bf16 v[112:115], v[12:13], a[12:13], v[112:115]\n"
"v_mfma_f32_4x4x4_16b_bf16 v[116:119], v[12:13], a[14:15], v[116:119]\n"
"s_waitcnt vmcnt(10)\n"
"v_mfma_f32_4x4x4_16b_bf16 v[120:123], v[12:13], a[44:45], v[120:123]\n"
"v_mfma_f32_4x4x4_16b_bf16 v[124:127], v[12:13], a[46:47], v[124:127]\n"
"s_waitcnt vmcnt(14)\n"
"v_mfma_f32_4x4x4_16b_bf16 v[128:131], v[12:13], a[76:77], v[128:131]\n"
"v_mfma_f32_4x4x4_16b_bf16 v[132:135], v[12:13], a[78:79], v[132:135]\n"
"v_mfma_f32_4x4x4_16b_bf16 v[112:115], v[12:13], a[20:21], v[112:115]\n"
"v_mfma_f32_4x4x4_16b_bf16 v[116:119], v[12:13], a[22:23], v[116:119]\n"
"s_waitcnt vmcnt(9)\n"
"v_mfma_f32_4x4x4_16b_bf16 v[120:123], v[12:13], a[52:53], v[120:123]\n"
"v_mfma_f32_4x4x4_16b_bf16 v[124:127], v[12:13], a[54:55], v[124:127]\n"
"s_waitcnt vmcnt(13)\n"
"v_mfma_f32_4x4x4_16b_bf16 v[128:131], v[12:13], a[84:85], v[128:131]\n"
"v_mfma_f32_4x4x4_16b_bf16 v[132:135], v[12:13], a[86:87], v[132:135]\n"
"v_mfma_f32_4x4x4_16b_bf16 v[112:115], v[12:13], a[28:29], v[112:115]\n"
"v_mfma_f32_4x4x4_16b_bf16 v[116:119], v[12:13], a[30:31], v[116:119]\n"
"s_waitcnt vmcnt(8)\n"
"v_mfma_f32_4x4x4_16b_bf16 v[120:123], v[12:13], a[60:61], v[120:123]\n"
"v_mfma_f32_4x4x4_16b_bf16 v[124:127], v[12:13], a[62:63], v[124:127]\n"
"s_waitcnt vmcnt(12)\n"
"v_mfma_f32_4x4x4_16b_bf16 v[128:131], v[12:13], a[92:93], v[128:131]\n"
"v_mfma_f32_4x4x4_16b_bf16 v[132:135], v[12:13], a[94:95], v[132:135]\n"
"v_mul_f32 v48, 0xbfb8aa3b, v112\n"
"v_mul_f32 v49, 0xbfb8aa3b, v113\n"
"v_mul_f32 v50, 0xbfb8aa3b, v114\n"
"v_mul_f32 v51, 0xbfb8aa3b, v115\n"
"v_exp_f32 v48, v48\n"
"v_exp_f32 v49, v49\n"
"v_exp_f32 v50, v50\n"
"v_exp_f32 v51, v51\n"
"v_add_f32 v48, 1.0, v48\n"
"v_add_f32 v49, 1.0, v49\n"
"v_add_f32 v50, 1.0, v50\n"
"v_add_f32 v51, 1.0, v51\n"
"v_rcp_f32 v48, v48\n"
"v_rcp_f32 v49, v49\n"
"v_rcp_f32 v50, v50\n"
"v_rcp_f32 v51, v51\n"
"v_mul_f32 v112, v112, v48\n"
"v_mul_f32 v113, v113, v49\n"
"v_mul_f32 v114, v114, v50\n"
"v_mul_f32 v115, v115, v51\n"
"v_mul_f32 v52, 0xbfb8aa3b, v116\n"
"v_mul_f32 v53, 0xbfb8aa3b, v117\n"
"v_mul_f32 v54, 0xbfb8aa3b, v118\n"
"v_mul_f32 v55, 0xbfb8aa3b, v119\n"
"v_exp_f32 v52, v52\n"
"v_exp_f32 v53, v53\n"
"v_exp_f32 v54, v54\n"
"v_exp_f32 v55, v55\n"
"v_add_f32 v52, 1.0, v52\n"
"v_add_f32 v53, 1.0, v53\n"
"v_add_f32 v54, 1.0, v54\n"
"v_add_f32 v55, 1.0, v55\n"
"v_rcp_f32 v52, v52\n"
"v_rcp_f32 v53, v53\n"
"v_rcp_f32 v54, v54\n"
"v_rcp_f32 v55, v55\n"
"v_mul_f32 v116, v116, v52\n"
"v_mul_f32 v117, v117, v53\n"
"v_mul_f32 v118, v118, v54\n"
"v_mul_f32 v119, v119, v55\n"
"v_mul_f32 v56, 0xbfb8aa3b, v120\n"
"v_mul_f32 v57, 0xbfb8aa3b, v121\n"
"v_mul_f32 v58, 0xbfb8aa3b, v122\n"
"v_mul_f32 v59, 0xbfb8aa3b, v123\n"
"v_exp_f32 v56, v56\n"
"v_exp_f32 v57, v57\n"
"v_exp_f32 v58, v58\n"
"v_exp_f32 v59, v59\n"
"v_add_f32 v56, 1.0, v56\n"
"v_add_f32 v57, 1.0, v57\n"
"v_add_f32 v58, 1.0, v58\n"
"v_add_f32 v59, 1.0, v59\n"
"v_rcp_f32 v56, v56\n"
"v_rcp_f32 v57, v57\n"
"v_rcp_f32 v58, v58\n"
"v_rcp_f32 v59, v59\n"
"v_mul_f32 v120, v120, v56\n"
"v_mul_f32 v121, v121, v57\n"
"v_mul_f32 v122, v122, v58\n"
"v_mul_f32 v123, v123, v59\n"
"v_mul_f32 v60, 0xbfb8aa3b, v124\n"
"v_mul_f32 v61, 0xbfb8aa3b, v125\n"
"v_mul_f32 v62, 0xbfb8aa3b, v126\n"
"v_mul_f32 v63, 0xbfb8aa3b, v127\n"
"v_exp_f32 v60, v60\n"
"v_exp_f32 v61, v61\n"
"v_exp_f32 v62, v62\n"
"v_exp_f32 v63, v63\n"
"v_add_f32 v60, 1.0, v60\n"
"v_add_f32 v61, 1.0, v61\n"
"v_add_f32 v62, 1.0, v62\n"
"v_add_f32 v63, 1.0, v63\n"
"v_rcp_f32 v60, v60\n"
"v_rcp_f32 v61, v61\n"
"v_rcp_f32 v62, v62\n"
"v_rcp_f32 v63, v63\n"
"v_mul_f32 v124, v124, v60\n"
"v_mul_f32 v125, v125, v61\n"
"v_mul_f32 v126, v126, v62\n"
"v_mul_f32 v127, v127, v63\n"
"v_mul_f32 v48, 0xbfb8aa3b, v128\n"
"v_mul_f32 v49, 0xbfb8aa3b, v129\n"
"v_mul_f32 v50, 0xbfb8aa3b, v130\n"
"v_mul_f32 v51, 0xbfb8aa3b, v131\n"
"v_exp_f32 v48, v48\n"
"v_exp_f32 v49, v49\n"
"v_exp_f32 v50, v50\n"
"v_exp_f32 v51, v51\n"
"v_add_f32 v48, 1.0, v48\n"
"v_add_f32 v49, 1.0, v49\n"
"v_add_f32 v50, 1.0, v50\n"
"v_add_f32 v51, 1.0, v51\n"
"v_rcp_f32 v48, v48\n"
"v_rcp_f32 v49, v49\n"
"v_rcp_f32 v50, v50\n"
"v_rcp_f32 v51, v51\n"
"v_mul_f32 v128, v128, v48\n"
"v_mul_f32 v129, v129, v49\n"
"v_mul_f32 v130, v130, v50\n"
"v_mul_f32 v131, v131, v51\n"
"v_mul_f32 v52, 0xbfb8aa3b, v132\n"
"v_mul_f32 v53, 0xbfb8aa3b, v133\n"
"v_mul_f32 v54, 0xbfb8aa3b, v134\n"
"v_mul_f32 v55, 0xbfb8aa3b, v135\n"
"v_exp_f32 v52, v52\n"
"v_exp_f32 v53, v53\n"
"v_exp_f32 v54, v54\n"
"v_exp_f32 v55, v55\n"
"v_add_f32 v52, 1.0, v52\n"
"v_add_f32 v53, 1.0, v53\n"
"v_add_f32 v54, 1.0, v54\n"
"v_add_f32 v55, 1.0, v55\n"
"v_rcp_f32 v52, v52\n"
"v_rcp_f32 v53, v53\n"
"v_rcp_f32 v54, v54\n"
"v_rcp_f32 v55, v55\n"
"v_mul_f32 v132, v132, v52\n"
"v_mul_f32 v133, v133, v53\n"
"v_mul_f32 v134, v134, v54\n"
"v_mul_f32 v135, v135, v55\n"
"s_waitcnt vmcnt(7)\n"
"v_mfma_f32_4x4x4_16b_bf16 v[136:139], v[12:13], a[0:1], 0\n"
"v_mfma_f32_4x4x4_16b_bf16 v[140:143], v[12:13], a[2:3], 0\n"
"s_waitcnt vmcnt(3)\n"
"v_mfma_f32_4x4x4_16b_bf16 v[144:147], v[12:13], a[32:33], 0\n"
"v_mfma_f32_4x4x4_16b_bf16 v[148:151], v[12:13], a[34:35], 0\n"
"s_waitcnt vmcnt(3)\n"
"v_mfma_f32_4x4x4_16b_bf16 v[152:155], v[12:13], a[64:65], 0\n"
"v_mfma_f32_4x4x4_16b_bf16 v[156:159], v[12:13], a[66:67], 0\n"
"v_mfma_f32_4x4x4_16b_bf16 v[136:139], v[12:13], a[8:9], v[136:139]\n"
"v_mfma_f32_4x4x4_16b_bf16 v[140:143], v[12:13], a[10:11], v[140:143]\n"
"s_waitcnt vmcnt(2)\n"
"v_mfma_f32_4x4x4_16b_bf16 v[144:147], v[12:13], a[40:41], v[144:147]\n"
"v_mfma_f32_4x4x4_16b_bf16 v[148:151], v[12:13], a[42:43], v[148:151]\n"
"s_waitcnt vmcnt(2)\n"
"v_mfma_f32_4x4x4_16b_bf16 v[152:155], v[12:13], a[72:73], v[152:155]\n"
"v_mfma_f32_4x4x4_16b_bf16 v[156:159], v[12:13], a[74:75], v[156:159]\n"
"v_mfma_f32_4x4x4_16b_bf16 v[136:139], v[12:13], a[16:17], v[136:139]\n"
"v_mfma_f32_4x4x4_16b_bf16 v[140:143], v[12:13], a[18:19], v[140:143]\n"
"s_waitcnt vmcnt(1)\n"
"v_mfma_f32_4x4x4_16b_bf16 v[144:147], v[12:13], a[48:49], v[144:147]\n"
"v_mfma_f32_4x4x4_16b_bf16 v[148:151], v[12:13], a[50:51], v[148:151]\n"
"s_waitcnt vmcnt(1)\n"
"v_mfma_f32_4x4x4_16b_bf16 v[152:155], v[12:13], a[80:81], v[152:155]\n"
"v_mfma_f32_4x4x4_16b_bf16 v[156:159], v[12:13], a[82:83], v[156:159]\n"
"v_mfma_f32_4x4x4_16b_bf16 v[136:139], v[12:13], a[24:25], v[136:139]\n"
"v_mfma_f32_4x4x4_16b_bf16 v[140:143], v[12:13], a[26:27], v[140:143]\n"
"s_waitcnt vmcnt(0)\n"
"v_mfma_f32_4x4x4_16b_bf16 v[144:147], v[12:13], a[56:57], v[144:147]\n"
"v_mfma_f32_4x4x4_16b_bf16 v[148:151], v[12:13], a[58:59], v[148:151]\n"
"s_waitcnt vmcnt(0)\n"
"v_mfma_f32_4x4x4_16b_bf16 v[152:155], v[12:13], a[88:89], v[152:155]\n"
"v_mfma_f32_4x4x4_16b_bf16 v[156:159], v[12:13], a[90:91], v[156:159]\n"
"v_lshlrev_b32 v164, 16, v80\n"
"v_and_b32 v165, 0xffff0000, v80\n"
"v_lshlrev_b32 v166, 16, v81\n"
"v_and_b32 v167, 0xffff0000, v81\n"
"v_fma_f32 v164, v136, v112, v164\n"
"v_fma_f32 v165, v137, v113, v165\n"
"v_fma_f32 v166, v138, v114, v166\n"
"v_fma_f32 v167, v139, v115, v167\n"
"v_cvt_pk_bf16_f32 v80, v164, v165\n"
"v_cvt_pk_bf16_f32 v81, v166, v167\n"
"s_add_u32 s76, s84, 0\n"
"buffer_store_dwordx2 v[80:81], v21, s[40:43], s76 offen\n"
"v_lshlrev_b32 v168, 16, v82\n"
"v_and_b32 v169, 0xffff0000, v82\n"
"v_lshlrev_b32 v170, 16, v83\n"
"v_and_b32 v171, 0xffff0000, v83\n"
"v_fma_f32 v168, v140, v116, v168\n"
"v_fma_f32 v169, v141, v117, v169\n"
"v_fma_f32 v170, v142, v118, v170\n"
"v_fma_f32 v171, v143, v119, v171\n"
"v_cvt_pk_bf16_f32 v82, v168, v169\n"
"v_cvt_pk_bf16_f32 v83, v170, v171\n"
"s_add_u32 s76, s84, 0x4800\n"
"buffer_store_dwordx2 v[82:83], v21, s[40:43], s76 offen\n"
"v_lshlrev_b32 v172, 16, v84\n"
"v_and_b32 v173, 0xffff0000, v84\n"
"v_lshlrev_b32 v174, 16, v85\n"
"v_and_b32 v175, 0xffff0000, v85\n"
"v_fma_f32 v172, v144, v120, v172\n"
"v_fma_f32 v173, v145, v121, v173\n"
"v_fma_f32 v174, v146, v122, v174\n"
"v_fma_f32 v175, v147, v123, v175\n"
"v_cvt_pk_bf16_f32 v84, v172, v173\n"
"v_cvt_pk_bf16_f32 v85, v174, v175\n"
"s_add_u32 s76, s85, 0\n"
"buffer_store_dwordx2 v[84:85], v21, s[40:43], s76 offen\n"
"v_lshlrev_b32 v164, 16, v86\n"
"v_and_b32 v165, 0xffff0000, v86\n"
"v_lshlrev_b32 v166, 16, v87\n"
"v_and_b32 v167, 0xffff0000, v87\n"
"v_fma_f32 v164, v148, v124, v164\n"
"v_fma_f32 v165, v149, v125, v165\n"
"v_fma_f32 v166, v150, v126, v166\n"
"v_fma_f32 v167, v151, v127, v167\n"
"v_cvt_pk_bf16_f32 v86, v164, v165\n"
"v_cvt_pk_bf16_f32 v87, v166, v167\n"
"s_add_u32 s76, s85, 0x4800\n"
"buffer_store_dwordx2 v[86:87], v21, s[40:43], s76 offen\n"
"v_lshlrev_b32 v168, 16, v88\n"
"v_and_b32 v169, 0xffff0000, v88\n"
"v_lshlrev_b32 v170, 16, v89\n"
"v_and_b32 v171, 0xffff0000, v89\n"
"v_fma_f32 v168, v152, v128, v168\n"
"v_fma_f32 v169, v153, v129, v169\n"
"v_fma_f32 v170, v154, v130, v170\n"
"v_fma_f32 v171, v155, v131, v171\n"
"v_cvt_pk_bf16_f32 v88, v168, v169\n"
"v_cvt_pk_bf16_f32 v89, v170, v171\n"
"s_cmp_ge_u32 s80, 2\n"
"s_cbranch_scc1 OUTEND_%=\n"
"s_add_u32 s76, s86, 0\n"
"buffer_store_dwordx2 v[88:89], v21, s[40:43], s76 offen\n"
"v_lshlrev_b32 v172, 16, v90\n"
"v_and_b32 v173, 0xffff0000, v90\n"
"v_lshlrev_b32 v174, 16, v91\n"
"v_and_b32 v175, 0xffff0000, v91\n"
"v_fma_f32 v172, v156, v132, v172\n"
"v_fma_f32 v173, v157, v133, v173\n"
"v_fma_f32 v174, v158, v134, v174\n"
"v_fma_f32 v175, v159, v135, v175\n"
"v_cvt_pk_bf16_f32 v90, v172, v173\n"
"v_cvt_pk_bf16_f32 v91, v174, v175\n"
"s_add_u32 s76, s86, 0x4800\n"
"buffer_store_dwordx2 v[90:91], v21, s[40:43], s76 offen\n"
"OUTEND_%=:\n"
"s_or_b32 s76, s72, s74\n"
"s_cmp_lg_u32 s76, 0\n"
"s_cbranch_scc1 NOEPO_%=\n"
"s_lshl_b32 s76, s75, 8\n"
"v_mov_b32 v72, s76\n"
"v_mov_b32 v74, s88\n"
"v_mov_b32 v75, s89\n"
"s_mov_b64 exec, 1\n"
"global_store_dwordx2 v72, v[74:75], s[92:93] sc1\n"
"s_mov_b64 exec, -1\n"
"NOEPO_%=:\n"
"s_waitcnt vmcnt(0)\n"
"s_branch KEND_%=\n"
"FALLBACK_%=:\n"
"s_waitcnt vmcnt(0) lgkmcnt(0)\n"
"s_barrier\n"
"s_mov_b32 s79, 1\n"
"s_lshl_b32 s78, s72, 12\n"
"s_mov_b32 s94, s78\n"
"v_mov_b32 v240, 0xff61b1e6\n"
"v_mov_b32 v244, 0xff61b1e6\n"
"v_mov_b32 v248, 0xff61b1e6\n"
"v_mov_b32 v252, 0xff61b1e6\n"
"s_mov_b32 s96, 32\n"
"FBL_%=:\n"
"s_add_u32 m0, s78, 0\n"
"s_nop 0\n"
"buffer_load_dwordx4 v110, s[40:43], s94 offen sc1 lds\n"
"s_add_u32 m0, s78, 0x400\n"
"s_nop 0\n"
"buffer_load_dwordx4 v111, s[40:43], s94 offen sc1 lds\n"
"s_add_u32 m0, s78, 0x800\n"
"s_nop 0\n"
"buffer_load_dwordx4 v112, s[40:43], s94 offen sc1 lds\n"
"s_add_u32 m0, s78, 0xc00\n"
"s_nop 0\n"
"buffer_load_dwordx4 v113, s[40:43], s94 offen sc1 lds\n"
"s_add_u32 s94, s94, 0x4000\n"
"s_waitcnt vmcnt(0)\n"
"s_barrier\n"
"ds_read_b128 a[192:195], v104\n"
"ds_read_b128 a[196:199], v105\n"
"ds_read_b128 a[200:203], v106\n"
"ds_read_b128 a[204:207], v107\n"
"ds_read_b128 a[208:211], v104 offset:4096\n"
"ds_read_b128 a[212:215], v105 offset:4096\n"
"ds_read_b128 a[216:219], v106 offset:4096\n"
"ds_read_b128 a[220:223], v107 offset:4096\n"
"ds_read_b128 a[224:227], v104 offset:8192\n"
"ds_read_b128 a[228:231], v105 offset:8192\n"
"ds_read_b128 a[232:235], v106 offset:8192\n"
"ds_read_b128 a[236:239], v107 offset:8192\n"
"ds_read_b128 a[240:243], v104 offset:12288\n"
"ds_read_b128 a[244:247], v105 offset:12288\n"
"ds_read_b128 a[248:251], v106 offset:12288\n"
"ds_read_b128 a[252:255], v107 offset:12288\n"
"s_waitcnt lgkmcnt(0)\n"
"v_mfma_f32_16x16x32_bf16 v[192:195], a[192:195], a[128:131], 0\n"
"v_mfma_f32_16x16x32_bf16 v[192:195], a[196:199], a[132:135], v[192:195]\n"
"v_mfma_f32_16x16x32_bf16 v[192:195], a[200:203], a[136:139], v[192:195]\n"
"v_mfma_f32_16x16x32_bf16 v[192:195], a[204:207], a[140:143], v[192:195]\n"
"v_mfma_f32_16x16x32_bf16 v[196:199], a[208:211], a[128:131], 0\n"
"v_mfma_f32_16x16x32_bf16 v[196:199], a[212:215], a[132:135], v[196:199]\n"
"v_mfma_f32_16x16x32_bf16 v[196:199], a[216:219], a[136:139], v[196:199]\n"
"v_mfma_f32_16x16x32_bf16 v[196:199], a[220:223], a[140:143], v[196:199]\n"
"v_mfma_f32_16x16x32_bf16 v[200:203], a[224:227], a[128:131], 0\n"
"v_mfma_f32_16x16x32_bf16 v[200:203], a[228:231], a[132:135], v[200:203]\n"
"v_mfma_f32_16x16x32_bf16 v[200:203], a[232:235], a[136:139], v[200:203]\n"
"v_mfma_f32_16x16x32_bf16 v[200:203], a[236:239], a[140:143], v[200:203]\n"
"v_mfma_f32_16x16x32_bf16 v[204:207], a[240:243], a[128:131], 0\n"
"v_mfma_f32_16x16x32_bf16 v[204:207], a[244:247], a[132:135], v[204:207]\n"
"v_mfma_f32_16x16x32_bf16 v[204:207], a[248:251], a[136:139], v[204:207]\n"
"v_mfma_f32_16x16x32_bf16 v[204:207], a[252:255], a[140:143], v[204:207]\n"
"v_max3_f32 v240, v240, v192, v193\n"
"v_max3_f32 v240, v240, v194, v195\n"
"v_max3_f32 v240, v240, v196, v197\n"
"v_max3_f32 v240, v240, v198, v199\n"
"v_max3_f32 v240, v240, v200, v201\n"
"v_max3_f32 v240, v240, v202, v203\n"
"s_nop 1\n"
"v_max3_f32 v240, v240, v204, v205\n"
"v_max3_f32 v240, v240, v206, v207\n"
"v_mfma_f32_16x16x32_bf16 v[208:211], a[192:195], a[144:147], 0\n"
"v_mfma_f32_16x16x32_bf16 v[208:211], a[196:199], a[148:151], v[208:211]\n"
"v_mfma_f32_16x16x32_bf16 v[208:211], a[200:203], a[152:155], v[208:211]\n"
"v_mfma_f32_16x16x32_bf16 v[208:211], a[204:207], a[156:159], v[208:211]\n"
"v_mfma_f32_16x16x32_bf16 v[212:215], a[208:211], a[144:147], 0\n"
"v_mfma_f32_16x16x32_bf16 v[212:215], a[212:215], a[148:151], v[212:215]\n"
"v_mfma_f32_16x16x32_bf16 v[212:215], a[216:219], a[152:155], v[212:215]\n"
"v_mfma_f32_16x16x32_bf16 v[212:215], a[220:223], a[156:159], v[212:215]\n"
"v_mfma_f32_16x16x32_bf16 v[216:219], a[224:227], a[144:147], 0\n"
"v_mfma_f32_16x16x32_bf16 v[216:219], a[228:231], a[148:151], v[216:219]\n"
"v_mfma_f32_16x16x32_bf16 v[216:219], a[232:235], a[152:155], v[216:219]\n"
"v_mfma_f32_16x16x32_bf16 v[216:219], a[236:239], a[156:159], v[216:219]\n"
"v_mfma_f32_16x16x32_bf16 v[220:223], a[240:243], a[144:147], 0\n"
"v_mfma_f32_16x16x32_bf16 v[220:223], a[244:247], a[148:151], v[220:223]\n"
"v_mfma_f32_16x16x32_bf16 v[220:223], a[248:251], a[152:155], v[220:223]\n"
"v_mfma_f32_16x16x32_bf16 v[220:223], a[252:255], a[156:159], v[220:223]\n"
"v_max3_f32 v244, v244, v208, v209\n"
"v_max3_f32 v244, v244, v210, v211\n"
"v_max3_f32 v244, v244, v212, v213\n"
"v_max3_f32 v244, v244, v214, v215\n"
"v_max3_f32 v244, v244, v216, v217\n"
"v_max3_f32 v244, v244, v218, v219\n"
"s_nop 1\n"
"v_max3_f32 v244, v244, v220, v221\n"
"v_max3_f32 v244, v244, v222, v223\n"
"v_mfma_f32_16x16x32_bf16 v[192:195], a[192:195], a[160:163], 0\n"
"v_mfma_f32_16x16x32_bf16 v[192:195], a[196:199], a[164:167], v[192:195]\n"
"v_mfma_f32_16x16x32_bf16 v[192:195], a[200:203], a[168:171], v[192:195]\n"
"v_mfma_f32_16x16x32_bf16 v[192:195], a[204:207], a[172:175], v[192:195]\n"
"v_mfma_f32_16x16x32_bf16 v[196:199], a[208:211], a[160:163], 0\n"
"v_mfma_f32_16x16x32_bf16 v[196:199], a[212:215], a[164:167], v[196:199]\n"
"v_mfma_f32_16x16x32_bf16 v[196:199], a[216:219], a[168:171], v[196:199]\n"
"v_mfma_f32_16x16x32_bf16 v[196:199], a[220:223], a[172:175], v[196:199]\n"
"v_mfma_f32_16x16x32_bf16 v[200:203], a[224:227], a[160:163], 0\n"
"v_mfma_f32_16x16x32_bf16 v[200:203], a[228:231], a[164:167], v[200:203]\n"
"v_mfma_f32_16x16x32_bf16 v[200:203], a[232:235], a[168:171], v[200:203]\n"
"v_mfma_f32_16x16x32_bf16 v[200:203], a[236:239], a[172:175], v[200:203]\n"
"v_mfma_f32_16x16x32_bf16 v[204:207], a[240:243], a[160:163], 0\n"
"v_mfma_f32_16x16x32_bf16 v[204:207], a[244:247], a[164:167], v[204:207]\n"
"v_mfma_f32_16x16x32_bf16 v[204:207], a[248:251], a[168:171], v[204:207]\n"
"v_mfma_f32_16x16x32_bf16 v[204:207], a[252:255], a[172:175], v[204:207]\n"
"v_max3_f32 v248, v248, v192, v193\n"
"v_max3_f32 v248, v248, v194, v195\n"
"v_max3_f32 v248, v248, v196, v197\n"
"v_max3_f32 v248, v248, v198, v199\n"
"v_max3_f32 v248, v248, v200, v201\n"
"v_max3_f32 v248, v248, v202, v203\n"
"s_nop 1\n"
"v_max3_f32 v248, v248, v204, v205\n"
"v_max3_f32 v248, v248, v206, v207\n"
"v_mfma_f32_16x16x32_bf16 v[208:211], a[192:195], a[176:179], 0\n"
"v_mfma_f32_16x16x32_bf16 v[208:211], a[196:199], a[180:183], v[208:211]\n"
"v_mfma_f32_16x16x32_bf16 v[208:211], a[200:203], a[184:187], v[208:211]\n"
"v_mfma_f32_16x16x32_bf16 v[208:211], a[204:207], a[188:191], v[208:211]\n"
"v_mfma_f32_16x16x32_bf16 v[212:215], a[208:211], a[176:179], 0\n"
"v_mfma_f32_16x16x32_bf16 v[212:215], a[212:215], a[180:183], v[212:215]\n"
"v_mfma_f32_16x16x32_bf16 v[212:215], a[216:219], a[184:187], v[212:215]\n"
"v_mfma_f32_16x16x32_bf16 v[212:215], a[220:223], a[188:191], v[212:215]\n"
"v_mfma_f32_16x16x32_bf16 v[216:219], a[224:227], a[176:179], 0\n"
"v_mfma_f32_16x16x32_bf16 v[216:219], a[228:231], a[180:183], v[216:219]\n"
"v_mfma_f32_16x16x32_bf16 v[216:219], a[232:235], a[184:187], v[216:219]\n"
"v_mfma_f32_16x16x32_bf16 v[216:219], a[236:239], a[188:191], v[216:219]\n"
"v_mfma_f32_16x16x32_bf16 v[220:223], a[240:243], a[176:179], 0\n"
"v_mfma_f32_16x16x32_bf16 v[220:223], a[244:247], a[180:183], v[220:223]\n"
"v_mfma_f32_16x16x32_bf16 v[220:223], a[248:251], a[184:187], v[220:223]\n"
"v_mfma_f32_16x16x32_bf16 v[220:223], a[252:255], a[188:191], v[220:223]\n"
"v_max3_f32 v252, v252, v208, v209\n"
"v_max3_f32 v252, v252, v210, v211\n"
"v_max3_f32 v252, v252, v212, v213\n"
"v_max3_f32 v252, v252, v214, v215\n"
"v_max3_f32 v252, v252, v216, v217\n"
"v_max3_f32 v252, v252, v218, v219\n"
"s_nop 1\n"
"v_max3_f32 v252, v252, v220, v221\n"
"v_max3_f32 v252, v252, v222, v223\n"
"s_barrier\n"
"s_sub_u32 s96, s96, 1\n"
"s_cmp_lg_u32 s96, 0\n"
"s_cbranch_scc1 FBL_%=\n"
"v_mov_b32 v224, v240\n"
"v_mov_b32 v226, v244\n"
"v_mov_b32 v228, v248\n"
"v_mov_b32 v230, v252\n"
"v_mov_b32 v225, v224\n"
"v_mov_b32 v227, v226\n"
"v_mov_b32 v229, v228\n"
"v_mov_b32 v231, v230\n"
"v_permlane32_swap_b32 v224, v225\n"
"v_permlane32_swap_b32 v226, v227\n"
"v_permlane32_swap_b32 v228, v229\n"
"v_permlane32_swap_b32 v230, v231\n"
"v_max_f32 v224, v224, v225\n"
"v_max_f32 v226, v226, v227\n"
"v_max_f32 v228, v228, v229\n"
"v_max_f32 v230, v230, v231\n"
"v_mov_b32 v225, v224\n"
"v_mov_b32 v227, v226\n"
"v_mov_b32 v229, v228\n"
"v_mov_b32 v231, v230\n"
"v_permlane16_swap_b32 v224, v225\n"
"v_permlane16_swap_b32 v226, v227\n"
"v_permlane16_swap_b32 v228, v229\n"
"v_permlane16_swap_b32 v230, v231\n"
"v_max_f32 v224, v224, v225\n"
"v_max_f32 v226, v226, v227\n"
"v_max_f32 v228, v228, v229\n"
"v_max_f32 v230, v230, v231\n"
"v_sub_f32 v240, 0, v224\n"
"v_sub_f32 v241, 0, v224\n"
"v_sub_f32 v242, 0, v224\n"
"v_sub_f32 v243, 0, v224\n"
"v_sub_f32 v244, 0, v226\n"
"v_sub_f32 v245, 0, v226\n"
"v_sub_f32 v246, 0, v226\n"
"v_sub_f32 v247, 0, v226\n"
"v_sub_f32 v248, 0, v228\n"
"v_sub_f32 v249, 0, v228\n"
"v_sub_f32 v250, 0, v228\n"
"v_sub_f32 v251, 0, v228\n"
"v_sub_f32 v252, 0, v230\n"
"v_sub_f32 v253, 0, v230\n"
"v_sub_f32 v254, 0, v230\n"
"v_sub_f32 v255, 0, v230\n"
"s_mov_b32 s94, s78\n"
"s_mov_b32 s95, s78\n"
"s_add_u32 m0, s78, 0\n"
"s_add_u32 s77, s78, 0\n"
"buffer_load_dwordx4 v110, s[40:43], s77 offen sc1 lds\n"
"s_add_u32 m0, s78, 0x400\n"
"s_add_u32 s77, s78, 0\n"
"buffer_load_dwordx4 v111, s[40:43], s77 offen sc1 lds\n"
"s_add_u32 m0, s78, 0x800\n"
"s_add_u32 s77, s78, 0\n"
"buffer_load_dwordx4 v112, s[40:43], s77 offen sc1 lds\n"
"s_add_u32 m0, s78, 0xc00\n"
"s_add_u32 s77, s78, 0\n"
"buffer_load_dwordx4 v113, s[40:43], s77 offen sc1 lds\n"
"s_add_u32 m0, s78, 0x10000\n"
"s_add_u32 s77, s78, 0\n"
"buffer_load_dwordx4 v114, s[44:47], s77 offen sc1 lds\n"
"s_add_u32 m0, s78, 0x10400\n"
"s_add_u32 s77, s78, 0\n"
"buffer_load_dwordx4 v115, s[44:47], s77 offen sc1 lds\n"
"s_add_u32 m0, s78, 0x10800\n"
"s_add_u32 s77, s78, 0\n"
"buffer_load_dwordx4 v116, s[44:47], s77 offen sc1 lds\n"
"s_add_u32 m0, s78, 0x10c00\n"
"s_add_u32 s77, s78, 0\n"
"buffer_load_dwordx4 v117, s[44:47], s77 offen sc1 lds\n"
"s_add_u32 m0, s78, 0x4000\n"
"s_add_u32 s77, s78, 0x4000\n"
"buffer_load_dwordx4 v110, s[40:43], s77 offen sc1 lds\n"
"s_add_u32 m0, s78, 0x4400\n"
"s_add_u32 s77, s78, 0x4000\n"
"buffer_load_dwordx4 v111, s[40:43], s77 offen sc1 lds\n"
"s_add_u32 m0, s78, 0x4800\n"
"s_add_u32 s77, s78, 0x4000\n"
"buffer_load_dwordx4 v112, s[40:43], s77 offen sc1 lds\n"
"s_add_u32 m0, s78, 0x4c00\n"
"s_add_u32 s77, s78, 0x4000\n"
"buffer_load_dwordx4 v113, s[40:43], s77 offen sc1 lds\n"
"s_add_u32 m0, s78, 0x14000\n"
"s_add_u32 s77, s78, 0x4000\n"
"buffer_load_dwordx4 v114, s[44:47], s77 offen sc1 lds\n"
"s_add_u32 m0, s78, 0x14400\n"
"s_add_u32 s77, s78, 0x4000\n"
"buffer_load_dwordx4 v115, s[44:47], s77 offen sc1 lds\n"
"s_add_u32 m0, s78, 0x14800\n"
"s_add_u32 s77, s78, 0x4000\n"
"buffer_load_dwordx4 v116, s[44:47], s77 offen sc1 lds\n"
"s_add_u32 m0, s78, 0x14c00\n"
"s_add_u32 s77, s78, 0x4000\n"
"buffer_load_dwordx4 v117, s[44:47], s77 offen sc1 lds\n"
"s_add_u32 m0, s78, 0x8000\n"
"s_add_u32 s77, s78, 0x8000\n"
"buffer_load_dwordx4 v110, s[40:43], s77 offen sc1 lds\n"
"s_add_u32 m0, s78, 0x8400\n"
"s_add_u32 s77, s78, 0x8000\n"
"buffer_load_dwordx4 v111, s[40:43], s77 offen sc1 lds\n"
"s_add_u32 m0, s78, 0x8800\n"
"s_add_u32 s77, s78, 0x8000\n"
"buffer_load_dwordx4 v112, s[40:43], s77 offen sc1 lds\n"
"s_add_u32 m0, s78, 0x8c00\n"
"s_add_u32 s77, s78, 0x8000\n"
"buffer_load_dwordx4 v113, s[40:43], s77 offen sc1 lds\n"
"s_add_u32 m0, s78, 0xc000\n"
"s_add_u32 s77, s78, 0xc000\n"
"buffer_load_dwordx4 v110, s[40:43], s77 offen sc1 lds\n"
"s_add_u32 m0, s78, 0xc400\n"
"s_add_u32 s77, s78, 0xc000\n"
"buffer_load_dwordx4 v111, s[40:43], s77 offen sc1 lds\n"
"s_add_u32 m0, s78, 0xc800\n"
"s_add_u32 s77, s78, 0xc000\n"
"buffer_load_dwordx4 v112, s[40:43], s77 offen sc1 lds\n"
"s_add_u32 m0, s78, 0xcc00\n"
"s_add_u32 s77, s78, 0xc000\n"
"buffer_load_dwordx4 v113, s[40:43], s77 offen sc1 lds\n"
"s_add_u32 s94, s94, 0x10000\n"
"s_add_u32 s95, s95, 0x10000\n"
"s_waitcnt vmcnt(0)\n"
"s_barrier\n"
"ds_read_b128 a[192:195], v104\n"
"ds_read_b128 a[196:199], v105\n"
"ds_read_b128 a[200:203], v106\n"
"ds_read_b128 a[204:207], v107\n"
"ds_read_b128 a[208:211], v104 offset:4096\n"
"ds_read_b128 a[212:215], v105 offset:4096\n"
"ds_read_b128 a[216:219], v106 offset:4096\n"
"ds_read_b128 a[220:223], v107 offset:4096\n"
"ds_read_b128 a[224:227], v104 offset:8192\n"
"ds_read_b128 a[228:231], v105 offset:8192\n"
"ds_read_b128 a[232:235], v106 offset:8192\n"
"ds_read_b128 a[236:239], v107 offset:8192\n"
"ds_read_b128 a[240:243], v104 offset:12288\n"
"ds_read_b128 a[244:247], v105 offset:12288\n"
"ds_read_b128 a[248:251], v106 offset:12288\n"
"ds_read_b128 a[252:255], v107 offset:12288\n"
"ds_read_b64_tr_b16 v[16:17], v108\n"
"ds_read_b64_tr_b16 v[18:19], v108 offset:4096\n"
"ds_read_b64_tr_b16 v[20:21], v108 offset:8192\n"
"ds_read_b64_tr_b16 v[22:23], v108 offset:12288\n"
"ds_read_b64_tr_b16 v[24:25], v109\n"
"ds_read_b64_tr_b16 v[26:27], v109 offset:4096\n"
"ds_read_b64_tr_b16 v[28:29], v109 offset:8192\n"
"ds_read_b64_tr_b16 v[30:31], v109 offset:12288\n"
"ds_read_b64_tr_b16 v[32:33], v108 offset:512\n"
"ds_read_b64_tr_b16 v[34:35], v108 offset:4608\n"
"ds_read_b64_tr_b16 v[36:37], v108 offset:8704\n"
"ds_read_b64_tr_b16 v[38:39], v108 offset:12800\n"
"ds_read_b64_tr_b16 v[40:41], v109 offset:512\n"
"ds_read_b64_tr_b16 v[42:43], v109 offset:4608\n"
"ds_read_b64_tr_b16 v[44:45], v109 offset:8704\n"
"ds_read_b64_tr_b16 v[46:47], v109 offset:12800\n"
"ds_read_b64_tr_b16 v[48:49], v108 offset:1024\n"
"ds_read_b64_tr_b16 v[50:51], v108 offset:5120\n"
"ds_read_b64_tr_b16 v[52:53], v108 offset:9216\n"
"ds_read_b64_tr_b16 v[54:55], v108 offset:13312\n"
"ds_read_b64_tr_b16 v[56:57], v109 offset:1024\n"
"ds_read_b64_tr_b16 v[58:59], v109 offset:5120\n"
"ds_read_b64_tr_b16 v[60:61], v109 offset:9216\n"
"ds_read_b64_tr_b16 v[62:63], v109 offset:13312\n"
"ds_read_b64_tr_b16 v[64:65], v108 offset:1536\n"
"ds_read_b64_tr_b16 v[66:67], v108 offset:5632\n"
"ds_read_b64_tr_b16 v[68:69], v108 offset:9728\n"
"ds_read_b64_tr_b16 v[70:71], v108 offset:13824\n"
"ds_read_b64_tr_b16 v[72:73], v109 offset:1536\n"
"ds_read_b64_tr_b16 v[74:75], v109 offset:5632\n"
"ds_read_b64_tr_b16 v[76:77], v109 offset:9728\n"
"ds_read_b64_tr_b16 v[78:79], v109 offset:13824\n"
"v_mov_b32 v96, 0x3f803f80\n"
"v_mov_b32 v97, 0x3f803f80\n"
"v_mov_b32 v98, 0x3f803f80\n"
"v_mov_b32 v99, 0x3f803f80\n"
"s_waitcnt lgkmcnt(0)\n"
"s_branch ATT_RESTART_%=\n"
"KEND_%=:\n"
  :: "s"(x), "s"(k), "s"(v), "s"(wq), "s"(wo), "s"(w1), "s"(w3), "s"(w2), "s"(ws), "s"(out), "v"(threadIdx.x), "s"(blockIdx.x), "s"((unsigned)(size_t)lds)
  : "v4", "v5", "v6", "v7", "v8", "v9", "v10", "v11", "v12", "v13", "v14", "v15", "v16", "v17", "v18", "v19", "v20", "v21", "v22", "v23", "v24", "v25", "v26", "v27", "v28", "v29", "v30", "v31", "v32", "v33", "v34", "v35", "v36", "v37", "v38", "v39", "v40", "v41", "v42", "v43", "v44", "v45", "v46", "v47", "v48", "v49", "v50", "v51", "v52", "v53", "v54", "v55", "v56", "v57", "v58", "v59", "v60", "v61", "v62", "v63", "v64", "v65", "v66", "v67", "v68", "v69", "v70", "v71", "v72", "v73", "v74", "v75", "v76", "v77", "v78", "v79", "v80", "v81", "v82", "v83", "v84", "v85", "v86", "v87", "v88", "v89", "v90", "v91", "v92", "v93", "v94", "v95", "v96", "v97", "v98", "v99", "v100", "v101", "v102", "v103", "v104", "v105", "v106", "v107", "v108", "v109", "v110", "v111", "v112", "v113", "v114", "v115", "v116", "v117", "v118", "v119", "v120", "v121", "v122", "v123", "v124", "v125", "v126", "v127", "v128", "v129", "v130", "v131", "v132", "v133", "v134", "v135", "v136", "v137", "v138", "v139", "v140", "v141", "v142", "v143", "v144", "v145", "v146", "v147", "v148", "v149", "v150", "v151", "v152", "v153", "v154", "v155", "v156", "v157", "v158", "v159", "v160", "v161", "v162", "v163", "v164", "v165", "v166", "v167", "v168", "v169", "v170", "v171", "v172", "v173", "v174", "v175", "v176", "v177", "v178", "v179", "v180", "v181", "v182", "v183", "v184", "v185", "v186", "v187", "v188", "v189", "v190", "v191", "v192", "v193", "v194", "v195", "v196", "v197", "v198", "v199", "v200", "v201", "v202", "v203", "v204", "v205", "v206", "v207", "v208", "v209", "v210", "v211", "v212", "v213", "v214", "v215", "v216", "v217", "v218", "v219", "v220", "v221", "v222", "v223", "v224", "v225", "v226", "v227", "v228", "v229", "v230", "v231", "v232", "v233", "v234", "v235", "v236", "v237", "v238", "v239", "v240", "v241", "v242", "v243", "v244", "v245", "v246", "v247", "v248", "v249", "v250", "v251", "v252", "v253", "v254", "v255", "a0", "a1", "a2", "a3", "a4", "a5", "a6", "a7", "a8", "a9", "a10", "a11", "a12", "a13", "a14", "a15", "a16", "a17", "a18", "a19", "a20", "a21", "a22", "a23", "a24", "a25", "a26", "a27", "a28", "a29", "a30", "a31", "a32", "a33", "a34", "a35", "a36", "a37", "a38", "a39", "a40", "a41", "a42", "a43", "a44", "a45", "a46", "a47", "a48", "a49", "a50", "a51", "a52", "a53", "a54", "a55", "a56", "a57", "a58", "a59", "a60", "a61", "a62", "a63", "a64", "a65", "a66", "a67", "a68", "a69", "a70", "a71", "a72", "a73", "a74", "a75", "a76", "a77", "a78", "a79", "a80", "a81", "a82", "a83", "a84", "a85", "a86", "a87", "a88", "a89", "a90", "a91", "a92", "a93", "a94", "a95", "a96", "a97", "a98", "a99", "a100", "a101", "a102", "a103", "a104", "a105", "a106", "a107", "a108", "a109", "a110", "a111", "a112", "a113", "a114", "a115", "a116", "a117", "a118", "a119", "a120", "a121", "a122", "a123", "a124", "a125", "a126", "a127", "a128", "a129", "a130", "a131", "a132", "a133", "a134", "a135", "a136", "a137", "a138", "a139", "a140", "a141", "a142", "a143", "a144", "a145", "a146", "a147", "a148", "a149", "a150", "a151", "a152", "a153", "a154", "a155", "a156", "a157", "a158", "a159", "a160", "a161", "a162", "a163", "a164", "a165", "a166", "a167", "a168", "a169", "a170", "a171", "a172", "a173", "a174", "a175", "a176", "a177", "a178", "a179", "a180", "a181", "a182", "a183", "a184", "a185", "a186", "a187", "a188", "a189", "a190", "a191", "a192", "a193", "a194", "a195", "a196", "a197", "a198", "a199", "a200", "a201", "a202", "a203", "a204", "a205", "a206", "a207", "a208", "a209", "a210", "a211", "a212", "a213", "a214", "a215", "a216", "a217", "a218", "a219", "a220", "a221", "a222", "a223", "a224", "a225", "a226", "a227", "a228", "a229", "a230", "a231", "a232", "a233", "a234", "a235", "a236", "a237", "a238", "a239", "a240", "a241", "a242", "a243", "a244", "a245", "a246", "a247", "a248", "a249", "a250", "a251", "a252", "a253", "a254", "a255", "s40", "s41", "s42", "s43", "s44", "s45", "s46", "s47", "s48", "s49", "s50", "s51", "s52", "s53", "s54", "s55", "s56", "s57", "s58", "s59", "s60", "s61", "s62", "s63", "s64", "s65", "s66", "s67", "s68", "s69", "s70", "s71", "s72", "s73", "s74", "s75", "s76", "s77", "s78", "s79", "s80", "s81", "s82", "s83", "s84", "s85", "s86", "s87", "s88", "s89", "s90", "s91", "s92", "s93", "s94", "s95", "s96", "s97", "s98", "s99", "vcc", "memory");
}

extern "C" void kernel_launch(void* const* d_in, const int* in_sizes, int n_in, void* d_out, int out_size, void* d_ws, size_t ws_size, hipStream_t stream) {
  char* ws = (char*)d_ws + 0;
  k_main<<<256, 256, 0, stream>>>(d_in[0], d_in[1], d_in[2], d_in[3], d_in[4], d_in[6], d_in[7], d_in[8], ws, d_out);
}
```
